```python
import torch

def _softmax(x, axis=-1):
    return torch.softmax(x, dim=axis)
def _gelu(x):
    return torch.nn.functional.gelu(x, approximate="tanh")
def _silu(x):
    return torch.nn.functional.silu(x)
def _sigmoid(x):
    return torch.sigmoid(x)
def _layernorm(x, axis=-1, eps=1e-5):
    m = x.mean(dim=axis, keepdim=True)
    v = x.var(dim=axis, keepdim=True, unbiased=False)
    return (x - m) / torch.sqrt(v + eps)
def _scatter_add(tgt, idx, src, axis=0):
    return tgt.clone().index_add_(axis, idx, src)
def _segment_sum(src, seg, n):
    out = torch.zeros((n,) + src.shape[1:], dtype=src.dtype)
    return out.index_add_(0, seg, src)
def _pad(x, pw):
    flat = tuple(v for pair in reversed(pw) for v in pair)
    return torch.nn.functional.pad(x, flat)

kernel_name = 'recurrent_scan__compressed_kv_cla__dense_activation_mish__prompt'

def setup_inputs():

    _w = (lambda a: (lambda t: t.to(torch.bfloat16) if t.is_floating_point() else t)(torch.as_tensor(a)))
    def _A_setup():
        B, H, Sq, Sk, D, M = 1, 32, 2048, 2048, 128, 640
        Dc = 4 * D
        return {
            "x": _w((lambda *s: torch.randn(*s, dtype=torch.bfloat16) * 0.05)(B, Sq, M)),
            "c_kv": _w((lambda *s: torch.randn(*s, dtype=torch.bfloat16) * 0.8)(B, Sk, Dc)),
            "w_q": _w((lambda *s: torch.randn(*s, dtype=torch.bfloat16) * 1.6)(M, H, D) * (M ** -0.5)),
            "w_uk": _w((lambda *s: torch.randn(*s, dtype=torch.bfloat16) * 1.6)(Dc, H, D) * (Dc ** -0.5)),
            "w_uv": _w((lambda *s: torch.randn(*s, dtype=torch.bfloat16) * 0.8)(Dc, H, D) * (Dc ** -0.5)),
            "w_o": _w((lambda *s: torch.randn(*s, dtype=torch.bfloat16) * 0.8)(H, D, M) * ((H * D) ** -0.5)),
        }
    _A_in = _A_setup()

    def _B_setup():
        B, S, D, F = 1, 2048, 640, 1728
        return {
            "x": _w((lambda *s: torch.randn(*s, dtype=torch.bfloat16) * 0.05)(B, S, D)),
            "w1": _w((lambda *s: torch.randn(*s, dtype=torch.bfloat16) * 0.8)(D, F) * (D ** -0.5)),
            "b1": _w((lambda *s: torch.randn(*s, dtype=torch.bfloat16) * 0.8)(F)),
            "w2": _w((lambda *s: torch.randn(*s, dtype=torch.bfloat16) * 0.2)(F, D) * (F ** -0.5)),
        }
    _B_in = _B_setup()

    return {
        "A_x": _A_in["x"],
        "A_c_kv": _A_in["c_kv"],
        "A_w_q": _A_in["w_q"],
        "A_w_uk": _A_in["w_uk"],
        "A_w_uv": _A_in["w_uv"],
        "A_w_o": _A_in["w_o"],
        "B_x": _B_in["x"],
        "B_w1": _B_in["w1"],
        "B_b1": _B_in["b1"],
        "B_w2": _B_in["w2"],
    }

def reference(A_x, A_c_kv, A_w_q, A_w_uk, A_w_uv, A_w_o, B_x, B_w1, B_b1, B_w2):
    def _A_ref(x, c_kv, w_q, w_uk, w_uv, w_o):
        B, Sq, M = x.shape[0], x.shape[1], x.shape[2]
        Sk = c_kv.shape[1]
        H, D = w_q.shape[1], w_q.shape[2]
        q = torch.einsum("bqm,mhd->bhqd", x, w_q)
        k = torch.einsum("bkc,chd->bhkd", c_kv, w_uk)
        v = torch.einsum("bkc,chd->bhkd", c_kv, w_uv)
        scores = torch.einsum("bhqd,bhkd->bhqk", q, k) * (D ** -0.5)
        off = Sk - Sq
        qi = torch.arange(Sq)[:, None]
        kj = torch.arange(Sk)[None, :]
        cmask = kj <= (qi + off)
        scores = torch.where(cmask[None, None], scores, -1e9)
        p = _softmax(scores, axis=-1)
        ctx = torch.einsum("bhqk,bhkd->bhqd", p, v)
        return torch.einsum("bhqd,hdm->bqm", ctx, w_o)

    def _B_ref(x, w1, b1, w2):
        h = torch.einsum("bsd,df->bsf", x, w1) + b1
        sp = torch.log1p(torch.exp(-torch.abs(h))) + torch.where(h > 0, h, torch.zeros_like(h))
        act = h * torch.tanh(sp)
        return torch.einsum("bsf,fd->bsd", act, w2)

    x = A_x
    xn = _layernorm(x, axis=-1)
    s = _A_ref(xn, A_c_kv, A_w_q, A_w_uk, A_w_uv, A_w_o)
    g = _silu(_B_ref(xn, B_w1, B_b1, B_w2))
    return x + s * g
```

```cpp
#include <hip/hip_runtime.h>
#include <hip/hip_bf16.h>
#include <stdint.h>
typedef unsigned short bf16_t;
__device__ __forceinline__ float bf2f(bf16_t b){ return __uint_as_float(((unsigned)b)<<16); }
__device__ __forceinline__ bf16_t f2bf(float f){ unsigned u=__float_as_uint(f); u += 0x7FFFu + ((u>>16)&1u); return (bf16_t)(u>>16); }

__global__ void ln_kernel(const bf16_t* x, bf16_t* xn){
  int row = blockIdx.x*4 + (threadIdx.x>>6), lane = threadIdx.x&63;
  const bf16_t* xr = x + (size_t)row*640;
  float v[10]; float s=0;
  for(int i=0;i<10;i++){ v[i]=bf2f(xr[lane+64*i]); s+=v[i]; }
  for(int o=32;o;o>>=1) s+=__shfl_xor(s,o);
  float m=s/640.f; float q=0;
  for(int i=0;i<10;i++){ float d=v[i]-m; q+=d*d; }
  for(int o=32;o;o>>=1) q+=__shfl_xor(q,o);
  float r=1.f/sqrtf(q/640.f+1e-5f);
  for(int i=0;i<10;i++) xn[(size_t)row*640+lane+64*i]=f2bf((v[i]-m)*r);
}
template<int MODE>
__global__ void gemm_kernel(const bf16_t* A, const bf16_t* W, void* C, const bf16_t* bias, int M,int N,int K,int ldw, float scale){
  __shared__ float As[16][65], Ws[16][65];
  int tx=threadIdx.x&15, ty=threadIdx.x>>4;
  int m0=blockIdx.y*64, n0=blockIdx.x*64;
  float acc[4][4]={};
  for(int k0=0;k0<K;k0+=16){
    for(int i=threadIdx.x;i<1024;i+=256){ int r=i>>4,c=i&15; As[c][r]=bf2f(A[(size_t)(m0+r)*K+k0+c]); }
    for(int i=threadIdx.x;i<1024;i+=256){ int r=i>>6,c=i&63; Ws[r][c]=bf2f(W[(size_t)(k0+r)*ldw+n0+c]); }
    __syncthreads();
    for(int k=0;k<16;k++){
      float a[4],b[4];
      for(int i=0;i<4;i++){a[i]=As[k][ty*4+i]; b[i]=Ws[k][tx*4+i];}
      for(int i=0;i<4;i++)for(int j=0;j<4;j++)acc[i][j]+=a[i]*b[j];
    }
    __syncthreads();
  }
  for(int i=0;i<4;i++)for(int j=0;j<4;j++){
    int m=m0+ty*4+i,n=n0+tx*4+j; float v=acc[i][j];
    if(MODE==0) ((bf16_t*)C)[(size_t)m*N+n]=f2bf(v*scale);
    else if(MODE==1){ float h=v+bf2f(bias[n]); float sp=log1pf(expf(-fabsf(h)))+fmaxf(h,0.f); ((bf16_t*)C)[(size_t)m*N+n]=f2bf(h*tanhf(sp)); }
    else ((float*)C)[(size_t)m*N+n]=v;
  }
}
__global__ void attn_kernel(const bf16_t* Q,const bf16_t* Kt,const bf16_t* V,bf16_t* ctx){
  __shared__ float sc[2048]; __shared__ float qv[128]; __shared__ float red[2];
  int q=blockIdx.x,h=blockIdx.y,t=threadIdx.x;
  qv[t]=bf2f(Q[(size_t)q*4096+h*128+t]);
  __syncthreads();
  float mx=-1e30f;
  for(int k=t;k<=q;k+=128){ const bf16_t* kr=Kt+(size_t)k*4096+h*128; float s=0; for(int d=0;d<128;d++) s+=qv[d]*bf2f(kr[d]); sc[k]=s; mx=fmaxf(mx,s);}
  for(int o=32;o;o>>=1) mx=fmaxf(mx,__shfl_xor(mx,o));
  if((t&63)==0) red[t>>6]=mx; __syncthreads(); mx=fmaxf(red[0],red[1]); __syncthreads();
  float l=0;
  for(int k=t;k<=q;k+=128){ float p=exp2f(sc[k]-mx); l+=p; sc[k]=bf2f(f2bf(p)); }
  for(int o=32;o;o>>=1) l+=__shfl_xor(l,o);
  if((t&63)==0) red[t>>6]=l; __syncthreads(); l=red[0]+red[1];
  float o=0;
  for(int k=0;k<=q;k++) o+=sc[k]*bf2f(V[(size_t)k*4096+h*128+t]);
  ctx[(size_t)q*4096+h*128+t]=f2bf(o/l);
}
__global__ void final_kernel(const bf16_t* x,const float* s,const float* g,bf16_t* out,int n){
  int i=blockIdx.x*256+threadIdx.x; if(i<n){ float gg=g[i]; gg=gg/(1.f+expf(-gg)); out[i]=f2bf(bf2f(x[i])+s[i]*gg);} }

extern "C" void kernel_launch(void* const* d_in, const int* in_sizes, int n_in, void* d_out, int out_size, void* d_ws, size_t ws_size, hipStream_t stream){
  const bf16_t* x=(const bf16_t*)d_in[0]; const bf16_t* ckv=(const bf16_t*)d_in[1];
  const bf16_t* wq=(const bf16_t*)d_in[2]; const bf16_t* wuk=(const bf16_t*)d_in[3]; const bf16_t* wuv=(const bf16_t*)d_in[4]; const bf16_t* wo=(const bf16_t*)d_in[5];
  const bf16_t* w1=(const bf16_t*)d_in[7]; const bf16_t* b1=(const bf16_t*)d_in[8]; const bf16_t* w2=(const bf16_t*)d_in[9];
  char* ws=(char*)d_ws;
  bf16_t* xn=(bf16_t*)ws; ws+= 2048*640*2;
  bf16_t* Q=(bf16_t*)ws; ws+=2048*4096*2;
  bf16_t* K=(bf16_t*)ws; ws+=2048*4096*2;
  bf16_t* V=(bf16_t*)ws; ws+=2048*4096*2;
  bf16_t* ctx=(bf16_t*)ws; ws+=2048*4096*2;
  bf16_t* act=(bf16_t*)ws; ws+=2048*1728*2;
  float* s=(float*)ws; ws+=2048*640*4;
  float* g=(float*)ws; ws+=2048*640*4;
  ln_kernel<<<512,256,0,stream>>>(x,xn);
  gemm_kernel<0><<<dim3(64,32),256,0,stream>>>(xn,wq,Q,nullptr,2048,4096,640,4096,0.08838834764831845f*1.4426950408889634f);
  gemm_kernel<0><<<dim3(64,32),256,0,stream>>>(ckv,wuk,K,nullptr,2048,4096,512,4096,1.f);
  gemm_kernel<0><<<dim3(64,32),256,0,stream>>>(ckv,wuv,V,nullptr,2048,4096,512,4096,1.f);
  gemm_kernel<1><<<dim3(27,32),256,0,stream>>>(xn,w1,act,b1,2048,1728,640,1728,1.f);
  gemm_kernel<2><<<dim3(10,32),256,0,stream>>>(act,w2,g,nullptr,2048,640,1728,640,1.f);
  attn_kernel<<<dim3(2048,32),128,0,stream>>>(Q,K,V,ctx);
  gemm_kernel<2><<<dim3(10,32),256,0,stream>>>(ctx,wo,s,nullptr,2048,640,4096,640,1.f);
  final_kernel<<<(2048*640+255)/256,256,0,stream>>>(x,s,g,(bf16_t*)d_out,2048*640);
}
```

```cpp
#include <hip/hip_runtime.h>
#include <stdint.h>
#include <utility>

typedef unsigned short bf16_t;
typedef short bf16x8 __attribute__((ext_vector_type(8)));
typedef short s16x4 __attribute__((ext_vector_type(4)));
typedef short v4i16_t __attribute__((ext_vector_type(4)));
typedef float f32x4 __attribute__((ext_vector_type(4)));
typedef float f32x2 __attribute__((ext_vector_type(2)));
typedef unsigned u32x4 __attribute__((ext_vector_type(4)));
typedef unsigned u32x2 __attribute__((ext_vector_type(2)));
typedef __bf16 bf16x2_t __attribute__((ext_vector_type(2)));
typedef __amdgpu_buffer_rsrc_t rsrc_t;
#define LAS __attribute__((address_space(3)))
#define GAS __attribute__((address_space(1)))
typedef LAS char* lptr;
typedef LAS const char* lcptr;
typedef GAS unsigned gu32;
#define DI __device__ __forceinline__
#define SBAR() __builtin_amdgcn_sched_barrier(0)
#define PIN(x) asm volatile("" : "+v"(x))
#define MF16(a, b, c) __builtin_amdgcn_mfma_f32_16x16x32_bf16(a, b, c, 0, 0, 0)
#define WAITV(n) asm volatile("s_waitcnt vmcnt(" #n ")" ::: "memory")
#define WAITL(n) asm volatile("s_waitcnt lgkmcnt(" #n ")" ::: "memory")
#define BARRIER() do { asm volatile("" ::: "memory"); __builtin_amdgcn_s_barrier(); asm volatile("" ::: "memory"); } while (0)

#define UPTO 4
#define STAMPS 0

constexpr unsigned OFF_FLAGS = 0;
constexpr unsigned OFF_XN = 65536;
constexpr unsigned OFF_K = OFF_XN + 2621440;
constexpr unsigned OFF_V = OFF_K + 16777216;
constexpr unsigned OFF_QS = OFF_V + 16777216;
constexpr unsigned OFF_ACT = OFF_QS + 8388608;
constexpr unsigned OFF_CTX = OFF_ACT + 7077888;
constexpr unsigned OFF_WP = OFF_CTX + 16777216;
constexpr unsigned OFF_ST = OFF_WP + 182 * 40 * 1024;
constexpr unsigned WS_BYTES = OFF_ST + 256 * 16 * 8;
constexpr unsigned FL_P0 = 0, FL_KV = 256, FL_ACT = 512, FL_CTX = 768;
constexpr int SC1 = 16;

DI unsigned cvtpk(float lo, float hi) { f32x2 v = {lo, hi}; bf16x2_t b = __builtin_convertvector(v, bf16x2_t); return __builtin_bit_cast(unsigned, b); }
DI u32x4 pack8(f32x4 a, f32x4 b) { u32x4 w = {cvtpk(a[0], a[1]), cvtpk(a[2], a[3]), cvtpk(b[0], b[1]), cvtpk(b[2], b[3])}; return w; }
DI float bflo(unsigned u) { return __uint_as_float(u << 16); }
DI float bfhi(unsigned u) { return __uint_as_float(u & 0xffff0000u); }
DI s16x4 vtr(lcptr p) { return __builtin_bit_cast(s16x4, __builtin_amdgcn_ds_read_tr16_b64_v4i16((LAS v4i16_t*)p)); }
DI bf16x8 cat(s16x4 a, s16x4 b) { return (bf16x8){a[0], a[1], a[2], a[3], b[0], b[1], b[2], b[3]}; }
DI bf16x8 ld128(lcptr p) { return *(const LAS bf16x8*)p; }
DI bf16x8 asbf(u32x4 v) { return __builtin_bit_cast(bf16x8, v); }
DI float fadd_s(float a, float b) { float r; asm("v_add_f32_e32 %0, %1, %2" : "=v"(r) : "v"(a), "v"(b)); return r; }
DI rsrc_t mk_rsrc(const void* p, unsigned bytes) { return __builtin_amdgcn_make_buffer_rsrc((void*)p, (short)0, (int)bytes, 0x00020000); }
DI void dma16(rsrc_t r, unsigned voff, unsigned soff, unsigned ldsoff) {
    asm volatile("s_mov_b32 m0, %0\n\ts_nop 3\n\tbuffer_load_dwordx4 %1, %2, %3 offen lds" ::"s"(ldsoff), "v"(voff), "s"(r), "s"(soff) : "memory");
}
DI void dma16x4(rsrc_t r, unsigned voff, unsigned soff, unsigned ldsoff) {
    asm volatile("s_mov_b32 m0, %0\n\ts_nop 3\n\tbuffer_load_dwordx4 %1, %2, %3 offen lds\n\tbuffer_load_dwordx4 %1, %2, %3 offen offset:1024 lds\n\t"
                 "buffer_load_dwordx4 %1, %2, %3 offen offset:2048 lds\n\tbuffer_load_dwordx4 %1, %2, %3 offen offset:3072 lds" ::"s"(ldsoff), "v"(voff), "s"(r), "s"(soff) : "memory");
}
DI void st16_sc1(rsrc_t r, u32x4 v, unsigned voff, unsigned soff) { asm volatile("s_nop 4\n\tbuffer_store_dwordx4 %0, %1, %2, %3 offen sc1\n\ts_nop 1" ::"v"(v), "v"(voff), "s"(r), "s"(soff) : "memory"); }
DI void st16(rsrc_t r, u32x4 v, unsigned voff, unsigned soff) { asm volatile("s_nop 4\n\tbuffer_store_dwordx4 %0, %1, %2, %3 offen\n\ts_nop 1" ::"v"(v), "v"(voff), "s"(r), "s"(soff) : "memory"); }
template <int OFF> DI void st16_sc1o(rsrc_t r, u32x4 v, unsigned voff, unsigned soff) { asm volatile("s_nop 4\n\tbuffer_store_dwordx4 %0, %1, %2, %3 offen offset:%4 sc1\n\ts_nop 1" ::"v"(v), "v"(voff), "s"(r), "s"(soff), "i"(OFF) : "memory"); }
DI u32x4 ld16(rsrc_t r, unsigned voff, unsigned soff) { return __builtin_amdgcn_raw_buffer_load_b128(r, voff, soff, 0); }
template <class T> DI T sel(bool c, T a, T b) { return c ? a : b; }
DI int lane_id() { int l; asm volatile("v_mbcnt_lo_u32_b32 %0, -1, 0\n\tv_mbcnt_hi_u32_b32 %0, -1, %0" : "=v"(l)); return l; }
DI void set_flag(gu32* flags, unsigned idx, unsigned token) { __hip_atomic_store(flags + idx * 16, token, __ATOMIC_RELAXED, __HIP_MEMORY_SCOPE_AGENT); }
DI void wait_flags(gu32* flags, int idx0, int idx1, unsigned token) {
    const gu32* p0 = flags + (idx0 >= 0 ? idx0 : 0) * 16;
    const gu32* p1 = flags + (idx1 >= 0 ? idx1 : 0) * 16;
    const unsigned long long t0 = __builtin_amdgcn_s_memrealtime();
    for (;;) {
        const unsigned v0 = __hip_atomic_load(p0, __ATOMIC_RELAXED, __HIP_MEMORY_SCOPE_AGENT);
        const unsigned v1 = __hip_atomic_load(p1, __ATOMIC_RELAXED, __HIP_MEMORY_SCOPE_AGENT);
        if (__all((idx0 < 0 || v0 == token) && (idx1 < 0 || v1 == token))) break;
        if (__builtin_amdgcn_s_memrealtime() - t0 > 2000000ull) break;
        __builtin_amdgcn_s_sleep(2);
    }
    __builtin_amdgcn_fence(__ATOMIC_ACQUIRE, "agent");
    asm volatile("s_waitcnt vmcnt(0)" ::: "memory");
}
#define STAMP(i) do { } while (0)

struct P1Ctx { rsrc_t sx, sw; unsigned vx[2], vw[2]; unsigned sxo, swo; int w; lcptr xb[2]; lcptr wb[4]; };
DI void p1_dma(const P1Ctx& c, int st, int ks) {
#pragma unroll
    for (int i = 0; i < 4; ++i) dma16(c.sx, c.vx[i & 1], c.sxo + i * 8192 + ks * 128, st * 32768 + (4 * c.w + i) * 1024);
#pragma unroll
    for (int i = 0; i < 4; ++i) dma16(c.sw, c.vw[i & 1], c.swo + i * 65536 + ks * 524288, 65536 + st * 32768 + (4 * c.w + i) * 1024);
}
DI void p1_dma1(const P1Ctx& c, int st, int ks, int i) {
    if (i < 4) dma16(c.sx, c.vx[i & 1], c.sxo + i * 8192 + ks * 128, st * 32768 + (4 * c.w + i) * 1024);
    else dma16(c.sw, c.vw[i & 1], c.swo + (i - 4) * 65536 + ks * 524288, 65536 + st * 32768 + (4 * c.w + (i - 4)) * 1024);
}
template <int ST, int UP, int I> DI void p1_rdw(const P1Ctx& c, bf16x8 (&wf)[4], s16x4 (&wl)[4]) {
    constexpr int nt = I >> 1, rd = I & 1;
    const s16x4 v = vtr(c.wb[nt] + ST * 32768 + UP * 4096 + rd * 512);
    if constexpr (rd == 0) wl[nt] = v; else wf[nt] = cat(wl[nt], v);
}
template <int ST, int G> DI bf16x8 p1_rdx(const P1Ctx& c) { return ld128(c.xb[G >> 3] + ST * 32768 + (G & 7) * 2048); }
template <bool ISV, int ST, bool DMA, bool NEXT, int G> DI void p1_group(const P1Ctx& c, int ks, f32x4 (&acc)[4][8], bf16x8 (&wA)[4], bf16x8 (&wB)[4], s16x4 (&wl)[4], bf16x8 (&xr)[4]) {
    constexpr int up = G >> 3, mt = G & 7;
    if constexpr (G == 13) { asm volatile("s_waitcnt vmcnt(0) lgkmcnt(0)" ::: "memory"); BARRIER(); SBAR(); }
    const bf16x8 x = xr[G & 3];
#pragma unroll
    for (int nt = 0; nt < 4; ++nt) {
        const bf16x8 wv = up == 0 ? wA[nt] : wB[nt];
        if constexpr (ISV) acc[nt][mt] = MF16(x, wv, acc[nt][mt]); else acc[nt][mt] = MF16(wv, x, acc[nt][mt]);
        if (nt == 0) {
            if constexpr (G + 3 < 16) xr[(G + 3) & 3] = p1_rdx<ST, G + 3>(c);
            else if constexpr (NEXT) xr[(G + 3) & 3] = p1_rdx<ST ^ 1, G + 3 - 16>(c);
        }
        if constexpr (G >= 2 && G < 6) { if (nt == 1) p1_rdw<ST, 1, 2 * (G - 2)>(c, wB, wl); if (nt == 2) p1_rdw<ST, 1, 2 * (G - 2) + 1>(c, wB, wl); }
        if constexpr (G >= 13 && NEXT) {
            if (nt == 1) p1_rdw<ST ^ 1, 0, 3 * (G - 13)>(c, wA, wl);
            if (nt == 2) p1_rdw<ST ^ 1, 0, 3 * (G - 13) + 1>(c, wA, wl);
            if constexpr (G < 15) { if (nt == 3) p1_rdw<ST ^ 1, 0, 3 * (G - 13) + 2>(c, wA, wl); }
        }
        if constexpr (G >= 13 && DMA) { constexpr int d0 = 3 * (G - 13); if (nt >= 1 && d0 + nt - 1 < 8) p1_dma1(c, ST, ks + 2, d0 + nt - 1); }
        SBAR();
    }
}
template <bool ISV, int ST, bool DMA, bool NEXT, int... Gs> DI void p1_step(const P1Ctx& c, int ks, f32x4 (&acc)[4][8], bf16x8 (&wA)[4], bf16x8 (&wB)[4], s16x4 (&wl)[4], bf16x8 (&xr)[4], std::integer_sequence<int, Gs...>) {
    (p1_group<ISV, ST, DMA, NEXT, Gs>(c, ks, acc, wA, wB, wl, xr), ...);
}
template <bool ISV> DI void p1_main(const P1Ctx& c, u32x4 (&kv)[16]) {
    f32x4 acc[4][8];
#pragma unroll
    for (int a = 0; a < 4; ++a)
#pragma unroll
        for (int b = 0; b < 8; ++b) acc[a][b] = (f32x4){0.f, 0.f, 0.f, 0.f};
    bf16x8 wA[4], wB[4], xr[4]; s16x4 wl[4];
    asm volatile("" ::: "memory");
    p1_rdw<0, 0, 0>(c, wA, wl); p1_rdw<0, 0, 1>(c, wA, wl); p1_rdw<0, 0, 2>(c, wA, wl); p1_rdw<0, 0, 3>(c, wA, wl);
    p1_rdw<0, 0, 4>(c, wA, wl); p1_rdw<0, 0, 5>(c, wA, wl); p1_rdw<0, 0, 6>(c, wA, wl); p1_rdw<0, 0, 7>(c, wA, wl);
    xr[0] = p1_rdx<0, 0>(c); xr[1] = p1_rdx<0, 1>(c); xr[2] = p1_rdx<0, 2>(c);
    SBAR();
    constexpr auto seq = std::make_integer_sequence<int, 16>{};
    for (int ks = 0; ks < 6; ks += 2) {
        p1_step<ISV, 0, true, true>(c, ks, acc, wA, wB, wl, xr, seq);
        p1_step<ISV, 1, true, true>(c, ks + 1, acc, wA, wB, wl, xr, seq);
    }
    p1_step<ISV, 0, false, true>(c, 6, acc, wA, wB, wl, xr, seq);
    p1_step<ISV, 1, false, false>(c, 7, acc, wA, wB, wl, xr, seq);
#pragma unroll
    for (int a = 0; a < 8; ++a)
#pragma unroll
        for (int b = 0; b < 2; ++b) {
            if constexpr (ISV) kv[a * 2 + b] = pack8(acc[(a & 1) * 2 + b][(a >> 1) * 2], acc[(a & 1) * 2 + b][(a >> 1) * 2 + 1]);
            else kv[a * 2 + b] = pack8(acc[2 * b][a], acc[2 * b + 1][a]);
        }
}

struct P2Ctx { rsrc_t sws, swq, sw1; unsigned vl16, vwq, vw1; unsigned sxo, swqo, sw1o, skv; int w; lcptr xb; lcptr wq[4]; lcptr wf[2]; };
template <bool HASF> DI void p2_dma(const P2Ctx& c, int st, int ks) {
#pragma unroll
    for (int i = 0; i < 4; ++i) dma16(c.sws, c.vl16, c.sxo + (i >> 1) * 20480 + (i & 1) * 1024 + ks * 2048, st * 32768 + (4 * c.w + i) * 1024);
    dma16(c.swq, c.vwq, c.swqo + ks * 524288, 65536 + st * 24576 + c.w * 1024);
    dma16(c.swq, c.vwq, c.swqo + 128 + ks * 524288, 65536 + st * 24576 + 8192 + c.w * 1024);
    if constexpr (HASF) dma16(c.sw1, c.vw1, c.sw1o + ks * (64 * 3456), 65536 + st * 24576 + 16384 + c.w * 1024);
}
template <bool HASF, int ST, int I> DI bf16x8 p2_rdw(const P2Ctx& c) {
    constexpr int NW = HASF ? 6 : 4, up = I / NW, n = I % NW;
    lcptr p = (n < 4 ? c.wq[n & 3] : c.wf[n & 1]) + ST * 24576 + up * 4096;
    return cat(vtr(p), vtr(p + 512));
}
template <int ST, int UP, int MT> DI bf16x8 p2_rdx(const P2Ctx& c) { return ld128(c.xb + ST * 32768 + MT * 2048 + UP * 1024); }

template <bool HASF, int KS, int G> DI void p2_group(const P2Ctx& c, f32x4 (&acc)[6][4], bf16x8 (&wr)[4], bf16x8 (&x0)[4], bf16x8 (&x1)[4], const u32x4 (&kv)[16], bool isv) {
    constexpr int NW = HASF ? 6 : 4, NG = 2 * NW, ST = KS & 1, up = G / NW, n = G % NW;
    constexpr bool LAST = (KS == 9);
    if constexpr (G == NG - 3) {
        asm volatile("s_waitcnt vmcnt(0) lgkmcnt(0)" ::: "memory"); BARRIER();
        if constexpr (KS + 2 < 10) p2_dma<HASF>(c, ST, KS + 2);
        if constexpr (KS < 8) {
            constexpr int i0 = 2 * KS, i1 = 2 * KS + 1;
            st16_sc1(c.sws, kv[i0], c.vl16, c.skv + (isv ? (unsigned)(((i0 >> 3) * 16 + ((i0 >> 2) & 1) * 8 + (i0 & 3)) * 1024) : (unsigned)((i0 >> 1) * 4096 + (i0 & 1) * 1024)));
            st16_sc1(c.sws, kv[i1], c.vl16, c.skv + (isv ? (unsigned)(((i1 >> 3) * 16 + ((i1 >> 2) & 1) * 8 + (i1 & 3)) * 1024) : (unsigned)((i1 >> 1) * 4096 + (i1 & 1) * 1024)));
        }
        SBAR();
    }
    const bf16x8 wcur = wr[G & 3];
#pragma unroll
    for (int mt = 0; mt < 4; ++mt) {
        acc[n][mt] = MF16(wcur, up == 0 ? x0[mt] : x1[mt], acc[n][mt]);
        if (mt == 0) {
            if constexpr (G + 3 < NG) wr[(G + 3) & 3] = p2_rdw<HASF, ST, G + 3>(c);
            else if constexpr (!LAST) wr[(G + 3) & 3] = p2_rdw<HASF, ST ^ 1, G + 3 - NG>(c);
        }
        if (mt == 1) {
            if constexpr (G < 4) x1[G] = p2_rdx<ST, 1, G>(c);
            else if constexpr (G >= NG - 3 && !LAST) x0[G - (NG - 3)] = p2_rdx<ST ^ 1, 0, G - (NG - 3)>(c);
        }
        if (mt == 2) { if constexpr (G == NG - 1 && !LAST) x0[3] = p2_rdx<ST ^ 1, 0, 3>(c); }
        SBAR();
    }
}
template <bool HASF, int KS, int... Gs> DI void p2_step(const P2Ctx& c, f32x4 (&acc)[6][4], bf16x8 (&wr)[4], bf16x8 (&x0)[4], bf16x8 (&x1)[4], const u32x4 (&kv)[16], bool isv, std::integer_sequence<int, Gs...>) {
    (p2_group<HASF, KS, Gs>(c, acc, wr, x0, x1, kv, isv), ...);
}
template <bool HASF, int... KSs> DI void p2_main(const P2Ctx& c, f32x4 (&acc)[6][4], const u32x4 (&kv)[16], bool isv, std::integer_sequence<int, KSs...>) {
    bf16x8 wr[4], x0[4], x1[4];
    asm volatile("" ::: "memory");
    wr[0] = p2_rdw<HASF, 0, 0>(c); wr[1] = p2_rdw<HASF, 0, 1>(c); wr[2] = p2_rdw<HASF, 0, 2>(c);
    x0[0] = p2_rdx<0, 0, 0>(c); x0[1] = p2_rdx<0, 0, 1>(c); x0[2] = p2_rdx<0, 0, 2>(c); x0[3] = p2_rdx<0, 0, 3>(c);
    SBAR();
    (p2_step<HASF, KSs>(c, acc, wr, x0, x1, kv, isv, std::make_integer_sequence<int, (HASF ? 12 : 8)>{}), ...);
}
DI float mish(float h) {
    const float t = __builtin_amdgcn_exp2f(__builtin_fminf(h, 20.f) * 1.4426950408889634f);
    const float n = t * (t + 2.f);
    return h * n * __builtin_amdgcn_rcpf(n + 2.f);
}

constexpr int PD = 8;
struct AttnRegs { f32x4 o[8][2]; f32x4 s[4][2]; bf16x8 q[2][4]; u32x4 pk[2][2]; f32x4 negm[2]; bf16x8 op[32]; };
template <int I> DI void a_read(AttnRegs& r, lcptr vp, lcptr kp) {
    if constexpr (I < 16) r.op[I] = ld128(vp + I * 1024);
    else if constexpr (I < 32) r.op[I] = ld128(kp + (((I - 16) & 3) * 4 + ((I - 16) >> 2)) * 1024);
}
template <int I> DI void a_slot(AttnRegs& r, lcptr vp, lcptr kp) {
    if constexpr (I < 16) { constexpr int ks = I >> 3, dt = I & 7;
        r.o[dt][0] = MF16(r.op[I], asbf(r.pk[0][ks]), r.o[dt][0]); r.o[dt][1] = MF16(r.op[I], asbf(r.pk[1][ks]), r.o[dt][1]); }
    else { constexpr int u = (I - 16) >> 2, kt = (I - 16) & 3;
        r.s[kt][0] = MF16(r.op[I], r.q[0][u], u == 0 ? r.negm[0] : r.s[kt][0]); r.s[kt][1] = MF16(r.op[I], r.q[1][u], u == 0 ? r.negm[1] : r.s[kt][1]); }
    a_read<I + PD>(r, vp, kp);
    SBAR();
}
template <int B, int... Is> DI void a_run(AttnRegs& r, lcptr vp, lcptr kp, std::integer_sequence<int, Is...>) { (a_slot<B + Is>(r, vp, kp), ...); }
template <int B, int... Is> DI void a_pre(AttnRegs& r, lcptr vp, lcptr kp, std::integer_sequence<int, Is...>) { (a_read<B + Is>(r, vp, kp), ...); }

DI void a_exp_pack(AttnRegs& r, float& a0, float& a1) {
    a0 = 0.f; a1 = 0.f;
#pragma unroll
    for (int ks = 0; ks < 2; ++ks)
#pragma unroll
        for (int qt = 0; qt < 2; ++qt) {
            float e[8];
#pragma unroll
            for (int i = 0; i < 4; ++i) { e[i] = __builtin_amdgcn_exp2f(r.s[2 * ks][qt][i]); e[4 + i] = __builtin_amdgcn_exp2f(r.s[2 * ks + 1][qt][i]); }
#pragma unroll
            for (int i = 0; i < 8; ++i) { if (qt == 0) a0 += e[i]; else a1 += e[i]; }
            r.pk[qt][ks] = (u32x4){cvtpk(e[0], e[1]), cvtpk(e[2], e[3]), cvtpk(e[4], e[5]), cvtpk(e[6], e[7])};
        }
}
DI float xmax4(float v) { v = __builtin_fmaxf(v, __shfl_xor(v, 16)); return __builtin_fmaxf(v, __shfl_xor(v, 32)); }
DI float xsum4(float v) { v += __shfl_xor(v, 16); return v + __shfl_xor(v, 32); }

struct AttnCtx { rsrc_t sws; unsigned vl16, kbase, vbase, qso, ctxl; int nL, grp, qg, j, rowrel, swap_s, first_s; lcptr kl, vl; };
struct AttnSm { float m0, m1, l0, l1, ll0, ll1; };
DI void a_dmaK(const AttnCtx& c, int n) { dma16x4(c.sws, c.vl16, c.kbase + n * 16384 + c.qg * 4096, (n & 3) * 16384 + c.qg * 4096); }
DI void a_dmaV(const AttnCtx& c, int n) { dma16x4(c.sws, c.vl16, c.vbase + n * 16384 + c.qg * 4096, 65536 + (n & 3) * 16384 + c.qg * 4096); }
DI void a_slotfull(const AttnCtx& c, AttnRegs& r, AttnSm& z, const int s) {
    const int nL = c.nL, grp = c.grp;
    const bool SWAP = (s == c.swap_s), FIN = (s == c.swap_s + 1), FIRST = (s == c.first_s) || FIN;
    const int tcur = s < nL ? s : 2 * s - nL + grp;
    asm volatile("s_waitcnt vmcnt(0)" ::: "memory");
    auto issue_dma = [&]() { const int s2 = s + 2, s1 = s + 1;
        if (s2 <= 16 && (grp == 0 || s2 >= nL)) a_dmaK(c, s2 < nL ? s2 : 2 * s2 - nL + grp);
        if (s1 <= 16 && (grp == 0 || s1 >= nL)) a_dmaV(c, s1 < nL ? s1 : 2 * s1 - nL + grp); };
    if (SWAP) {
#pragma unroll
        for (int i = 0; i < 8; ++i) r.q[i >> 2][i & 3] = asbf(ld16(c.sws, c.vl16 + (i & 3) * 1024, c.qso + (i >> 2) * 4096));
    } else issue_dma();
    if (FIN) {
        const float i0 = __builtin_amdgcn_rcpf(xsum4(z.ll0)), i1 = __builtin_amdgcn_rcpf(xsum4(z.ll1));
        const unsigned c1o = c.ctxl + 131072;
        st16_sc1o<0>(c.sws, pack8(r.o[0][0] * i0, r.o[1][0] * i0), c.vl16, c.ctxl); st16_sc1o<1024>(c.sws, pack8(r.o[2][0] * i0, r.o[3][0] * i0), c.vl16, c.ctxl);
        st16_sc1o<2048>(c.sws, pack8(r.o[4][0] * i0, r.o[5][0] * i0), c.vl16, c.ctxl); st16_sc1o<3072>(c.sws, pack8(r.o[6][0] * i0, r.o[7][0] * i0), c.vl16, c.ctxl);
        st16_sc1o<0>(c.sws, pack8(r.o[0][1] * i1, r.o[1][1] * i1), c.vl16, c1o); st16_sc1o<1024>(c.sws, pack8(r.o[2][1] * i1, r.o[3][1] * i1), c.vl16, c1o);
        st16_sc1o<2048>(c.sws, pack8(r.o[4][1] * i1, r.o[5][1] * i1), c.vl16, c1o); st16_sc1o<3072>(c.sws, pack8(r.o[6][1] * i1, r.o[7][1] * i1), c.vl16, c1o);
#pragma unroll
        for (int d = 0; d < 8; ++d) { r.o[d][0] = (f32x4){0.f, 0.f, 0.f, 0.f}; r.o[d][1] = (f32x4){0.f, 0.f, 0.f, 0.f}; }
    }
    {
        const bool heavy = (grp == 1) || (s >= nL);
        const int qb = heavy ? 15 - c.j : c.j, kbrel = 64 * tcur - 128 * qb;
        if (kbrel >= 0) {
            const int dq = c.rowrel - kbrel;
#pragma unroll
            for (int kt = 0; kt < 4; ++kt)
#pragma unroll
                for (int i = 0; i < 4; ++i) {
                    if (16 * kt + i > dq) r.s[kt][0][i] = -__builtin_inff();
                    if (16 * kt + i > dq + 16) r.s[kt][1][i] = -__builtin_inff();
                }
        }
    }
    float a0 = 0.f, a1 = 0.f;
    bool redo = FIRST;
    if (!FIRST) { a_exp_pack(r, a0, a1); redo = __builtin_expect(__any(!(a0 <= 65536.f) || !(a1 <= 65536.f)), 0); }
    if (redo) {
        float x0 = r.s[0][0][0], x1 = r.s[0][1][0];
#pragma unroll
        for (int kt = 0; kt < 4; ++kt)
#pragma unroll
            for (int i = 0; i < 4; ++i) { x0 = __builtin_fmaxf(x0, r.s[kt][0][i]); x1 = __builtin_fmaxf(x1, r.s[kt][1][i]); }
        x0 = xmax4(x0); x1 = xmax4(x1);
        if (!FIRST) { x0 = __builtin_fmaxf(x0, 0.f); x1 = __builtin_fmaxf(x1, 0.f);
            const float f0 = __builtin_amdgcn_exp2f(-x0), f1 = __builtin_amdgcn_exp2f(-x1);
#pragma unroll
            for (int d = 0; d < 8; ++d) { r.o[d][0] *= f0; r.o[d][1] *= f1; }
            z.l0 *= f0; z.l1 *= f1; }
        z.m0 += x0; z.m1 += x1;
        r.negm[0] = (f32x4){-z.m0, -z.m0, -z.m0, -z.m0}; r.negm[1] = (f32x4){-z.m1, -z.m1, -z.m1, -z.m1};
#pragma unroll
        for (int kt = 0; kt < 4; ++kt) { r.s[kt][0] -= x0; r.s[kt][1] -= x1; }
        a_exp_pack(r, a0, a1);
    }
    z.l0 += a0; z.l1 += a1;
    if (SWAP) {
        z.ll0 = z.l0; z.ll1 = z.l1; z.l0 = 0.f; z.l1 = 0.f; z.m0 = 0.f; z.m1 = 0.f;
        r.negm[0] = (f32x4){0.f, 0.f, 0.f, 0.f}; r.negm[1] = (f32x4){0.f, 0.f, 0.f, 0.f};
#pragma unroll
        for (int i = 0; i < 8; ++i) PIN(r.q[i >> 2][i & 3]);
        issue_dma();
    }
    const int snext = s + 1, tnext = snext < nL ? snext : 2 * snext - nL + grp;
    const lcptr vp = c.vl + (tcur & 3) * 16384, kp = c.kl + (tnext & 3) * 16384;
    SBAR();
    a_pre<0>(r, vp, kp, std::make_integer_sequence<int, PD>{});
    SBAR(); BARRIER(); SBAR();
    __builtin_amdgcn_s_setprio(1);
    a_run<0>(r, vp, kp, std::make_integer_sequence<int, 16>{});
    if (s < 16) a_run<16>(r, vp, kp, std::make_integer_sequence<int, 16>{});
    __builtin_amdgcn_s_setprio(0);
    SBAR(); BARRIER(); SBAR();
}

__global__ __launch_bounds__(512, 2) void fused_kernel(const bf16_t* __restrict__ x, const bf16_t* __restrict__ ckv, const bf16_t* __restrict__ wq, const bf16_t* __restrict__ wuk,
                                                       const bf16_t* __restrict__ wuv, const bf16_t* __restrict__ wo, const bf16_t* __restrict__ w1, const bf16_t* __restrict__ b1,
                                                       const bf16_t* __restrict__ w2, bf16_t* __restrict__ out, char* __restrict__ ws) {
    __shared__ __attribute__((aligned(1024))) char smem[163840];
    const lptr lds = (lptr)smem;
    const int b = blockIdx.x, xcd = b & 7, bi = b >> 3, h = xcd * 4 + (bi >> 3), j = bi & 7;
    const int w = __builtin_amdgcn_readfirstlane(threadIdx.x >> 6);
    gu32* flags = (gu32*)(ws + OFF_FLAGS);
    const unsigned token = 1u;
    const rsrc_t sws = mk_rsrc(ws, WS_BYTES);
    STAMP(0);
    u32x4 kv[16];
    P1Ctx c1;
    {
        const int l = lane_id(), tid = w * 64 + l, g = l >> 4, q = (l & 15) >> 2;
        const int nb = w >> 1;
        c1.w = w;
        c1.sx = mk_rsrc(ckv, 2048 * 512 * 2);
        c1.sw = mk_rsrc(nb < 2 ? wuk : wuv, 512 * 4096 * 2);
#pragma unroll
        for (int par = 0; par < 2; ++par) {
            c1.vx[par] = (l >> 3) * 1024 + (((l & 7) ^ (4 * par + (l >> 4))) * 16);
            c1.vw[par] = (l >> 3) * 8192 + (((l & 7) ^ ((((l >> 4) & 1) | (par << 1)) << 1)) * 16);
        }
        c1.sxo = (256 * j + 32 * w) * 1024;
        c1.swo = (32 * (w & 1)) * 8192 + (h * 128 + 64 * (nb & 1)) * 2;
        const int wn = w & 3, wm = w >> 2, f = (l & 15) >> 1, s = ((q >> 1) & 1) | ((g & 1) << 1);
        c1.xb[0] = lds + wm * 16384 + (l & 15) * 128 + ((g ^ f) * 16);
        c1.xb[1] = lds + wm * 16384 + (l & 15) * 128 + (((g ^ f) ^ 4) * 16);
#pragma unroll
        for (int nt = 0; nt < 4; ++nt) c1.wb[nt] = lds + 65536 + wn * 8192 + (8 * g + q) * 128 + ((nt ^ s) * 32) + (l & 3) * 8;
    }
    p1_dma(c1, 0, 0); p1_dma(c1, 1, 1);
    {
        const int l = lane_id(), tid = w * 64 + l, g = l >> 4, q = (l & 15) >> 2;
        constexpr unsigned PK = 131072, LNS = 131072 + 20480;
        const rsrc_t swo_ = mk_rsrc(wo, 4096 * 640 * 2), sw2_ = mk_rsrc(w2, 1728 * 640 * 2);
        auto pack_dma = [&](int v) {
            const int u = v >> 1, half = v & 1;
#pragma unroll
            for (int i = 0; i < 3; ++i) { const int p = w + 8 * i; if (p < 20) { const int cidx = 64 * p + l, row = cidx / 40, cc = cidx - row * 40;
                if (u < 128) dma16(swo_, row * 1280 + cc * 16, u * 32 * 1280 + half * 640, PK + p * 1024); else dma16(sw2_, row * 1280 + cc * 16, (u - 128) * 32 * 1280 + half * 640, PK + p * 1024); } }
        };
        auto pack_out = [&](int v) {
            const int u = v >> 1, half = v & 1;
#pragma unroll
            for (int i = 0; i < 3; ++i) { const int nt = w + 8 * i; if (nt < 20) { lcptr p = lds + PK + (4 * g + q) * 640 + nt * 32 + (l & 3) * 8;
                const bf16x8 fr = cat(vtr(p), vtr(p + 16 * 640));
                st16_sc1(sws, __builtin_bit_cast(u32x4, fr), l * 16, OFF_WP + (u * 40 + half * 20 + nt) * 1024); } }
        };
        pack_dma(b);
        const int row = 8 * b + w;
        const u32x4* xr = (const u32x4*)(x + row * 640);
        const u32x4 c0 = xr[l]; u32x4 c1v = (u32x4){0u, 0u, 0u, 0u}; if (l < 16) c1v = xr[64 + l];
        float v[16];
#pragma unroll
        for (int i = 0; i < 4; ++i) { v[2 * i] = bflo(c0[i]); v[2 * i + 1] = bfhi(c0[i]); v[8 + 2 * i] = bflo(c1v[i]); v[8 + 2 * i + 1] = bfhi(c1v[i]); }
        float s = 0.f;
#pragma unroll
        for (int i = 0; i < 16; ++i) s += v[i];
#pragma unroll
        for (int o = 32; o; o >>= 1) s += __shfl_xor(s, o);
        const float mean = s * (1.f / 640.f);
        float qq = 0.f;
#pragma unroll
        for (int i = 0; i < 8; ++i) { v[i] -= mean; qq += v[i] * v[i]; }
#pragma unroll
        for (int i = 8; i < 16; ++i) { v[i] = l < 16 ? v[i] - mean : 0.f; qq += v[i] * v[i]; }
#pragma unroll
        for (int o = 32; o; o >>= 1) qq += __shfl_xor(qq, o);
        const float rstd = 1.f / __builtin_sqrtf(qq * (1.f / 640.f) + 1e-5f);
        *(LAS u32x4*)(lds + LNS + (l * 8 + w) * 16) = (u32x4){cvtpk(v[0] * rstd, v[1] * rstd), cvtpk(v[2] * rstd, v[3] * rstd), cvtpk(v[4] * rstd, v[5] * rstd), cvtpk(v[6] * rstd, v[7] * rstd)};
        if (l < 16) *(LAS u32x4*)(lds + LNS + ((64 + l) * 8 + w) * 16) = (u32x4){cvtpk(v[8] * rstd, v[9] * rstd), cvtpk(v[10] * rstd, v[11] * rstd), cvtpk(v[12] * rstd, v[13] * rstd), cvtpk(v[14] * rstd, v[15] * rstd)};
        asm volatile("s_waitcnt vmcnt(0) lgkmcnt(0)" ::: "memory"); BARRIER();
        {
            const unsigned xo = OFF_XN + (b >> 1) * 20480 + (b & 1) * 128;
            { const int cch = tid >> 3, r = tid & 7; st16_sc1(sws, *(const LAS u32x4*)(lds + LNS + tid * 16), (cch >> 2) * 1024 + (cch & 3) * 256 + r * 16, xo); }
            if (tid < 128) { const int t2 = 512 + tid, cch = t2 >> 3, r = t2 & 7; st16_sc1(sws, *(const LAS u32x4*)(lds + LNS + t2 * 16), (cch >> 2) * 1024 + (cch & 3) * 256 + r * 16, xo); }
        }
        pack_out(b);
        if (b < 108) {
            asm volatile("s_waitcnt lgkmcnt(0)" ::: "memory"); BARRIER();
            pack_dma(256 + b);
            asm volatile("s_waitcnt vmcnt(0)" ::: "memory"); BARRIER();
            pack_out(256 + b);
        }
        asm volatile("s_waitcnt vmcnt(0)" ::: "memory"); BARRIER();
        if (tid == 0) set_flag(flags, FL_P0 + b, token);
    }
    STAMP(1);
    const bool isv = (w & 3) >= 2;
    if (isv) p1_main<true>(c1, kv); else p1_main<false>(c1, kv);
    STAMP(2);
    unsigned skv;
    { const int wn = w & 3, wm = w >> 2;
      skv = isv ? OFF_V + (unsigned)(h * 32 + 4 * j + 2 * wm) * 16384 + (wn - 2) * 4096 : OFF_K + (unsigned)((h * 128 + 16 * j + 8 * wm) * 4 + 2 * wn) * 1024; }
    const bool hasf = h < 27;
    bf16x8 qreg[2][4];
    {
        P2Ctx c2;
        const int l = lane_id(), tid = w * 64 + l, g = l >> 4, q = (l & 15) >> 2;
        const int wn = w & 1, wm = w >> 1;
        c2.w = w; c2.sws = sws; c2.swq = mk_rsrc(wq, 640 * 4096 * 2); c2.sw1 = mk_rsrc(w1, 640 * 1728 * 2);
        c2.vl16 = l * 16;
        const int swz = ((l >> 4) & 1) | ((w & 1) << 1);
        c2.vwq = (l >> 3) * 8192 + (((l & 7) ^ (swz << 1)) * 16);
        c2.vw1 = (l >> 3) * 3456 + (((l & 7) ^ (swz << 1)) * 16);
        { const int mtl = 2 * w, mtg = mtl < 8 ? 8 * j + mtl : 8 * (15 - j) + (mtl - 8); c2.sxo = OFF_XN + mtg * 20480; }
        c2.swqo = (8 * w) * 8192 + h * 256;
        c2.sw1o = (8 * w) * 3456 + h * 128;
        c2.skv = skv;
        const int s = ((q >> 1) & 1) | ((g & 1) << 1);
        c2.xb = lds + wm * 8192 + l * 16;
#pragma unroll
        for (int nt = 0; nt < 4; ++nt) c2.wq[nt] = lds + 65536 + wn * 8192 + (8 * g + q) * 128 + ((nt ^ s) * 32) + (l & 3) * 8;
#pragma unroll
        for (int ft = 0; ft < 2; ++ft) c2.wf[ft] = lds + 65536 + 16384 + (8 * g + q) * 128 + (((2 * wn + ft) ^ s) * 32) + (l & 3) * 8;
        if (w == 0) wait_flags(flags, l < 32 ? (int)FL_P0 + (l < 16 ? 16 * j + l : 16 * (15 - j) + (l - 16)) : -1, -1, token);
        BARRIER();
        if (hasf) { p2_dma<true>(c2, 0, 0); p2_dma<true>(c2, 1, 1); } else { p2_dma<false>(c2, 0, 0); p2_dma<false>(c2, 1, 1); }
        f32x4 acc[6][4];
#pragma unroll
        for (int a = 0; a < 6; ++a)
#pragma unroll
            for (int m = 0; m < 4; ++m) acc[a][m] = (f32x4){0.f, 0.f, 0.f, 0.f};
        asm volatile("s_waitcnt vmcnt(0)" ::: "memory"); BARRIER();
        STAMP(3);
        if (hasf) p2_main<true>(c2, acc, kv, isv, std::make_integer_sequence<int, 10>{}); else p2_main<false>(c2, acc, kv, isv, std::make_integer_sequence<int, 10>{});
        asm volatile("s_waitcnt vmcnt(0) lgkmcnt(0)" ::: "memory"); BARRIER();
        if (tid == 0) set_flag(flags, FL_KV + h * 8 + j, token);
        STAMP(4);
        constexpr float C2 = 0.08838834764831845f * 1.4426950408889634f;
#pragma unroll
        for (int mt = 0; mt < 4; ++mt)
#pragma unroll
            for (int a = 0; a < 2; ++a) {
                const u32x4 fr = pack8(acc[2 * a][mt] * C2, acc[2 * a + 1][mt] * C2);
                *(LAS u32x4*)(lds + 98304 + ((4 * wm + mt) * 4 + 2 * wn + a) * 1024 + l * 16) = fr;
                if (wm >= 2) st16(sws, fr, l * 16, OFF_QS + b * 32768 + ((4 * (wm - 2) + mt) * 4 + 2 * wn + a) * 1024);
            }
        if (hasf) {
            const int f0 = 64 * h + 32 * wn + 4 * g;
            const u32x2 bA = *(const u32x2*)(b1 + f0), bB = *(const u32x2*)(b1 + f0 + 16);
            const f32x4 biasA = {bflo(bA[0]), bfhi(bA[0]), bflo(bA[1]), bfhi(bA[1])}, biasB = {bflo(bB[0]), bfhi(bB[0]), bflo(bB[1]), bfhi(bB[1])};
#pragma unroll
            for (int mt = 0; mt < 4; ++mt) {
                f32x4 hA = acc[4][mt] + biasA, hB = acc[5][mt] + biasB;
#pragma unroll
                for (int i = 0; i < 4; ++i) { hA[i] = mish(hA[i]); hB[i] = mish(hB[i]); }
                const int mtl = 4 * wm + mt, mtg = mtl < 8 ? 8 * j + mtl : 8 * (15 - j) + (mtl - 8);
                st16_sc1(sws, pack8(hA, hB), l * 16, OFF_ACT + (mtg * 54 + 2 * h + wn) * 1024);
            }
        }
        asm volatile("s_waitcnt vmcnt(0) lgkmcnt(0)" ::: "memory"); BARRIER();
        if (tid == 0) set_flag(flags, FL_ACT + j * 32 + h, token);
        { const int grp = w >> 2, qg = w & 3;
#pragma unroll
          for (int qt = 0; qt < 2; ++qt)
#pragma unroll
              for (int u = 0; u < 4; ++u) qreg[qt][u] = ld128(lds + 98304 + ((8 * grp + 2 * qg + qt) * 4 + u) * 1024 + l * 16); }
    }
    STAMP(5);
    {
        const int l = lane_id(), tid = w * 64 + l, g = l >> 4;
        const int grp = w >> 2, qg = w & 3;
        const int nL = 2 * j + 2;
        const unsigned vl16 = l * 16;
        const unsigned kbase = OFF_K + (unsigned)h * 524288, vbase = OFF_V + (unsigned)h * 524288;
        if (w == 0) wait_flags(flags, l < 8 ? (int)FL_KV + h * 8 + l : -1, -1, token);
        asm volatile("s_waitcnt lgkmcnt(0)" ::: "memory");
        BARRIER();
        AttnCtx c3;
        c3.sws = sws; c3.vl16 = vl16; c3.kbase = kbase; c3.vbase = vbase; c3.qso = OFF_QS + b * 32768 + 2 * qg * 4096; c3.ctxl = OFF_CTX + ((8 * j + 2 * qg) * 128 + 4 * h) * 1024;
        c3.nL = nL; c3.grp = grp; c3.qg = qg; c3.j = j; c3.rowrel = 32 * qg + (l & 15) - 4 * g; c3.kl = lds + l * 16; c3.vl = lds + 65536 + l * 16;
        if (grp == 0) { a_dmaK(c3, 0); a_dmaV(c3, 0); } else { a_dmaK(c3, 1); }
        AttnRegs r;
#pragma unroll
        for (int qt = 0; qt < 2; ++qt)
#pragma unroll
            for (int u = 0; u < 4; ++u) r.q[qt][u] = qreg[qt][u];
#pragma unroll
        for (int d = 0; d < 8; ++d) { r.o[d][0] = (f32x4){0.f, 0.f, 0.f, 0.f}; r.o[d][1] = (f32x4){0.f, 0.f, 0.f, 0.f}; }
        r.negm[0] = (f32x4){0.f, 0.f, 0.f, 0.f}; r.negm[1] = (f32x4){0.f, 0.f, 0.f, 0.f};
        AttnSm z; z.m0 = 0.f; z.m1 = 0.f; z.l0 = 0.f; z.l1 = 0.f; z.ll0 = 0.f; z.ll1 = 0.f;
        asm volatile("s_waitcnt vmcnt(0)" ::: "memory"); BARRIER();
        STAMP(6);
        a_pre<16>(r, c3.vl, c3.kl, std::make_integer_sequence<int, PD>{});
        if (grp == 1) BARRIER();
        a_run<16>(r, c3.vl, c3.kl, std::make_integer_sequence<int, 16>{});
        BARRIER();
        c3.swap_s = grp == 0 ? nL - 1 : 99; c3.first_s = 0;
        asm volatile("" : "+s"(c3.swap_s), "+s"(c3.first_s));
#pragma nounroll
        for (int s = 0; s <= 16; ++s) a_slotfull(c3, r, z, s);
        float m0 = z.m0, m1 = z.m1, l0 = z.l0, l1 = z.l1;
        if (grp == 0) BARRIER();
        STAMP(7);
        l0 = xsum4(l0); l1 = xsum4(l1);
        {
            const lptr xo = lds + (qg * 2 + grp) * 9216;
#pragma unroll
            for (int d = 0; d < 4; ++d)
#pragma unroll
                for (int qt = 0; qt < 2; ++qt) *(LAS f32x4*)(xo + (d * 2 + qt) * 1024 + l * 16) = sel(grp == 0, r.o[4 + d][qt], r.o[d][qt]);
            *(LAS f32x4*)(xo + 8192 + l * 16) = (f32x4){m0, m1, l0, l1};
            asm volatile("s_waitcnt lgkmcnt(0)" ::: "memory"); BARRIER();
            const lcptr xi = lds + (qg * 2 + (grp ^ 1)) * 9216;
            const f32x4 st = *(const LAS f32x4*)(xi + 8192 + l * 16);
            const float M0 = __builtin_fmaxf(m0, st[0]), M1 = __builtin_fmaxf(m1, st[1]);
            const float wa0 = __builtin_amdgcn_exp2f(m0 - M0), wb0 = __builtin_amdgcn_exp2f(st[0] - M0), wa1 = __builtin_amdgcn_exp2f(m1 - M1), wb1 = __builtin_amdgcn_exp2f(st[1] - M1);
            const float i0 = __builtin_amdgcn_rcpf(wa0 * l0 + wb0 * st[2]), i1 = __builtin_amdgcn_rcpf(wa1 * l1 + wb1 * st[3]);
            const float ca0 = wa0 * i0, cb0 = wb0 * i0, ca1 = wa1 * i1, cb1 = wb1 * i1;
#pragma unroll
            for (int up = 0; up < 2; ++up)
#pragma unroll
                for (int qt = 0; qt < 2; ++qt) {
                    const f32x4 pa = *(const LAS f32x4*)(xi + ((2 * up) * 2 + qt) * 1024 + l * 16), pb = *(const LAS f32x4*)(xi + ((2 * up + 1) * 2 + qt) * 1024 + l * 16);
                    const f32x4 oa = sel(grp == 0, r.o[2 * up][qt], r.o[4 + 2 * up][qt]), ob = sel(grp == 0, r.o[2 * up + 1][qt], r.o[4 + 2 * up + 1][qt]);
                    const float ca = qt ? ca1 : ca0, cb = qt ? cb1 : cb0;
                    st16_sc1(sws, pack8(oa * ca + pa * cb, ob * ca + pb * cb), vl16, OFF_CTX + ((8 * (15 - j) + 2 * qg + qt) * 128 + 4 * h + 2 * grp + up) * 1024);
                }
        }
        asm volatile("s_waitcnt vmcnt(0) lgkmcnt(0)" ::: "memory"); BARRIER();
        if (tid == 0) set_flag(flags, FL_CTX + j * 32 + h, token);
    }
    STAMP(8);
    {
        const int l = lane_id(), tid = w * 64 + l, g = l >> 4;
        const int rb = (xcd >> 1) * 8 + (bi >> 2), cb = (xcd & 1) * 4 + (bi & 3);
        const int xq = rb >> 1, jj = xq < 8 ? xq : 15 - xq;
        const unsigned vl16 = l * 16;
        const unsigned wpo = OFF_WP + cb * 5 * 1024;
        f32x4 gs[3];
        auto gemm = [&](unsigned abase, unsigned astride, int u0, int nsteps, int ulimit, auto fin) {
            f32x4 acc[5][4];
#pragma unroll
            for (int n = 0; n < 5; ++n)
#pragma unroll
                for (int m = 0; m < 4; ++m) acc[n][m] = (f32x4){0.f, 0.f, 0.f, 0.f};
            u32x4 xa[3][4], wa[3][5];
            auto load = [&](int sidx, u32x4 (&xd)[4], u32x4 (&wd)[5]) {
                const int u = w + 8 * sidx; const bool ok = u < ulimit;
                const unsigned ao = ok ? abase + u * 1024 : 0xF0000000u, wo_ = ok ? wpo + (u0 + u) * 40960 : 0xF0000000u;
#pragma unroll
                for (int m = 0; m < 4; ++m) xd[m] = ld16(sws, vl16, ao + m * astride);
#pragma unroll
                for (int n = 0; n < 5; ++n) wd[n] = ld16(sws, vl16, wo_ + n * 1024);
            };
            auto mm = [&](const u32x4 (&xd)[4], const u32x4 (&wd)[5]) {
#pragma unroll
                for (int n = 0; n < 5; ++n)
#pragma unroll
                    for (int m = 0; m < 4; ++m) acc[n][m] = MF16(asbf(wd[n]), asbf(xd[m]), acc[n][m]);
            };
            load(0, xa[0], wa[0]); load(1, xa[1], wa[1]);
            int sidx = 0;
            for (; sidx + 3 <= nsteps; sidx += 3) {
                load(sidx + 2, xa[2], wa[2]); SBAR(); mm(xa[0], wa[0]); SBAR();
                load(sidx + 3, xa[0], wa[0]); SBAR(); mm(xa[1], wa[1]); SBAR();
                load(sidx + 4, xa[1], wa[1]); SBAR(); mm(xa[2], wa[2]); SBAR();
            }
            if (sidx < nsteps) { mm(xa[0], wa[0]); }
            if (sidx + 1 < nsteps) { mm(xa[1], wa[1]); }
            BARRIER();
#pragma unroll
            for (int n = 0; n < 5; ++n)
#pragma unroll
                for (int m = 0; m < 4; ++m) *(LAS f32x4*)(lds + w * 20480 + (n * 4 + m) * 1024 + l * 16) = acc[n][m];
            asm volatile("s_waitcnt lgkmcnt(0)" ::: "memory"); BARRIER();
#pragma unroll
            for (int i = 0; i < 3; ++i) {
                const int t = w < 4 ? 3 * w + i : 12 + 2 * (w - 4) + i;
                if (i < 2 || w < 4) {
                    f32x4 sum = *(const LAS f32x4*)(lds + t * 1024 + l * 16);
#pragma unroll
                    for (int ww = 1; ww < 8; ++ww) sum += *(const LAS f32x4*)(lds + ww * 20480 + t * 1024 + l * 16);
                    fin(i, t, sum);
                }
                asm volatile("" ::: "memory"); SBAR();
            }
        };
        if (w == 0) {
            wait_flags(flags, (int)FL_P0 + l, (int)FL_P0 + 64 + l, token);
            wait_flags(flags, (int)FL_P0 + 128 + l, (int)FL_P0 + 192 + l, token);
            wait_flags(flags, l < 32 ? (int)FL_ACT + jj * 32 + l : -1, -1, token);
        }
        BARRIER();
        gemm(OFF_ACT + (4 * rb) * 54 * 1024, 54 * 1024, 128, 7, 54, [&](int i, int, f32x4 v) {
#pragma unroll
            for (int e = 0; e < 4; ++e) v[e] = v[e] * __builtin_amdgcn_rcpf(1.f + __builtin_amdgcn_exp2f(-1.4426950408889634f * v[e]));
            gs[i] = v; PIN(gs[i]); });
        STAMP(9);
        if (w == 0) wait_flags(flags, l < 32 ? (int)FL_CTX + jj * 32 + l : -1, -1, token);
        BARRIER();
        STAMP(10);
        gemm(OFF_CTX + (4 * rb) * 128 * 1024, 128 * 1024, 0, 16, 128, [&](int i, int t, f32x4 v) {
            const int n = t >> 2, m = t & 3;
            const int row = 64 * rb + 16 * m + (l & 15), col = 80 * cb + 16 * n + 4 * g;
            const u32x2 xv = *(const u32x2*)(x + row * 640 + col);
            const f32x4 o = {bflo(xv[0]) + v[0] * gs[i][0], bfhi(xv[0]) + v[1] * gs[i][1], bflo(xv[1]) + v[2] * gs[i][2], bfhi(xv[1]) + v[3] * gs[i][3]};
            *(u32x2*)(out + row * 640 + col) = (u32x2){cvtpk(o[0], o[1]), cvtpk(o[2], o[3])}; });
    }
    STAMP(11);
}

extern "C" void kernel_launch(void* const* d_in, const int* in_sizes, int n_in, void* d_out, int out_size, void* d_ws, size_t ws_size, hipStream_t stream) {
    (void)in_sizes; (void)n_in; (void)out_size; (void)ws_size;
    (void)hipMemsetAsync(d_ws, 0, 65536, stream);
    fused_kernel<<<256, 512, 0, stream>>>((const bf16_t*)d_in[0], (const bf16_t*)d_in[1], (const bf16_t*)d_in[2], (const bf16_t*)d_in[3], (const bf16_t*)d_in[4],
                                          (const bf16_t*)d_in[5], (const bf16_t*)d_in[7], (const bf16_t*)d_in[8], (const bf16_t*)d_in[9], (bf16_t*)d_out, (char*)d_ws);
}
```

```cpp
#include <hip/hip_runtime.h>
#include <stdint.h>
#include <utility>

typedef unsigned short bf16_t;
typedef short bf16x8 __attribute__((ext_vector_type(8)));
typedef short s16x4 __attribute__((ext_vector_type(4)));
typedef short v4i16_t __attribute__((ext_vector_type(4)));
typedef float f32x4 __attribute__((ext_vector_type(4)));
typedef float f32x2 __attribute__((ext_vector_type(2)));
typedef unsigned u32x4 __attribute__((ext_vector_type(4)));
typedef unsigned u32x2 __attribute__((ext_vector_type(2)));
typedef __bf16 bf16x2_t __attribute__((ext_vector_type(2)));
typedef __amdgpu_buffer_rsrc_t rsrc_t;
typedef unsigned long long u64;
#define LAS __attribute__((address_space(3)))
#define GAS __attribute__((address_space(1)))
typedef LAS char* lptr;
typedef LAS const char* lcptr;
typedef GAS u64 gu64;
#define DI __device__ __forceinline__
#define SBAR() __builtin_amdgcn_sched_barrier(0)
#define PIN(x) asm volatile("" : "+v"(x))
#define MF16(a, b, c) __builtin_amdgcn_mfma_f32_16x16x32_bf16(a, b, c, 0, 0, 0)
#define WAITV(n) asm volatile("s_waitcnt vmcnt(" #n ")" ::: "memory")
#define WAITL(n) asm volatile("s_waitcnt lgkmcnt(" #n ")" ::: "memory")
#define BARRIER() do { asm volatile("" ::: "memory"); __builtin_amdgcn_s_barrier(); asm volatile("" ::: "memory"); } while (0)

#define UPTO 4
#define STAMPS 0

constexpr unsigned OFF_FLAGS = 0;
constexpr unsigned OFF_XN = 65536;
constexpr unsigned OFF_K = OFF_XN + 2621440;
constexpr unsigned OFF_V = OFF_K + 16777216;
constexpr unsigned OFF_ACT = OFF_V + 16777216 + 8388608;
constexpr unsigned OFF_CTX = OFF_ACT + 7077888;
constexpr unsigned OFF_WP = OFF_CTX + 16777216;
constexpr unsigned OFF_ST = OFF_WP + 182 * 40 * 1024;
constexpr unsigned WS_BYTES = OFF_ST + 256 * 16 * 8;
constexpr unsigned FL_P0 = 0, FL_KV = 256, FL_ACT = 512, FL_CTX = 768;
constexpr int SC1 = 16;

DI unsigned cvtpk(float lo, float hi) { f32x2 v = {lo, hi}; bf16x2_t b = __builtin_convertvector(v, bf16x2_t); return __builtin_bit_cast(unsigned, b); }
DI u32x4 pack8(f32x4 a, f32x4 b) { u32x4 w = {cvtpk(a[0], a[1]), cvtpk(a[2], a[3]), cvtpk(b[0], b[1]), cvtpk(b[2], b[3])}; return w; }
DI float bflo(unsigned u) { return __uint_as_float(u << 16); }
DI float bfhi(unsigned u) { return __uint_as_float(u & 0xffff0000u); }
DI s16x4 vtr(lcptr p) { return __builtin_bit_cast(s16x4, __builtin_amdgcn_ds_read_tr16_b64_v4i16((LAS v4i16_t*)p)); }
DI bf16x8 cat(s16x4 a, s16x4 b) { return (bf16x8){a[0], a[1], a[2], a[3], b[0], b[1], b[2], b[3]}; }
DI bf16x8 ld128(lcptr p) { return *(const LAS bf16x8*)p; }
DI bf16x8 asbf(u32x4 v) { return __builtin_bit_cast(bf16x8, v); }
DI rsrc_t mk_rsrc(const void* p, unsigned bytes) { return __builtin_amdgcn_make_buffer_rsrc((void*)p, (short)0, (int)bytes, 0x00020000); }
DI void dma16(rsrc_t r, unsigned voff, unsigned soff, unsigned ldsoff) {
    asm volatile("s_mov_b32 m0, %0\n\ts_nop 3\n\tbuffer_load_dwordx4 %1, %2, %3 offen lds" ::"s"(ldsoff), "v"(voff), "s"(r), "s"(soff) : "memory");
}
DI void dma16x4(rsrc_t r, unsigned voff, unsigned soff, unsigned ldsoff) {
    asm volatile("s_mov_b32 m0, %0\n\ts_nop 3\n\tbuffer_load_dwordx4 %1, %2, %3 offen lds\n\tbuffer_load_dwordx4 %1, %2, %3 offen offset:1024 lds\n\t"
                 "buffer_load_dwordx4 %1, %2, %3 offen offset:2048 lds\n\tbuffer_load_dwordx4 %1, %2, %3 offen offset:3072 lds" ::"s"(ldsoff), "v"(voff), "s"(r), "s"(soff) : "memory");
}
DI void st16_sc1(rsrc_t r, u32x4 v, unsigned voff, unsigned soff) { asm volatile("s_nop 4\n\tbuffer_store_dwordx4 %0, %1, %2, %3 offen sc1\n\ts_nop 1" ::"v"(v), "v"(voff), "s"(r), "s"(soff) : "memory"); }
template <int OFF> DI void st16_sc1o(rsrc_t r, u32x4 v, unsigned voff, unsigned soff) { asm volatile("s_nop 4\n\tbuffer_store_dwordx4 %0, %1, %2, %3 offen offset:%4 sc1\n\ts_nop 1" ::"v"(v), "v"(voff), "s"(r), "s"(soff), "i"(OFF) : "memory"); }
DI u32x4 ld16(rsrc_t r, unsigned voff, unsigned soff) { return __builtin_amdgcn_raw_buffer_load_b128(r, voff, soff, 0); }
DI u32x4 ld16_sc1(rsrc_t r, unsigned voff, unsigned soff) { return __builtin_amdgcn_raw_buffer_load_b128(r, voff, soff, SC1); }
template <class T> DI T sel(bool c, T a, T b) { return c ? a : b; }
DI int lane_id() { int l; asm volatile("v_mbcnt_lo_u32_b32 %0, -1, 0\n\tv_mbcnt_hi_u32_b32 %0, -1, %0" : "=v"(l)); return l; }
DI void set_flag(gu64* flags, unsigned idx, u64 token) { __hip_atomic_store(flags + idx * 8, token, __ATOMIC_RELAXED, __HIP_MEMORY_SCOPE_AGENT); }
DI u64 poll_flag(gu64* flags, unsigned idx) { return __hip_atomic_load(flags + idx * 8, __ATOMIC_RELAXED, __HIP_MEMORY_SCOPE_AGENT); }
DI void spin_flags(gu64* flags, unsigned idx, u64 token) {
    const unsigned long long t0 = __builtin_amdgcn_s_memrealtime();
    for (;;) {
        __builtin_amdgcn_s_sleep(1);
        const u64 v = poll_flag(flags, idx);
        if (__all(v == token)) break;
        if (__builtin_amdgcn_s_memrealtime() - t0 > 2000000ull) break;
    }
}
DI void check_flags(gu64* flags, unsigned idx, u64 seen, u64 token) { if (!__all(seen == token)) spin_flags(flags, idx, token); }
DI void acquire_issue() { asm volatile("buffer_inv sc1" ::: "memory"); }
DI u64 uni64(u64 v) { return ((u64)(unsigned)__builtin_amdgcn_readfirstlane((unsigned)(v >> 32)) << 32) | (unsigned)__builtin_amdgcn_readfirstlane((unsigned)v); }
#define STAMP(i) do { } while (0)
#define CSTAMP(i) do { } while (0)

struct Sync { gu64* flags; u64 token; int b, h, j, w; };

struct P2Ctx { rsrc_t sws, swq, sw1; unsigned vl16, vwq, vw1; unsigned sxo, swqo, sw1o, skv; int w; lcptr xb; lcptr wq[4]; lcptr wf[2]; };
DI void p2_dma1(const P2Ctx& c, int st, int ks, int i) {
    if (i < 4) dma16(c.sws, c.vl16, c.sxo + (i >> 1) * 20480 + (i & 1) * 1024 + ks * 2048, st * 32768 + (4 * c.w + i) * 1024);
    else if (i < 6) dma16(c.swq, c.vwq, c.swqo + (i - 4) * 128 + ks * 524288, 65536 + st * 24576 + (i - 4) * 8192 + c.w * 1024);
    else dma16(c.sw1, c.vw1, c.sw1o + ks * (64 * 3456), 65536 + st * 24576 + 16384 + c.w * 1024);
}

struct P1Ctx { rsrc_t sx, sw; unsigned vx[2], vw[2]; unsigned sxo, swo; int w; lcptr xb[2]; lcptr wb[4]; };
DI void p1_dma1(const P1Ctx& c, int st, int ks, int i) {
    if (i < 4) dma16(c.sx, c.vx[i & 1], c.sxo + i * 8192 + ks * 128, st * 32768 + (4 * c.w + i) * 1024);
    else dma16(c.sw, c.vw[i & 1], c.swo + (i - 4) * 65536 + ks * 524288, 65536 + st * 32768 + (4 * c.w + (i - 4)) * 1024);
}
DI void p1_dma(const P1Ctx& c, int st, int ks) {
#pragma unroll
    for (int i = 0; i < 8; ++i) p1_dma1(c, st, ks, i);
}
template <int ST, int UP, int I> DI void p1_rdw(const P1Ctx& c, bf16x8 (&wf)[4], s16x4 (&wl)[4]) {
    constexpr int nt = I >> 1, rd = I & 1;
    const s16x4 v = vtr(c.wb[nt] + ST * 32768 + UP * 4096 + rd * 512);
    if constexpr (rd == 0) wl[nt] = v; else wf[nt] = cat(wl[nt], v);
}
template <int ST, int G> DI bf16x8 p1_rdx(const P1Ctx& c) { return ld128(c.xb[G >> 3] + ST * 32768 + (G & 7) * 2048); }
template <bool ISV, int KS, int G> DI void p1_group(const P1Ctx& c, const P2Ctx& c2, const Sync& y, u64& seen, f32x4 (&acc)[4][8], bf16x8 (&wA)[4], bf16x8 (&wB)[4], s16x4 (&wl)[4], bf16x8 (&xr)[4]) {
    constexpr int up = G >> 3, mt = G & 7, ST = KS & 1;
    constexpr bool NEXT = KS < 7;
    if constexpr (G == 13) {
        asm volatile("s_waitcnt vmcnt(0) lgkmcnt(0)" ::: "memory"); BARRIER();
        if constexpr (!ISV) {
            if constexpr (KS == 0) { if (y.w == 0 && lane_id() == 0) set_flag(y.flags, FL_P0 + y.b, y.token); }
            if constexpr (KS == 5) { if (y.w == 0) { const int l = lane_id() & 31; check_flags(y.flags, FL_P0 + (l < 16 ? 16 * y.j + l : 16 * (15 - y.j) + (l - 16)), seen, y.token); } }
        }
        SBAR();
    }
    const bf16x8 x = xr[G & 3];
#pragma unroll
    for (int nt = 0; nt < 4; ++nt) {
        const bf16x8 wv = up == 0 ? wA[nt] : wB[nt];
        if constexpr (ISV) acc[nt][mt] = MF16(x, wv, acc[nt][mt]); else acc[nt][mt] = MF16(wv, x, acc[nt][mt]);
        if (nt == 0) {
            if constexpr (G + 3 < 16) xr[(G + 3) & 3] = p1_rdx<ST, G + 3>(c);
            else if constexpr (NEXT) xr[(G + 3) & 3] = p1_rdx<ST ^ 1, G + 3 - 16>(c);
        }
        if constexpr (G >= 2 && G < 6) { if (nt == 1) p1_rdw<ST, 1, 2 * (G - 2)>(c, wB, wl); if (nt == 2) p1_rdw<ST, 1, 2 * (G - 2) + 1>(c, wB, wl); }
        if constexpr (G >= 13 && NEXT) {
            if (nt == 1) p1_rdw<ST ^ 1, 0, 3 * (G - 13)>(c, wA, wl);
            if (nt == 2) p1_rdw<ST ^ 1, 0, 3 * (G - 13) + 1>(c, wA, wl);
            if constexpr (G < 15) { if (nt == 3) p1_rdw<ST ^ 1, 0, 3 * (G - 13) + 2>(c, wA, wl); }
        }
        if constexpr (G >= 13) { constexpr int d0 = 3 * (G - 13);
            if constexpr (KS < 6) { if (nt >= 1 && d0 + nt - 1 < 8) p1_dma1(c, ST, KS + 2, d0 + nt - 1); }
            else { if (nt >= 1 && d0 + nt - 1 < 7) p2_dma1(c2, ST, KS - 6, d0 + nt - 1); } }
        SBAR();
    }
    if constexpr (G == 15 && !ISV) {
        if constexpr (KS == 4) { if (y.w == 0) { const int l = lane_id() & 31; seen = poll_flag(y.flags, FL_P0 + (l < 16 ? 16 * y.j + l : 16 * (15 - y.j) + (l - 16))); } SBAR(); }
        if constexpr (KS == 5) { if (y.w == 0) acquire_issue(); SBAR(); }
    }
}
template <bool ISV, int KS, int... Gs> DI void p1_step(const P1Ctx& c, const P2Ctx& c2, const Sync& y, u64& seen, f32x4 (&acc)[4][8], bf16x8 (&wA)[4], bf16x8 (&wB)[4], s16x4 (&wl)[4], bf16x8 (&xr)[4], std::integer_sequence<int, Gs...>) {
    (p1_group<ISV, KS, Gs>(c, c2, y, seen, acc, wA, wB, wl, xr), ...);
}
template <bool ISV, int... KSs> DI void p1_main(const P1Ctx& c, const P2Ctx& c2, const Sync& y, u32x4 (&kv)[16], std::integer_sequence<int, KSs...>) {
    f32x4 acc[4][8];
#pragma unroll
    for (int a = 0; a < 4; ++a)
#pragma unroll
        for (int b = 0; b < 8; ++b) acc[a][b] = (f32x4){0.f, 0.f, 0.f, 0.f};
    bf16x8 wA[4], wB[4], xr[4]; s16x4 wl[4];
    u64 seen = 0;
    asm volatile("" ::: "memory");
    p1_rdw<0, 0, 0>(c, wA, wl); p1_rdw<0, 0, 1>(c, wA, wl); p1_rdw<0, 0, 2>(c, wA, wl); p1_rdw<0, 0, 3>(c, wA, wl);
    p1_rdw<0, 0, 4>(c, wA, wl); p1_rdw<0, 0, 5>(c, wA, wl); p1_rdw<0, 0, 6>(c, wA, wl); p1_rdw<0, 0, 7>(c, wA, wl);
    xr[0] = p1_rdx<0, 0>(c); xr[1] = p1_rdx<0, 1>(c); xr[2] = p1_rdx<0, 2>(c);
    SBAR();
    (p1_step<ISV, KSs>(c, c2, y, seen, acc, wA, wB, wl, xr, std::make_integer_sequence<int, 16>{}), ...);
#pragma unroll
    for (int a = 0; a < 8; ++a)
#pragma unroll
        for (int b = 0; b < 2; ++b) {
            if constexpr (ISV) kv[a * 2 + b] = pack8(acc[(a & 1) * 2 + b][(a >> 1) * 2], acc[(a & 1) * 2 + b][(a >> 1) * 2 + 1]);
            else kv[a * 2 + b] = pack8(acc[2 * b][a], acc[2 * b + 1][a]);
        }
}

template <bool HASF, int ST, int I> DI bf16x8 p2_rdw(const P2Ctx& c) {
    constexpr int NW = HASF ? 6 : 4, up = I / NW, n = I % NW;
    lcptr p = (n < 4 ? c.wq[n & 3] : c.wf[n & 1]) + ST * 24576 + up * 4096;
    return cat(vtr(p), vtr(p + 512));
}
template <int ST, int UP, int MT> DI bf16x8 p2_rdx(const P2Ctx& c) { return ld128(c.xb + ST * 32768 + MT * 2048 + UP * 1024); }
template <int I> DI void p2_kvst(const P2Ctx& c, const u32x4 (&kv)[16], bool isv) {
    st16_sc1(c.sws, kv[I], c.vl16, c.skv + (isv ? (unsigned)(((I >> 3) * 16 + ((I >> 2) & 1) * 8 + (I & 3)) * 1024) : (unsigned)((I >> 1) * 4096 + (I & 1) * 1024)));
}
template <bool HASF, int KS, int G> DI void p2_group(const P2Ctx& c, const Sync& y, u64& seen, f32x4 (&acc)[6][4], bf16x8 (&wr)[4], bf16x8 (&x0)[4], bf16x8 (&x1)[4], const u32x4 (&kv)[16], bool isv) {
    constexpr int NW = HASF ? 6 : 4, NG = 2 * NW, ST = KS & 1, up = G / NW, n = G % NW;
    constexpr bool LAST = (KS == 9);
    if constexpr (G == NG - 3) {
        if constexpr (KS == 0) asm volatile("s_waitcnt vmcnt(4) lgkmcnt(0)" ::: "memory");
        else if constexpr (KS <= 6) asm volatile("s_waitcnt vmcnt(2) lgkmcnt(0)" ::: "memory");
        else asm volatile("s_waitcnt vmcnt(0) lgkmcnt(0)" ::: "memory");
        BARRIER();
        if constexpr (KS + 2 < 10) {
#pragma unroll
            for (int i = 0; i < (HASF ? 7 : 6); ++i) p2_dma1(c, ST, KS + 2, i);
        }
        if constexpr (KS < 6) { p2_kvst<4 + 2 * KS>(c, kv, isv); p2_kvst<5 + 2 * KS>(c, kv, isv); }
        if constexpr (KS == 7) { if (y.w == 0 && lane_id() == 0) set_flag(y.flags, FL_KV + y.h * 8 + y.j, y.token); }
        if constexpr (KS == 8) { if (y.w == 0) seen = poll_flag(y.flags, FL_KV + y.h * 8 + (lane_id() & 7)); }
        if constexpr (KS == 9) { if (y.w == 0) { check_flags(y.flags, FL_KV + y.h * 8 + (lane_id() & 7), seen, y.token); acquire_issue(); } }
        SBAR();
    }
    const bf16x8 wcur = wr[G & 3];
#pragma unroll
    for (int mt = 0; mt < 4; ++mt) {
        acc[n][mt] = MF16(wcur, up == 0 ? x0[mt] : x1[mt], acc[n][mt]);
        if (mt == 0) {
            if constexpr (G + 3 < NG) wr[(G + 3) & 3] = p2_rdw<HASF, ST, G + 3>(c);
            else if constexpr (!LAST) wr[(G + 3) & 3] = p2_rdw<HASF, ST ^ 1, G + 3 - NG>(c);
        }
        if (mt == 1) {
            if constexpr (G < 4) x1[G] = p2_rdx<ST, 1, G>(c);
            else if constexpr (G >= NG - 3 && !LAST) x0[G - (NG - 3)] = p2_rdx<ST ^ 1, 0, G - (NG - 3)>(c);
        }
        if (mt == 2) { if constexpr (G == NG - 1 && !LAST) x0[3] = p2_rdx<ST ^ 1, 0, 3>(c); }
        SBAR();
    }
}
template <bool HASF, int KS, int... Gs> DI void p2_step(const P2Ctx& c, const Sync& y, u64& seen, f32x4 (&acc)[6][4], bf16x8 (&wr)[4], bf16x8 (&x0)[4], bf16x8 (&x1)[4], const u32x4 (&kv)[16], bool isv, std::integer_sequence<int, Gs...>) {
    (p2_group<HASF, KS, Gs>(c, y, seen, acc, wr, x0, x1, kv, isv), ...);
}
template <bool HASF, int... KSs> DI void p2_main(const P2Ctx& c, const Sync& y, f32x4 (&acc)[6][4], const u32x4 (&kv)[16], bool isv, std::integer_sequence<int, KSs...>) {
    bf16x8 wr[4], x0[4], x1[4];
    u64 seen = 0;
    asm volatile("" ::: "memory");
    wr[0] = p2_rdw<HASF, 0, 0>(c); wr[1] = p2_rdw<HASF, 0, 1>(c); wr[2] = p2_rdw<HASF, 0, 2>(c);
    x0[0] = p2_rdx<0, 0, 0>(c); x0[1] = p2_rdx<0, 0, 1>(c); x0[2] = p2_rdx<0, 0, 2>(c); x0[3] = p2_rdx<0, 0, 3>(c);
    SBAR();
    (p2_step<HASF, KSs>(c, y, seen, acc, wr, x0, x1, kv, isv, std::make_integer_sequence<int, (HASF ? 12 : 8)>{}), ...);
}
DI float mish(float h) {
    const float t = __builtin_amdgcn_exp2f(__builtin_fminf(h, 20.f) * 1.4426950408889634f);
    const float n = t * (t + 2.f);
    return h * n * __builtin_amdgcn_rcpf(n + 2.f);
}

constexpr int PD = 8;
struct AttnRegs { f32x4 o[8][2]; f32x4 s[4][2]; bf16x8 q[2][4]; u32x4 pk[2][2]; f32x4 negm[2]; bf16x8 op[32]; };
template <int I> DI void a_read(AttnRegs& r, lcptr vp, lcptr kp) {
    if constexpr (I < 16) r.op[I] = ld128(vp + I * 1024);
    else if constexpr (I < 32) r.op[I] = ld128(kp + (((I - 16) & 3) * 4 + ((I - 16) >> 2)) * 1024);
}
template <int I> DI void a_slot(AttnRegs& r, lcptr vp, lcptr kp) {
    if constexpr (I < 16) { constexpr int ks = I >> 3, dt = I & 7;
        r.o[dt][0] = MF16(r.op[I], asbf(r.pk[0][ks]), r.o[dt][0]); r.o[dt][1] = MF16(r.op[I], asbf(r.pk[1][ks]), r.o[dt][1]); }
    else { constexpr int u = (I - 16) >> 2, kt = (I - 16) & 3;
        r.s[kt][0] = MF16(r.op[I], r.q[0][u], u == 0 ? r.negm[0] : r.s[kt][0]); r.s[kt][1] = MF16(r.op[I], r.q[1][u], u == 0 ? r.negm[1] : r.s[kt][1]); }
    a_read<I + PD>(r, vp, kp);
    SBAR();
}
template <int B, int... Is> DI void a_run(AttnRegs& r, lcptr vp, lcptr kp, std::integer_sequence<int, Is...>) { (a_slot<B + Is>(r, vp, kp), ...); }
template <int B, int... Is> DI void a_pre(AttnRegs& r, lcptr vp, lcptr kp, std::integer_sequence<int, Is...>) { (a_read<B + Is>(r, vp, kp), ...); }

DI void a_exp_pack(AttnRegs& r, float& a0, float& a1) {
    a0 = 0.f; a1 = 0.f;
#pragma unroll
    for (int ks = 0; ks < 2; ++ks)
#pragma unroll
        for (int qt = 0; qt < 2; ++qt) {
            float e[8];
#pragma unroll
            for (int i = 0; i < 4; ++i) { e[i] = __builtin_amdgcn_exp2f(r.s[2 * ks][qt][i]); e[4 + i] = __builtin_amdgcn_exp2f(r.s[2 * ks + 1][qt][i]); }
#pragma unroll
            for (int i = 0; i < 8; ++i) { if (qt == 0) a0 += e[i]; else a1 += e[i]; }
            r.pk[qt][ks] = (u32x4){cvtpk(e[0], e[1]), cvtpk(e[2], e[3]), cvtpk(e[4], e[5]), cvtpk(e[6], e[7])};
        }
}
DI float xmax4(float v) { v = __builtin_fmaxf(v, __shfl_xor(v, 16)); return __builtin_fmaxf(v, __shfl_xor(v, 32)); }
DI float xsum4(float v) { v += __shfl_xor(v, 16); return v + __shfl_xor(v, 32); }

struct AttnCtx { rsrc_t sws; unsigned vl16, kbase, vbase, ctxl; int nL, grp, qg, j, rowrel, swap_s, first_s; lcptr kl, vl, ql; };
struct AttnSm { float m0, m1, l0, l1, ll0, ll1; };
DI void a_dmaK(const AttnCtx& c, int n) { dma16x4(c.sws, c.vl16, c.kbase + n * 16384 + c.qg * 4096, (n & 3) * 16384 + c.qg * 4096); }
DI void a_dmaV(const AttnCtx& c, int n) { dma16x4(c.sws, c.vl16, c.vbase + n * 16384 + c.qg * 4096, 65536 + (n & 3) * 16384 + c.qg * 4096); }
DI void a_slotfull(const AttnCtx& c, const Sync& y, AttnRegs& r, AttnSm& z, const int s) {
    const int nL = c.nL, grp = c.grp;
    const bool SWAP = (s == c.swap_s), FIN = (s == c.swap_s + 1), FIRST = (s == c.first_s) || FIN;
    const int tcur = s < nL ? s : 2 * s - nL + grp;
    {
        const int s2 = s + 2, s1 = s + 1;
        if (s2 <= 16 && (grp == 0 || s2 >= nL)) a_dmaK(c, s2 < nL ? s2 : 2 * s2 - nL + grp);
        if (s1 <= 16 && (grp == 0 || s1 >= nL)) a_dmaV(c, s1 < nL ? s1 : 2 * s1 - nL + grp);
    }
    if (s == 2) { if (y.w == 0 && lane_id() == 0) set_flag(y.flags, FL_ACT + y.j * 32 + y.h, y.token); }
    if (SWAP) {
        asm volatile("ds_read_b128 %0, %8\n\tds_read_b128 %1, %8 offset:1024\n\tds_read_b128 %2, %8 offset:2048\n\tds_read_b128 %3, %8 offset:3072\n\t"
                     "ds_read_b128 %4, %8 offset:4096\n\tds_read_b128 %5, %8 offset:5120\n\tds_read_b128 %6, %8 offset:6144\n\tds_read_b128 %7, %8 offset:7168\n\ts_waitcnt lgkmcnt(0)"
                     : "=&v"(r.q[0][0]), "=&v"(r.q[0][1]), "=&v"(r.q[0][2]), "=&v"(r.q[0][3]), "=&v"(r.q[1][0]), "=&v"(r.q[1][1]), "=&v"(r.q[1][2]), "=&v"(r.q[1][3])
                     : "v"(c.ql) : "memory");
    }
    if (FIN) {
        const float i0 = __builtin_amdgcn_rcpf(xsum4(z.ll0)), i1 = __builtin_amdgcn_rcpf(xsum4(z.ll1));
        const unsigned c1o = c.ctxl + 131072;
        st16_sc1o<0>(c.sws, pack8(r.o[0][0] * i0, r.o[1][0] * i0), c.vl16, c.ctxl); st16_sc1o<1024>(c.sws, pack8(r.o[2][0] * i0, r.o[3][0] * i0), c.vl16, c.ctxl);
        st16_sc1o<2048>(c.sws, pack8(r.o[4][0] * i0, r.o[5][0] * i0), c.vl16, c.ctxl); st16_sc1o<3072>(c.sws, pack8(r.o[6][0] * i0, r.o[7][0] * i0), c.vl16, c.ctxl);
        st16_sc1o<0>(c.sws, pack8(r.o[0][1] * i1, r.o[1][1] * i1), c.vl16, c1o); st16_sc1o<1024>(c.sws, pack8(r.o[2][1] * i1, r.o[3][1] * i1), c.vl16, c1o);
        st16_sc1o<2048>(c.sws, pack8(r.o[4][1] * i1, r.o[5][1] * i1), c.vl16, c1o); st16_sc1o<3072>(c.sws, pack8(r.o[6][1] * i1, r.o[7][1] * i1), c.vl16, c1o);
#pragma unroll
        for (int d = 0; d < 8; ++d) { r.o[d][0] = (f32x4){0.f, 0.f, 0.f, 0.f}; r.o[d][1] = (f32x4){0.f, 0.f, 0.f, 0.f}; }
    }
    {
        const bool heavy = (grp == 1) || (s >= nL);
        const int qb = heavy ? 15 - c.j : c.j, kbrel = 64 * tcur - 128 * qb;
        if (kbrel >= 0) {
            const int dq = c.rowrel - kbrel;
#pragma unroll
            for (int kt = 0; kt < 4; ++kt)
#pragma unroll
                for (int i = 0; i < 4; ++i) {
                    if (16 * kt + i > dq) r.s[kt][0][i] = -__builtin_inff();
                    if (16 * kt + i > dq + 16) r.s[kt][1][i] = -__builtin_inff();
                }
        }
    }
    float a0 = 0.f, a1 = 0.f;
    bool redo = FIRST;
    if (!FIRST) { a_exp_pack(r, a0, a1); redo = __builtin_expect(__any(!(a0 <= 65536.f) || !(a1 <= 65536.f)), 0); }
    if (redo) {
        float x0 = r.s[0][0][0], x1 = r.s[0][1][0];
#pragma unroll
        for (int kt = 0; kt < 4; ++kt)
#pragma unroll
            for (int i = 0; i < 4; ++i) { x0 = __builtin_fmaxf(x0, r.s[kt][0][i]); x1 = __builtin_fmaxf(x1, r.s[kt][1][i]); }
        x0 = xmax4(x0); x1 = xmax4(x1);
        if (!FIRST) { x0 = __builtin_fmaxf(x0, 0.f); x1 = __builtin_fmaxf(x1, 0.f);
            const float f0 = __builtin_amdgcn_exp2f(-x0), f1 = __builtin_amdgcn_exp2f(-x1);
#pragma unroll
            for (int d = 0; d < 8; ++d) { r.o[d][0] *= f0; r.o[d][1] *= f1; }
            z.l0 *= f0; z.l1 *= f1; }
        z.m0 += x0; z.m1 += x1;
        r.negm[0] = (f32x4){-z.m0, -z.m0, -z.m0, -z.m0}; r.negm[1] = (f32x4){-z.m1, -z.m1, -z.m1, -z.m1};
#pragma unroll
        for (int kt = 0; kt < 4; ++kt) { r.s[kt][0] -= x0; r.s[kt][1] -= x1; }
        a_exp_pack(r, a0, a1);
    }
    z.l0 += a0; z.l1 += a1;
    if (SWAP) {
        z.ll0 = z.l0; z.ll1 = z.l1; z.l0 = 0.f; z.l1 = 0.f; z.m0 = 0.f; z.m1 = 0.f;
        r.negm[0] = (f32x4){0.f, 0.f, 0.f, 0.f}; r.negm[1] = (f32x4){0.f, 0.f, 0.f, 0.f};
    }
    const int snext = s + 1, tnext = snext < nL ? snext : 2 * snext - nL + grp;
    const lcptr vp = c.vl + (tcur & 3) * 16384, kp = c.kl + (tnext & 3) * 16384;
    SBAR();
    a_pre<0>(r, vp, kp, std::make_integer_sequence<int, PD>{});
    SBAR(); BARRIER(); SBAR();
    __builtin_amdgcn_s_setprio(1);
    a_run<0>(r, vp, kp, std::make_integer_sequence<int, 16>{});
    if (s < 16) a_run<16>(r, vp, kp, std::make_integer_sequence<int, 16>{});
    __builtin_amdgcn_s_setprio(0);
    SBAR();
    if (FIN) WAITV(8); else WAITV(0);
    BARRIER(); SBAR();
}

__global__ __launch_bounds__(512, 2) void fused_kernel(const bf16_t* __restrict__ x, const bf16_t* __restrict__ ckv, const bf16_t* __restrict__ wq, const bf16_t* __restrict__ wuk,
                                                       const bf16_t* __restrict__ wuv, const bf16_t* __restrict__ wo, const bf16_t* __restrict__ w1, const bf16_t* __restrict__ b1,
                                                       const bf16_t* __restrict__ w2, bf16_t* __restrict__ out, char* __restrict__ ws) {
    __shared__ __attribute__((aligned(1024))) char smem[163840];
    const lptr lds = (lptr)smem;
    const int b = blockIdx.x, xcd = b & 7, bi = b >> 3, h = xcd * 4 + (bi >> 3), j = bi & 7;
    const int w = __builtin_amdgcn_readfirstlane(threadIdx.x >> 6);
    const rsrc_t sws = mk_rsrc(ws, WS_BYTES);
    STAMP(0);
    Sync y;
    y.flags = (gu64*)(ws + OFF_FLAGS); y.b = b; y.h = h; y.j = j; y.w = w;
    asm volatile("s_load_dwordx2 %0, %1, 0x20\n\ts_waitcnt lgkmcnt(0)" : "=s"(y.token) : "s"(ws) : "memory");
    y.token += 1;
    u32x4 kv[16];
    P1Ctx c1;
    P2Ctx c2;
    const bool isv = (w & 3) >= 2;
    {
        const int l = lane_id(), g = l >> 4, q = (l & 15) >> 2;
        u32x4 xr[10], xo0, xo1;
        {
            const rsrc_t sxi = mk_rsrc(x, 2048 * 640 * 2);
            const unsigned vx = (l & 7) * 1280 + (l >> 3) * 16, so = b * 8 * 1280;
#define LDX(dst, soff, off) asm volatile("buffer_load_dwordx4 %0, %1, %2, %3 offen offset:" #off : "=v"(dst) : "v"(vx), "s"(sxi), "s"(soff) : "memory")
            asm volatile("s_nop 4" ::: "memory");
            LDX(xr[0], so, 0); LDX(xr[1], so, 128); LDX(xr[2], so, 256); LDX(xr[3], so, 384); LDX(xr[4], so, 512);
            LDX(xr[5], so, 640); LDX(xr[6], so, 768); LDX(xr[7], so, 896); LDX(xr[8], so, 1024); LDX(xr[9], so, 1152);
            const unsigned so0 = so + w * 128, so1 = so + (8 + (w & 1)) * 128;
            asm volatile("s_nop 4" ::: "memory");
            LDX(xo0, so0, 0); LDX(xo1, so1, 0);
#undef LDX
        }
        {
            const int nb = w >> 1;
            c1.w = w;
            c1.sx = mk_rsrc(ckv, 2048 * 512 * 2);
            c1.sw = mk_rsrc(nb < 2 ? wuk : wuv, 512 * 4096 * 2);
#pragma unroll
            for (int par = 0; par < 2; ++par) {
                c1.vx[par] = (l >> 3) * 1024 + (((l & 7) ^ (4 * par + (l >> 4))) * 16);
                c1.vw[par] = (l >> 3) * 8192 + (((l & 7) ^ ((((l >> 4) & 1) | (par << 1)) << 1)) * 16);
            }
            c1.sxo = (256 * j + 32 * w) * 1024;
            c1.swo = (32 * (w & 1)) * 8192 + (h * 128 + 64 * (nb & 1)) * 2;
            const int wn = w & 3, wm = w >> 2, f = (l & 15) >> 1, s = ((q >> 1) & 1) | ((g & 1) << 1);
            c1.xb[0] = lds + wm * 16384 + (l & 15) * 128 + ((g ^ f) * 16);
            c1.xb[1] = lds + wm * 16384 + (l & 15) * 128 + (((g ^ f) ^ 4) * 16);
#pragma unroll
            for (int nt = 0; nt < 4; ++nt) c1.wb[nt] = lds + 65536 + wn * 8192 + (8 * g + q) * 128 + ((nt ^ s) * 32) + (l & 3) * 8;
        }
        p1_dma(c1, 0, 0);
        const int gw = b * 8 + w, pu = gw / 10, pq = gw - pu * 10;
        const bool hasjob = gw < 1820;
        {
            const rsrc_t sp = mk_rsrc(pu < 128 ? wo : w2, pu < 128 ? 4096 * 640 * 2 : 1728 * 640 * 2);
            const unsigned so = hasjob ? (unsigned)((pu < 128 ? pu : pu - 128) * 32 * 1280 + pq * 128) : 0xF0000000u;
#pragma unroll
            for (int i = 0; i < 4; ++i) dma16(sp, (l >> 3) * 1280 + (l & 7) * 16, so + i * 8 * 1280, 131072 + w * 4096 + i * 1024);
        }
        p1_dma(c1, 1, 1);
        asm volatile("s_waitcnt vmcnt(20)" : "+v"(xr[0]), "+v"(xr[1]), "+v"(xr[2]), "+v"(xr[3]), "+v"(xr[4]), "+v"(xr[5]), "+v"(xr[6]), "+v"(xr[7]), "+v"(xr[8]), "+v"(xr[9]), "+v"(xo0), "+v"(xo1) :: "memory");
        {
            f32x2 sa = {0.f, 0.f};
#pragma unroll
            for (int i = 0; i < 10; ++i)
#pragma unroll
                for (int e = 0; e < 4; ++e) sa += (f32x2){bflo(xr[i][e]), bfhi(xr[i][e])};
            float s = sa[0] + sa[1];
            s += __shfl_xor(s, 8); s += __shfl_xor(s, 16); s += __shfl_xor(s, 32);
            const float mean = s * (1.f / 640.f);
            f32x2 qa = {0.f, 0.f};
#pragma unroll
            for (int i = 0; i < 10; ++i)
#pragma unroll
                for (int e = 0; e < 4; ++e) { const f32x2 d = (f32x2){bflo(xr[i][e]), bfhi(xr[i][e])} - mean; qa += d * d; }
            float qq = qa[0] + qa[1];
            qq += __shfl_xor(qq, 8); qq += __shfl_xor(qq, 16); qq += __shfl_xor(qq, 32);
            const float rstd = 1.f / __builtin_sqrtf(qq * (1.f / 640.f) + 1e-5f);
            const unsigned vo = (l >> 3) * 256 + (l & 7) * 16, xo = OFF_XN + (b >> 1) * 20480 + (b & 1) * 128;
            u32x4 o0, o1;
#pragma unroll
            for (int e = 0; e < 4; ++e) { o0[e] = cvtpk((bflo(xo0[e]) - mean) * rstd, (bfhi(xo0[e]) - mean) * rstd); o1[e] = cvtpk((bflo(xo1[e]) - mean) * rstd, (bfhi(xo1[e]) - mean) * rstd); }
            st16_sc1(sws, o0, vo, xo + w * 2048);
            if (w < 2) st16_sc1(sws, o1, vo, xo + (8 + w) * 2048);
        }
        {
            c2.w = w; c2.sws = sws; c2.swq = mk_rsrc(wq, 640 * 4096 * 2); c2.sw1 = mk_rsrc(w1, 640 * 1728 * 2);
            c2.vl16 = l * 16;
            const int swz = ((l >> 4) & 1) | ((w & 1) << 1);
            c2.vwq = (l >> 3) * 8192 + (((l & 7) ^ (swz << 1)) * 16);
            c2.vw1 = (l >> 3) * 3456 + (((l & 7) ^ (swz << 1)) * 16);
            { const int mtl = 2 * w, mtg = mtl < 8 ? 8 * j + mtl : 8 * (15 - j) + (mtl - 8); c2.sxo = OFF_XN + mtg * 20480; }
            c2.swqo = (8 * w) * 8192 + h * 256;
            c2.sw1o = (8 * w) * 3456 + h * 128;
            { const int wn = w & 3, wm = w >> 2;
              c2.skv = isv ? OFF_V + (unsigned)(h * 32 + 4 * j + 2 * wm) * 16384 + (wn - 2) * 4096 : OFF_K + (unsigned)((h * 128 + 16 * j + 8 * wm) * 4 + 2 * wn) * 1024; }
        }
        WAITV(9);
        {
            lcptr p = lds + 131072 + w * 4096 + (4 * g + q) * 128 + (l & 3) * 8;
            bf16x8 fr[4];
#pragma unroll
            for (int i = 0; i < 4; ++i) fr[i] = cat(vtr(p + i * 32), vtr(p + i * 32 + 16 * 128));
            if (hasjob) {
#pragma unroll
                for (int i = 0; i < 4; ++i) st16_sc1(sws, __builtin_bit_cast(u32x4, fr[i]), l * 16, OFF_WP + (pu * 40 + pq * 4 + i) * 1024);
            }
        }
        BARRIER();
    }
    STAMP(1); CSTAMP(14);
    if (isv) p1_main<true>(c1, c2, y, kv, std::make_integer_sequence<int, 8>{}); else p1_main<false>(c1, c2, y, kv, std::make_integer_sequence<int, 8>{});
    STAMP(2); CSTAMP(15);
    p2_kvst<0>(c2, kv, isv); p2_kvst<1>(c2, kv, isv); p2_kvst<2>(c2, kv, isv); p2_kvst<3>(c2, kv, isv);
    const bool hasf = h < 27;
    AttnCtx c3;
    AttnRegs r;
    {
        const int l = lane_id(), g = l >> 4, q = (l & 15) >> 2;
        const int wn = w & 1, wm = w >> 1;
        const int s = ((q >> 1) & 1) | ((g & 1) << 1);
        c2.xb = lds + wm * 8192 + l * 16;
#pragma unroll
        for (int nt = 0; nt < 4; ++nt) c2.wq[nt] = lds + 65536 + wn * 8192 + (8 * g + q) * 128 + ((nt ^ s) * 32) + (l & 3) * 8;
#pragma unroll
        for (int ft = 0; ft < 2; ++ft) c2.wf[ft] = lds + 65536 + 16384 + (8 * g + q) * 128 + (((2 * wn + ft) ^ s) * 32) + (l & 3) * 8;
        f32x4 acc[6][4];
#pragma unroll
        for (int a = 0; a < 6; ++a)
#pragma unroll
            for (int m = 0; m < 4; ++m) acc[a][m] = (f32x4){0.f, 0.f, 0.f, 0.f};
        STAMP(3);
        if (hasf) p2_main<true>(c2, y, acc, kv, isv, std::make_integer_sequence<int, 10>{}); else p2_main<false>(c2, y, acc, kv, isv, std::make_integer_sequence<int, 10>{});
        STAMP(4);
        constexpr float C2 = 0.08838834764831845f * 1.4426950408889634f;
#pragma unroll
        for (int mt = 0; mt < 4; ++mt)
#pragma unroll
            for (int a = 0; a < 2; ++a)
                *(LAS u32x4*)(lds + 98304 + ((4 * wm + mt) * 4 + 2 * wn + a) * 1024 + l * 16) = pack8(acc[2 * a][mt] * C2, acc[2 * a + 1][mt] * C2);
        u32x4 actf[4];
        if (hasf) {
            const int f0 = 64 * h + 32 * wn + 4 * g;
            const u32x2 bA = *(const u32x2*)(b1 + f0), bB = *(const u32x2*)(b1 + f0 + 16);
            const f32x4 biasA = {bflo(bA[0]), bfhi(bA[0]), bflo(bA[1]), bfhi(bA[1])}, biasB = {bflo(bB[0]), bfhi(bB[0]), bflo(bB[1]), bfhi(bB[1])};
#pragma unroll
            for (int mt = 0; mt < 4; ++mt) {
                f32x4 hA = acc[4][mt] + biasA, hB = acc[5][mt] + biasB;
#pragma unroll
                for (int i = 0; i < 4; ++i) { hA[i] = mish(hA[i]); hB[i] = mish(hB[i]); }
                actf[mt] = pack8(hA, hB);
            }
        }
        asm volatile("s_waitcnt vmcnt(0) lgkmcnt(0)" ::: "memory"); BARRIER();
        const int grp = w >> 2, qg = w & 3;
        c3.sws = sws; c3.vl16 = l * 16; c3.kbase = OFF_K + (unsigned)h * 524288; c3.vbase = OFF_V + (unsigned)h * 524288;
        c3.ctxl = OFF_CTX + ((8 * j + 2 * qg) * 128 + 4 * h) * 1024;
        c3.nL = 2 * j + 2; c3.grp = grp; c3.qg = qg; c3.j = j; c3.rowrel = 32 * qg + (l & 15) - 4 * g; c3.kl = lds + l * 16; c3.vl = lds + 65536 + l * 16;
        c3.ql = lds + 131072 + qg * 8192 + l * 16;
        if (grp == 0) { a_dmaK(c3, 0); a_dmaV(c3, 0); } else { a_dmaK(c3, 1); }
        if (hasf) {
#pragma unroll
            for (int mt = 0; mt < 4; ++mt) { const int mtl = 4 * wm + mt, mtg = mtl < 8 ? 8 * j + mtl : 8 * (15 - j) + (mtl - 8);
                st16_sc1(sws, actf[mt], l * 16, OFF_ACT + (mtg * 54 + 2 * h + wn) * 1024); }
        }
#pragma unroll
        for (int qt = 0; qt < 2; ++qt)
#pragma unroll
            for (int u = 0; u < 4; ++u) r.q[qt][u] = ld128(lds + 98304 + ((8 * grp + 2 * qg + qt) * 4 + u) * 1024 + l * 16);
    }
    STAMP(5);
    {
        const int l = lane_id();
        const int grp = c3.grp, qg = c3.qg;
        const unsigned vl16 = c3.vl16;
#pragma unroll
        for (int d = 0; d < 8; ++d) { r.o[d][0] = (f32x4){0.f, 0.f, 0.f, 0.f}; r.o[d][1] = (f32x4){0.f, 0.f, 0.f, 0.f}; }
        r.negm[0] = (f32x4){0.f, 0.f, 0.f, 0.f}; r.negm[1] = (f32x4){0.f, 0.f, 0.f, 0.f};
        AttnSm z; z.m0 = 0.f; z.m1 = 0.f; z.l0 = 0.f; z.l1 = 0.f; z.ll0 = 0.f; z.ll1 = 0.f;
        if (hasf) asm volatile("s_waitcnt vmcnt(4) lgkmcnt(0)" ::: "memory"); else asm volatile("s_waitcnt vmcnt(0) lgkmcnt(0)" ::: "memory");
        BARRIER();
        STAMP(6); CSTAMP(12);
        a_pre<16>(r, c3.vl, c3.kl, std::make_integer_sequence<int, PD>{});
        if (grp == 1) BARRIER();
        a_run<16>(r, c3.vl, c3.kl, std::make_integer_sequence<int, 16>{});
        BARRIER();
        c3.swap_s = grp == 0 ? c3.nL - 1 : 99; c3.first_s = 0;
        asm volatile("" : "+s"(c3.swap_s), "+s"(c3.first_s));
#pragma nounroll
        for (int s = 0; s <= 16; ++s) a_slotfull(c3, y, r, z, s);
        float m0 = z.m0, m1 = z.m1, l0 = z.l0, l1 = z.l1;
        if (grp == 0) BARRIER();
        STAMP(7); CSTAMP(13);
        l0 = xsum4(l0); l1 = xsum4(l1);
        {
            const lptr xo = lds + (qg * 2 + grp) * 9216;
#pragma unroll
            for (int d = 0; d < 4; ++d)
#pragma unroll
                for (int qt = 0; qt < 2; ++qt) *(LAS f32x4*)(xo + (d * 2 + qt) * 1024 + l * 16) = sel(grp == 0, r.o[4 + d][qt], r.o[d][qt]);
            *(LAS f32x4*)(xo + 8192 + l * 16) = (f32x4){m0, m1, l0, l1};
            asm volatile("s_waitcnt lgkmcnt(0)" ::: "memory"); BARRIER();
            const lcptr xi = lds + (qg * 2 + (grp ^ 1)) * 9216;
            const f32x4 st = *(const LAS f32x4*)(xi + 8192 + l * 16);
            const float M0 = __builtin_fmaxf(m0, st[0]), M1 = __builtin_fmaxf(m1, st[1]);
            const float wa0 = __builtin_amdgcn_exp2f(m0 - M0), wb0 = __builtin_amdgcn_exp2f(st[0] - M0), wa1 = __builtin_amdgcn_exp2f(m1 - M1), wb1 = __builtin_amdgcn_exp2f(st[1] - M1);
            const float i0 = __builtin_amdgcn_rcpf(wa0 * l0 + wb0 * st[2]), i1 = __builtin_amdgcn_rcpf(wa1 * l1 + wb1 * st[3]);
            const float ca0 = wa0 * i0, cb0 = wb0 * i0, ca1 = wa1 * i1, cb1 = wb1 * i1;
#pragma unroll
            for (int up = 0; up < 2; ++up)
#pragma unroll
                for (int qt = 0; qt < 2; ++qt) {
                    const f32x4 pa = *(const LAS f32x4*)(xi + ((2 * up) * 2 + qt) * 1024 + l * 16), pb = *(const LAS f32x4*)(xi + ((2 * up + 1) * 2 + qt) * 1024 + l * 16);
                    const f32x4 oa = sel(grp == 0, r.o[2 * up][qt], r.o[4 + 2 * up][qt]), ob = sel(grp == 0, r.o[2 * up + 1][qt], r.o[4 + 2 * up + 1][qt]);
                    const float ca = qt ? ca1 : ca0, cb = qt ? cb1 : cb0;
                    st16_sc1(sws, pack8(oa * ca + pa * cb, ob * ca + pb * cb), vl16, OFF_CTX + ((8 * (15 - j) + 2 * qg + qt) * 128 + 4 * h + 2 * grp + up) * 1024);
                }
        }
        asm volatile("s_waitcnt vmcnt(0) lgkmcnt(0)" ::: "memory"); BARRIER();
        if (w == 0 && l == 0) set_flag(y.flags, FL_CTX + j * 32 + h, y.token);
    }
    STAMP(8);
    {
        const int l = lane_id(), g = l >> 4;
        const int rb = (xcd >> 1) * 8 + (bi >> 2), cb = (xcd & 1) * 4 + (bi & 3);
        const int xq = rb >> 1, jj = xq < 8 ? xq : 15 - xq;
        const unsigned vl16 = l * 16;
        const unsigned wpo = OFF_WP + cb * 5 * 1024;
        f32x4 gs[3];
        auto gemm = [&](unsigned abase, unsigned astride, int u0, int nsteps, int ulimit, auto fin) {
            f32x4 acc[5][4];
#pragma unroll
            for (int n = 0; n < 5; ++n)
#pragma unroll
                for (int m = 0; m < 4; ++m) acc[n][m] = (f32x4){0.f, 0.f, 0.f, 0.f};
            u32x4 xa[3][4], wa[3][5];
            auto load = [&](int sidx, u32x4 (&xd)[4], u32x4 (&wd)[5]) {
                const int u = w + 8 * sidx; const bool ok = u < ulimit;
                const unsigned ao = ok ? abase + u * 1024 : 0xF0000000u, wo_ = ok ? wpo + (u0 + u) * 40960 : 0xF0000000u;
#pragma unroll
                for (int m = 0; m < 4; ++m) xd[m] = ld16_sc1(sws, vl16, ao + m * astride);
#pragma unroll
                for (int n = 0; n < 5; ++n) wd[n] = ld16_sc1(sws, vl16, wo_ + n * 1024);
            };
            auto mm = [&](const u32x4 (&xd)[4], const u32x4 (&wd)[5]) {
#pragma unroll
                for (int n = 0; n < 5; ++n)
#pragma unroll
                    for (int m = 0; m < 4; ++m) acc[n][m] = MF16(asbf(wd[n]), asbf(xd[m]), acc[n][m]);
            };
            load(0, xa[0], wa[0]); load(1, xa[1], wa[1]);
            int sidx = 0;
            for (; sidx + 3 <= nsteps; sidx += 3) {
                load(sidx + 2, xa[2], wa[2]); SBAR(); mm(xa[0], wa[0]); SBAR();
                load(sidx + 3, xa[0], wa[0]); SBAR(); mm(xa[1], wa[1]); SBAR();
                load(sidx + 4, xa[1], wa[1]); SBAR(); mm(xa[2], wa[2]); SBAR();
            }
            if (sidx < nsteps) { mm(xa[0], wa[0]); }
            if (sidx + 1 < nsteps) { mm(xa[1], wa[1]); }
            BARRIER();
#pragma unroll
            for (int n = 0; n < 5; ++n)
#pragma unroll
                for (int m = 0; m < 4; ++m) *(LAS f32x4*)(lds + w * 20480 + (n * 4 + m) * 1024 + l * 16) = acc[n][m];
            asm volatile("s_waitcnt lgkmcnt(0)" ::: "memory"); BARRIER();
#pragma unroll
            for (int i = 0; i < 3; ++i) {
                const int t = w < 4 ? 3 * w + i : 12 + 2 * (w - 4) + i;
                if (i < 2 || w < 4) {
                    f32x4 sum = *(const LAS f32x4*)(lds + t * 1024 + l * 16);
#pragma unroll
                    for (int ww = 1; ww < 8; ++ww) sum += *(const LAS f32x4*)(lds + ww * 20480 + t * 1024 + l * 16);
                    fin(i, t, sum);
                }
                asm volatile("" ::: "memory"); SBAR();
            }
        };
        if (w == 0) {
            spin_flags(y.flags, FL_P0 + l, y.token); spin_flags(y.flags, FL_P0 + 64 + l, y.token);
            spin_flags(y.flags, FL_P0 + 128 + l, y.token); spin_flags(y.flags, FL_P0 + 192 + l, y.token);
            spin_flags(y.flags, FL_ACT + jj * 32 + (l & 31), y.token);
        }
        BARRIER();
        gemm(OFF_ACT + (4 * rb) * 54 * 1024, 54 * 1024, 128, 7, 54, [&](int i, int, f32x4 v) {
#pragma unroll
            for (int e = 0; e < 4; ++e) v[e] = v[e] * __builtin_amdgcn_rcpf(1.f + __builtin_amdgcn_exp2f(-1.4426950408889634f * v[e]));
            gs[i] = v; PIN(gs[i]); });
        STAMP(9);
        if (w == 0) spin_flags(y.flags, FL_CTX + jj * 32 + (l & 31), y.token);
        BARRIER();
        STAMP(10);
        gemm(OFF_CTX + (4 * rb) * 128 * 1024, 128 * 1024, 0, 16, 128, [&](int i, int t, f32x4 v) {
            const int n = t >> 2, m = t & 3;
            const int row = 64 * rb + 16 * m + (l & 15), col = 80 * cb + 16 * n + 4 * g;
            const u32x2 xv = *(const u32x2*)(x + row * 640 + col);
            const f32x4 o = {bflo(xv[0]) + v[0] * gs[i][0], bfhi(xv[0]) + v[1] * gs[i][1], bflo(xv[1]) + v[2] * gs[i][2], bfhi(xv[1]) + v[3] * gs[i][3]};
            *(u32x2*)(out + row * 640 + col) = (u32x2){cvtpk(o[0], o[1]), cvtpk(o[2], o[3])}; });
        if (b == 0 && w == 0 && l == 0) __hip_atomic_store(y.flags + 4, y.token, __ATOMIC_RELAXED, __HIP_MEMORY_SCOPE_AGENT);
    }
    STAMP(11);
}

extern "C" void kernel_launch(void* const* d_in, const int* in_sizes, int n_in, void* d_out, int out_size, void* d_ws, size_t ws_size, hipStream_t stream) {
    (void)in_sizes; (void)n_in; (void)out_size; (void)ws_size;
    fused_kernel<<<256, 512, 0, stream>>>((const bf16_t*)d_in[0], (const bf16_t*)d_in[1], (const bf16_t*)d_in[2], (const bf16_t*)d_in[3], (const bf16_t*)d_in[4],
                                          (const bf16_t*)d_in[5], (const bf16_t*)d_in[7], (const bf16_t*)d_in[8], (const bf16_t*)d_in[9], (bf16_t*)d_out, (char*)d_ws);
}
```

```cpp
#include <hip/hip_runtime.h>
#include <stdint.h>
#include <utility>

typedef unsigned short bf16_t;
typedef short bf16x8 __attribute__((ext_vector_type(8)));
typedef short s16x4 __attribute__((ext_vector_type(4)));
typedef short v4i16_t __attribute__((ext_vector_type(4)));
typedef float f32x4 __attribute__((ext_vector_type(4)));
typedef float f32x2 __attribute__((ext_vector_type(2)));
typedef unsigned u32x4 __attribute__((ext_vector_type(4)));
typedef unsigned u32x2 __attribute__((ext_vector_type(2)));
typedef __bf16 bf16x2_t __attribute__((ext_vector_type(2)));
typedef __amdgpu_buffer_rsrc_t rsrc_t;
typedef unsigned long long u64;
#define LAS __attribute__((address_space(3)))
#define GAS __attribute__((address_space(1)))
typedef LAS char* lptr;
typedef LAS const char* lcptr;
typedef GAS u64 gu64;
#define DI __device__ __forceinline__
#define SBAR() __builtin_amdgcn_sched_barrier(0)
#define PIN(x) asm volatile("" : "+v"(x))
#define MF16(a, b, c) __builtin_amdgcn_mfma_f32_16x16x32_bf16(a, b, c, 0, 0, 0)
#define WAITV(n) asm volatile("s_waitcnt vmcnt(" #n ")" ::: "memory")
#define WAITL(n) asm volatile("s_waitcnt lgkmcnt(" #n ")" ::: "memory")
#define BARRIER() do { asm volatile("" ::: "memory"); __builtin_amdgcn_s_barrier(); asm volatile("" ::: "memory"); } while (0)

#define UPTO 4
#define STAMPS 0

constexpr unsigned OFF_FLAGS = 0;
constexpr unsigned OFF_XN = 65536;
constexpr unsigned OFF_K = OFF_XN + 2621440;
constexpr unsigned OFF_V = OFF_K + 16777216;
constexpr unsigned OFF_ACT = OFF_V + 16777216 + 8388608;
constexpr unsigned OFF_CTX = OFF_ACT + 7077888;
constexpr unsigned OFF_WP = OFF_CTX + 16777216;
constexpr unsigned OFF_ST = OFF_WP + 182 * 40 * 1024;
constexpr unsigned WS_BYTES = OFF_ST + 256 * 16 * 8 + 256 * 2 * 16 * 4;
constexpr unsigned FL_P0 = 0, FL_KV = 256, FL_ACT = 512, FL_CTX = 768;
constexpr int SC1 = 16;

DI unsigned cvtpk(float lo, float hi) { f32x2 v = {lo, hi}; bf16x2_t b = __builtin_convertvector(v, bf16x2_t); return __builtin_bit_cast(unsigned, b); }
DI u32x4 pack8(f32x4 a, f32x4 b) { u32x4 w = {cvtpk(a[0], a[1]), cvtpk(a[2], a[3]), cvtpk(b[0], b[1]), cvtpk(b[2], b[3])}; return w; }
DI float bflo(unsigned u) { return __uint_as_float(u << 16); }
DI float bfhi(unsigned u) { return __uint_as_float(u & 0xffff0000u); }
DI s16x4 vtr(lcptr p) { return __builtin_bit_cast(s16x4, __builtin_amdgcn_ds_read_tr16_b64_v4i16((LAS v4i16_t*)p)); }
DI bf16x8 cat(s16x4 a, s16x4 b) { return (bf16x8){a[0], a[1], a[2], a[3], b[0], b[1], b[2], b[3]}; }
DI bf16x8 ld128(lcptr p) { return *(const LAS bf16x8*)p; }
DI bf16x8 asbf(u32x4 v) { return __builtin_bit_cast(bf16x8, v); }
DI rsrc_t mk_rsrc(const void* p, unsigned bytes) { return __builtin_amdgcn_make_buffer_rsrc((void*)p, (short)0, (int)bytes, 0x00020000); }
DI void dma16(rsrc_t r, unsigned voff, unsigned soff, unsigned ldsoff) {
    asm volatile("s_mov_b32 m0, %0\n\ts_nop 3\n\tbuffer_load_dwordx4 %1, %2, %3 offen lds" ::"s"(ldsoff), "v"(voff), "s"(r), "s"(soff) : "memory");
}
DI void dma16x4(rsrc_t r, unsigned voff, unsigned soff, unsigned ldsoff) {
    asm volatile("s_mov_b32 m0, %0\n\ts_nop 3\n\tbuffer_load_dwordx4 %1, %2, %3 offen lds\n\tbuffer_load_dwordx4 %1, %2, %3 offen offset:1024 lds\n\t"
                 "buffer_load_dwordx4 %1, %2, %3 offen offset:2048 lds\n\tbuffer_load_dwordx4 %1, %2, %3 offen offset:3072 lds" ::"s"(ldsoff), "v"(voff), "s"(r), "s"(soff) : "memory");
}
DI void st16_sc1(rsrc_t r, u32x4 v, unsigned voff, unsigned soff) { asm volatile("s_nop 4\n\tbuffer_store_dwordx4 %0, %1, %2, %3 offen sc1\n\ts_nop 1" ::"v"(v), "v"(voff), "s"(r), "s"(soff) : "memory"); }
template <int OFF> DI void st16_sc1o(rsrc_t r, u32x4 v, unsigned voff, unsigned soff) { asm volatile("s_nop 4\n\tbuffer_store_dwordx4 %0, %1, %2, %3 offen offset:%4 sc1\n\ts_nop 1" ::"v"(v), "v"(voff), "s"(r), "s"(soff), "i"(OFF) : "memory"); }
DI u32x4 ld16(rsrc_t r, unsigned voff, unsigned soff) { return __builtin_amdgcn_raw_buffer_load_b128(r, voff, soff, 0); }
#define P4AUX SC1
DI u32x4 ld16_sc1(rsrc_t r, unsigned voff, unsigned soff) { return __builtin_amdgcn_raw_buffer_load_b128(r, voff, soff, P4AUX); }
template <class T> DI T sel(bool c, T a, T b) { return c ? a : b; }
DI int lane_id() { int l; asm volatile("v_mbcnt_lo_u32_b32 %0, -1, 0\n\tv_mbcnt_hi_u32_b32 %0, -1, %0" : "=v"(l)); return l; }
DI void set_flag(gu64* flags, unsigned idx, u64 token) { __hip_atomic_store(flags + idx * 8, token, __ATOMIC_RELAXED, __HIP_MEMORY_SCOPE_AGENT); }
DI u64 poll_flag(gu64* flags, unsigned idx) { return __hip_atomic_load(flags + idx * 8, __ATOMIC_RELAXED, __HIP_MEMORY_SCOPE_AGENT); }
DI void spin_flags(gu64* flags, unsigned idx, u64 token) {
    const unsigned long long t0 = __builtin_amdgcn_s_memrealtime();
    for (;;) {
        __builtin_amdgcn_s_sleep(1);
        const u64 v = poll_flag(flags, idx);
        if (__all(v == token)) break;
        if (__builtin_amdgcn_s_memrealtime() - t0 > 2000000ull) break;
    }
}
DI void check_flags(gu64* flags, unsigned idx, u64 seen, u64 token) { if (!__all(seen == token)) spin_flags(flags, idx, token); }
DI void acquire_issue() { asm volatile("buffer_inv sc1" ::: "memory"); }
DI u64 uni64(u64 v) { return ((u64)(unsigned)__builtin_amdgcn_readfirstlane((unsigned)(v >> 32)) << 32) | (unsigned)__builtin_amdgcn_readfirstlane((unsigned)v); }
#define STAMP(i) do { } while (0)
#define CSTAMP(i) do { } while (0)

struct Sync { gu64* flags; u64 token; int b, h, j, w; };

struct P2Ctx { rsrc_t sws, swq, sw1; unsigned vl16, vwq, vw1; unsigned sxo, swqo, sw1o, skv; int w; lcptr xb; lcptr wq[4]; lcptr wf[2]; };
DI void p2_dma1(const P2Ctx& c, int st, int ks, int i) {
    if (i < 4) dma16(c.sws, c.vl16, c.sxo + (i >> 1) * 20480 + (i & 1) * 1024 + ks * 2048, st * 32768 + (4 * c.w + i) * 1024);
    else if (i < 6) dma16(c.swq, c.vwq, c.swqo + (i - 4) * 128 + ks * 524288, 65536 + st * 24576 + (i - 4) * 8192 + c.w * 1024);
    else dma16(c.sw1, c.vw1, c.sw1o + ks * (64 * 3456), 65536 + st * 24576 + 16384 + c.w * 1024);
}

struct P1Ctx { rsrc_t sx, sw, sws; unsigned vx[2], vw[2]; unsigned sxo, swo; int w; lcptr xb[2]; lcptr wb[4]; const bf16_t *wo, *w2; lcptr lds; };
DI void pack_fetch(const P1Ctx& c, int b) {
    const int l = lane_id(), gw = b * 8 + c.w, pu = gw / 10, pq = gw - pu * 10;
    const rsrc_t sp = mk_rsrc(pu < 128 ? c.wo : c.w2, pu < 128 ? 4096 * 640 * 2 : 1728 * 640 * 2);
    const unsigned so = gw < 1820 ? (unsigned)((pu < 128 ? pu : pu - 128) * 32 * 1280 + pq * 128) : 0xF0000000u;
#pragma unroll
    for (int i = 0; i < 4; ++i) dma16(sp, (l >> 3) * 1280 + (l & 7) * 16, so + i * 8 * 1280, 131072 + c.w * 4096 + i * 1024);
}
DI void pack_store(const P1Ctx& c, int b) {
    const int l = lane_id(), g = l >> 4, q = (l & 15) >> 2, gw = b * 8 + c.w, pu = gw / 10, pq = gw - pu * 10;
    lcptr p = c.lds + 131072 + c.w * 4096 + (4 * g + q) * 128 + (l & 3) * 8;
    bf16x8 fr[4];
#pragma unroll
    for (int i = 0; i < 4; ++i) fr[i] = cat(vtr(p + i * 32), vtr(p + i * 32 + 16 * 128));
    if (gw < 1820) {
#pragma unroll
        for (int i = 0; i < 4; ++i) st16_sc1(c.sws, __builtin_bit_cast(u32x4, fr[i]), l * 16, OFF_WP + (pu * 40 + pq * 4 + i) * 1024);
    }
}
DI void p1_dma1(const P1Ctx& c, int st, int ks, int i) {
    if (i < 4) dma16(c.sx, c.vx[i & 1], c.sxo + i * 8192 + ks * 128, st * 32768 + (4 * c.w + i) * 1024);
    else dma16(c.sw, c.vw[i & 1], c.swo + (i - 4) * 65536 + ks * 524288, 65536 + st * 32768 + (4 * c.w + (i - 4)) * 1024);
}
DI void p1_dma(const P1Ctx& c, int st, int ks) {
#pragma unroll
    for (int i = 0; i < 8; ++i) p1_dma1(c, st, ks, i);
}
template <int ST, int UP, int I> DI void p1_rdw(const P1Ctx& c, bf16x8 (&wf)[4], s16x4 (&wl)[4]) {
    constexpr int nt = I >> 1, rd = I & 1;
    const s16x4 v = vtr(c.wb[nt] + ST * 32768 + UP * 4096 + rd * 512);
    if constexpr (rd == 0) wl[nt] = v; else wf[nt] = cat(wl[nt], v);
}
template <int ST, int G> DI bf16x8 p1_rdx(const P1Ctx& c) { return ld128(c.xb[G >> 3] + ST * 32768 + (G & 7) * 2048); }
template <bool ISV, int KS, int G> DI void p1_group(const P1Ctx& c, const P2Ctx& c2, const Sync& y, u64& seen, unsigned& sink, f32x4 (&acc)[4][8], bf16x8 (&wA)[4], bf16x8 (&wB)[4], s16x4 (&wl)[4], bf16x8 (&xr)[4]) {
    constexpr int up = G >> 3, mt = G & 7, ST = KS & 1;
    constexpr bool NEXT = KS < 7;
    if constexpr (G == 13) {
        if constexpr (KS == 0) asm volatile("s_waitcnt vmcnt(4) lgkmcnt(0)" ::: "memory");
        else asm volatile("s_waitcnt vmcnt(0) lgkmcnt(0)" ::: "memory");
        if constexpr (KS == 1) asm volatile("" :: "v"(sink));
        BARRIER();
        if constexpr (!ISV) {
            if constexpr (KS == 1) { if (y.w == 0 && lane_id() == 0) set_flag(y.flags, FL_P0 + y.b, y.token); }
            if constexpr (KS == 5) { if (y.w == 0) { const int l = lane_id() & 31; check_flags(y.flags, FL_P0 + (l < 16 ? 16 * y.j + l : 16 * (15 - y.j) + (l - 16)), seen, y.token); } }
        }
        SBAR();
    }
    const bf16x8 x = xr[G & 3];
#pragma unroll
    for (int nt = 0; nt < 4; ++nt) {
        const bf16x8 wv = up == 0 ? wA[nt] : wB[nt];
        if constexpr (ISV) acc[nt][mt] = MF16(x, wv, acc[nt][mt]); else acc[nt][mt] = MF16(wv, x, acc[nt][mt]);
        if (nt == 0) {
            if constexpr (G + 3 < 16) xr[(G + 3) & 3] = p1_rdx<ST, G + 3>(c);
            else if constexpr (NEXT) xr[(G + 3) & 3] = p1_rdx<ST ^ 1, G + 3 - 16>(c);
        }
        if constexpr (G >= 2 && G < 6) { if (nt == 1) p1_rdw<ST, 1, 2 * (G - 2)>(c, wB, wl); if (nt == 2) p1_rdw<ST, 1, 2 * (G - 2) + 1>(c, wB, wl); }
        if constexpr (G >= 13 && NEXT) {
            if (nt == 1) p1_rdw<ST ^ 1, 0, 3 * (G - 13)>(c, wA, wl);
            if (nt == 2) p1_rdw<ST ^ 1, 0, 3 * (G - 13) + 1>(c, wA, wl);
            if constexpr (G < 15) { if (nt == 3) p1_rdw<ST ^ 1, 0, 3 * (G - 13) + 2>(c, wA, wl); }
        }
        if constexpr (G >= 13) { constexpr int d0 = 3 * (G - 13);
            if constexpr (KS < 6) { if (nt >= 1 && d0 + nt - 1 < 8) p1_dma1(c, ST, KS + 2, d0 + nt - 1); }
            else { if (nt >= 1 && d0 + nt - 1 < 7) p2_dma1(c2, ST, KS - 6, d0 + nt - 1); } }
        SBAR();
    }
    if constexpr (G == 15 && KS == 4) { pack_fetch(c, y.b); SBAR(); }
    if constexpr (G == 15 && KS == 5) { pack_store(c, y.b); SBAR(); }
    if constexpr (G == 15 && !ISV) {
        if constexpr (KS == 4) { if (y.w == 0) { const int l = lane_id() & 31; seen = poll_flag(y.flags, FL_P0 + (l < 16 ? 16 * y.j + l : 16 * (15 - y.j) + (l - 16))); } SBAR(); }
        if constexpr (KS == 5) { if (y.w == 0) acquire_issue(); SBAR(); }
    }
}
template <bool ISV, int KS, int... Gs> DI void p1_step(const P1Ctx& c, const P2Ctx& c2, const Sync& y, u64& seen, unsigned& sink, f32x4 (&acc)[4][8], bf16x8 (&wA)[4], bf16x8 (&wB)[4], s16x4 (&wl)[4], bf16x8 (&xr)[4], std::integer_sequence<int, Gs...>) {
    (p1_group<ISV, KS, Gs>(c, c2, y, seen, sink, acc, wA, wB, wl, xr), ...);
}
template <bool ISV, int... KSs> DI void p1_main(const P1Ctx& c, const P2Ctx& c2, const Sync& y, unsigned& sink, u32x4 (&kv)[16], std::integer_sequence<int, KSs...>) {
    f32x4 acc[4][8];
#pragma unroll
    for (int a = 0; a < 4; ++a)
#pragma unroll
        for (int b = 0; b < 8; ++b) acc[a][b] = (f32x4){0.f, 0.f, 0.f, 0.f};
    bf16x8 wA[4], wB[4], xr[4]; s16x4 wl[4];
    u64 seen = 0;
    asm volatile("" ::: "memory");
    p1_rdw<0, 0, 0>(c, wA, wl); p1_rdw<0, 0, 1>(c, wA, wl); p1_rdw<0, 0, 2>(c, wA, wl); p1_rdw<0, 0, 3>(c, wA, wl);
    p1_rdw<0, 0, 4>(c, wA, wl); p1_rdw<0, 0, 5>(c, wA, wl); p1_rdw<0, 0, 6>(c, wA, wl); p1_rdw<0, 0, 7>(c, wA, wl);
    xr[0] = p1_rdx<0, 0>(c); xr[1] = p1_rdx<0, 1>(c); xr[2] = p1_rdx<0, 2>(c);
    SBAR();
    (p1_step<ISV, KSs>(c, c2, y, seen, sink, acc, wA, wB, wl, xr, std::make_integer_sequence<int, 16>{}), ...);
#pragma unroll
    for (int a = 0; a < 8; ++a)
#pragma unroll
        for (int b = 0; b < 2; ++b) {
            if constexpr (ISV) kv[a * 2 + b] = pack8(acc[(a & 1) * 2 + b][(a >> 1) * 2], acc[(a & 1) * 2 + b][(a >> 1) * 2 + 1]);
            else kv[a * 2 + b] = pack8(acc[2 * b][a], acc[2 * b + 1][a]);
        }
}

template <bool HASF, int ST, int I> DI bf16x8 p2_rdw(const P2Ctx& c) {
    constexpr int NW = HASF ? 6 : 4, up = I / NW, n = I % NW;
    lcptr p = (n < 4 ? c.wq[n & 3] : c.wf[n & 1]) + ST * 24576 + up * 4096;
    return cat(vtr(p), vtr(p + 512));
}
template <int ST, int UP, int MT> DI bf16x8 p2_rdx(const P2Ctx& c) { return ld128(c.xb + ST * 32768 + MT * 2048 + UP * 1024); }
template <int I> DI void p2_kvst(const P2Ctx& c, const u32x4 (&kv)[16], bool isv) {
    st16_sc1(c.sws, kv[I], c.vl16, c.skv + (isv ? (unsigned)(((I >> 3) * 16 + ((I >> 2) & 1) * 8 + (I & 3)) * 1024) : (unsigned)((I >> 1) * 4096 + (I & 1) * 1024)));
}
template <bool HASF, int KS, int G> DI void p2_group(const P2Ctx& c, const Sync& y, u64& seen, f32x4 (&acc)[6][4], bf16x8 (&wr)[4], bf16x8 (&x0)[4], bf16x8 (&x1)[4], const u32x4 (&kv)[16], bool isv) {
    constexpr int NW = HASF ? 6 : 4, NG = 2 * NW, ST = KS & 1, up = G / NW, n = G % NW;
    constexpr bool LAST = (KS == 9);
    if constexpr (G == NG - 3) {
        if constexpr (KS == 0) asm volatile("s_waitcnt vmcnt(4) lgkmcnt(0)" ::: "memory");
        else if constexpr (KS <= 6) asm volatile("s_waitcnt vmcnt(2) lgkmcnt(0)" ::: "memory");
        else asm volatile("s_waitcnt vmcnt(0) lgkmcnt(0)" ::: "memory");
        BARRIER();
        if constexpr (KS + 2 < 10) {
#pragma unroll
            for (int i = 0; i < (HASF ? 7 : 6); ++i) p2_dma1(c, ST, KS + 2, i);
        }
        if constexpr (KS < 6) { p2_kvst<4 + 2 * KS>(c, kv, isv); p2_kvst<5 + 2 * KS>(c, kv, isv); }
        if constexpr (KS == 7) { if (y.w == 0 && lane_id() == 0) set_flag(y.flags, FL_KV + y.h * 8 + y.j, y.token); }
        if constexpr (KS == 8) { if (y.w == 0) seen = poll_flag(y.flags, FL_KV + y.h * 8 + (lane_id() & 7)); }
        if constexpr (KS == 9) { if (y.w == 0) { check_flags(y.flags, FL_KV + y.h * 8 + (lane_id() & 7), seen, y.token); acquire_issue(); } }
        SBAR();
    }
    const bf16x8 wcur = wr[G & 3];
#pragma unroll
    for (int mt = 0; mt < 4; ++mt) {
        acc[n][mt] = MF16(wcur, up == 0 ? x0[mt] : x1[mt], acc[n][mt]);
        if (mt == 0) {
            if constexpr (G + 3 < NG) wr[(G + 3) & 3] = p2_rdw<HASF, ST, G + 3>(c);
            else if constexpr (!LAST) wr[(G + 3) & 3] = p2_rdw<HASF, ST ^ 1, G + 3 - NG>(c);
        }
        if (mt == 1) {
            if constexpr (G < 4) x1[G] = p2_rdx<ST, 1, G>(c);
            else if constexpr (G >= NG - 3 && !LAST) x0[G - (NG - 3)] = p2_rdx<ST ^ 1, 0, G - (NG - 3)>(c);
        }
        if (mt == 2) { if constexpr (G == NG - 1 && !LAST) x0[3] = p2_rdx<ST ^ 1, 0, 3>(c); }
        SBAR();
    }
}
template <bool HASF, int KS, int... Gs> DI void p2_step(const P2Ctx& c, const Sync& y, u64& seen, f32x4 (&acc)[6][4], bf16x8 (&wr)[4], bf16x8 (&x0)[4], bf16x8 (&x1)[4], const u32x4 (&kv)[16], bool isv, std::integer_sequence<int, Gs...>) {
    (p2_group<HASF, KS, Gs>(c, y, seen, acc, wr, x0, x1, kv, isv), ...);
}
template <bool HASF, int... KSs> DI void p2_main(const P2Ctx& c, const Sync& y, f32x4 (&acc)[6][4], const u32x4 (&kv)[16], bool isv, std::integer_sequence<int, KSs...>) {
    bf16x8 wr[4], x0[4], x1[4];
    u64 seen = 0;
    asm volatile("" ::: "memory");
    wr[0] = p2_rdw<HASF, 0, 0>(c); wr[1] = p2_rdw<HASF, 0, 1>(c); wr[2] = p2_rdw<HASF, 0, 2>(c);
    x0[0] = p2_rdx<0, 0, 0>(c); x0[1] = p2_rdx<0, 0, 1>(c); x0[2] = p2_rdx<0, 0, 2>(c); x0[3] = p2_rdx<0, 0, 3>(c);
    SBAR();
    (p2_step<HASF, KSs>(c, y, seen, acc, wr, x0, x1, kv, isv, std::make_integer_sequence<int, (HASF ? 12 : 8)>{}), ...);
}
DI float mish(float h) {
    const float t = __builtin_amdgcn_exp2f(__builtin_fminf(h, 20.f) * 1.4426950408889634f);
    const float n = t * (t + 2.f);
    return h * n * __builtin_amdgcn_rcpf(n + 2.f);
}

constexpr int PD = 8;
#define PRIOM 1
#define PRIOV 0
#define ADDMODE 1
#define ABL_NOEARLY 0
#define APRE_EARLY 1
#define LATE_STEP 7
#define LATE_OFF 2
struct AttnRegs { f32x4 o[8][2]; f32x4 s[4][2]; bf16x8 q[2][4]; u32x4 pk[2][2]; f32x4 negm[2]; bf16x8 op[32]; };
template <int I> DI void a_read(AttnRegs& r, lcptr vp, lcptr kp) {
    if constexpr (I < 16) r.op[I] = ld128(vp + I * 1024);
    else if constexpr (I < 32) r.op[I] = ld128(kp + (((I - 16) & 3) * 4 + ((I - 16) >> 2)) * 1024);
}
struct Piece { rsrc_t sws; unsigned vl16, soff, ldso; bool on; };
template <int P, bool COND> DI void piece(const Piece& d) {
    if ((!COND && !ABL_NOEARLY) || (COND && d.on)) asm volatile("s_mov_b32 m0, %0\n\ts_nop 3\n\tbuffer_load_dwordx4 %1, %2, %3 offen offset:%4 lds" ::"s"(d.ldso), "v"(d.vl16), "s"(d.sws), "s"(d.soff), "i"(P * 1024) : "memory");
}
template <int I, int DMA> DI void a_slot(AttnRegs& r, lcptr vp, lcptr kp, const Piece& late) {
    if constexpr (I < 16) { constexpr int ks = I >> 3, dt = I & 7;
        r.o[dt][0] = MF16(r.op[I], asbf(r.pk[0][ks]), r.o[dt][0]); r.o[dt][1] = MF16(r.op[I], asbf(r.pk[1][ks]), r.o[dt][1]); }
    else { constexpr int u = (I - 16) >> 2, kt = (I - 16) & 3;
        r.s[kt][0] = MF16(r.op[I], r.q[0][u], u == 0 ? r.negm[0] : r.s[kt][0]); r.s[kt][1] = MF16(r.op[I], r.q[1][u], u == 0 ? r.negm[1] : r.s[kt][1]); }
    a_read<I + PD>(r, vp, kp);
    SBAR();
    if constexpr (DMA != 0 && (I % LATE_STEP) == LATE_OFF && I / LATE_STEP < 4) { piece<I / LATE_STEP, DMA == 2>(late); SBAR(); }
}
template <int B, int DMA, int... Is> DI void a_run(AttnRegs& r, lcptr vp, lcptr kp, const Piece& late, std::integer_sequence<int, Is...>) { (a_slot<B + Is, DMA>(r, vp, kp, late), ...); }
template <int B, int... Is> DI void a_pre(AttnRegs& r, lcptr vp, lcptr kp, std::integer_sequence<int, Is...>) { (a_read<B + Is>(r, vp, kp), ...); }

template <int Q> DI void a_exp_quarter(AttnRegs& r, float& a0, float& a1) {
    constexpr int ks = Q >> 1, qt = Q & 1;
    float one = 1.0f; asm("" : "+s"(one));
    float e[8];
#pragma unroll
    for (int i = 0; i < 4; ++i) { e[i] = __builtin_amdgcn_exp2f(r.s[2 * ks][qt][i]); e[4 + i] = __builtin_amdgcn_exp2f(r.s[2 * ks + 1][qt][i]); }
#pragma unroll
    for (int i = 0; i < 8; ++i) {
        if (qt == 0) a0 += e[i]; else a1 = __builtin_fmaf(e[i], one, a1);
    }
    r.pk[qt][ks] = (u32x4){cvtpk(e[0], e[1]), cvtpk(e[2], e[3]), cvtpk(e[4], e[5]), cvtpk(e[6], e[7])};
}
template <bool DMA> DI void a_exp_pack(AttnRegs& r, float& a0, float& a1, const Piece& early) {
    a0 = 0.f; a1 = 0.f;
    if constexpr (DMA) { piece<0, false>(early); SBAR(); }
    a_exp_quarter<0>(r, a0, a1);
    if constexpr (DMA) { SBAR(); piece<1, false>(early); SBAR(); }
    a_exp_quarter<1>(r, a0, a1);
    if constexpr (DMA) { SBAR(); piece<2, false>(early); SBAR(); }
    a_exp_quarter<2>(r, a0, a1);
    if constexpr (DMA) { SBAR(); piece<3, false>(early); SBAR(); }
    a_exp_quarter<3>(r, a0, a1);
}
DI float xmax4(float v) {
    const u32x2 a = __builtin_amdgcn_permlane16_swap(__float_as_uint(v), __float_as_uint(v), false, false);
    v = __builtin_fmaxf(__uint_as_float(a[0]), __uint_as_float(a[1]));
    const u32x2 c = __builtin_amdgcn_permlane32_swap(__float_as_uint(v), __float_as_uint(v), false, false);
    return __builtin_fmaxf(__uint_as_float(c[0]), __uint_as_float(c[1]));
}
DI float xsum4(float v) {
    const u32x2 a = __builtin_amdgcn_permlane16_swap(__float_as_uint(v), __float_as_uint(v), false, false);
    v = __uint_as_float(a[0]) + __uint_as_float(a[1]);
    const u32x2 c = __builtin_amdgcn_permlane32_swap(__float_as_uint(v), __float_as_uint(v), false, false);
    return __uint_as_float(c[0]) + __uint_as_float(c[1]);
}
constexpr float SUM_HI = 1.152921504606846976e18f, SUM_LO = 8.673617379884035e-19f;

struct AttnCtx { rsrc_t sws; unsigned vl16, kbase, vbase, ctxl; int nL, grp, qg, j, rowrel, swap_s, first_s; lcptr kl, vl, ql; };
struct AttnSm { float m0, m1, l0, l1, ll0, ll1; };
DI void a_dmaK(const AttnCtx& c, int n) { dma16x4(c.sws, c.vl16, c.kbase + n * 16384 + c.qg * 4096, (n & 3) * 16384 + c.qg * 4096); }
DI void a_dmaV(const AttnCtx& c, int n) { dma16x4(c.sws, c.vl16, c.vbase + n * 16384 + c.qg * 4096, 65536 + (n & 3) * 16384 + c.qg * 4096); }
#define PT(i) do { } while (0)
#define PARG
#define PPASS
#define PPASSG
DI void a_slotfull(const AttnCtx& c, const Sync& y, AttnRegs& r, AttnSm& z, const int s PARG) {
    const int nL = c.nL, grp = c.grp;
    const bool SWAP = (s == c.swap_s), FIN = (s == c.swap_s + 1), FIRST = (s == c.first_s) || FIN;
    const int tcur = s < nL ? s : 2 * s - nL + grp;
    Piece early, late;
    {
        const int s2 = s + 2, s1 = s + 1;
        const bool eK = grp == 1 && s2 < nL;
        const int te = eK ? s2 : (s1 < nL ? s1 : 2 * s1 - nL + grp), tl = 2 * s2 - nL + grp;
        early.sws = c.sws; early.vl16 = c.vl16; late.sws = c.sws; late.vl16 = c.vl16;
        early.on = eK || (s1 <= 16 && (grp == 0 || s1 >= nL));
        early.soff = (eK ? c.kbase : c.vbase) + te * 16384 + c.qg * 4096; early.ldso = (eK ? 0 : 65536) + (te & 3) * 16384 + c.qg * 4096;
        late.on = s2 <= 16 && s2 >= nL;
        late.soff = c.kbase + tl * 16384 + c.qg * 4096; late.ldso = (tl & 3) * 16384 + c.qg * 4096;
    }
    const int snext = s + 1, tnext = snext < nL ? snext : 2 * snext - nL + grp;
    const lcptr vp = c.vl + (tcur & 3) * 16384, kp = c.kl + (tnext & 3) * 16384;
    a_pre<0>(r, vp, kp, std::make_integer_sequence<int, PD>{});
    SBAR();
    if (SWAP) {
        asm volatile("ds_read_b128 %0, %8\n\tds_read_b128 %1, %8 offset:1024\n\tds_read_b128 %2, %8 offset:2048\n\tds_read_b128 %3, %8 offset:3072\n\t"
                     "ds_read_b128 %4, %8 offset:4096\n\tds_read_b128 %5, %8 offset:5120\n\tds_read_b128 %6, %8 offset:6144\n\tds_read_b128 %7, %8 offset:7168\n\ts_waitcnt lgkmcnt(0)"
                     : "=&v"(r.q[0][0]), "=&v"(r.q[0][1]), "=&v"(r.q[0][2]), "=&v"(r.q[0][3]), "=&v"(r.q[1][0]), "=&v"(r.q[1][1]), "=&v"(r.q[1][2]), "=&v"(r.q[1][3])
                     : "v"(c.ql) : "memory");
    }
    if (FIN) {
        const float i0 = __builtin_amdgcn_rcpf(xsum4(z.ll0)), i1 = __builtin_amdgcn_rcpf(xsum4(z.ll1));
        const unsigned c1o = c.ctxl + 131072;
        st16_sc1o<0>(c.sws, pack8(r.o[0][0] * i0, r.o[1][0] * i0), c.vl16, c.ctxl); st16_sc1o<1024>(c.sws, pack8(r.o[2][0] * i0, r.o[3][0] * i0), c.vl16, c.ctxl);
        st16_sc1o<2048>(c.sws, pack8(r.o[4][0] * i0, r.o[5][0] * i0), c.vl16, c.ctxl); st16_sc1o<3072>(c.sws, pack8(r.o[6][0] * i0, r.o[7][0] * i0), c.vl16, c.ctxl);
        st16_sc1o<0>(c.sws, pack8(r.o[0][1] * i1, r.o[1][1] * i1), c.vl16, c1o); st16_sc1o<1024>(c.sws, pack8(r.o[2][1] * i1, r.o[3][1] * i1), c.vl16, c1o);
        st16_sc1o<2048>(c.sws, pack8(r.o[4][1] * i1, r.o[5][1] * i1), c.vl16, c1o); st16_sc1o<3072>(c.sws, pack8(r.o[6][1] * i1, r.o[7][1] * i1), c.vl16, c1o);
#pragma unroll
        for (int d = 0; d < 8; ++d) { r.o[d][0] = (f32x4){0.f, 0.f, 0.f, 0.f}; r.o[d][1] = (f32x4){0.f, 0.f, 0.f, 0.f}; }
    }
    {
        const bool heavy = (grp == 1) || (s >= nL);
        const int qb = heavy ? 15 - c.j : c.j, kb = 64 * tcur - 128 * qb - 32 * c.qg;
        if (kb >= -32) {
            const float NI = -__builtin_inff();
            const f32x4 ninf = {NI, NI, NI, NI};
            f32x4 s00 = r.s[0][0], s10 = r.s[1][0], s20 = r.s[2][0], s30 = r.s[3][0], s01 = r.s[0][1], s11 = r.s[1][1], s21 = r.s[2][1], s31 = r.s[3][1];
            const int dq = c.rowrel;
            if (kb > 0) { s00 = ninf; s10 = ninf; s20 = ninf; s30 = ninf; s01 = ninf; s11 = ninf; s21 = ninf; s31 = ninf; }
            else if (kb == 0) {
#pragma unroll
                for (int i = 0; i < 4; ++i) { s00[i] = sel(i > dq, NI, s00[i]); s11[i] = sel(i > dq, NI, s11[i]); }
                s10 = ninf; s20 = ninf; s30 = ninf; s21 = ninf; s31 = ninf;
            } else {
#pragma unroll
                for (int i = 0; i < 4; ++i) { s20[i] = sel(i > dq, NI, s20[i]); s31[i] = sel(i > dq, NI, s31[i]); }
                s30 = ninf;
            }
            r.s[0][0] = s00; r.s[1][0] = s10; r.s[2][0] = s20; r.s[3][0] = s30; r.s[0][1] = s01; r.s[1][1] = s11; r.s[2][1] = s21; r.s[3][1] = s31;
        }
    }
    float a0, a1;
    if (early.on) dma16x4(c.sws, c.vl16, early.soff, early.ldso);
    a_exp_pack<false>(r, a0, a1, early);
    const float lo = FIRST ? SUM_LO : 0.f;
    if (__builtin_expect(__any(!(a0 <= SUM_HI) || !(a1 <= SUM_HI) || a0 < lo || a1 < lo), 0)) {
        float x0 = r.s[0][0][0], x1 = r.s[0][1][0];
#pragma unroll
        for (int kt = 0; kt < 4; ++kt)
#pragma unroll
            for (int i = 0; i < 4; ++i) { x0 = __builtin_fmaxf(x0, r.s[kt][0][i]); x1 = __builtin_fmaxf(x1, r.s[kt][1][i]); }
        x0 = xmax4(x0); x1 = xmax4(x1);
        if (!FIRST) { x0 = __builtin_fmaxf(x0, 0.f); x1 = __builtin_fmaxf(x1, 0.f);
            const float f0 = __builtin_amdgcn_exp2f(-x0), f1 = __builtin_amdgcn_exp2f(-x1);
#pragma unroll
            for (int d = 0; d < 8; ++d) { r.o[d][0] *= f0; r.o[d][1] *= f1; }
            z.l0 *= f0; z.l1 *= f1; }
        z.m0 += x0; z.m1 += x1;
        r.negm[0] = (f32x4){-z.m0, -z.m0, -z.m0, -z.m0}; r.negm[1] = (f32x4){-z.m1, -z.m1, -z.m1, -z.m1};
#pragma unroll
        for (int kt = 0; kt < 4; ++kt) { r.s[kt][0] -= x0; r.s[kt][1] -= x1; }
        a_exp_pack<false>(r, a0, a1, early);
    }
    z.l0 += a0; z.l1 += a1;
    if (SWAP) {
        z.ll0 = z.l0; z.ll1 = z.l1; z.l0 = 0.f; z.l1 = 0.f; z.m0 = 0.f; z.m1 = 0.f;
        r.negm[0] = (f32x4){0.f, 0.f, 0.f, 0.f}; r.negm[1] = (f32x4){0.f, 0.f, 0.f, 0.f};
    }
    SBAR();
    PT(0);
    if (FIN) { if (early.on) WAITV(12); else WAITV(8); } else { if (early.on) WAITV(4); else WAITV(0); }
    PT(1);
    BARRIER(); SBAR();
    PT(2);
    __builtin_amdgcn_s_setprio(PRIOM);
    a_run<0, 2>(r, vp, kp, late, std::make_integer_sequence<int, 16>{});
    if (s < 16) a_run<16, 2>(r, vp, kp, late, std::make_integer_sequence<int, 16>{});
    __builtin_amdgcn_s_setprio(PRIOV);
    SBAR();
    PT(3);
    if (late.on) WAITV(4); else WAITV(0);
    PT(4);
    BARRIER(); SBAR();
    PT(5);
}

struct Lean { unsigned e_soff, e_lds, l_soff, l_lds, voff, koff; };
template <bool LATE> DI void a_slot_lean(const AttnCtx& c, AttnRegs& r, AttnSm& z, const Lean& st PARG) {
    const lcptr vp = c.vl + st.voff, kp = c.kl + st.koff;
    Piece early, late;
    early.sws = c.sws; early.vl16 = c.vl16; early.soff = st.e_soff; early.ldso = st.e_lds; early.on = true;
    late.sws = c.sws; late.vl16 = c.vl16; late.soff = st.l_soff; late.ldso = st.l_lds; late.on = true;
    a_pre<0>(r, vp, kp, std::make_integer_sequence<int, PD>{});
    SBAR();
    float a0, a1;
    a_exp_pack<true>(r, a0, a1, early);
    if (__builtin_expect(__any(!(a0 <= SUM_HI) || !(a1 <= SUM_HI)), 0)) {
        float x0 = 0.f, x1 = 0.f;
#pragma unroll
        for (int kt = 0; kt < 4; ++kt)
#pragma unroll
            for (int i = 0; i < 4; ++i) { x0 = __builtin_fmaxf(x0, r.s[kt][0][i]); x1 = __builtin_fmaxf(x1, r.s[kt][1][i]); }
        x0 = xmax4(x0); x1 = xmax4(x1);
        const float f0 = __builtin_amdgcn_exp2f(-x0), f1 = __builtin_amdgcn_exp2f(-x1);
#pragma unroll
        for (int d = 0; d < 8; ++d) { r.o[d][0] *= f0; r.o[d][1] *= f1; }
        z.l0 *= f0; z.l1 *= f1;
        z.m0 += x0; z.m1 += x1;
        r.negm[0] = (f32x4){-z.m0, -z.m0, -z.m0, -z.m0}; r.negm[1] = (f32x4){-z.m1, -z.m1, -z.m1, -z.m1};
#pragma unroll
        for (int kt = 0; kt < 4; ++kt) { r.s[kt][0] -= x0; r.s[kt][1] -= x1; }
        a_exp_pack<false>(r, a0, a1, early);
    }
    z.l0 += a0; z.l1 += a1;
    SBAR();
    PT(0);
    WAITV(4);
    PT(1);
    BARRIER(); SBAR();
    PT(2);
    __builtin_amdgcn_s_setprio(PRIOM);
    a_run<0, LATE ? 1 : 0>(r, vp, kp, late, std::make_integer_sequence<int, 32>{});
    __builtin_amdgcn_s_setprio(PRIOV);
    SBAR();
    PT(3);
    if constexpr (LATE) WAITV(4); else WAITV(0);
    PT(4);
    BARRIER(); SBAR();
    PT(5);
}

#define P4NS 4
struct P4Ctx { rsrc_t sws; unsigned vl16, ag, as, wg, wsb; int w, jj; lptr red; };
struct P4Regs { f32x4 acc[5][4]; u32x4 xa[P4NS][4], wa[P4NS][5]; u64 seen; };
template <int S> DI void p4_load(const P4Ctx& c, P4Regs& r, int t) {
    const bool isg = t < 8;
    const int u = isg ? t : t - 8;
    const bool ok = isg ? (t < 6 || (t == 6 && c.w < 6)) : t < 24;
    const unsigned ao = ok ? (isg ? c.ag : c.as) + u * 8192 : 0xF0000000u, wo_ = ok ? (isg ? c.wg : c.wsb) + u * 8 * 40960 : 0xF0000000u, st = isg ? 54 * 1024 : 128 * 1024;
#pragma unroll
    for (int m = 0; m < 4; ++m) r.xa[S][m] = ld16_sc1(c.sws, c.vl16, ao + m * st);
#pragma unroll
    for (int n = 0; n < 5; ++n) r.wa[S][n] = ld16_sc1(c.sws, c.vl16, wo_ + n * 1024);
}
DI f32x4 p4_sum(const P4Ctx& c, int t) {
    lcptr p = c.red + t * 1024;
    f32x4 sum = (*(const LAS f32x4*)(p) + *(const LAS f32x4*)(p + 20480)) + (*(const LAS f32x4*)(p + 40960) + *(const LAS f32x4*)(p + 61440));
    p += 81920;
    return sum + ((*(const LAS f32x4*)(p) + *(const LAS f32x4*)(p + 20480)) + (*(const LAS f32x4*)(p + 40960) + *(const LAS f32x4*)(p + 61440)));
}
DI void p4_put(const P4Ctx& c, P4Regs& r) {
    asm volatile("s_nop 7\n\ts_nop 7\n\ts_nop 7" ::: "memory");
#pragma unroll
    for (int n = 0; n < 5; ++n)
#pragma unroll
        for (int m = 0; m < 4; ++m) *(LAS f32x4*)(c.red + c.w * 20480 + (n * 4 + m) * 1024) = r.acc[n][m];
}
template <int S> DI void p4_mm(P4Regs& r) {
    SBAR();
#pragma unroll
    for (int n = 0; n < 5; ++n)
#pragma unroll
        for (int m = 0; m < 4; ++m) asm volatile("v_mfma_f32_16x16x32_bf16 %0, %1, %2, %0" : "+v"(r.acc[n][m]) : "v"(r.wa[S][n]), "v"(r.xa[S][m]));
    SBAR();
}
DI void p4_zero(P4Regs& r) {
#pragma unroll
    for (int n = 0; n < 5; ++n)
#pragma unroll
        for (int m = 0; m < 4; ++m) r.acc[n][m] = (f32x4){0.f, 0.f, 0.f, 0.f};
    asm volatile("s_nop 1" ::: "memory");
}
DI void p4_main(const P4Ctx& c, const Sync& y, P4Regs& r) {
    p4_zero(r);
#pragma nounroll
    for (int q = 0; q < 6; ++q) {
        p4_load<3>(c, r, 4 * q + 3); p4_mm<0>(r);
        if (q == 1) check_flags(y.flags, FL_CTX + c.jj * 32 + (lane_id() & 31), r.seen, y.token);
        p4_load<0>(c, r, 4 * q + 4); p4_mm<1>(r);
        p4_load<1>(c, r, 4 * q + 5); p4_mm<2>(r);
        if (q == 0) r.seen = poll_flag(y.flags, FL_CTX + c.jj * 32 + (lane_id() & 31));
        p4_load<2>(c, r, 4 * q + 6); p4_mm<3>(r);
        if (q == 1) { p4_put(c, r); p4_zero(r); }
        if (q == 2) {
            asm volatile("s_waitcnt lgkmcnt(0)" ::: "memory"); BARRIER();
#pragma unroll
            for (int i = 0; i < 3; ++i) {
                if (i < 2 || c.w < 4) {
                    const int t = c.w < 4 ? 3 * c.w + i : 12 + 2 * (c.w - 4) + i;
                    f32x4 v = p4_sum(c, t);
#pragma unroll
                    for (int e = 0; e < 4; ++e) v[e] = v[e] * __builtin_amdgcn_rcpf(1.f + __builtin_amdgcn_exp2f(-1.4426950408889634f * v[e]));
                    *(LAS f32x4*)(c.red + t * 1024) = v;
                }
                SBAR();
            }
        }
    }
}

__global__ __launch_bounds__(512, 2) void fused_kernel(const bf16_t* __restrict__ x, const bf16_t* __restrict__ ckv, const bf16_t* __restrict__ wq, const bf16_t* __restrict__ wuk,
                                                       const bf16_t* __restrict__ wuv, const bf16_t* __restrict__ wo, const bf16_t* __restrict__ w1, const bf16_t* __restrict__ b1,
                                                       const bf16_t* __restrict__ w2, bf16_t* __restrict__ out, char* __restrict__ ws) {
    __shared__ __attribute__((aligned(1024))) char smem[163840];
    const lptr lds = (lptr)smem;
    const int b = blockIdx.x, xcd = b & 7, bi = b >> 3, h = xcd * 4 + (bi >> 3), j = bi & 7;
    const int w = __builtin_amdgcn_readfirstlane(threadIdx.x >> 6);
    const rsrc_t sws = mk_rsrc(ws, WS_BYTES);
    STAMP(0);
    Sync y;
    y.flags = (gu64*)(ws + OFF_FLAGS); y.b = b; y.h = h; y.j = j; y.w = w;
    asm volatile("s_load_dwordx2 %0, %1, 0x20\n\ts_waitcnt lgkmcnt(0)" : "=s"(y.token) : "s"(ws) : "memory");
    y.token += 1;
    u32x4 kv[16];
    P1Ctx c1;
    P2Ctx c2;
    const bool isv = (w & 3) >= 2;
    unsigned sink = 0;
    {
        const int l = lane_id(), g = l >> 4, q = (l & 15) >> 2;
        {
            const int nb = w >> 1;
            c1.w = w; c1.sws = sws; c1.wo = wo; c1.w2 = w2; c1.lds = lds;
            c1.sx = mk_rsrc(ckv, 2048 * 512 * 2);
            c1.sw = mk_rsrc(nb < 2 ? wuk : wuv, 512 * 4096 * 2);
#pragma unroll
            for (int par = 0; par < 2; ++par) {
                c1.vx[par] = (l >> 3) * 1024 + (((l & 7) ^ (4 * par + (l >> 4))) * 16);
                c1.vw[par] = (l >> 3) * 8192 + (((l & 7) ^ ((((l >> 4) & 1) | (par << 1)) << 1)) * 16);
            }
            c1.sxo = (256 * j + 32 * w) * 1024;
            c1.swo = (32 * (w & 1)) * 8192 + (h * 128 + 64 * (nb & 1)) * 2;
            const int wn = w & 3, wm = w >> 2, f = (l & 15) >> 1, s = ((q >> 1) & 1) | ((g & 1) << 1);
            c1.xb[0] = lds + wm * 16384 + (l & 15) * 128 + ((g ^ f) * 16);
            c1.xb[1] = lds + wm * 16384 + (l & 15) * 128 + (((g ^ f) ^ 4) * 16);
#pragma unroll
            for (int nt = 0; nt < 4; ++nt) c1.wb[nt] = lds + 65536 + wn * 8192 + (8 * g + q) * 128 + ((nt ^ s) * 32) + (l & 3) * 8;
        }
        p1_dma(c1, 0, 0);
        u32x4 xo0, xo1;
        {
            const rsrc_t sxi = mk_rsrc(x, 2048 * 640 * 2);
            const unsigned vx = (l & 7) * 1280 + (l >> 3) * 16, so0 = b * 8 * 1280 + w * 128, so1 = b * 8 * 1280 + (8 + (w & 1)) * 128;
            asm volatile("s_nop 4\n\tbuffer_load_dwordx4 %0, %2, %3, %4 offen\n\tbuffer_load_dwordx4 %1, %2, %3, %5 offen" : "=&v"(xo0), "=&v"(xo1) : "v"(vx), "s"(sxi), "s"(so0), "s"(so1) : "memory");
        }
        p1_dma(c1, 1, 1);
        {
            const char* pa = (const char*)ckv + (size_t)(256 * j + 64 * (h & 3) + 8 * w + (l >> 3)) * 1024 + (l & 7) * 128;
            const char* pb;
            if (w < 4) pb = (const char*)((w & 1) ? wuv : wuk) + (size_t)(64 * j + 32 * (w >> 1) + (l >> 1)) * 8192 + h * 256 + (l & 1) * 128;
            else { const int L = (w - 4) * 64 + l;
                   pb = (L >= 160 && L < 240 && h < 27) ? (const char*)w1 + (size_t)(80 * j + L - 160) * 3456 + h * 128
                                                        : (const char*)wq + (size_t)(80 * j + ((L < 160 ? L : 0) >> 1)) * 8192 + h * 256 + (L & 1) * 128; }
            const int gw = b * 8 + w < 1820 ? b * 8 + w : 0, pu = gw / 10, pq = gw - pu * 10;
            const char* pc = (const char*)(pu < 128 ? wo : w2) + (size_t)((pu < 128 ? pu : pu - 128) * 32 + (l & 31)) * 1280 + pq * 128;
            asm volatile("global_load_dword %0, %1, off\n\tglobal_load_dword %0, %2, off\n\tglobal_load_dword %0, %3, off" : "+v"(sink) : "v"(pa), "v"(pb), "v"(pc) : "memory");
        }
        asm volatile("s_waitcnt vmcnt(11)" : "+v"(xo0), "+v"(xo1) :: "memory");
        float v0[8], v1[8];
#pragma unroll
        for (int e = 0; e < 4; ++e) { v0[2 * e] = bflo(xo0[e]); v0[2 * e + 1] = bfhi(xo0[e]); v1[2 * e] = bflo(xo1[e]); v1[2 * e + 1] = bfhi(xo1[e]); }
        auto red8 = [](float t) { t += __uint_as_float(__builtin_amdgcn_update_dpp(0u, __float_as_uint(t), 0x128, 0xf, 0xf, false)); return xsum4(t); };
        {
            const float m0 = red8(((v0[0] + v0[1]) + (v0[2] + v0[3])) + ((v0[4] + v0[5]) + (v0[6] + v0[7]))) * (1.f / 64.f);
            const float m1 = red8(((v1[0] + v1[1]) + (v1[2] + v1[3])) + ((v1[4] + v1[5]) + (v1[6] + v1[7]))) * (1.f / 64.f);
            float q0 = 0.f, q1 = 0.f;
#pragma unroll
            for (int e = 0; e < 8; ++e) { const float d0 = v0[e] - m0, d1 = v1[e] - m1; q0 += d0 * d0; q1 += d1 * d1; }
            q0 = red8(q0); q1 = red8(q1);
            if (l < 8) { *(LAS f32x2*)(lds + 162816 + l * 80 + w * 8) = (f32x2){m0, q0}; if (w < 2) *(LAS f32x2*)(lds + 162816 + l * 80 + 64 + w * 8) = (f32x2){m1, q1}; }
        }
        asm volatile("s_waitcnt lgkmcnt(0)" ::: "memory"); BARRIER();
        {
            f32x4 t[5];
#pragma unroll
            for (int i = 0; i < 5; ++i) t[i] = *(const LAS f32x4*)(lds + 162816 + (l & 7) * 80 + i * 16);
            float mean = 0.f;
#pragma unroll
            for (int i = 0; i < 5; ++i) mean += t[i][0] + t[i][2];
            mean *= 0.1f;
            float m2 = 0.f;
#pragma unroll
            for (int i = 0; i < 5; ++i) { const float d0 = t[i][0] - mean, d1 = t[i][2] - mean; m2 += (t[i][1] + t[i][3]) + 64.f * (d0 * d0 + d1 * d1); }
            const float rstd = 1.f / __builtin_sqrtf(m2 * (1.f / 640.f) + 1e-5f);
            const unsigned vo = (l >> 3) * 256 + (l & 7) * 16, xo = OFF_XN + (b >> 1) * 20480 + (b & 1) * 128;
            u32x4 o0, o1;
#pragma unroll
            for (int e = 0; e < 4; ++e) { o0[e] = cvtpk((v0[2 * e] - mean) * rstd, (v0[2 * e + 1] - mean) * rstd); o1[e] = cvtpk((v1[2 * e] - mean) * rstd, (v1[2 * e + 1] - mean) * rstd); }
            st16_sc1(sws, o0, vo, xo + w * 2048);
            if (w < 2) st16_sc1(sws, o1, vo, xo + (8 + w) * 2048);
        }
        {
            c2.w = w; c2.sws = sws; c2.swq = mk_rsrc(wq, 640 * 4096 * 2); c2.sw1 = mk_rsrc(w1, 640 * 1728 * 2);
            c2.vl16 = l * 16;
            const int swz = ((l >> 4) & 1) | ((w & 1) << 1);
            c2.vwq = (l >> 3) * 8192 + (((l & 7) ^ (swz << 1)) * 16);
            c2.vw1 = (l >> 3) * 3456 + (((l & 7) ^ (swz << 1)) * 16);
            { const int mtl = 2 * w, mtg = mtl < 8 ? 8 * j + mtl : 8 * (15 - j) + (mtl - 8); c2.sxo = OFF_XN + mtg * 20480; }
            c2.swqo = (8 * w) * 8192 + h * 256;
            c2.sw1o = (8 * w) * 3456 + h * 128;
            { const int wn = w & 3, wm = w >> 2;
              c2.skv = isv ? OFF_V + (unsigned)(h * 32 + 4 * j + 2 * wm) * 16384 + (wn - 2) * 4096 : OFF_K + (unsigned)((h * 128 + 16 * j + 8 * wm) * 4 + 2 * wn) * 1024; }
        }
    }
    STAMP(1); CSTAMP(14);
    if (isv) p1_main<true>(c1, c2, y, sink, kv, std::make_integer_sequence<int, 8>{}); else p1_main<false>(c1, c2, y, sink, kv, std::make_integer_sequence<int, 8>{});
    STAMP(2); CSTAMP(15);
    p2_kvst<0>(c2, kv, isv); p2_kvst<1>(c2, kv, isv); p2_kvst<2>(c2, kv, isv); p2_kvst<3>(c2, kv, isv);
    const bool hasf = h < 27;
    AttnCtx c3;
    AttnRegs r;
    {
        const int l = lane_id(), g = l >> 4, q = (l & 15) >> 2;
        const int wn = w & 1, wm = w >> 1;
        const int s = ((q >> 1) & 1) | ((g & 1) << 1);
        c2.xb = lds + wm * 8192 + l * 16;
#pragma unroll
        for (int nt = 0; nt < 4; ++nt) c2.wq[nt] = lds + 65536 + wn * 8192 + (8 * g + q) * 128 + ((nt ^ s) * 32) + (l & 3) * 8;
#pragma unroll
        for (int ft = 0; ft < 2; ++ft) c2.wf[ft] = lds + 65536 + 16384 + (8 * g + q) * 128 + (((2 * wn + ft) ^ s) * 32) + (l & 3) * 8;
        f32x4 acc[6][4];
#pragma unroll
        for (int a = 0; a < 6; ++a)
#pragma unroll
            for (int m = 0; m < 4; ++m) acc[a][m] = (f32x4){0.f, 0.f, 0.f, 0.f};
        STAMP(3);
        if (hasf) p2_main<true>(c2, y, acc, kv, isv, std::make_integer_sequence<int, 10>{}); else p2_main<false>(c2, y, acc, kv, isv, std::make_integer_sequence<int, 10>{});
        STAMP(4);
        constexpr float C2 = 0.08838834764831845f * 1.4426950408889634f;
#pragma unroll
        for (int mt = 0; mt < 4; ++mt)
#pragma unroll
            for (int a = 0; a < 2; ++a)
                *(LAS u32x4*)(lds + 98304 + ((4 * wm + mt) * 4 + 2 * wn + a) * 1024 + l * 16) = pack8(acc[2 * a][mt] * C2, acc[2 * a + 1][mt] * C2);
        u32x4 actf[4];
        if (hasf) {
            const int f0 = 64 * h + 32 * wn + 4 * g;
            const u32x2 bA = *(const u32x2*)(b1 + f0), bB = *(const u32x2*)(b1 + f0 + 16);
            const f32x4 biasA = {bflo(bA[0]), bfhi(bA[0]), bflo(bA[1]), bfhi(bA[1])}, biasB = {bflo(bB[0]), bfhi(bB[0]), bflo(bB[1]), bfhi(bB[1])};
#pragma unroll
            for (int mt = 0; mt < 4; ++mt) {
                f32x4 hA = acc[4][mt] + biasA, hB = acc[5][mt] + biasB;
#pragma unroll
                for (int i = 0; i < 4; ++i) { hA[i] = mish(hA[i]); hB[i] = mish(hB[i]); }
                actf[mt] = pack8(hA, hB);
            }
        }
        asm volatile("s_waitcnt vmcnt(0) lgkmcnt(0)" ::: "memory"); BARRIER();
        const int grp = w >> 2, qg = w & 3;
        c3.sws = sws; c3.vl16 = l * 16; c3.kbase = OFF_K + (unsigned)h * 524288; c3.vbase = OFF_V + (unsigned)h * 524288;
        c3.ctxl = OFF_CTX + ((8 * j + 2 * qg) * 128 + 4 * h) * 1024;
        c3.nL = 2 * j + 2; c3.grp = grp; c3.qg = qg; c3.j = j; c3.rowrel = (l & 15) - 4 * g; c3.kl = lds + l * 16; c3.vl = lds + 65536 + l * 16;
        c3.ql = lds + 131072 + qg * 8192 + l * 16;
        if (grp == 0) { a_dmaK(c3, 0); a_dmaV(c3, 0); } else { a_dmaK(c3, 1); }
        if (hasf) {
#pragma unroll
            for (int mt = 0; mt < 4; ++mt) { const int mtl = 4 * wm + mt, mtg = mtl < 8 ? 8 * j + mtl : 8 * (15 - j) + (mtl - 8);
                st16_sc1(sws, actf[mt], l * 16, OFF_ACT + (mtg * 54 + 2 * h + wn) * 1024); }
        }
#pragma unroll
        for (int qt = 0; qt < 2; ++qt)
#pragma unroll
            for (int u = 0; u < 4; ++u) r.q[qt][u] = ld128(lds + 98304 + ((8 * grp + 2 * qg + qt) * 4 + u) * 1024 + l * 16);
    }
    STAMP(5);
    {
        const int l = lane_id();
        const int grp = c3.grp, qg = c3.qg;
        const unsigned vl16 = c3.vl16;
#pragma unroll
        for (int d = 0; d < 8; ++d) { r.o[d][0] = (f32x4){0.f, 0.f, 0.f, 0.f}; r.o[d][1] = (f32x4){0.f, 0.f, 0.f, 0.f}; }
        r.negm[0] = (f32x4){0.f, 0.f, 0.f, 0.f}; r.negm[1] = (f32x4){0.f, 0.f, 0.f, 0.f};
        AttnSm z; z.m0 = 0.f; z.m1 = 0.f; z.l0 = 0.f; z.l1 = 0.f; z.ll0 = 0.f; z.ll1 = 0.f;
        if (hasf) asm volatile("s_waitcnt vmcnt(4) lgkmcnt(0)" ::: "memory"); else asm volatile("s_waitcnt vmcnt(0) lgkmcnt(0)" ::: "memory");
        BARRIER();
        STAMP(6); CSTAMP(12);
        a_pre<16>(r, c3.vl, c3.kl, std::make_integer_sequence<int, PD>{});
        if (grp == 1) BARRIER();
        { Piece none; none.on = false; none.sws = sws; none.vl16 = 0; none.soff = 0; none.ldso = 0; a_run<16, 0>(r, c3.vl, c3.kl, none, std::make_integer_sequence<int, 16>{}); }
        BARRIER();
        c3.swap_s = grp == 0 ? c3.nL - 1 : 99; c3.first_s = 0;
        asm volatile("" : "+s"(c3.swap_s), "+s"(c3.first_s));
        {
            const int nL = c3.nL;
            auto lean_init = [&](int s) {
                Lean st;
                const bool eK = grp == 1 && s + 2 < nL;
                const int t0 = s < nL ? s : 2 * s - nL + grp, t1 = s + 1 < nL ? s + 1 : 2 * s + 2 - nL + grp, t2 = s + 2 < nL ? s + 2 : 2 * s + 4 - nL + grp;
                const int te = eK ? t2 : t1;
                st.e_soff = (eK ? c3.kbase : c3.vbase) + te * 16384 + qg * 4096; st.e_lds = (eK ? 0 : 65536) + (te & 3) * 16384 + qg * 4096;
                st.l_soff = c3.kbase + t2 * 16384 + qg * 4096; st.l_lds = (t2 & 3) * 16384 + qg * 4096;
                st.voff = (t0 & 3) * 16384; st.koff = (t1 & 3) * 16384;
                return st;
            };
            int s = 0, phase = 0;
#pragma nounroll
            for (;;) {
                const int gend = phase == 0 ? 1 : (phase == 1 ? (grp == 0 ? nL + 1 : nL - 1) : 17);
#pragma nounroll
                while (s < gend) { a_slotfull(c3, y, r, z, s PPASSG); ++s; }
                if (phase == 0) { if (w == 0 && l == 0) set_flag(y.flags, FL_ACT + j * 32 + h, y.token); }
                if (phase == 2 || s > 16) break;
                if (phase == 1 && s <= 14) {
                    Lean st = lean_init(s);
#pragma nounroll
                    do { a_slot_lean<true>(c3, r, z, st PPASS);
                         st.e_soff += 32768; st.e_lds ^= 32768; st.l_soff += 32768; st.l_lds ^= 32768; st.voff ^= 32768; st.koff ^= 32768; ++s; } while (s <= 14);
                }
                const int send = phase == 0 ? nL - 2 : 16;
                if (s < send) {
                    Lean st = lean_init(s);
#pragma nounroll
                    do { a_slot_lean<false>(c3, r, z, st PPASS);
                         st.e_soff += 16384; st.e_lds = (st.e_lds & ~0xC000u) | ((st.e_lds + 16384) & 0xC000u); st.voff = (st.voff + 16384) & 0xC000u; st.koff = (st.koff + 16384) & 0xC000u; ++s; } while (s < send);
                }
                ++phase;
            }
        }
        float m0 = z.m0, m1 = z.m1, l0 = z.l0, l1 = z.l1;
        if (grp == 0) BARRIER();
        STAMP(7); CSTAMP(13);
        u64 seenA[4];
#pragma unroll
        for (int i = 0; i < 4; ++i) seenA[i] = poll_flag(y.flags, FL_ACT + 64 * i + l);
        l0 = xsum4(l0); l1 = xsum4(l1);
        {
            const lptr xo = lds + (qg * 2 + grp) * 9216;
#pragma unroll
            for (int d = 0; d < 4; ++d)
#pragma unroll
                for (int qt = 0; qt < 2; ++qt) *(LAS f32x4*)(xo + (d * 2 + qt) * 1024 + l * 16) = sel(grp == 0, r.o[4 + d][qt], r.o[d][qt]);
            *(LAS f32x4*)(xo + 8192 + l * 16) = (f32x4){m0, m1, l0, l1};
            asm volatile("s_waitcnt lgkmcnt(0)" ::: "memory"); BARRIER();
#pragma unroll
            for (int i = 0; i < 4; ++i) check_flags(y.flags, FL_ACT + 64 * i + l, seenA[i], y.token);
            const lcptr xi = lds + (qg * 2 + (grp ^ 1)) * 9216;
            const f32x4 st = *(const LAS f32x4*)(xi + 8192 + l * 16);
            const float M0 = __builtin_fmaxf(m0, st[0]), M1 = __builtin_fmaxf(m1, st[1]);
            const float wa0 = __builtin_amdgcn_exp2f(m0 - M0), wb0 = __builtin_amdgcn_exp2f(st[0] - M0), wa1 = __builtin_amdgcn_exp2f(m1 - M1), wb1 = __builtin_amdgcn_exp2f(st[1] - M1);
            const float i0 = __builtin_amdgcn_rcpf(wa0 * l0 + wb0 * st[2]), i1 = __builtin_amdgcn_rcpf(wa1 * l1 + wb1 * st[3]);
            const float ca0 = wa0 * i0, cb0 = wb0 * i0, ca1 = wa1 * i1, cb1 = wb1 * i1;
#pragma unroll
            for (int up = 0; up < 2; ++up)
#pragma unroll
                for (int qt = 0; qt < 2; ++qt) {
                    const f32x4 pa = *(const LAS f32x4*)(xi + ((2 * up) * 2 + qt) * 1024 + l * 16), pb = *(const LAS f32x4*)(xi + ((2 * up + 1) * 2 + qt) * 1024 + l * 16);
                    const f32x4 oa = sel(grp == 0, r.o[2 * up][qt], r.o[4 + 2 * up][qt]), ob = sel(grp == 0, r.o[2 * up + 1][qt], r.o[4 + 2 * up + 1][qt]);
                    const float ca = qt ? ca1 : ca0, cb = qt ? cb1 : cb0;
                    st16_sc1(sws, pack8(oa * ca + pa * cb, ob * ca + pb * cb), vl16, OFF_CTX + ((8 * (15 - j) + 2 * qg + qt) * 128 + 4 * h + 2 * grp + up) * 1024);
                }
        }
    }
    STAMP(8);
    {
        const int rb = (xcd >> 1) * 8 + (bi >> 2), cb = (xcd & 1) * 4 + (bi & 3);
        const int xq = rb >> 1;
        P4Ctx c4;
        c4.sws = sws; c4.vl16 = lane_id() * 16; c4.w = w; c4.jj = xq < 8 ? xq : 15 - xq; c4.red = lds + c4.vl16;
        c4.ag = OFF_ACT + (4 * rb * 54 + w) * 1024; c4.as = OFF_CTX + (4 * rb * 128 + w) * 1024;
        c4.wg = OFF_WP + ((128 + w) * 40 + cb * 5) * 1024; c4.wsb = OFF_WP + (w * 40 + cb * 5) * 1024;
        P4Regs r4;
        r4.seen = 0;
        p4_load<0>(c4, r4, 0); p4_load<1>(c4, r4, 1); p4_load<2>(c4, r4, 2);
        asm volatile("s_waitcnt vmcnt(27) lgkmcnt(0)" ::: "memory");
        BARRIER();
        if (w == 0 && lane_id() == 0) set_flag(y.flags, FL_CTX + j * 32 + h, y.token);
        STAMP(9);
        p4_main(c4, y, r4);
        STAMP(10);
        const int l = lane_id(), row0 = 64 * rb + (l & 15), col0 = 80 * cb + 4 * (l >> 4);
        f32x4 gsv[3]; u32x2 xvv[3];
#pragma unroll
        for (int i = 0; i < 3; ++i) { const int t = w < 4 ? 3 * w + i : 12 + 2 * (w - 4) + (i & 1);
            gsv[i] = *(const LAS f32x4*)(c4.red + t * 1024); xvv[i] = *(const u32x2*)(x + (row0 + (t & 3) * 16) * 640 + col0 + (t >> 2) * 16); }
        asm volatile("s_waitcnt lgkmcnt(0)" ::: "memory"); BARRIER();
        p4_put(c4, r4);
        asm volatile("s_waitcnt lgkmcnt(0)" ::: "memory"); BARRIER();
#pragma unroll
        for (int i = 0; i < 3; ++i) {
            if (i < 2 || w < 4) {
                const int t = w < 4 ? 3 * w + i : 12 + 2 * (w - 4) + i;
                const f32x4 v = p4_sum(c4, t), gs = gsv[i];
                const u32x2 xv = xvv[i];
                const f32x4 o = {bflo(xv[0]) + v[0] * gs[0], bfhi(xv[0]) + v[1] * gs[1], bflo(xv[1]) + v[2] * gs[2], bfhi(xv[1]) + v[3] * gs[3]};
                *(u32x2*)(out + (row0 + (t & 3) * 16) * 640 + col0 + (t >> 2) * 16) = (u32x2){cvtpk(o[0], o[1]), cvtpk(o[2], o[3])};
            }
            SBAR();
        }
        if (b == 0 && w == 0 && l == 0) __hip_atomic_store(y.flags + 4, y.token, __ATOMIC_RELAXED, __HIP_MEMORY_SCOPE_AGENT);
    }
    STAMP(11);
}

extern "C" void kernel_launch(void* const* d_in, const int* in_sizes, int n_in, void* d_out, int out_size, void* d_ws, size_t ws_size, hipStream_t stream) {
    (void)in_sizes; (void)n_in; (void)out_size; (void)ws_size;
    fused_kernel<<<256, 512, 0, stream>>>((const bf16_t*)d_in[0], (const bf16_t*)d_in[1], (const bf16_t*)d_in[2], (const bf16_t*)d_in[3], (const bf16_t*)d_in[4],
                                          (const bf16_t*)d_in[5], (const bf16_t*)d_in[7], (const bf16_t*)d_in[8], (const bf16_t*)d_in[9], (bf16_t*)d_out, (char*)d_ws);
}
```

```cpp
#include <hip/hip_runtime.h>
#include <stdint.h>
#include <utility>

typedef unsigned short bf16_t;
typedef short bf16x8 __attribute__((ext_vector_type(8)));
typedef short s16x4 __attribute__((ext_vector_type(4)));
typedef short v4i16_t __attribute__((ext_vector_type(4)));
typedef float f32x4 __attribute__((ext_vector_type(4)));
typedef float f32x2 __attribute__((ext_vector_type(2)));
typedef unsigned u32x4 __attribute__((ext_vector_type(4)));
typedef unsigned u32x2 __attribute__((ext_vector_type(2)));
typedef __bf16 bf16x2_t __attribute__((ext_vector_type(2)));
typedef __amdgpu_buffer_rsrc_t rsrc_t;
typedef unsigned long long u64;
#define LAS __attribute__((address_space(3)))
#define GAS __attribute__((address_space(1)))
typedef LAS char* lptr;
typedef LAS const char* lcptr;
typedef GAS u64 gu64;
#define DI __device__ __forceinline__
#define SBAR() __builtin_amdgcn_sched_barrier(0)
#define PIN(x) asm volatile("" : "+v"(x))
#define MF16(a, b, c) __builtin_amdgcn_mfma_f32_16x16x32_bf16(a, b, c, 0, 0, 0)
#define WAITV(n) asm volatile("s_waitcnt vmcnt(" #n ")" ::: "memory")
#define WAITL(n) asm volatile("s_waitcnt lgkmcnt(" #n ")" ::: "memory")
#define BARRIER() do { asm volatile("" ::: "memory"); __builtin_amdgcn_s_barrier(); asm volatile("" ::: "memory"); } while (0)

#define UPTO 4
#define XST 0
#define STAMPS 0

constexpr unsigned OFF_FLAGS = 0;
constexpr unsigned OFF_XN = 65536;
constexpr unsigned OFF_K = OFF_XN + 2621440;
constexpr unsigned OFF_V = OFF_K + 16777216;
constexpr unsigned OFF_ACT = OFF_V + 16777216 + 8388608;
constexpr unsigned OFF_CTX = OFF_ACT + 7077888;
constexpr unsigned OFF_WP = OFF_CTX + 16777216;
constexpr unsigned OFF_ST = OFF_WP + 182 * 40 * 1024;
constexpr unsigned WS_BYTES = OFF_ST + 256 * 16 * 8 + 256 * 2 * 16 * 4;
constexpr unsigned FL_P0 = 0, FL_KV = 256, FL_ACT = 512, FL_CTX = 768;
constexpr int SC1 = 16;

DI unsigned cvtpk(float lo, float hi) { f32x2 v = {lo, hi}; bf16x2_t b = __builtin_convertvector(v, bf16x2_t); return __builtin_bit_cast(unsigned, b); }
DI u32x4 pack8(f32x4 a, f32x4 b) { u32x4 w = {cvtpk(a[0], a[1]), cvtpk(a[2], a[3]), cvtpk(b[0], b[1]), cvtpk(b[2], b[3])}; return w; }
DI float bflo(unsigned u) { return __uint_as_float(u << 16); }
DI float bfhi(unsigned u) { return __uint_as_float(u & 0xffff0000u); }
DI s16x4 vtr(lcptr p) { return __builtin_bit_cast(s16x4, __builtin_amdgcn_ds_read_tr16_b64_v4i16((LAS v4i16_t*)p)); }
DI bf16x8 cat(s16x4 a, s16x4 b) { return (bf16x8){a[0], a[1], a[2], a[3], b[0], b[1], b[2], b[3]}; }
DI bf16x8 ld128(lcptr p) { return *(const LAS bf16x8*)p; }
DI bf16x8 asbf(u32x4 v) { return __builtin_bit_cast(bf16x8, v); }
DI rsrc_t mk_rsrc(const void* p, unsigned bytes) { return __builtin_amdgcn_make_buffer_rsrc((void*)p, (short)0, (int)bytes, 0x00020000); }
DI void dma16(rsrc_t r, unsigned voff, unsigned soff, unsigned ldsoff) {
    asm volatile("s_mov_b32 m0, %0\n\ts_nop 3\n\tbuffer_load_dwordx4 %1, %2, %3 offen lds" ::"s"(ldsoff), "v"(voff), "s"(r), "s"(soff) : "memory");
}
DI void dma16x4(rsrc_t r, unsigned voff, unsigned soff, unsigned ldsoff) {
    asm volatile("s_mov_b32 m0, %0\n\ts_nop 3\n\tbuffer_load_dwordx4 %1, %2, %3 offen lds\n\tbuffer_load_dwordx4 %1, %2, %3 offen offset:1024 lds\n\t"
                 "buffer_load_dwordx4 %1, %2, %3 offen offset:2048 lds\n\tbuffer_load_dwordx4 %1, %2, %3 offen offset:3072 lds" ::"s"(ldsoff), "v"(voff), "s"(r), "s"(soff) : "memory");
}
DI void st16_sc1(rsrc_t r, u32x4 v, unsigned voff, unsigned soff) { asm volatile("s_nop 4\n\tbuffer_store_dwordx4 %0, %1, %2, %3 offen sc1\n\ts_nop 1" ::"v"(v), "v"(voff), "s"(r), "s"(soff) : "memory"); }
template <int OFF> DI void st16_sc1o(rsrc_t r, u32x4 v, unsigned voff, unsigned soff) { asm volatile("s_nop 4\n\tbuffer_store_dwordx4 %0, %1, %2, %3 offen offset:%4 sc1\n\ts_nop 1" ::"v"(v), "v"(voff), "s"(r), "s"(soff), "i"(OFF) : "memory"); }
DI u32x4 ld16(rsrc_t r, unsigned voff, unsigned soff) { return __builtin_amdgcn_raw_buffer_load_b128(r, voff, soff, 0); }
#define P4AUX SC1
DI u32x4 ld16_sc1(rsrc_t r, unsigned voff, unsigned soff) { return __builtin_amdgcn_raw_buffer_load_b128(r, voff, soff, P4AUX); }
template <class T> DI T sel(bool c, T a, T b) { return c ? a : b; }
DI int lane_id() { int l; asm volatile("v_mbcnt_lo_u32_b32 %0, -1, 0\n\tv_mbcnt_hi_u32_b32 %0, -1, %0" : "=v"(l)); return l; }
DI void set_flag(gu64* flags, unsigned idx, u64 token) { __hip_atomic_store(flags + idx * 8, token, __ATOMIC_RELAXED, __HIP_MEMORY_SCOPE_AGENT); }
DI u64 poll_flag(gu64* flags, unsigned idx) { return __hip_atomic_load(flags + idx * 8, __ATOMIC_RELAXED, __HIP_MEMORY_SCOPE_AGENT); }
DI void spin_flags(gu64* flags, unsigned idx, u64 token) {
    const unsigned long long t0 = __builtin_amdgcn_s_memrealtime();
    for (;;) {
        __builtin_amdgcn_s_sleep(1);
        const u64 v = poll_flag(flags, idx);
        if (__all(v == token)) break;
        if (__builtin_amdgcn_s_memrealtime() - t0 > 2000000ull) break;
    }
}
DI void check_flags(gu64* flags, unsigned idx, u64 seen, u64 token) { if (!__all(seen == token)) spin_flags(flags, idx, token); }
DI void acquire_issue() { asm volatile("buffer_inv sc1" ::: "memory"); }
DI u64 uni64(u64 v) { return ((u64)(unsigned)__builtin_amdgcn_readfirstlane((unsigned)(v >> 32)) << 32) | (unsigned)__builtin_amdgcn_readfirstlane((unsigned)v); }
#define STAMP(i) do { } while (0)
#define STAMPW(i, wv) do { } while (0)
#define CSTAMP(i) do { } while (0)

struct Sync { gu64* flags; u64 token; int b, h, j, w; };
#define YSTAMP(i) do { } while (0)

struct P2Ctx { rsrc_t sws, swq, sw1; unsigned vl16, vwq, vw1; unsigned sxo, swqo, sw1o, skv; int w; lcptr xb; lcptr wq[4]; lcptr wf[2]; };
DI void p2_dma1(const P2Ctx& c, int st, int ks, int i) {
    if (i < 4) dma16(c.sws, c.vl16, c.sxo + (i >> 1) * 20480 + (i & 1) * 1024 + ks * 2048, st * 32768 + (4 * c.w + i) * 1024);
    else if (i < 6) dma16(c.swq, c.vwq, c.swqo + (i - 4) * 128 + ks * 524288, 65536 + st * 24576 + (i - 4) * 8192 + c.w * 1024);
    else dma16(c.sw1, c.vw1, c.sw1o + ks * (64 * 3456), 65536 + st * 24576 + 16384 + c.w * 1024);
}

struct P1Ctx { rsrc_t sx, sw, sws; unsigned vx[2], vw[2]; unsigned sxo, swo; int w; lcptr xb[2]; lcptr wb[4]; const bf16_t *wo, *w2, *wq, *w1; lcptr lds; };
DI void pack_fetch(const P1Ctx& c, int b) {
    const int l = lane_id(), gw = b * 8 + c.w, pu = gw / 10, pq = gw - pu * 10;
    const rsrc_t sp = mk_rsrc(pu < 128 ? c.wo : c.w2, pu < 128 ? 4096 * 640 * 2 : 1728 * 640 * 2);
    const unsigned so = gw < 1820 ? (unsigned)((pu < 128 ? pu : pu - 128) * 32 * 1280 + pq * 128) : 0xF0000000u;
#pragma unroll
    for (int i = 0; i < 4; ++i) dma16(sp, (l >> 3) * 1280 + (l & 7) * 16, so + i * 8 * 1280, 131072 + c.w * 4096 + i * 1024);
}
DI void pack_store(const P1Ctx& c, int b) {
    const int l = lane_id(), g = l >> 4, q = (l & 15) >> 2, gw = b * 8 + c.w, pu = gw / 10, pq = gw - pu * 10;
    lcptr p = c.lds + 131072 + c.w * 4096 + (4 * g + q) * 128 + (l & 3) * 8;
    bf16x8 fr[4];
#pragma unroll
    for (int i = 0; i < 4; ++i) fr[i] = cat(vtr(p + i * 32), vtr(p + i * 32 + 16 * 128));
    if (gw < 1820) {
#pragma unroll
        for (int i = 0; i < 4; ++i) st16_sc1(c.sws, __builtin_bit_cast(u32x4, fr[i]), l * 16, OFF_WP + (pu * 40 + pq * 4 + i) * 1024);
    }
}
DI void p1_dma1(const P1Ctx& c, int st, int ks, int i) {
    if (i < 4) dma16(c.sx, c.vx[i & 1], c.sxo + i * 8192 + ks * 128, st * 32768 + (4 * c.w + i) * 1024);
    else dma16(c.sw, c.vw[i & 1], c.swo + (i - 4) * 65536 + ks * 524288, 65536 + st * 32768 + (4 * c.w + (i - 4)) * 1024);
}
DI void p1_dma(const P1Ctx& c, int st, int ks) {
#pragma unroll
    for (int i = 0; i < 8; ++i) p1_dma1(c, st, ks, i);
}
template <int ST, int UP, int I> DI void p1_rdw(const P1Ctx& c, bf16x8 (&wf)[4], s16x4 (&wl)[4]) {
    constexpr int nt = I >> 1, rd = I & 1;
    const s16x4 v = vtr(c.wb[nt] + ST * 32768 + UP * 4096 + rd * 512);
    if constexpr (rd == 0) wl[nt] = v; else wf[nt] = cat(wl[nt], v);
}
template <int ST, int G> DI bf16x8 p1_rdx(const P1Ctx& c) { return ld128(c.xb[G >> 3] + ST * 32768 + (G & 7) * 2048); }
template <bool ISV, int KS, int G> DI void p1_group(const P1Ctx& c, const P2Ctx& c2, const Sync& y, u64& seen, unsigned& sink, f32x4 (&acc)[4][8], bf16x8 (&wA)[4], bf16x8 (&wB)[4], s16x4 (&wl)[4], bf16x8 (&xr)[4]) {
    constexpr int up = G >> 3, mt = G & 7, ST = KS & 1;
    constexpr bool NEXT = KS < 7;
    if constexpr (G == 13) {
        if constexpr (KS == 0) asm volatile("s_waitcnt vmcnt(3) lgkmcnt(0)" ::: "memory");
        else if constexpr (KS == 1 || KS == 3) asm volatile("s_waitcnt vmcnt(1) lgkmcnt(0)" ::: "memory");
        else asm volatile("s_waitcnt vmcnt(0) lgkmcnt(0)" ::: "memory");
        if constexpr (KS == 4) asm volatile("" :: "v"(sink));
        BARRIER();
        if constexpr (!ISV) {
            if constexpr (KS == 1) { if (y.w == 0 && lane_id() == 0) set_flag(y.flags, FL_P0 + y.b, y.token); }
            if constexpr (KS == 5) { if (y.w == 0) { const int l = lane_id() & 31; check_flags(y.flags, FL_P0 + (l < 16 ? 16 * y.j + l : 16 * (15 - y.j) + (l - 16)), seen, y.token); } }
        }
        SBAR();
    }
    const bf16x8 x = xr[G & 3];
#pragma unroll
    for (int nt = 0; nt < 4; ++nt) {
        const bf16x8 wv = up == 0 ? wA[nt] : wB[nt];
        if constexpr (ISV) acc[nt][mt] = MF16(x, wv, acc[nt][mt]); else acc[nt][mt] = MF16(wv, x, acc[nt][mt]);
        if (nt == 0) {
            if constexpr (G + 3 < 16) xr[(G + 3) & 3] = p1_rdx<ST, G + 3>(c);
            else if constexpr (NEXT) xr[(G + 3) & 3] = p1_rdx<ST ^ 1, G + 3 - 16>(c);
        }
        if constexpr (G >= 2 && G < 6) { if (nt == 1) p1_rdw<ST, 1, 2 * (G - 2)>(c, wB, wl); if (nt == 2) p1_rdw<ST, 1, 2 * (G - 2) + 1>(c, wB, wl); }
        if constexpr (G >= 13 && NEXT) {
            if (nt == 1) p1_rdw<ST ^ 1, 0, 3 * (G - 13)>(c, wA, wl);
            if (nt == 2) p1_rdw<ST ^ 1, 0, 3 * (G - 13) + 1>(c, wA, wl);
            if constexpr (G < 15) { if (nt == 3) p1_rdw<ST ^ 1, 0, 3 * (G - 13) + 2>(c, wA, wl); }
        }
        if constexpr (G >= 13) { constexpr int d0 = 3 * (G - 13);
            if constexpr (KS < 6) { if (nt >= 1 && d0 + nt - 1 < 8) p1_dma1(c, ST, KS + 2, d0 + nt - 1); }
            else { if (nt >= 1 && d0 + nt - 1 < 7) p2_dma1(c2, ST, KS - 6, d0 + nt - 1); } }
        SBAR();
    }
    if constexpr (G == 15 && KS == 0) {
        const int l = lane_id(), L = (c.w & 3) * 64 + l;
        const char* pb = (L >= 160 && L < 240 && y.h < 27) ? (const char*)c.w1 + (size_t)(80 * y.j + L - 160) * 3456 + y.h * 128
                                                           : (const char*)c.wq + (size_t)(80 * y.j + ((L < 160 ? L : 0) >> 1)) * 8192 + y.h * 256 + (L & 1) * 128;
        asm volatile("global_load_dword %0, %1, off" : "+v"(sink) : "v"(pb) : "memory"); SBAR();
    }
    if constexpr (G == 15 && KS == 2) {
        const int l = lane_id(), gw = y.b * 8 + c.w < 1820 ? y.b * 8 + c.w : 0, pu = gw / 10, pq = gw - pu * 10;
        const char* pc = (const char*)(pu < 128 ? c.wo : c.w2) + (size_t)((pu < 128 ? pu : pu - 128) * 32 + (l & 31)) * 1280 + pq * 128;
        asm volatile("global_load_dword %0, %1, off" : "+v"(sink) : "v"(pc) : "memory"); SBAR();
    }
    if constexpr (G == 15 && KS == 4) { pack_fetch(c, y.b); SBAR(); }
    if constexpr (G == 15 && KS == 5) { pack_store(c, y.b); SBAR(); }
    if constexpr (G == 15 && !ISV) {
        if constexpr (KS == 4) { if (y.w == 0) { const int l = lane_id() & 31; seen = poll_flag(y.flags, FL_P0 + (l < 16 ? 16 * y.j + l : 16 * (15 - y.j) + (l - 16))); } SBAR(); }
        if constexpr (KS == 5) { if (y.w == 0) acquire_issue(); SBAR(); }
    }
}
template <bool ISV, int KS, int... Gs> DI void p1_step(const P1Ctx& c, const P2Ctx& c2, const Sync& y, u64& seen, unsigned& sink, f32x4 (&acc)[4][8], bf16x8 (&wA)[4], bf16x8 (&wB)[4], s16x4 (&wl)[4], bf16x8 (&xr)[4], std::integer_sequence<int, Gs...>) {
    (p1_group<ISV, KS, Gs>(c, c2, y, seen, sink, acc, wA, wB, wl, xr), ...);
}
template <bool ISV, int... KSs> DI void p1_main(const P1Ctx& c, const P2Ctx& c2, const Sync& y, unsigned& sink, u32x4 (&kv)[16], std::integer_sequence<int, KSs...>) {
    f32x4 acc[4][8];
#pragma unroll
    for (int a = 0; a < 4; ++a)
#pragma unroll
        for (int b = 0; b < 8; ++b) acc[a][b] = (f32x4){0.f, 0.f, 0.f, 0.f};
    bf16x8 wA[4], wB[4], xr[4]; s16x4 wl[4];
    u64 seen = 0;
    asm volatile("" ::: "memory");
    p1_rdw<0, 0, 0>(c, wA, wl); p1_rdw<0, 0, 1>(c, wA, wl); p1_rdw<0, 0, 2>(c, wA, wl); p1_rdw<0, 0, 3>(c, wA, wl);
    p1_rdw<0, 0, 4>(c, wA, wl); p1_rdw<0, 0, 5>(c, wA, wl); p1_rdw<0, 0, 6>(c, wA, wl); p1_rdw<0, 0, 7>(c, wA, wl);
    xr[0] = p1_rdx<0, 0>(c); xr[1] = p1_rdx<0, 1>(c); xr[2] = p1_rdx<0, 2>(c);
    SBAR();
    (p1_step<ISV, KSs>(c, c2, y, seen, sink, acc, wA, wB, wl, xr, std::make_integer_sequence<int, 16>{}), ...);
#pragma unroll
    for (int a = 0; a < 8; ++a)
#pragma unroll
        for (int b = 0; b < 2; ++b) {
            if constexpr (ISV) kv[a * 2 + b] = pack8(acc[(a & 1) * 2 + b][(a >> 1) * 2], acc[(a & 1) * 2 + b][(a >> 1) * 2 + 1]);
            else kv[a * 2 + b] = pack8(acc[2 * b][a], acc[2 * b + 1][a]);
        }
}

template <bool HASF, int ST, int I> DI bf16x8 p2_rdw(const P2Ctx& c) {
    constexpr int NW = HASF ? 6 : 4, up = I / NW, n = I % NW;
    lcptr p = (n < 4 ? c.wq[n & 3] : c.wf[n & 1]) + ST * 24576 + up * 4096;
    return cat(vtr(p), vtr(p + 512));
}
template <int ST, int UP, int MT> DI bf16x8 p2_rdx(const P2Ctx& c) { return ld128(c.xb + ST * 32768 + MT * 2048 + UP * 1024); }
template <int I> DI void p2_kvst(const P2Ctx& c, const u32x4 (&kv)[16], bool isv) {
    st16_sc1(c.sws, kv[I], c.vl16, c.skv + (isv ? (unsigned)(((I >> 3) * 16 + ((I >> 2) & 1) * 8 + (I & 3)) * 1024) : (unsigned)((I >> 1) * 4096 + (I & 1) * 1024)));
}
template <bool HASF, int KS, int G> DI void p2_group(const P2Ctx& c, const Sync& y, u64& seen, f32x4 (&acc)[6][4], bf16x8 (&wr)[4], bf16x8 (&x0)[4], bf16x8 (&x1)[4], const u32x4 (&kv)[16], bool isv) {
    constexpr int NW = HASF ? 6 : 4, NG = 2 * NW, ST = KS & 1, up = G / NW, n = G % NW;
    constexpr bool LAST = (KS == 9);
    if constexpr (G == NG - 3) {
        if constexpr (KS == 0) asm volatile("s_waitcnt vmcnt(4) lgkmcnt(0)" ::: "memory");
        else if constexpr (KS <= 6) asm volatile("s_waitcnt vmcnt(2) lgkmcnt(0)" ::: "memory");
        else asm volatile("s_waitcnt vmcnt(0) lgkmcnt(0)" ::: "memory");
        BARRIER();
        if constexpr (KS + 2 < 10) {
#pragma unroll
            for (int i = 0; i < (HASF ? 7 : 6); ++i) p2_dma1(c, ST, KS + 2, i);
        }
        if constexpr (KS < 6) { p2_kvst<4 + 2 * KS>(c, kv, isv); p2_kvst<5 + 2 * KS>(c, kv, isv); }
        if constexpr (KS == 7) { if (y.w == 0 && lane_id() == 0) set_flag(y.flags, FL_KV + y.h * 8 + y.j, y.token); }
        if constexpr (KS == 8) { if (y.w == 0) seen = poll_flag(y.flags, FL_KV + y.h * 8 + (lane_id() & 7)); }
        if constexpr (KS == 9) { if (y.w == 0) { check_flags(y.flags, FL_KV + y.h * 8 + (lane_id() & 7), seen, y.token); acquire_issue(); } }
        SBAR();
    }
    const bf16x8 wcur = wr[G & 3];
#pragma unroll
    for (int mt = 0; mt < 4; ++mt) {
        acc[n][mt] = MF16(wcur, up == 0 ? x0[mt] : x1[mt], acc[n][mt]);
        if (mt == 0) {
            if constexpr (G + 3 < NG) wr[(G + 3) & 3] = p2_rdw<HASF, ST, G + 3>(c);
            else if constexpr (!LAST) wr[(G + 3) & 3] = p2_rdw<HASF, ST ^ 1, G + 3 - NG>(c);
        }
        if (mt == 1) {
            if constexpr (G < 4) x1[G] = p2_rdx<ST, 1, G>(c);
            else if constexpr (G >= NG - 3 && !LAST) x0[G - (NG - 3)] = p2_rdx<ST ^ 1, 0, G - (NG - 3)>(c);
        }
        if (mt == 2) { if constexpr (G == NG - 1 && !LAST) x0[3] = p2_rdx<ST ^ 1, 0, 3>(c); }
        SBAR();
    }
}
template <bool HASF, int KS, int... Gs> DI void p2_step(const P2Ctx& c, const Sync& y, u64& seen, f32x4 (&acc)[6][4], bf16x8 (&wr)[4], bf16x8 (&x0)[4], bf16x8 (&x1)[4], const u32x4 (&kv)[16], bool isv, std::integer_sequence<int, Gs...>) {
    (p2_group<HASF, KS, Gs>(c, y, seen, acc, wr, x0, x1, kv, isv), ...);
}
template <bool HASF, int... KSs> DI void p2_main(const P2Ctx& c, const Sync& y, f32x4 (&acc)[6][4], const u32x4 (&kv)[16], bool isv, std::integer_sequence<int, KSs...>) {
    bf16x8 wr[4], x0[4], x1[4];
    u64 seen = 0;
    asm volatile("" ::: "memory");
    wr[0] = p2_rdw<HASF, 0, 0>(c); wr[1] = p2_rdw<HASF, 0, 1>(c); wr[2] = p2_rdw<HASF, 0, 2>(c);
    x0[0] = p2_rdx<0, 0, 0>(c); x0[1] = p2_rdx<0, 0, 1>(c); x0[2] = p2_rdx<0, 0, 2>(c); x0[3] = p2_rdx<0, 0, 3>(c);
    SBAR();
    (p2_step<HASF, KSs>(c, y, seen, acc, wr, x0, x1, kv, isv, std::make_integer_sequence<int, (HASF ? 12 : 8)>{}), ...);
}
DI float mish(float h) {
    const float t = __builtin_amdgcn_exp2f(__builtin_fminf(h, 20.f) * 1.4426950408889634f);
    const float n = t * (t + 2.f);
    return h * n * __builtin_amdgcn_rcpf(n + 2.f);
}

constexpr int PD = 8;
#define PRIOM 1
#define PRIOV 0
#define ADDMODE 1
#define ABL_NOEARLY 0
#define APRE_EARLY 1
#define LATE_STEP 7
#define LATE_OFF 2
struct AttnRegs { f32x4 o[8][2]; f32x4 s[4][2]; bf16x8 q[2][4]; u32x4 pk[2][2]; f32x4 negm[2]; bf16x8 op[32]; };
template <int I> DI void a_read(AttnRegs& r, lcptr vp, lcptr kp) {
    if constexpr (I < 16) r.op[I] = ld128(vp + I * 1024);
    else if constexpr (I < 32) r.op[I] = ld128(kp + (((I - 16) & 3) * 4 + ((I - 16) >> 2)) * 1024);
}
struct Piece { rsrc_t sws; unsigned vl16, soff, ldso; bool on; };
template <int P, bool COND> DI void piece(const Piece& d) {
    if ((!COND && !ABL_NOEARLY) || (COND && d.on)) asm volatile("s_mov_b32 m0, %0\n\ts_nop 3\n\tbuffer_load_dwordx4 %1, %2, %3 offen offset:%4 lds" ::"s"(d.ldso), "v"(d.vl16), "s"(d.sws), "s"(d.soff), "i"(P * 1024) : "memory");
}
template <int I, int DMA> DI void a_slot(AttnRegs& r, lcptr vp, lcptr kp, const Piece& late) {
    if constexpr (I < 16) { constexpr int ks = I >> 3, dt = I & 7;
        r.o[dt][0] = MF16(r.op[I], asbf(r.pk[0][ks]), r.o[dt][0]); r.o[dt][1] = MF16(r.op[I], asbf(r.pk[1][ks]), r.o[dt][1]); }
    else { constexpr int u = (I - 16) >> 2, kt = (I - 16) & 3;
        r.s[kt][0] = MF16(r.op[I], r.q[0][u], u == 0 ? r.negm[0] : r.s[kt][0]); r.s[kt][1] = MF16(r.op[I], r.q[1][u], u == 0 ? r.negm[1] : r.s[kt][1]); }
    a_read<I + PD>(r, vp, kp);
    SBAR();
    if constexpr (DMA != 0 && (I % LATE_STEP) == LATE_OFF && I / LATE_STEP < 4) { piece<I / LATE_STEP, DMA == 2>(late); SBAR(); }
}
template <int B, int DMA, int... Is> DI void a_run(AttnRegs& r, lcptr vp, lcptr kp, const Piece& late, std::integer_sequence<int, Is...>) { (a_slot<B + Is, DMA>(r, vp, kp, late), ...); }
template <int B, int... Is> DI void a_pre(AttnRegs& r, lcptr vp, lcptr kp, std::integer_sequence<int, Is...>) { (a_read<B + Is>(r, vp, kp), ...); }

template <int Q> DI void a_exp_quarter(AttnRegs& r, float& a0, float& a1) {
    constexpr int ks = Q >> 1, qt = Q & 1;
    float one = 1.0f; asm("" : "+s"(one));
    float e[8];
#pragma unroll
    for (int i = 0; i < 4; ++i) { e[i] = __builtin_amdgcn_exp2f(r.s[2 * ks][qt][i]); e[4 + i] = __builtin_amdgcn_exp2f(r.s[2 * ks + 1][qt][i]); }
#pragma unroll
    for (int i = 0; i < 8; ++i) {
        if (qt == 0) a0 += e[i]; else a1 = __builtin_fmaf(e[i], one, a1);
    }
    r.pk[qt][ks] = (u32x4){cvtpk(e[0], e[1]), cvtpk(e[2], e[3]), cvtpk(e[4], e[5]), cvtpk(e[6], e[7])};
}
template <bool DMA> DI void a_exp_pack(AttnRegs& r, float& a0, float& a1, const Piece& early) {
    a0 = 0.f; a1 = 0.f;
    if constexpr (DMA) { piece<0, false>(early); SBAR(); }
    a_exp_quarter<0>(r, a0, a1);
    if constexpr (DMA) { SBAR(); piece<1, false>(early); SBAR(); }
    a_exp_quarter<1>(r, a0, a1);
    if constexpr (DMA) { SBAR(); piece<2, false>(early); SBAR(); }
    a_exp_quarter<2>(r, a0, a1);
    if constexpr (DMA) { SBAR(); piece<3, false>(early); SBAR(); }
    a_exp_quarter<3>(r, a0, a1);
}
DI float xmax4(float v) {
    const u32x2 a = __builtin_amdgcn_permlane16_swap(__float_as_uint(v), __float_as_uint(v), false, false);
    v = __builtin_fmaxf(__uint_as_float(a[0]), __uint_as_float(a[1]));
    const u32x2 c = __builtin_amdgcn_permlane32_swap(__float_as_uint(v), __float_as_uint(v), false, false);
    return __builtin_fmaxf(__uint_as_float(c[0]), __uint_as_float(c[1]));
}
DI float xsum4(float v) {
    const u32x2 a = __builtin_amdgcn_permlane16_swap(__float_as_uint(v), __float_as_uint(v), false, false);
    v = __uint_as_float(a[0]) + __uint_as_float(a[1]);
    const u32x2 c = __builtin_amdgcn_permlane32_swap(__float_as_uint(v), __float_as_uint(v), false, false);
    return __uint_as_float(c[0]) + __uint_as_float(c[1]);
}
constexpr float SUM_HI = 1.152921504606846976e18f, SUM_LO = 8.673617379884035e-19f;

struct AttnCtx { rsrc_t sws; unsigned vl16, kbase, vbase, ctxl; int nL, grp, qg, j, rowrel, swap_s, first_s; lcptr kl, vl, ql; };
struct AttnSm { float m0, m1, l0, l1, ll0, ll1; };
DI void a_dmaK(const AttnCtx& c, int n) { dma16x4(c.sws, c.vl16, c.kbase + n * 16384 + c.qg * 4096, (n & 3) * 16384 + c.qg * 4096); }
DI void a_dmaV(const AttnCtx& c, int n) { dma16x4(c.sws, c.vl16, c.vbase + n * 16384 + c.qg * 4096, 65536 + (n & 3) * 16384 + c.qg * 4096); }
#define PT(i) do { } while (0)
#define PARG
#define PPASS
#define PPASSG
DI void a_slotfull(const AttnCtx& c, const Sync& y, AttnRegs& r, AttnSm& z, const int s PARG) {
    const int nL = c.nL, grp = c.grp;
    const bool SWAP = (s == c.swap_s), FIN = (s == c.swap_s + 1), FIRST = (s == c.first_s) || FIN;
    const int tcur = s < nL ? s : 2 * s - nL + grp;
    Piece early, late;
    {
        const int s2 = s + 2, s1 = s + 1;
        const bool eK = grp == 1 && s2 < nL;
        const int te = eK ? s2 : (s1 < nL ? s1 : 2 * s1 - nL + grp), tl = 2 * s2 - nL + grp;
        early.sws = c.sws; early.vl16 = c.vl16; late.sws = c.sws; late.vl16 = c.vl16;
        early.on = eK || (s1 <= 16 && (grp == 0 || s1 >= nL));
        early.soff = (eK ? c.kbase : c.vbase) + te * 16384 + c.qg * 4096; early.ldso = (eK ? 0 : 65536) + (te & 3) * 16384 + c.qg * 4096;
        late.on = s2 <= 16 && s2 >= nL;
        late.soff = c.kbase + tl * 16384 + c.qg * 4096; late.ldso = (tl & 3) * 16384 + c.qg * 4096;
    }
    const int snext = s + 1, tnext = snext < nL ? snext : 2 * snext - nL + grp;
    const lcptr vp = c.vl + (tcur & 3) * 16384, kp = c.kl + (tnext & 3) * 16384;
    a_pre<0>(r, vp, kp, std::make_integer_sequence<int, PD>{});
    SBAR();
    if (SWAP) {
        asm volatile("ds_read_b128 %0, %8\n\tds_read_b128 %1, %8 offset:1024\n\tds_read_b128 %2, %8 offset:2048\n\tds_read_b128 %3, %8 offset:3072\n\t"
                     "ds_read_b128 %4, %8 offset:4096\n\tds_read_b128 %5, %8 offset:5120\n\tds_read_b128 %6, %8 offset:6144\n\tds_read_b128 %7, %8 offset:7168\n\ts_waitcnt lgkmcnt(0)"
                     : "=&v"(r.q[0][0]), "=&v"(r.q[0][1]), "=&v"(r.q[0][2]), "=&v"(r.q[0][3]), "=&v"(r.q[1][0]), "=&v"(r.q[1][1]), "=&v"(r.q[1][2]), "=&v"(r.q[1][3])
                     : "v"(c.ql) : "memory");
    }
    if (FIN) {
        const float i0 = __builtin_amdgcn_rcpf(xsum4(z.ll0)), i1 = __builtin_amdgcn_rcpf(xsum4(z.ll1));
        const unsigned c1o = c.ctxl + 131072;
        st16_sc1o<0>(c.sws, pack8(r.o[0][0] * i0, r.o[1][0] * i0), c.vl16, c.ctxl); st16_sc1o<1024>(c.sws, pack8(r.o[2][0] * i0, r.o[3][0] * i0), c.vl16, c.ctxl);
        st16_sc1o<2048>(c.sws, pack8(r.o[4][0] * i0, r.o[5][0] * i0), c.vl16, c.ctxl); st16_sc1o<3072>(c.sws, pack8(r.o[6][0] * i0, r.o[7][0] * i0), c.vl16, c.ctxl);
        st16_sc1o<0>(c.sws, pack8(r.o[0][1] * i1, r.o[1][1] * i1), c.vl16, c1o); st16_sc1o<1024>(c.sws, pack8(r.o[2][1] * i1, r.o[3][1] * i1), c.vl16, c1o);
        st16_sc1o<2048>(c.sws, pack8(r.o[4][1] * i1, r.o[5][1] * i1), c.vl16, c1o); st16_sc1o<3072>(c.sws, pack8(r.o[6][1] * i1, r.o[7][1] * i1), c.vl16, c1o);
#pragma unroll
        for (int d = 0; d < 8; ++d) { r.o[d][0] = (f32x4){0.f, 0.f, 0.f, 0.f}; r.o[d][1] = (f32x4){0.f, 0.f, 0.f, 0.f}; }
    }
    {
        const bool heavy = (grp == 1) || (s >= nL);
        const int qb = heavy ? 15 - c.j : c.j, kb = 64 * tcur - 128 * qb - 32 * c.qg;
        if (kb >= -32) {
            const float NI = -__builtin_inff();
            const f32x4 ninf = {NI, NI, NI, NI};
            f32x4 s00 = r.s[0][0], s10 = r.s[1][0], s20 = r.s[2][0], s30 = r.s[3][0], s01 = r.s[0][1], s11 = r.s[1][1], s21 = r.s[2][1], s31 = r.s[3][1];
            const int dq = c.rowrel;
            if (kb > 0) { s00 = ninf; s10 = ninf; s20 = ninf; s30 = ninf; s01 = ninf; s11 = ninf; s21 = ninf; s31 = ninf; }
            else if (kb == 0) {
#pragma unroll
                for (int i = 0; i < 4; ++i) { s00[i] = sel(i > dq, NI, s00[i]); s11[i] = sel(i > dq, NI, s11[i]); }
                s10 = ninf; s20 = ninf; s30 = ninf; s21 = ninf; s31 = ninf;
            } else {
#pragma unroll
                for (int i = 0; i < 4; ++i) { s20[i] = sel(i > dq, NI, s20[i]); s31[i] = sel(i > dq, NI, s31[i]); }
                s30 = ninf;
            }
            r.s[0][0] = s00; r.s[1][0] = s10; r.s[2][0] = s20; r.s[3][0] = s30; r.s[0][1] = s01; r.s[1][1] = s11; r.s[2][1] = s21; r.s[3][1] = s31;
        }
    }
    float a0, a1;
    if (early.on) dma16x4(c.sws, c.vl16, early.soff, early.ldso);
    a_exp_pack<false>(r, a0, a1, early);
    const float lo = FIRST ? SUM_LO : 0.f;
    if (__builtin_expect(__any(!(a0 <= SUM_HI) || !(a1 <= SUM_HI) || a0 < lo || a1 < lo), 0)) {
        float x0 = r.s[0][0][0], x1 = r.s[0][1][0];
#pragma unroll
        for (int kt = 0; kt < 4; ++kt)
#pragma unroll
            for (int i = 0; i < 4; ++i) { x0 = __builtin_fmaxf(x0, r.s[kt][0][i]); x1 = __builtin_fmaxf(x1, r.s[kt][1][i]); }
        x0 = xmax4(x0); x1 = xmax4(x1);
        if (!FIRST) { x0 = __builtin_fmaxf(x0, 0.f); x1 = __builtin_fmaxf(x1, 0.f);
            const float f0 = __builtin_amdgcn_exp2f(-x0), f1 = __builtin_amdgcn_exp2f(-x1);
#pragma unroll
            for (int d = 0; d < 8; ++d) { r.o[d][0] *= f0; r.o[d][1] *= f1; }
            z.l0 *= f0; z.l1 *= f1; }
        z.m0 += x0; z.m1 += x1;
        r.negm[0] = (f32x4){-z.m0, -z.m0, -z.m0, -z.m0}; r.negm[1] = (f32x4){-z.m1, -z.m1, -z.m1, -z.m1};
#pragma unroll
        for (int kt = 0; kt < 4; ++kt) { r.s[kt][0] -= x0; r.s[kt][1] -= x1; }
        a_exp_pack<false>(r, a0, a1, early);
    }
    z.l0 += a0; z.l1 += a1;
    if (SWAP) {
        z.ll0 = z.l0; z.ll1 = z.l1; z.l0 = 0.f; z.l1 = 0.f; z.m0 = 0.f; z.m1 = 0.f;
        r.negm[0] = (f32x4){0.f, 0.f, 0.f, 0.f}; r.negm[1] = (f32x4){0.f, 0.f, 0.f, 0.f};
    }
    SBAR();
    PT(0);
    if (FIN) { if (early.on) WAITV(12); else WAITV(8); } else { if (early.on) WAITV(4); else WAITV(0); }
    PT(1);
    BARRIER(); SBAR();
    PT(2);
    __builtin_amdgcn_s_setprio(PRIOM);
    a_run<0, 2>(r, vp, kp, late, std::make_integer_sequence<int, 16>{});
    if (s < 16) a_run<16, 2>(r, vp, kp, late, std::make_integer_sequence<int, 16>{});
    __builtin_amdgcn_s_setprio(PRIOV);
    SBAR();
    PT(3);
    if (late.on) WAITV(4); else WAITV(0);
    PT(4);
    BARRIER(); SBAR();
    PT(5);
}

struct Lean { unsigned e_soff, e_lds, l_soff, l_lds, voff, koff; };
template <bool LATE> DI void a_slot_lean(const AttnCtx& c, AttnRegs& r, AttnSm& z, const Lean& st PARG) {
    const lcptr vp = c.vl + st.voff, kp = c.kl + st.koff;
    Piece early, late;
    early.sws = c.sws; early.vl16 = c.vl16; early.soff = st.e_soff; early.ldso = st.e_lds; early.on = true;
    late.sws = c.sws; late.vl16 = c.vl16; late.soff = st.l_soff; late.ldso = st.l_lds; late.on = true;
    a_pre<0>(r, vp, kp, std::make_integer_sequence<int, PD>{});
    SBAR();
    float a0, a1;
    a_exp_pack<true>(r, a0, a1, early);
    if (__builtin_expect(__any(!(a0 <= SUM_HI) || !(a1 <= SUM_HI)), 0)) {
        float x0 = 0.f, x1 = 0.f;
#pragma unroll
        for (int kt = 0; kt < 4; ++kt)
#pragma unroll
            for (int i = 0; i < 4; ++i) { x0 = __builtin_fmaxf(x0, r.s[kt][0][i]); x1 = __builtin_fmaxf(x1, r.s[kt][1][i]); }
        x0 = xmax4(x0); x1 = xmax4(x1);
        const float f0 = __builtin_amdgcn_exp2f(-x0), f1 = __builtin_amdgcn_exp2f(-x1);
#pragma unroll
        for (int d = 0; d < 8; ++d) { r.o[d][0] *= f0; r.o[d][1] *= f1; }
        z.l0 *= f0; z.l1 *= f1;
        z.m0 += x0; z.m1 += x1;
        r.negm[0] = (f32x4){-z.m0, -z.m0, -z.m0, -z.m0}; r.negm[1] = (f32x4){-z.m1, -z.m1, -z.m1, -z.m1};
#pragma unroll
        for (int kt = 0; kt < 4; ++kt) { r.s[kt][0] -= x0; r.s[kt][1] -= x1; }
        a_exp_pack<false>(r, a0, a1, early);
    }
    z.l0 += a0; z.l1 += a1;
    SBAR();
    PT(0);
    WAITV(4);
    PT(1);
    BARRIER(); SBAR();
    PT(2);
    __builtin_amdgcn_s_setprio(PRIOM);
    a_run<0, LATE ? 1 : 0>(r, vp, kp, late, std::make_integer_sequence<int, 32>{});
    __builtin_amdgcn_s_setprio(PRIOV);
    SBAR();
    PT(3);
    if constexpr (LATE) WAITV(4); else WAITV(0);
    PT(4);
    BARRIER(); SBAR();
    PT(5);
}

#define P4BAR 1
#define P4NS 4
struct P4Ctx { rsrc_t sws; unsigned vl16, ag, as, wg, wsb; int w, jj; lptr red; };
struct P4Regs { f32x4 acc[5][4]; u32x4 xa[P4NS][4], wa[P4NS][5]; u64 seen; };
template <int S> DI void p4_load(const P4Ctx& c, P4Regs& r, int t) {
    const bool isg = t < 8;
    const int u = isg ? t : t - 8;
    const bool ok = isg ? (t < 6 || (t == 6 && c.w < 6)) : t < 24;
    const unsigned ao = ok ? (isg ? c.ag : c.as) + u * 8192 : 0xF0000000u, wo_ = ok ? (isg ? c.wg : c.wsb) + u * 8 * 40960 : 0xF0000000u, st = isg ? 54 * 1024 : 128 * 1024;
#pragma unroll
    for (int m = 0; m < 4; ++m) r.xa[S][m] = ld16_sc1(c.sws, c.vl16, ao + m * st);
#pragma unroll
    for (int n = 0; n < 5; ++n) r.wa[S][n] = ld16_sc1(c.sws, c.vl16, wo_ + n * 1024);
}
DI f32x4 p4_sum(const P4Ctx& c, int t) {
    lcptr p = c.red + t * 1024;
    f32x4 sum = (*(const LAS f32x4*)(p) + *(const LAS f32x4*)(p + 20480)) + (*(const LAS f32x4*)(p + 40960) + *(const LAS f32x4*)(p + 61440));
    p += 81920;
    return sum + ((*(const LAS f32x4*)(p) + *(const LAS f32x4*)(p + 20480)) + (*(const LAS f32x4*)(p + 40960) + *(const LAS f32x4*)(p + 61440)));
}
DI void p4_put(const P4Ctx& c, P4Regs& r) {
    asm volatile("s_nop 7\n\ts_nop 7\n\ts_nop 7" ::: "memory");
#pragma unroll
    for (int n = 0; n < 5; ++n)
#pragma unroll
        for (int m = 0; m < 4; ++m) *(LAS f32x4*)(c.red + c.w * 20480 + (n * 4 + m) * 1024) = r.acc[n][m];
}
template <int S> DI void p4_mm(P4Regs& r) {
    SBAR();
#pragma unroll
    for (int n = 0; n < 5; ++n)
#pragma unroll
        for (int m = 0; m < 4; ++m) asm volatile("v_mfma_f32_16x16x32_bf16 %0, %1, %2, %0" : "+v"(r.acc[n][m]) : "v"(r.wa[S][n]), "v"(r.xa[S][m]));
    SBAR();
}
DI void p4_zero(P4Regs& r) {
#pragma unroll
    for (int n = 0; n < 5; ++n)
#pragma unroll
        for (int m = 0; m < 4; ++m) r.acc[n][m] = (f32x4){0.f, 0.f, 0.f, 0.f};
    asm volatile("s_nop 1" ::: "memory");
}
DI void p4_main(const P4Ctx& c, const Sync& y, P4Regs& r) {
    p4_zero(r);
#pragma nounroll
    for (int q = 0; q < 6; ++q) {
        p4_load<3>(c, r, 4 * q + 3); p4_mm<0>(r);
        if (q == 1) check_flags(y.flags, FL_CTX + c.jj * 32 + (lane_id() & 31), r.seen, y.token);
        p4_load<0>(c, r, 4 * q + 4); p4_mm<1>(r);
        p4_load<1>(c, r, 4 * q + 5); p4_mm<2>(r);
        if (q == 0) r.seen = poll_flag(y.flags, FL_CTX + c.jj * 32 + (lane_id() & 31));
        p4_load<2>(c, r, 4 * q + 6); p4_mm<3>(r);
        if (q == 1) { p4_put(c, r); p4_zero(r); }
        if (q != 2) BARRIER();
        if (q == 2) {
            asm volatile("s_waitcnt lgkmcnt(0)" ::: "memory"); BARRIER();
#pragma unroll
            for (int i = 0; i < 3; ++i) {
                if (i < 2 || c.w < 4) {
                    const int t = c.w < 4 ? 3 * c.w + i : 12 + 2 * (c.w - 4) + i;
                    f32x4 v = p4_sum(c, t);
#pragma unroll
                    for (int e = 0; e < 4; ++e) v[e] = v[e] * __builtin_amdgcn_rcpf(1.f + __builtin_amdgcn_exp2f(-1.4426950408889634f * v[e]));
                    *(LAS f32x4*)(c.red + t * 1024) = v;
                }
                SBAR();
            }
        }
    }
}

__global__ __launch_bounds__(512, 2) void fused_kernel(const bf16_t* __restrict__ x, const bf16_t* __restrict__ ckv, const bf16_t* __restrict__ wq, const bf16_t* __restrict__ wuk,
                                                       const bf16_t* __restrict__ wuv, const bf16_t* __restrict__ wo, const bf16_t* __restrict__ w1, const bf16_t* __restrict__ b1,
                                                       const bf16_t* __restrict__ w2, bf16_t* __restrict__ out, char* __restrict__ ws) {
    __shared__ __attribute__((aligned(1024))) char smem[163840];
    const lptr lds = (lptr)smem;
    const int b = blockIdx.x, xcd = b & 7, bi = b >> 3, h = xcd * 4 + (bi >> 3), j = bi & 7;
    const int w = __builtin_amdgcn_readfirstlane(threadIdx.x >> 6);
    const rsrc_t sws = mk_rsrc(ws, WS_BYTES);
    STAMP(0);
    Sync y;
    y.flags = (gu64*)(ws + OFF_FLAGS); y.b = b; y.h = h; y.j = j; y.w = w;
    u64 epoch;
    asm volatile("s_load_dwordx2 %0, %1, 0x20" : "=s"(epoch) : "s"(ws) : "memory");
    u32x4 kv[16];
    P1Ctx c1;
    P2Ctx c2;
    const bool isv = (w & 3) >= 2;
    unsigned sink = 0;
    {
        const int l = lane_id(), g = l >> 4, q = (l & 15) >> 2;
        {
            const int nb = w >> 1;
            c1.w = w; c1.sws = sws; c1.wo = wo; c1.w2 = w2; c1.wq = wq; c1.w1 = w1; c1.lds = lds;
            c1.sx = mk_rsrc(ckv, 2048 * 512 * 2);
            c1.sw = mk_rsrc(nb < 2 ? wuk : wuv, 512 * 4096 * 2);
#pragma unroll
            for (int par = 0; par < 2; ++par) {
                c1.vx[par] = (l >> 3) * 1024 + (((l & 7) ^ (4 * par + (l >> 4))) * 16);
                c1.vw[par] = (l >> 3) * 8192 + (((l & 7) ^ ((((l >> 4) & 1) | (par << 1)) << 1)) * 16);
            }
            c1.sxo = (256 * j + 32 * w) * 1024;
            c1.swo = (32 * (w & 1)) * 8192 + (h * 128 + 64 * (nb & 1)) * 2;
            const int wn = w & 3, wm = w >> 2, f = (l & 15) >> 1, s = ((q >> 1) & 1) | ((g & 1) << 1);
            c1.xb[0] = lds + wm * 16384 + (l & 15) * 128 + ((g ^ f) * 16);
            c1.xb[1] = lds + wm * 16384 + (l & 15) * 128 + (((g ^ f) ^ 4) * 16);
#pragma unroll
            for (int nt = 0; nt < 4; ++nt) c1.wb[nt] = lds + 65536 + wn * 8192 + (8 * g + q) * 128 + ((nt ^ s) * 32) + (l & 3) * 8;
        }
        p1_dma(c1, 0, 0);
        u32x4 xo0, xo1;
        {
            const rsrc_t sxi = mk_rsrc(x, 2048 * 640 * 2);
            const unsigned vx = (l & 7) * 1280 + (l >> 3) * 16, so0 = b * 8 * 1280 + w * 128, so1 = b * 8 * 1280 + (8 + (w & 1)) * 128;
            asm volatile("s_nop 4\n\tbuffer_load_dwordx4 %0, %2, %3, %4 offen\n\tbuffer_load_dwordx4 %1, %2, %3, %5 offen" : "=&v"(xo0), "=&v"(xo1) : "v"(vx), "s"(sxi), "s"(so0), "s"(so1) : "memory");
        }
        p1_dma(c1, 1, 1);
        {
            const char* pa = (const char*)ckv + (size_t)(256 * j + 64 * (h & 3) + 8 * w + (l >> 3)) * 1024 + (l & 7) * 128;
            const char* pw = (const char*)((w & 1) ? wuv : wuk) + (size_t)(64 * j + 16 * (w >> 1) + ((l & 31) >> 1)) * 8192 + h * 256 + (l & 1) * 128;
            asm volatile("global_load_dword %0, %1, off\n\tglobal_load_dword %0, %2, off" : "+v"(sink) : "v"(pa), "v"(pw) : "memory");
        }
        asm volatile("s_waitcnt lgkmcnt(0)" : "+s"(epoch) :: "memory");
        y.token = epoch + 1;
        STAMP(12);
        asm volatile("s_waitcnt vmcnt(10)" : "+v"(xo0), "+v"(xo1) :: "memory");
        STAMP(13);
        float v0[8], v1[8];
#pragma unroll
        for (int e = 0; e < 4; ++e) { v0[2 * e] = bflo(xo0[e]); v0[2 * e + 1] = bfhi(xo0[e]); v1[2 * e] = bflo(xo1[e]); v1[2 * e + 1] = bfhi(xo1[e]); }
        auto red8 = [](float t) { t += __uint_as_float(__builtin_amdgcn_update_dpp(0u, __float_as_uint(t), 0x128, 0xf, 0xf, false)); return xsum4(t); };
        {
            const float m0 = red8(((v0[0] + v0[1]) + (v0[2] + v0[3])) + ((v0[4] + v0[5]) + (v0[6] + v0[7]))) * (1.f / 64.f);
            const float m1 = red8(((v1[0] + v1[1]) + (v1[2] + v1[3])) + ((v1[4] + v1[5]) + (v1[6] + v1[7]))) * (1.f / 64.f);
            float q0 = 0.f, q1 = 0.f;
#pragma unroll
            for (int e = 0; e < 8; ++e) { const float d0 = v0[e] - m0, d1 = v1[e] - m1; q0 += d0 * d0; q1 += d1 * d1; }
            q0 = red8(q0); q1 = red8(q1);
            if (l < 8) { *(LAS f32x2*)(lds + 162816 + l * 80 + w * 8) = (f32x2){m0, q0}; if (w < 2) *(LAS f32x2*)(lds + 162816 + l * 80 + 64 + w * 8) = (f32x2){m1, q1}; }
        }
        asm volatile("s_waitcnt lgkmcnt(0)" ::: "memory"); BARRIER();
        STAMP(14);
        {
            f32x4 t[5];
#pragma unroll
            for (int i = 0; i < 5; ++i) t[i] = *(const LAS f32x4*)(lds + 162816 + (l & 7) * 80 + i * 16);
            float mean = 0.f;
#pragma unroll
            for (int i = 0; i < 5; ++i) mean += t[i][0] + t[i][2];
            mean *= 0.1f;
            float m2 = 0.f;
#pragma unroll
            for (int i = 0; i < 5; ++i) { const float d0 = t[i][0] - mean, d1 = t[i][2] - mean; m2 += (t[i][1] + t[i][3]) + 64.f * (d0 * d0 + d1 * d1); }
            const float rstd = 1.f / __builtin_sqrtf(m2 * (1.f / 640.f) + 1e-5f);
            const unsigned vo = (l >> 3) * 256 + (l & 7) * 16, xo = OFF_XN + (b >> 1) * 20480 + (b & 1) * 128;
            u32x4 o0, o1;
#pragma unroll
            for (int e = 0; e < 4; ++e) { o0[e] = cvtpk((v0[2 * e] - mean) * rstd, (v0[2 * e + 1] - mean) * rstd); o1[e] = cvtpk((v1[2 * e] - mean) * rstd, (v1[2 * e + 1] - mean) * rstd); }
            st16_sc1(sws, o0, vo, xo + w * 2048);
            if (w < 2) st16_sc1(sws, o1, vo, xo + (8 + w) * 2048);
        }
        {
            c2.w = w; c2.sws = sws; c2.swq = mk_rsrc(wq, 640 * 4096 * 2); c2.sw1 = mk_rsrc(w1, 640 * 1728 * 2);
            c2.vl16 = l * 16;
            const int swz = ((l >> 4) & 1) | ((w & 1) << 1);
            c2.vwq = (l >> 3) * 8192 + (((l & 7) ^ (swz << 1)) * 16);
            c2.vw1 = (l >> 3) * 3456 + (((l & 7) ^ (swz << 1)) * 16);
            { const int mtl = 2 * w, mtg = mtl < 8 ? 8 * j + mtl : 8 * (15 - j) + (mtl - 8); c2.sxo = OFF_XN + mtg * 20480; }
            c2.swqo = (8 * w) * 8192 + h * 256;
            c2.sw1o = (8 * w) * 3456 + h * 128;
            { const int wn = w & 3, wm = w >> 2;
              c2.skv = isv ? OFF_V + (unsigned)(h * 32 + 4 * j + 2 * wm) * 16384 + (wn - 2) * 4096 : OFF_K + (unsigned)((h * 128 + 16 * j + 8 * wm) * 4 + 2 * wn) * 1024; }
        }
    }
    STAMP(1);
    if (isv) p1_main<true>(c1, c2, y, sink, kv, std::make_integer_sequence<int, 8>{}); else p1_main<false>(c1, c2, y, sink, kv, std::make_integer_sequence<int, 8>{});
    STAMP(2);
    p2_kvst<0>(c2, kv, isv); p2_kvst<1>(c2, kv, isv); p2_kvst<2>(c2, kv, isv); p2_kvst<3>(c2, kv, isv);
    const bool hasf = h < 27;
    AttnCtx c3;
    AttnRegs r;
    {
        const int l = lane_id(), g = l >> 4, q = (l & 15) >> 2;
        const int wn = w & 1, wm = w >> 1;
        const int s = ((q >> 1) & 1) | ((g & 1) << 1);
        c2.xb = lds + wm * 8192 + l * 16;
#pragma unroll
        for (int nt = 0; nt < 4; ++nt) c2.wq[nt] = lds + 65536 + wn * 8192 + (8 * g + q) * 128 + ((nt ^ s) * 32) + (l & 3) * 8;
#pragma unroll
        for (int ft = 0; ft < 2; ++ft) c2.wf[ft] = lds + 65536 + 16384 + (8 * g + q) * 128 + (((2 * wn + ft) ^ s) * 32) + (l & 3) * 8;
        f32x4 acc[6][4];
#pragma unroll
        for (int a = 0; a < 6; ++a)
#pragma unroll
            for (int m = 0; m < 4; ++m) acc[a][m] = (f32x4){0.f, 0.f, 0.f, 0.f};
        STAMP(3);
        if (hasf) p2_main<true>(c2, y, acc, kv, isv, std::make_integer_sequence<int, 10>{}); else p2_main<false>(c2, y, acc, kv, isv, std::make_integer_sequence<int, 10>{});
        STAMP(4);
        constexpr float C2 = 0.08838834764831845f * 1.4426950408889634f;
#pragma unroll
        for (int mt = 0; mt < 4; ++mt)
#pragma unroll
            for (int a = 0; a < 2; ++a)
                *(LAS u32x4*)(lds + 98304 + ((4 * wm + mt) * 4 + 2 * wn + a) * 1024 + l * 16) = pack8(acc[2 * a][mt] * C2, acc[2 * a + 1][mt] * C2);
        u32x4 actf[4];
        if (hasf) {
            const int f0 = 64 * h + 32 * wn + 4 * g;
            const u32x2 bA = *(const u32x2*)(b1 + f0), bB = *(const u32x2*)(b1 + f0 + 16);
            const f32x4 biasA = {bflo(bA[0]), bfhi(bA[0]), bflo(bA[1]), bfhi(bA[1])}, biasB = {bflo(bB[0]), bfhi(bB[0]), bflo(bB[1]), bfhi(bB[1])};
#pragma unroll
            for (int mt = 0; mt < 4; ++mt) {
                f32x4 hA = acc[4][mt] + biasA, hB = acc[5][mt] + biasB;
#pragma unroll
                for (int i = 0; i < 4; ++i) { hA[i] = mish(hA[i]); hB[i] = mish(hB[i]); }
                actf[mt] = pack8(hA, hB);
            }
        }
        asm volatile("s_waitcnt vmcnt(0) lgkmcnt(0)" ::: "memory"); BARRIER();
        const int grp = w >> 2, qg = w & 3;
        c3.sws = sws; c3.vl16 = l * 16; c3.kbase = OFF_K + (unsigned)h * 524288; c3.vbase = OFF_V + (unsigned)h * 524288;
        c3.ctxl = OFF_CTX + ((8 * j + 2 * qg) * 128 + 4 * h) * 1024;
        c3.nL = 2 * j + 2; c3.grp = grp; c3.qg = qg; c3.j = j; c3.rowrel = (l & 15) - 4 * g; c3.kl = lds + l * 16; c3.vl = lds + 65536 + l * 16;
        c3.ql = lds + 131072 + qg * 8192 + l * 16;
        if (grp == 0) { a_dmaK(c3, 0); a_dmaV(c3, 0); } else { a_dmaK(c3, 1); }
        if (hasf) {
#pragma unroll
            for (int mt = 0; mt < 4; ++mt) { const int mtl = 4 * wm + mt, mtg = mtl < 8 ? 8 * j + mtl : 8 * (15 - j) + (mtl - 8);
                st16_sc1(sws, actf[mt], l * 16, OFF_ACT + (mtg * 54 + 2 * h + wn) * 1024); }
        }
#pragma unroll
        for (int qt = 0; qt < 2; ++qt)
#pragma unroll
            for (int u = 0; u < 4; ++u) r.q[qt][u] = ld128(lds + 98304 + ((8 * grp + 2 * qg + qt) * 4 + u) * 1024 + l * 16);
    }
    STAMP(5);
    {
        const int l = lane_id();
        const int grp = c3.grp, qg = c3.qg;
        const unsigned vl16 = c3.vl16;
#pragma unroll
        for (int d = 0; d < 8; ++d) { r.o[d][0] = (f32x4){0.f, 0.f, 0.f, 0.f}; r.o[d][1] = (f32x4){0.f, 0.f, 0.f, 0.f}; }
        r.negm[0] = (f32x4){0.f, 0.f, 0.f, 0.f}; r.negm[1] = (f32x4){0.f, 0.f, 0.f, 0.f};
        AttnSm z; z.m0 = 0.f; z.m1 = 0.f; z.l0 = 0.f; z.l1 = 0.f; z.ll0 = 0.f; z.ll1 = 0.f;
        if (hasf) asm volatile("s_waitcnt vmcnt(4) lgkmcnt(0)" ::: "memory"); else asm volatile("s_waitcnt vmcnt(0) lgkmcnt(0)" ::: "memory");
        BARRIER();
        STAMP(6);
        a_pre<16>(r, c3.vl, c3.kl, std::make_integer_sequence<int, PD>{});
        if (grp == 1) BARRIER();
        { Piece none; none.on = false; none.sws = sws; none.vl16 = 0; none.soff = 0; none.ldso = 0; a_run<16, 0>(r, c3.vl, c3.kl, none, std::make_integer_sequence<int, 16>{}); }
        BARRIER();
        c3.swap_s = grp == 0 ? c3.nL - 1 : 99; c3.first_s = 0;
        asm volatile("" : "+s"(c3.swap_s), "+s"(c3.first_s));
        {
            const int nL = c3.nL;
            auto lean_init = [&](int s) {
                Lean st;
                const bool eK = grp == 1 && s + 2 < nL;
                const int t0 = s < nL ? s : 2 * s - nL + grp, t1 = s + 1 < nL ? s + 1 : 2 * s + 2 - nL + grp, t2 = s + 2 < nL ? s + 2 : 2 * s + 4 - nL + grp;
                const int te = eK ? t2 : t1;
                st.e_soff = (eK ? c3.kbase : c3.vbase) + te * 16384 + qg * 4096; st.e_lds = (eK ? 0 : 65536) + (te & 3) * 16384 + qg * 4096;
                st.l_soff = c3.kbase + t2 * 16384 + qg * 4096; st.l_lds = (t2 & 3) * 16384 + qg * 4096;
                st.voff = (t0 & 3) * 16384; st.koff = (t1 & 3) * 16384;
                return st;
            };
            int s = 0, phase = 0;
#pragma nounroll
            for (;;) {
                const int gend = phase == 0 ? 1 : (phase == 1 ? (grp == 0 ? nL + 1 : nL - 1) : 17);
#pragma nounroll
                while (s < gend) { a_slotfull(c3, y, r, z, s PPASSG); ++s; }
                if (phase == 0) { if (w == 0 && l == 0) set_flag(y.flags, FL_ACT + j * 32 + h, y.token); }
                if (phase == 2 || s > 16) break;
                if (phase == 1 && s <= 14) {
                    Lean st = lean_init(s);
#pragma nounroll
                    do { a_slot_lean<true>(c3, r, z, st PPASS);
                         st.e_soff += 32768; st.e_lds ^= 32768; st.l_soff += 32768; st.l_lds ^= 32768; st.voff ^= 32768; st.koff ^= 32768; ++s; } while (s <= 14);
                }
                const int send = phase == 0 ? nL - 2 : 16;
                if (s < send) {
                    Lean st = lean_init(s);
#pragma nounroll
                    do { a_slot_lean<false>(c3, r, z, st PPASS);
                         st.e_soff += 16384; st.e_lds = (st.e_lds & ~0xC000u) | ((st.e_lds + 16384) & 0xC000u); st.voff = (st.voff + 16384) & 0xC000u; st.koff = (st.koff + 16384) & 0xC000u; ++s; } while (s < send);
                }
                ++phase;
            }
        }
        float m0 = z.m0, m1 = z.m1, l0 = z.l0, l1 = z.l1;
        if (grp == 0) BARRIER();
        STAMP(7);
        u64 seenA[4];
#pragma unroll
        for (int i = 0; i < 4; ++i) seenA[i] = poll_flag(y.flags, FL_ACT + 64 * i + l);
        l0 = xsum4(l0); l1 = xsum4(l1);
        {
            const lptr xo = lds + (qg * 2 + grp) * 9216;
#pragma unroll
            for (int d = 0; d < 4; ++d)
#pragma unroll
                for (int qt = 0; qt < 2; ++qt) *(LAS f32x4*)(xo + (d * 2 + qt) * 1024 + l * 16) = sel(grp == 0, r.o[4 + d][qt], r.o[d][qt]);
            *(LAS f32x4*)(xo + 8192 + l * 16) = (f32x4){m0, m1, l0, l1};
            asm volatile("s_waitcnt lgkmcnt(0)" ::: "memory"); BARRIER();
#pragma unroll
            for (int i = 0; i < 4; ++i) check_flags(y.flags, FL_ACT + 64 * i + l, seenA[i], y.token);
            const lcptr xi = lds + (qg * 2 + (grp ^ 1)) * 9216;
            const f32x4 st = *(const LAS f32x4*)(xi + 8192 + l * 16);
            const float M0 = __builtin_fmaxf(m0, st[0]), M1 = __builtin_fmaxf(m1, st[1]);
            const float wa0 = __builtin_amdgcn_exp2f(m0 - M0), wb0 = __builtin_amdgcn_exp2f(st[0] - M0), wa1 = __builtin_amdgcn_exp2f(m1 - M1), wb1 = __builtin_amdgcn_exp2f(st[1] - M1);
            const float i0 = __builtin_amdgcn_rcpf(wa0 * l0 + wb0 * st[2]), i1 = __builtin_amdgcn_rcpf(wa1 * l1 + wb1 * st[3]);
            const float ca0 = wa0 * i0, cb0 = wb0 * i0, ca1 = wa1 * i1, cb1 = wb1 * i1;
#pragma unroll
            for (int up = 0; up < 2; ++up)
#pragma unroll
                for (int qt = 0; qt < 2; ++qt) {
                    const f32x4 pa = *(const LAS f32x4*)(xi + ((2 * up) * 2 + qt) * 1024 + l * 16), pb = *(const LAS f32x4*)(xi + ((2 * up + 1) * 2 + qt) * 1024 + l * 16);
                    const f32x4 oa = sel(grp == 0, r.o[2 * up][qt], r.o[4 + 2 * up][qt]), ob = sel(grp == 0, r.o[2 * up + 1][qt], r.o[4 + 2 * up + 1][qt]);
                    const float ca = qt ? ca1 : ca0, cb = qt ? cb1 : cb0;
                    st16_sc1(sws, pack8(oa * ca + pa * cb, ob * ca + pb * cb), vl16, OFF_CTX + ((8 * (15 - j) + 2 * qg + qt) * 128 + 4 * h + 2 * grp + up) * 1024);
                }
        }
    }
    STAMP(8);
    {
        const int rb = (xcd >> 1) * 8 + (bi >> 2), cb = (xcd & 1) * 4 + (bi & 3);
        const int xq = rb >> 1;
        P4Ctx c4;
        c4.sws = sws; c4.vl16 = lane_id() * 16; c4.w = w; c4.jj = xq < 8 ? xq : 15 - xq; c4.red = lds + c4.vl16;
        c4.ag = OFF_ACT + (4 * rb * 54 + w) * 1024; c4.as = OFF_CTX + (4 * rb * 128 + w) * 1024;
        c4.wg = OFF_WP + ((128 + w) * 40 + cb * 5) * 1024; c4.wsb = OFF_WP + (w * 40 + cb * 5) * 1024;
        P4Regs r4;
        r4.seen = 0;
        p4_load<0>(c4, r4, 0); p4_load<1>(c4, r4, 1); p4_load<2>(c4, r4, 2);
        asm volatile("s_waitcnt vmcnt(27) lgkmcnt(0)" ::: "memory");
        BARRIER();
        if (w == 0 && lane_id() == 0) set_flag(y.flags, FL_CTX + j * 32 + h, y.token);
        STAMP(9);
        p4_main(c4, y, r4);
        STAMP(10);
        const int l = lane_id(), row0 = 64 * rb + (l & 15), col0 = 80 * cb + 4 * (l >> 4);
        f32x4 gsv[3]; u32x2 xvv[3];
#pragma unroll
        for (int i = 0; i < 3; ++i) { const int t = w < 4 ? 3 * w + i : 12 + 2 * (w - 4) + (i & 1);
            gsv[i] = *(const LAS f32x4*)(c4.red + t * 1024); xvv[i] = *(const u32x2*)(x + (row0 + (t & 3) * 16) * 640 + col0 + (t >> 2) * 16); }
        asm volatile("s_waitcnt lgkmcnt(0)" ::: "memory"); BARRIER();
        p4_put(c4, r4);
        asm volatile("s_waitcnt lgkmcnt(0)" ::: "memory"); BARRIER();
#pragma unroll
        for (int i = 0; i < 3; ++i) {
            if (i < 2 || w < 4) {
                const int t = w < 4 ? 3 * w + i : 12 + 2 * (w - 4) + i;
                const f32x4 v = p4_sum(c4, t), gs = gsv[i];
                const u32x2 xv = xvv[i];
                const f32x4 o = {bflo(xv[0]) + v[0] * gs[0], bfhi(xv[0]) + v[1] * gs[1], bflo(xv[1]) + v[2] * gs[2], bfhi(xv[1]) + v[3] * gs[3]};
                *(u32x2*)(out + (row0 + (t & 3) * 16) * 640 + col0 + (t >> 2) * 16) = (u32x2){cvtpk(o[0], o[1]), cvtpk(o[2], o[3])};
            }
            SBAR();
        }
        if (b == 0 && w == 0 && l == 0) __hip_atomic_store(y.flags + 4, y.token, __ATOMIC_RELAXED, __HIP_MEMORY_SCOPE_AGENT);
    }
    STAMP(11);
}

extern "C" void kernel_launch(void* const* d_in, const int* in_sizes, int n_in, void* d_out, int out_size, void* d_ws, size_t ws_size, hipStream_t stream) {
    (void)in_sizes; (void)n_in; (void)out_size; (void)ws_size;
    fused_kernel<<<256, 512, 0, stream>>>((const bf16_t*)d_in[0], (const bf16_t*)d_in[1], (const bf16_t*)d_in[2], (const bf16_t*)d_in[3], (const bf16_t*)d_in[4],
                                          (const bf16_t*)d_in[5], (const bf16_t*)d_in[7], (const bf16_t*)d_in[8], (const bf16_t*)d_in[9], (bf16_t*)d_out, (char*)d_ws);
}
```

```cpp
#include <hip/hip_runtime.h>
#include <stdint.h>
#include <utility>

typedef unsigned short bf16_t;
typedef short bf16x8 __attribute__((ext_vector_type(8)));
typedef short s16x4 __attribute__((ext_vector_type(4)));
typedef short v4i16_t __attribute__((ext_vector_type(4)));
typedef float f32x4 __attribute__((ext_vector_type(4)));
typedef float f32x2 __attribute__((ext_vector_type(2)));
typedef unsigned u32x4 __attribute__((ext_vector_type(4)));
typedef unsigned u32x2 __attribute__((ext_vector_type(2)));
typedef __bf16 bf16x2_t __attribute__((ext_vector_type(2)));
typedef __amdgpu_buffer_rsrc_t rsrc_t;
typedef unsigned long long u64;
#define LAS __attribute__((address_space(3)))
#define GAS __attribute__((address_space(1)))
typedef LAS char* lptr;
typedef LAS const char* lcptr;
typedef GAS u64 gu64;
#define DI __device__ __forceinline__
#define SBAR() __builtin_amdgcn_sched_barrier(0)
#define PIN(x) asm volatile("" : "+v"(x))
#define MF16(a, b, c) __builtin_amdgcn_mfma_f32_16x16x32_bf16(a, b, c, 0, 0, 0)
#define WAITV(n) asm volatile("s_waitcnt vmcnt(" #n ")" ::: "memory")
#define WAITL(n) asm volatile("s_waitcnt lgkmcnt(" #n ")" ::: "memory")
#define BARRIER() do { asm volatile("" ::: "memory"); __builtin_amdgcn_s_barrier(); asm volatile("" ::: "memory"); } while (0)

#define UPTO 4
#define STAG 1
#define SPREAD 1
#define SPOS 1
#define XST 0
#define STAMPS 0

constexpr unsigned OFF_FLAGS = 0;
constexpr unsigned OFF_XN = 65536;
constexpr unsigned OFF_K = OFF_XN + 2621440;
constexpr unsigned OFF_V = OFF_K + 16777216;
constexpr unsigned OFF_ACT = OFF_V + 16777216 + 8388608;
constexpr unsigned OFF_CTX = OFF_ACT + 7077888;
constexpr unsigned OFF_WP = OFF_CTX + 16777216;
constexpr unsigned OFF_ST = OFF_WP + 182 * 40 * 1024;
constexpr unsigned OFF_XG = OFF_ST + 65536;
constexpr unsigned OFF_XS = OFF_XG + 25165824;
constexpr unsigned WS_BYTES = OFF_XS + 25165824;
constexpr unsigned FL_P0 = 0, FL_KV = 256, FL_ACT = 512, FL_CTX = 768;
constexpr int SC1 = 16;

DI unsigned cvtpk(float lo, float hi) { f32x2 v = {lo, hi}; bf16x2_t b = __builtin_convertvector(v, bf16x2_t); return __builtin_bit_cast(unsigned, b); }
DI u32x4 pack8(f32x4 a, f32x4 b) { u32x4 w = {cvtpk(a[0], a[1]), cvtpk(a[2], a[3]), cvtpk(b[0], b[1]), cvtpk(b[2], b[3])}; return w; }
DI float bflo(unsigned u) { return __uint_as_float(u << 16); }
DI float bfhi(unsigned u) { return __uint_as_float(u & 0xffff0000u); }
DI s16x4 vtr(lcptr p) { return __builtin_bit_cast(s16x4, __builtin_amdgcn_ds_read_tr16_b64_v4i16((LAS v4i16_t*)p)); }
DI bf16x8 cat(s16x4 a, s16x4 b) { return (bf16x8){a[0], a[1], a[2], a[3], b[0], b[1], b[2], b[3]}; }
DI bf16x8 ld128(lcptr p) { return *(const LAS bf16x8*)p; }
DI bf16x8 asbf(u32x4 v) { return __builtin_bit_cast(bf16x8, v); }
DI rsrc_t mk_rsrc(const void* p, unsigned bytes) { return __builtin_amdgcn_make_buffer_rsrc((void*)p, (short)0, (int)bytes, 0x00020000); }
DI void dma16(rsrc_t r, unsigned voff, unsigned soff, unsigned ldsoff) {
    asm volatile("s_mov_b32 m0, %0\n\ts_nop 3\n\tbuffer_load_dwordx4 %1, %2, %3 offen lds" ::"s"(ldsoff), "v"(voff), "s"(r), "s"(soff) : "memory");
}
DI void dma16x4(rsrc_t r, unsigned voff, unsigned soff, unsigned ldsoff) {
    asm volatile("s_mov_b32 m0, %0\n\ts_nop 3\n\tbuffer_load_dwordx4 %1, %2, %3 offen lds\n\tbuffer_load_dwordx4 %1, %2, %3 offen offset:1024 lds\n\t"
                 "buffer_load_dwordx4 %1, %2, %3 offen offset:2048 lds\n\tbuffer_load_dwordx4 %1, %2, %3 offen offset:3072 lds" ::"s"(ldsoff), "v"(voff), "s"(r), "s"(soff) : "memory");
}
DI void st16_sc1(rsrc_t r, u32x4 v, unsigned voff, unsigned soff) { asm volatile("s_nop 4\n\tbuffer_store_dwordx4 %0, %1, %2, %3 offen sc1\n\ts_nop 1" ::"v"(v), "v"(voff), "s"(r), "s"(soff) : "memory"); }
template <int OFF> DI void st16_sc1o(rsrc_t r, u32x4 v, unsigned voff, unsigned soff) { asm volatile("s_nop 4\n\tbuffer_store_dwordx4 %0, %1, %2, %3 offen offset:%4 sc1\n\ts_nop 1" ::"v"(v), "v"(voff), "s"(r), "s"(soff), "i"(OFF) : "memory"); }
DI u32x4 ld16(rsrc_t r, unsigned voff, unsigned soff) { return __builtin_amdgcn_raw_buffer_load_b128(r, voff, soff, 0); }
#define P4AUX SC1
DI u32x4 ld16_sc1(rsrc_t r, unsigned voff, unsigned soff) { return __builtin_amdgcn_raw_buffer_load_b128(r, voff, soff, P4AUX); }
template <class T> DI T sel(bool c, T a, T b) { return c ? a : b; }
DI int lane_id() { int l; asm volatile("v_mbcnt_lo_u32_b32 %0, -1, 0\n\tv_mbcnt_hi_u32_b32 %0, -1, %0" : "=v"(l)); return l; }
DI void set_flag(gu64* flags, unsigned idx, u64 token) { __hip_atomic_store(flags + idx * 8, token, __ATOMIC_RELAXED, __HIP_MEMORY_SCOPE_AGENT); }
DI u64 poll_flag(gu64* flags, unsigned idx) { return __hip_atomic_load(flags + idx * 8, __ATOMIC_RELAXED, __HIP_MEMORY_SCOPE_AGENT); }
DI void spin_flags(gu64* flags, unsigned idx, u64 token) {
    const unsigned long long t0 = __builtin_amdgcn_s_memrealtime();
    for (;;) {
        __builtin_amdgcn_s_sleep(1);
        const u64 v = poll_flag(flags, idx);
        if (__all(v == token)) break;
        if (__builtin_amdgcn_s_memrealtime() - t0 > 2000000ull) break;
    }
}
DI void check_flags(gu64* flags, unsigned idx, u64 seen, u64 token) { if (!__all(seen == token)) spin_flags(flags, idx, token); }
DI void acquire_issue() { asm volatile("buffer_inv sc1" ::: "memory"); }
DI u64 uni64(u64 v) { return ((u64)(unsigned)__builtin_amdgcn_readfirstlane((unsigned)(v >> 32)) << 32) | (unsigned)__builtin_amdgcn_readfirstlane((unsigned)v); }
#define SCLK __builtin_amdgcn_s_memrealtime
#define STAMP(i) do { } while (0)
#define STAMPW(i, wv) do { } while (0)
#define CSTAMP(i) do { } while (0)

#define TS(i) do { } while (0)
#define TSARG
#define TSPASS
struct Sync { gu64* flags; u64 token; int b, h, j, w; };
#define YSTAMP(i) do { } while (0)

struct P2Ctx { rsrc_t sws, swq, sw1; unsigned vl16, vwq, vw1; unsigned sxo, swqo, sw1o, skv; int w; lcptr xb; lcptr wq[4]; lcptr wf[2]; };
DI void p2_dma1(const P2Ctx& c, int st, int ks, int i) {
    if (i < 4) dma16(c.sws, c.vl16, c.sxo + (i >> 1) * 20480 + (i & 1) * 1024 + ks * 2048, st * 32768 + (4 * c.w + i) * 1024);
    else if (i < 6) dma16(c.swq, c.vwq, c.swqo + (i - 4) * 128 + ks * 524288, 65536 + st * 24576 + (i - 4) * 8192 + c.w * 1024);
    else dma16(c.sw1, c.vw1, c.sw1o + ks * (64 * 3456), 65536 + st * 24576 + 16384 + c.w * 1024);
}

struct P1Ctx { rsrc_t sx, sw, sws; unsigned vx[2], vw[2]; unsigned sxo, swo; int w; lcptr xb[2]; lcptr wb[4]; const bf16_t *wo, *w2, *wq, *w1; lcptr lds; };
DI void pack_fetch(const P1Ctx& c, int b) {
    const int l = lane_id(), gw = b * 8 + c.w, pu = gw / 10, pq = gw - pu * 10;
    const rsrc_t sp = mk_rsrc(pu < 128 ? c.wo : c.w2, pu < 128 ? 4096 * 640 * 2 : 1728 * 640 * 2);
    const unsigned so = gw < 1820 ? (unsigned)((pu < 128 ? pu : pu - 128) * 32 * 1280 + pq * 128) : 0xF0000000u;
#pragma unroll
    for (int i = 0; i < 4; ++i) dma16(sp, (l >> 3) * 1280 + (l & 7) * 16, so + i * 8 * 1280, 131072 + c.w * 4096 + i * 1024);
}
DI void pack_store(const P1Ctx& c, int b) {
    const int l = lane_id(), g = l >> 4, q = (l & 15) >> 2, gw = b * 8 + c.w, pu = gw / 10, pq = gw - pu * 10;
    lcptr p = c.lds + 131072 + c.w * 4096 + (4 * g + q) * 128 + (l & 3) * 8;
    bf16x8 fr[4];
#pragma unroll
    for (int i = 0; i < 4; ++i) fr[i] = cat(vtr(p + i * 32), vtr(p + i * 32 + 16 * 128));
    if (gw < 1820) {
#pragma unroll
        for (int i = 0; i < 4; ++i) st16_sc1(c.sws, __builtin_bit_cast(u32x4, fr[i]), l * 16, OFF_WP + (pu * 40 + pq * 4 + i) * 1024);
    }
}
DI void p1_dma1(const P1Ctx& c, int st, int ks, int i) {
    if (i < 4) dma16(c.sx, c.vx[i & 1], c.sxo + i * 8192 + ks * 128, st * 32768 + (4 * c.w + i) * 1024);
    else dma16(c.sw, c.vw[i & 1], c.swo + (i - 4) * 65536 + ks * 524288, 65536 + st * 32768 + (4 * c.w + (i - 4)) * 1024);
}
DI void p1_dma(const P1Ctx& c, int st, int ks) {
#pragma unroll
    for (int i = 0; i < 8; ++i) p1_dma1(c, st, ks, i);
}
template <int ST, int UP, int I> DI void p1_rdw(const P1Ctx& c, bf16x8 (&wf)[4], s16x4 (&wl)[4]) {
    constexpr int nt = I >> 1, rd = I & 1;
    const s16x4 v = vtr(c.wb[nt] + ST * 32768 + UP * 4096 + rd * 512);
    if constexpr (rd == 0) wl[nt] = v; else wf[nt] = cat(wl[nt], v);
}
template <int ST, int G> DI bf16x8 p1_rdx(const P1Ctx& c) { return ld128(c.xb[G >> 3] + ST * 32768 + (G & 7) * 2048); }
template <int ST, int KSC, int K> DI bool p1_is(int ks) { if constexpr (KSC >= 0) return KSC == K; else if constexpr ((K & 1) != ST || K > 5) return false; else return ks == K; }
template <bool ISV, int ST, int KSC, int G> DI void p1_group(const P1Ctx& c, const P2Ctx& c2, const Sync& y, const int ks, u64& seen, unsigned& sink, f32x4 (&acc)[4][8], bf16x8 (&wA)[4], bf16x8 (&wB)[4], s16x4 (&wl)[4], bf16x8 (&xr)[4]) {
    constexpr int up = G >> 3, mt = G & 7;
    constexpr bool NEXT = KSC != 7;
    if constexpr (G == 13) {
        if (p1_is<ST, KSC, 0>(ks)) asm volatile("s_waitcnt vmcnt(2) lgkmcnt(0)" ::: "memory");
        else if (p1_is<ST, KSC, 1>(ks) || p1_is<ST, KSC, 3>(ks)) asm volatile("s_waitcnt vmcnt(1) lgkmcnt(0)" ::: "memory");
        else if (p1_is<ST, KSC, 5>(ks) || p1_is<ST, KSC, 7>(ks)) asm volatile("s_waitcnt vmcnt(4) lgkmcnt(0)" ::: "memory");
        else asm volatile("s_waitcnt vmcnt(0) lgkmcnt(0)" ::: "memory");
        if constexpr (ST == 0) asm volatile("" :: "v"(sink));
        BARRIER();
        if constexpr (!ISV) {
            if (p1_is<ST, KSC, 0>(ks)) { if (y.w == 0 && lane_id() == 0) set_flag(y.flags, FL_P0 + y.b, y.token); }
            if (p1_is<ST, KSC, 4>(ks)) { if (y.w == 0) { const int l = lane_id() & 31; check_flags(y.flags, FL_P0 + (l < 16 ? 16 * y.j + l : 16 * (15 - y.j) + (l - 16)), seen, y.token); } }
        }
        SBAR();
    }
    const bf16x8 x = xr[G & 3];
#pragma unroll
    for (int nt = 0; nt < 4; ++nt) {
        const bf16x8 wv = up == 0 ? wA[nt] : wB[nt];
        if constexpr (ISV) acc[nt][mt] = MF16(x, wv, acc[nt][mt]); else acc[nt][mt] = MF16(wv, x, acc[nt][mt]);
        if (nt == 0) {
            if constexpr (G + 3 < 16) xr[(G + 3) & 3] = p1_rdx<ST, G + 3>(c);
            else if constexpr (NEXT) xr[(G + 3) & 3] = p1_rdx<ST ^ 1, G + 3 - 16>(c);
        }
        if constexpr (G >= 2 && G < 6) { if (nt == 1) p1_rdw<ST, 1, 2 * (G - 2)>(c, wB, wl); if (nt == 2) p1_rdw<ST, 1, 2 * (G - 2) + 1>(c, wB, wl); }
        if constexpr (G >= 13 && NEXT) {
            if (nt == 1) p1_rdw<ST ^ 1, 0, 3 * (G - 13)>(c, wA, wl);
            if (nt == 2) p1_rdw<ST ^ 1, 0, 3 * (G - 13) + 1>(c, wA, wl);
            if constexpr (G < 15) { if (nt == 3) p1_rdw<ST ^ 1, 0, 3 * (G - 13) + 2>(c, wA, wl); }
        }
        if (nt == SPOS) {
            if constexpr (G >= 13) { constexpr int p = G - 13;
                if constexpr (KSC < 0) p1_dma1(c, ST, ks + 2, p);
                else if constexpr (KSC == 6) p2_dma1(c2, 0, 0, p);
                else p2_dma1(c2, 1, 1, p);
            } else if constexpr (G <= 4) { constexpr int p = G + 3;
                if constexpr (KSC == 7) { if constexpr (p < 7) p2_dma1(c2, 0, 0, p); }
                else { if (!p1_is<ST, KSC, 0>(ks)) p1_dma1(c, ST ^ 1, ks + 1, p); }
            }
        }
        SBAR();
    }
    if constexpr (G == 6) {
        if (p1_is<ST, KSC, 1>(ks)) {
            const int l = lane_id(), L = (c.w & 3) * 64 + l;
            const char* pb = (L >= 160 && L < 240 && y.h < 27) ? (const char*)c.w1 + (size_t)(80 * y.j + L - 160) * 3456 + y.h * 128
                                                               : (const char*)c.wq + (size_t)(80 * y.j + ((L < 160 ? L : 0) >> 1)) * 8192 + y.h * 256 + (L & 1) * 128;
            asm volatile("global_load_dword %0, %1, off" : "+v"(sink) : "v"(pb) : "memory");
        }
        if (p1_is<ST, KSC, 3>(ks)) {
            const int l = lane_id(), gw = y.b * 8 + c.w < 1820 ? y.b * 8 + c.w : 0, pu = gw / 10, pq = gw - pu * 10;
            const char* pc = (const char*)(pu < 128 ? c.wo : c.w2) + (size_t)((pu < 128 ? pu : pu - 128) * 32 + (l & 31)) * 1280 + pq * 128;
            asm volatile("global_load_dword %0, %1, off" : "+v"(sink) : "v"(pc) : "memory");
        }
        if constexpr (!ISV) { if (p1_is<ST, KSC, 4>(ks)) { if (y.w == 0) { const int l = lane_id() & 31; seen = poll_flag(y.flags, FL_P0 + (l < 16 ? 16 * y.j + l : 16 * (15 - y.j) + (l - 16))); } } }
        if (p1_is<ST, KSC, 5>(ks)) { pack_fetch(c, y.b); if constexpr (!ISV) { if (y.w == 0) acquire_issue(); } }
        if constexpr (KSC == 7) pack_store(c, y.b);
        SBAR();
    }
}
template <bool ISV, int ST, int KSC, int... Gs> DI void p1_step(const P1Ctx& c, const P2Ctx& c2, const Sync& y, const int ks, u64& seen, unsigned& sink, f32x4 (&acc)[4][8], bf16x8 (&wA)[4], bf16x8 (&wB)[4], s16x4 (&wl)[4], bf16x8 (&xr)[4], std::integer_sequence<int, Gs...>) {
    (p1_group<ISV, ST, KSC, Gs>(c, c2, y, ks, seen, sink, acc, wA, wB, wl, xr), ...);
}
template <bool ISV> DI void p1_main(const P1Ctx& c, const P2Ctx& c2, const Sync& y, unsigned& sink, u32x4 (&kv)[16]) {
    f32x4 acc[4][8];
#pragma unroll
    for (int a = 0; a < 4; ++a)
#pragma unroll
        for (int b = 0; b < 8; ++b) acc[a][b] = (f32x4){0.f, 0.f, 0.f, 0.f};
    bf16x8 wA[4], wB[4], xr[4]; s16x4 wl[4];
    u64 seen = 0;
    asm volatile("" ::: "memory");
    p1_rdw<0, 0, 0>(c, wA, wl); p1_rdw<0, 0, 1>(c, wA, wl); p1_rdw<0, 0, 2>(c, wA, wl); p1_rdw<0, 0, 3>(c, wA, wl);
    p1_rdw<0, 0, 4>(c, wA, wl); p1_rdw<0, 0, 5>(c, wA, wl); p1_rdw<0, 0, 6>(c, wA, wl); p1_rdw<0, 0, 7>(c, wA, wl);
    xr[0] = p1_rdx<0, 0>(c); xr[1] = p1_rdx<0, 1>(c); xr[2] = p1_rdx<0, 2>(c);
    SBAR();
    constexpr auto seq = std::make_integer_sequence<int, 16>{};
#pragma nounroll
    for (int ks = 0; ks < 6; ks += 2) {
        p1_step<ISV, 0, -1>(c, c2, y, ks, seen, sink, acc, wA, wB, wl, xr, seq);
        p1_step<ISV, 1, -1>(c, c2, y, ks + 1, seen, sink, acc, wA, wB, wl, xr, seq);
    }
    p1_step<ISV, 0, 6>(c, c2, y, 6, seen, sink, acc, wA, wB, wl, xr, seq);
    p1_step<ISV, 1, 7>(c, c2, y, 7, seen, sink, acc, wA, wB, wl, xr, seq);
#pragma unroll
    for (int a = 0; a < 8; ++a)
#pragma unroll
        for (int b = 0; b < 2; ++b) {
            if constexpr (ISV) kv[a * 2 + b] = pack8(acc[(a & 1) * 2 + b][(a >> 1) * 2], acc[(a & 1) * 2 + b][(a >> 1) * 2 + 1]);
            else kv[a * 2 + b] = pack8(acc[2 * b][a], acc[2 * b + 1][a]);
        }
}

template <bool HASF, int ST, int I> DI bf16x8 p2_rdw(const P2Ctx& c) {
    constexpr int NW = HASF ? 6 : 4, up = I / NW, n = I % NW;
    lcptr p = (n < 4 ? c.wq[n & 3] : c.wf[n & 1]) + ST * 24576 + up * 4096;
    return cat(vtr(p), vtr(p + 512));
}
template <int ST, int UP, int MT> DI bf16x8 p2_rdx(const P2Ctx& c) { return ld128(c.xb + ST * 32768 + MT * 2048 + UP * 1024); }
template <int I> DI void p2_kvst(const P2Ctx& c, const u32x4 (&kv)[16], bool isv) {
    st16_sc1(c.sws, kv[I], c.vl16, c.skv + (isv ? (unsigned)(((I >> 3) * 16 + ((I >> 2) & 1) * 8 + (I & 3)) * 1024) : (unsigned)((I >> 1) * 4096 + (I & 1) * 1024)));
}
template <bool HASF, int IV> DI void p2_extras(const P2Ctx& c, const u32x4 (&kv)[16], bool isv) {
    if constexpr (IV < 6) { p2_kvst<4 + 2 * IV>(c, kv, isv); p2_kvst<5 + 2 * IV>(c, kv, isv); }
}
template <bool HASF, int KS, int G> DI void p2_group(const P2Ctx& c, const Sync& y, u64& seen, f32x4 (&acc)[6][4], bf16x8 (&wr)[4], bf16x8 (&x0)[4], bf16x8 (&x1)[4], const u32x4 (&kv)[16], bool isv) {
    constexpr int NW = HASF ? 6 : 4, NG = 2 * NW, ST = KS & 1, up = G / NW, n = G % NW;
    constexpr int NP = HASF ? 7 : 6, BD0 = HASF ? 2 : 0;
    constexpr bool LAST = (KS == 9);
    if constexpr (G == NG - 3) {
        if constexpr (KS == 0 && !SPREAD) asm volatile("s_waitcnt vmcnt(4) lgkmcnt(0)" ::: "memory");
        else if constexpr (KS == 0) asm volatile("s_waitcnt vmcnt(0) lgkmcnt(0)" ::: "memory");
        else if constexpr (KS <= 6) asm volatile("s_waitcnt vmcnt(2) lgkmcnt(0)" ::: "memory");
        else asm volatile("s_waitcnt vmcnt(0) lgkmcnt(0)" ::: "memory");
        BARRIER();
        if constexpr (KS == 7) { if (y.w == 0 && lane_id() == 0) set_flag(y.flags, FL_KV + y.h * 8 + y.j, y.token); }
        if constexpr (KS == 8) { if (y.w == 0) seen = poll_flag(y.flags, FL_KV + y.h * 8 + (lane_id() & 7)); }
        if constexpr (KS == 9) { if (y.w == 0) { check_flags(y.flags, FL_KV + y.h * 8 + (lane_id() & 7), seen, y.token); acquire_issue(); } }
        SBAR();
    }
    const bf16x8 wcur = wr[G & 3];
#pragma unroll
    for (int mt = 0; mt < 4; ++mt) {
        acc[n][mt] = MF16(wcur, up == 0 ? x0[mt] : x1[mt], acc[n][mt]);
        if (mt == 0) {
            if constexpr (G + 3 < NG) wr[(G + 3) & 3] = p2_rdw<HASF, ST, G + 3>(c);
            else if constexpr (!LAST) wr[(G + 3) & 3] = p2_rdw<HASF, ST ^ 1, G + 3 - NG>(c);
        }
        if (mt == 1) {
            if constexpr (G < 4) x1[G] = p2_rdx<ST, 1, G>(c);
            else if constexpr (G >= NG - 3 && !LAST) x0[G - (NG - 3)] = p2_rdx<ST ^ 1, 0, G - (NG - 3)>(c);
        }
        if (mt == 2) { if constexpr (G == NG - 1 && !LAST) x0[3] = p2_rdx<ST ^ 1, 0, 3>(c); }
        if (mt == SPOS) {
            if constexpr (G >= NG - 3) { if constexpr (KS + 2 < 10) p2_dma1(c, ST, KS + 2, G - (NG - 3)); }
            else if constexpr (G + 3 < NP) { if constexpr (KS + 1 < 10) p2_dma1(c, ST ^ 1, KS + 1, G + 3); }
        }
        SBAR();
    }
    if constexpr (G == NP - 3 && KS >= 1) { p2_extras<HASF, KS - 1>(c, kv, isv); SBAR(); }
}
template <bool HASF, int KS, int... Gs> DI void p2_step(const P2Ctx& c, const Sync& y, u64& seen, f32x4 (&acc)[6][4], bf16x8 (&wr)[4], bf16x8 (&x0)[4], bf16x8 (&x1)[4], const u32x4 (&kv)[16], bool isv, std::integer_sequence<int, Gs...>) {
    (p2_group<HASF, KS, Gs>(c, y, seen, acc, wr, x0, x1, kv, isv), ...);
}
template <bool HASF, int... KSs> DI void p2_main(const P2Ctx& c, const Sync& y, f32x4 (&acc)[6][4], const u32x4 (&kv)[16], bool isv, std::integer_sequence<int, KSs...>) {
    bf16x8 wr[4], x0[4], x1[4];
    u64 seen = 0;
    asm volatile("" ::: "memory");
    wr[0] = p2_rdw<HASF, 0, 0>(c); wr[1] = p2_rdw<HASF, 0, 1>(c); wr[2] = p2_rdw<HASF, 0, 2>(c);
    x0[0] = p2_rdx<0, 0, 0>(c); x0[1] = p2_rdx<0, 0, 1>(c); x0[2] = p2_rdx<0, 0, 2>(c); x0[3] = p2_rdx<0, 0, 3>(c);
    SBAR();
    (p2_step<HASF, KSs>(c, y, seen, acc, wr, x0, x1, kv, isv, std::make_integer_sequence<int, (HASF ? 12 : 8)>{}), ...);
}
DI float mish(float h) {
    const float t = __builtin_amdgcn_exp2f(__builtin_fminf(h, 20.f) * 1.4426950408889634f);
    const float n = t * (t + 2.f);
    return h * n * __builtin_amdgcn_rcpf(n + 2.f);
}

constexpr int PD = 8;
#define PRIOM 1
#define PRIOV 0
#define ADDMODE 1
#define ABL_NOEARLY 0
#define APRE_EARLY 1
#define LATE_STEP 7
#define LATE_OFF 2
struct AttnRegs { f32x4 o[8][2]; f32x4 s[4][2]; bf16x8 q[2][4]; u32x4 pk[2][2]; f32x4 negm[2]; bf16x8 op[32]; };
template <int I> DI void a_read(AttnRegs& r, lcptr vp, lcptr kp) {
    if constexpr (I < 16) r.op[I] = ld128(vp + I * 1024);
    else if constexpr (I < 32) r.op[I] = ld128(kp + (((I - 16) & 3) * 4 + ((I - 16) >> 2)) * 1024);
}
struct Piece { rsrc_t sws; unsigned vl16, soff, ldso; bool on; };
template <int P, bool COND> DI void piece(const Piece& d) {
    if ((!COND && !ABL_NOEARLY) || (COND && d.on)) asm volatile("s_mov_b32 m0, %0\n\ts_nop 3\n\tbuffer_load_dwordx4 %1, %2, %3 offen offset:%4 lds" ::"s"(d.ldso), "v"(d.vl16), "s"(d.sws), "s"(d.soff), "i"(P * 1024) : "memory");
}
template <int I, int DMA> DI void a_slot(AttnRegs& r, lcptr vp, lcptr kp, const Piece& late) {
    if constexpr (I < 16) { constexpr int ks = I >> 3, dt = I & 7;
        r.o[dt][0] = MF16(r.op[I], asbf(r.pk[0][ks]), r.o[dt][0]); r.o[dt][1] = MF16(r.op[I], asbf(r.pk[1][ks]), r.o[dt][1]); }
    else { constexpr int u = (I - 16) >> 2, kt = (I - 16) & 3;
        r.s[kt][0] = MF16(r.op[I], r.q[0][u], u == 0 ? r.negm[0] : r.s[kt][0]); r.s[kt][1] = MF16(r.op[I], r.q[1][u], u == 0 ? r.negm[1] : r.s[kt][1]); }
    a_read<I + PD>(r, vp, kp);
    SBAR();
    if constexpr (DMA != 0 && (I % LATE_STEP) == LATE_OFF && I / LATE_STEP < 4) { piece<I / LATE_STEP, DMA == 2>(late); SBAR(); }
}
template <int B, int DMA, int... Is> DI void a_run(AttnRegs& r, lcptr vp, lcptr kp, const Piece& late, std::integer_sequence<int, Is...>) { (a_slot<B + Is, DMA>(r, vp, kp, late), ...); }
template <int B, int... Is> DI void a_pre(AttnRegs& r, lcptr vp, lcptr kp, std::integer_sequence<int, Is...>) { (a_read<B + Is>(r, vp, kp), ...); }

template <int Q> DI void a_exp_quarter(AttnRegs& r, float& a0, float& a1) {
    constexpr int ks = Q >> 1, qt = Q & 1;
    float one = 1.0f; asm("" : "+s"(one));
    float e[8];
#pragma unroll
    for (int i = 0; i < 4; ++i) { e[i] = __builtin_amdgcn_exp2f(r.s[2 * ks][qt][i]); e[4 + i] = __builtin_amdgcn_exp2f(r.s[2 * ks + 1][qt][i]); }
#pragma unroll
    for (int i = 0; i < 8; ++i) {
        if (qt == 0) a0 += e[i]; else a1 = __builtin_fmaf(e[i], one, a1);
    }
    r.pk[qt][ks] = (u32x4){cvtpk(e[0], e[1]), cvtpk(e[2], e[3]), cvtpk(e[4], e[5]), cvtpk(e[6], e[7])};
}
template <bool DMA> DI void a_exp_pack(AttnRegs& r, float& a0, float& a1, const Piece& early) {
    a0 = 0.f; a1 = 0.f;
    if constexpr (DMA) { piece<0, false>(early); SBAR(); }
    a_exp_quarter<0>(r, a0, a1);
    if constexpr (DMA) { SBAR(); piece<1, false>(early); SBAR(); }
    a_exp_quarter<1>(r, a0, a1);
    if constexpr (DMA) { SBAR(); piece<2, false>(early); SBAR(); }
    a_exp_quarter<2>(r, a0, a1);
    if constexpr (DMA) { SBAR(); piece<3, false>(early); SBAR(); }
    a_exp_quarter<3>(r, a0, a1);
}
DI float xmax4(float v) {
    const u32x2 a = __builtin_amdgcn_permlane16_swap(__float_as_uint(v), __float_as_uint(v), false, false);
    v = __builtin_fmaxf(__uint_as_float(a[0]), __uint_as_float(a[1]));
    const u32x2 c = __builtin_amdgcn_permlane32_swap(__float_as_uint(v), __float_as_uint(v), false, false);
    return __builtin_fmaxf(__uint_as_float(c[0]), __uint_as_float(c[1]));
}
DI float xsum4(float v) {
    const u32x2 a = __builtin_amdgcn_permlane16_swap(__float_as_uint(v), __float_as_uint(v), false, false);
    v = __uint_as_float(a[0]) + __uint_as_float(a[1]);
    const u32x2 c = __builtin_amdgcn_permlane32_swap(__float_as_uint(v), __float_as_uint(v), false, false);
    return __uint_as_float(c[0]) + __uint_as_float(c[1]);
}
constexpr float SUM_HI = 1.152921504606846976e18f, SUM_LO = 8.673617379884035e-19f;

struct AttnCtx { rsrc_t sws; unsigned vl16, kbase, vbase, ctxl; int nL, grp, qg, j, rowrel, swap_s, first_s; lcptr kl, vl, ql; };
struct AttnSm { float m0, m1, l0, l1, ll0, ll1; };
DI void a_dmaK(const AttnCtx& c, int n) { dma16x4(c.sws, c.vl16, c.kbase + n * 16384 + c.qg * 4096, (n & 3) * 16384 + c.qg * 4096); }
DI void a_dmaV(const AttnCtx& c, int n) { dma16x4(c.sws, c.vl16, c.vbase + n * 16384 + c.qg * 4096, 65536 + (n & 3) * 16384 + c.qg * 4096); }
#define PT(i) do { } while (0)
#define PARG
#define PPASS
#define PPASSG
DI void a_slotfull(const AttnCtx& c, const Sync& y, AttnRegs& r, AttnSm& z, const int s PARG) {
    const int nL = c.nL, grp = c.grp;
    const bool SWAP = (s == c.swap_s), FIN = (s == c.swap_s + 1), FIRST = (s == c.first_s) || FIN;
    const int tcur = s < nL ? s : 2 * s - nL + grp;
    Piece early, late;
    {
        const int s2 = s + 2, s1 = s + 1;
        const bool eK = grp == 1 && s2 < nL;
        const int te = eK ? s2 : (s1 < nL ? s1 : 2 * s1 - nL + grp), tl = 2 * s2 - nL + grp;
        early.sws = c.sws; early.vl16 = c.vl16; late.sws = c.sws; late.vl16 = c.vl16;
        early.on = eK || (s1 <= 16 && (grp == 0 || s1 >= nL));
        early.soff = (eK ? c.kbase : c.vbase) + te * 16384 + c.qg * 4096; early.ldso = (eK ? 0 : 65536) + (te & 3) * 16384 + c.qg * 4096;
        late.on = s2 <= 16 && s2 >= nL;
        late.soff = c.kbase + tl * 16384 + c.qg * 4096; late.ldso = (tl & 3) * 16384 + c.qg * 4096;
    }
    const int snext = s + 1, tnext = snext < nL ? snext : 2 * snext - nL + grp;
    const lcptr vp = c.vl + (tcur & 3) * 16384, kp = c.kl + (tnext & 3) * 16384;
    a_pre<0>(r, vp, kp, std::make_integer_sequence<int, PD>{});
    SBAR();
    if (SWAP) {
        asm volatile("ds_read_b128 %0, %8\n\tds_read_b128 %1, %8 offset:1024\n\tds_read_b128 %2, %8 offset:2048\n\tds_read_b128 %3, %8 offset:3072\n\t"
                     "ds_read_b128 %4, %8 offset:4096\n\tds_read_b128 %5, %8 offset:5120\n\tds_read_b128 %6, %8 offset:6144\n\tds_read_b128 %7, %8 offset:7168\n\ts_waitcnt lgkmcnt(0)"
                     : "=&v"(r.q[0][0]), "=&v"(r.q[0][1]), "=&v"(r.q[0][2]), "=&v"(r.q[0][3]), "=&v"(r.q[1][0]), "=&v"(r.q[1][1]), "=&v"(r.q[1][2]), "=&v"(r.q[1][3])
                     : "v"(c.ql) : "memory");
    }
    if (FIN) {
        const float i0 = __builtin_amdgcn_rcpf(xsum4(z.ll0)), i1 = __builtin_amdgcn_rcpf(xsum4(z.ll1));
        const unsigned c1o = c.ctxl + 131072;
        st16_sc1o<0>(c.sws, pack8(r.o[0][0] * i0, r.o[1][0] * i0), c.vl16, c.ctxl); st16_sc1o<1024>(c.sws, pack8(r.o[2][0] * i0, r.o[3][0] * i0), c.vl16, c.ctxl);
        st16_sc1o<2048>(c.sws, pack8(r.o[4][0] * i0, r.o[5][0] * i0), c.vl16, c.ctxl); st16_sc1o<3072>(c.sws, pack8(r.o[6][0] * i0, r.o[7][0] * i0), c.vl16, c.ctxl);
        st16_sc1o<0>(c.sws, pack8(r.o[0][1] * i1, r.o[1][1] * i1), c.vl16, c1o); st16_sc1o<1024>(c.sws, pack8(r.o[2][1] * i1, r.o[3][1] * i1), c.vl16, c1o);
        st16_sc1o<2048>(c.sws, pack8(r.o[4][1] * i1, r.o[5][1] * i1), c.vl16, c1o); st16_sc1o<3072>(c.sws, pack8(r.o[6][1] * i1, r.o[7][1] * i1), c.vl16, c1o);
#pragma unroll
        for (int d = 0; d < 8; ++d) { r.o[d][0] = (f32x4){0.f, 0.f, 0.f, 0.f}; r.o[d][1] = (f32x4){0.f, 0.f, 0.f, 0.f}; }
    }
    {
        const bool heavy = (grp == 1) || (s >= nL);
        const int qb = heavy ? 15 - c.j : c.j, kb = 64 * tcur - 128 * qb - 32 * c.qg;
        if (kb >= -32) {
            const float NI = -__builtin_inff();
            const f32x4 ninf = {NI, NI, NI, NI};
            f32x4 s00 = r.s[0][0], s10 = r.s[1][0], s20 = r.s[2][0], s30 = r.s[3][0], s01 = r.s[0][1], s11 = r.s[1][1], s21 = r.s[2][1], s31 = r.s[3][1];
            const int dq = c.rowrel;
            if (kb > 0) { s00 = ninf; s10 = ninf; s20 = ninf; s30 = ninf; s01 = ninf; s11 = ninf; s21 = ninf; s31 = ninf; }
            else if (kb == 0) {
#pragma unroll
                for (int i = 0; i < 4; ++i) { s00[i] = sel(i > dq, NI, s00[i]); s11[i] = sel(i > dq, NI, s11[i]); }
                s10 = ninf; s20 = ninf; s30 = ninf; s21 = ninf; s31 = ninf;
            } else {
#pragma unroll
                for (int i = 0; i < 4; ++i) { s20[i] = sel(i > dq, NI, s20[i]); s31[i] = sel(i > dq, NI, s31[i]); }
                s30 = ninf;
            }
            r.s[0][0] = s00; r.s[1][0] = s10; r.s[2][0] = s20; r.s[3][0] = s30; r.s[0][1] = s01; r.s[1][1] = s11; r.s[2][1] = s21; r.s[3][1] = s31;
        }
    }
    float a0, a1;
    if (early.on) dma16x4(c.sws, c.vl16, early.soff, early.ldso);
    a_exp_pack<false>(r, a0, a1, early);
    const float lo = FIRST ? SUM_LO : 0.f;
    if (__builtin_expect(__any(!(a0 <= SUM_HI) || !(a1 <= SUM_HI) || a0 < lo || a1 < lo), 0)) {
        float x0 = r.s[0][0][0], x1 = r.s[0][1][0];
#pragma unroll
        for (int kt = 0; kt < 4; ++kt)
#pragma unroll
            for (int i = 0; i < 4; ++i) { x0 = __builtin_fmaxf(x0, r.s[kt][0][i]); x1 = __builtin_fmaxf(x1, r.s[kt][1][i]); }
        x0 = xmax4(x0); x1 = xmax4(x1);
        if (!FIRST) { x0 = __builtin_fmaxf(x0, 0.f); x1 = __builtin_fmaxf(x1, 0.f);
            const float f0 = __builtin_amdgcn_exp2f(-x0), f1 = __builtin_amdgcn_exp2f(-x1);
#pragma unroll
            for (int d = 0; d < 8; ++d) { r.o[d][0] *= f0; r.o[d][1] *= f1; }
            z.l0 *= f0; z.l1 *= f1; }
        z.m0 += x0; z.m1 += x1;
        r.negm[0] = (f32x4){-z.m0, -z.m0, -z.m0, -z.m0}; r.negm[1] = (f32x4){-z.m1, -z.m1, -z.m1, -z.m1};
#pragma unroll
        for (int kt = 0; kt < 4; ++kt) { r.s[kt][0] -= x0; r.s[kt][1] -= x1; }
        a_exp_pack<false>(r, a0, a1, early);
    }
    z.l0 += a0; z.l1 += a1;
    if (SWAP) {
        z.ll0 = z.l0; z.ll1 = z.l1; z.l0 = 0.f; z.l1 = 0.f; z.m0 = 0.f; z.m1 = 0.f;
        r.negm[0] = (f32x4){0.f, 0.f, 0.f, 0.f}; r.negm[1] = (f32x4){0.f, 0.f, 0.f, 0.f};
    }
    SBAR();
    PT(0);
    if (FIN) { if (early.on) WAITV(12); else WAITV(8); } else { if (early.on) WAITV(4); else WAITV(0); }
    PT(1);
    BARRIER(); SBAR();
    PT(2);
    __builtin_amdgcn_s_setprio(PRIOM);
    a_run<0, 2>(r, vp, kp, late, std::make_integer_sequence<int, 16>{});
    if (s < 16) a_run<16, 2>(r, vp, kp, late, std::make_integer_sequence<int, 16>{});
    __builtin_amdgcn_s_setprio(PRIOV);
    SBAR();
    PT(3);
    if (late.on) WAITV(4); else WAITV(0);
    PT(4);
    BARRIER(); SBAR();
    PT(5);
}

struct Lean { unsigned e_soff, e_lds, l_soff, l_lds, voff, koff; };
template <bool LATE> DI void a_slot_lean(const AttnCtx& c, AttnRegs& r, AttnSm& z, const Lean& st PARG) {
    const lcptr vp = c.vl + st.voff, kp = c.kl + st.koff;
    Piece early, late;
    early.sws = c.sws; early.vl16 = c.vl16; early.soff = st.e_soff; early.ldso = st.e_lds; early.on = true;
    late.sws = c.sws; late.vl16 = c.vl16; late.soff = st.l_soff; late.ldso = st.l_lds; late.on = true;
    a_pre<0>(r, vp, kp, std::make_integer_sequence<int, PD>{});
    SBAR();
    float a0, a1;
    a_exp_pack<true>(r, a0, a1, early);
    if (__builtin_expect(__any(!(a0 <= SUM_HI) || !(a1 <= SUM_HI)), 0)) {
        float x0 = 0.f, x1 = 0.f;
#pragma unroll
        for (int kt = 0; kt < 4; ++kt)
#pragma unroll
            for (int i = 0; i < 4; ++i) { x0 = __builtin_fmaxf(x0, r.s[kt][0][i]); x1 = __builtin_fmaxf(x1, r.s[kt][1][i]); }
        x0 = xmax4(x0); x1 = xmax4(x1);
        const float f0 = __builtin_amdgcn_exp2f(-x0), f1 = __builtin_amdgcn_exp2f(-x1);
#pragma unroll
        for (int d = 0; d < 8; ++d) { r.o[d][0] *= f0; r.o[d][1] *= f1; }
        z.l0 *= f0; z.l1 *= f1;
        z.m0 += x0; z.m1 += x1;
        r.negm[0] = (f32x4){-z.m0, -z.m0, -z.m0, -z.m0}; r.negm[1] = (f32x4){-z.m1, -z.m1, -z.m1, -z.m1};
#pragma unroll
        for (int kt = 0; kt < 4; ++kt) { r.s[kt][0] -= x0; r.s[kt][1] -= x1; }
        a_exp_pack<false>(r, a0, a1, early);
    }
    z.l0 += a0; z.l1 += a1;
    SBAR();
    PT(0);
    WAITV(4);
    PT(1);
    BARRIER(); SBAR();
    PT(2);
    __builtin_amdgcn_s_setprio(PRIOM);
    a_run<0, LATE ? 1 : 0>(r, vp, kp, late, std::make_integer_sequence<int, 32>{});
    __builtin_amdgcn_s_setprio(PRIOV);
    SBAR();
    PT(3);
    if constexpr (LATE) WAITV(4); else WAITV(0);
    PT(4);
    BARRIER(); SBAR();
    PT(5);
}

constexpr unsigned P4STG = 36864, P4TAG = 0x3FFu, P4OOB = 0xF0000000u;
struct P4Ctx { rsrc_t sws; unsigned vl16, ga, gw, gas, gws, dw0, dw1, dw2, lw, tag, so[3], so2[3], lo[3], lo2[3]; int w, q, o, ng, ra, rw; lcptr pa, pw; };
struct P4Regs { bf16x8 xa[2][2], wa[2][5]; };
struct P4Xch { f32x4 own[3]; u32x4 snd[9], rcv[9]; };
DI void mfa(f32x4& acc, bf16x8 a, bf16x8 b) { asm volatile("v_mfma_f32_16x16x32_bf16 %0, %1, %2, %0" : "+v"(acc) : "v"(a), "v"(b)); }
template <int OFF> DI void dma_sc1(const P4Ctx& c, unsigned soff, unsigned ldso) {
    asm volatile("s_mov_b32 m0, %0\n\ts_nop 3\n\tbuffer_load_dwordx4 %1, %2, %3 offen offset:%4 sc1 lds" ::"s"(ldso), "v"(c.vl16), "s"(c.sws), "s"(soff), "i"(OFF) : "memory");
}
template <int P> DI void p4_piece(P4Ctx& c, unsigned st) {
    if constexpr (P == 0) dma_sc1<0>(c, c.ga, st + c.w * 2048);
    else if constexpr (P == 1) { dma_sc1<1024>(c, c.ga, st + c.w * 2048); ++c.ra; c.ga = c.ra == c.ng ? c.gas : c.ga + 2048; }
    else if constexpr (P == 2) dma_sc1<0>(c, c.gw + c.dw0, st + c.lw);
    else if constexpr (P == 3) dma_sc1<0>(c, c.gw + c.dw1, st + c.lw + 8192);
    else { if (c.w < 4) dma_sc1<0>(c, c.gw + c.dw2, st + c.lw + 16384); ++c.rw; c.gw = c.rw == c.ng ? c.gws : c.gw + 81920; }
}
template <int B, int UP, int I> DI void p4_rd(const P4Ctx& c, P4Regs& r, unsigned st) {
    if constexpr (I == 0) r.wa[B][0] = ld128(c.pw + st + UP * 10240);
    else if constexpr (I <= 2) r.xa[B][I - 1] = ld128(c.pa + st + (I - 1) * 2048 + UP * 1024);
    else if constexpr (I < 7) r.wa[B][I - 2] = ld128(c.pw + st + UP * 10240 + (I - 2) * 1024);
}
template <int N> DI void p4_put(const P4Ctx& c, const P4Xch& x, unsigned xoff) { st16_sc1o<(N % 3) * 1024>(c.sws, x.snd[N], c.vl16, (N % 3 == 2 ? c.so2[N / 3] : c.so[N / 3]) + xoff); }
template <int N> DI void p4_get(const P4Ctx& c, P4Xch& x, unsigned xoff) {
    asm volatile("buffer_load_dwordx4 %0, %1, %2, %3 offen offset:%4 sc1" : "=v"(x.rcv[N]) : "v"(c.vl16), "s"(c.sws), "s"((N % 3 == 2 ? c.lo2[N / 3] : c.lo[N / 3]) + xoff), "i"((N % 3) * 1024) : "memory");
}
template <int O> DI void p4_split(const f32x4 (&acc)[10], unsigned tag, P4Xch& x) {
#pragma unroll
    for (int k = 0; k < 3; ++k) x.own[k] = acc[O + 4 * k < 10 ? O + 4 * k : O];
#pragma unroll
    for (int n = 0; n < 9; ++n) { const int i = ((O + 1 + n / 3) & 3) + 4 * (n % 3);
        const u32x4 v = __builtin_bit_cast(u32x4, acc[i < 10 ? i : 0]);
#pragma unroll
        for (int e = 0; e < 4; ++e) x.snd[n][e] = (v[e] & ~P4TAG) | tag; }
}
DI void p4_split(const P4Ctx& c, f32x4 (&a)[10], P4Xch& x) {
    asm volatile("s_nop 7\n\ts_nop 7\n\ts_nop 7" : "+v"(a[0]), "+v"(a[1]), "+v"(a[2]), "+v"(a[3]), "+v"(a[4]), "+v"(a[5]), "+v"(a[6]), "+v"(a[7]), "+v"(a[8]), "+v"(a[9]) :: "memory");
    switch (c.o) { case 0: p4_split<0>(a, c.tag, x); break; case 1: p4_split<1>(a, c.tag, x); break; case 2: p4_split<2>(a, c.tag, x); break; default: p4_split<3>(a, c.tag, x); break; }
}
DI bool p4_tagged(const P4Ctx& c, const P4Xch& x) {
    unsigned a = 0, b = 0;
#pragma unroll
    for (int n = 0; n < 9; ++n)
#pragma unroll
        for (int e = 0; e < 4; ++e) { if (n % 3 == 2) b |= x.rcv[n][e] ^ c.tag; else a |= x.rcv[n][e] ^ c.tag; }
    return __all(((a | (c.o < 2 ? b : 0u)) & P4TAG) == 0);
}
template <int... Ns> DI void p4_refetch(const P4Ctx& c, P4Xch& x, unsigned xoff, std::integer_sequence<int, Ns...>) {
    const unsigned long long t0 = __builtin_amdgcn_s_memrealtime();
    for (;;) {
        (p4_get<Ns>(c, x, xoff), ...);
        asm volatile("s_waitcnt vmcnt(0)" : "+v"(x.rcv[0]), "+v"(x.rcv[1]), "+v"(x.rcv[2]), "+v"(x.rcv[3]), "+v"(x.rcv[4]), "+v"(x.rcv[5]), "+v"(x.rcv[6]), "+v"(x.rcv[7]), "+v"(x.rcv[8]) :: "memory");
        if (p4_tagged(c, x)) break;
        if (__builtin_amdgcn_s_memrealtime() - t0 > 2000000ull) break;
        __builtin_amdgcn_s_sleep(1);
    }
}
DI f32x4 p4_total(const P4Xch& x, int k) { return (x.own[k] + __builtin_bit_cast(f32x4, x.rcv[k])) + (__builtin_bit_cast(f32x4, x.rcv[3 + k]) + __builtin_bit_cast(f32x4, x.rcv[6 + k])); }
template <int B, bool RD, int DMA, int PUT, int GET, int... Is> DI void p4_half(P4Ctx& c, P4Regs& r, P4Xch& x, f32x4 (&acc)[10], unsigned rs, unsigned ds, std::integer_sequence<int, Is...>) {
    auto one = [&](auto ic) {
        constexpr int I = decltype(ic)::value;
        mfa(acc[I], r.wa[B][I >> 1], r.xa[B][I & 1]);
        if constexpr (RD) p4_rd<B ^ 1, B ^ 1, I>(c, r, rs);
        if constexpr (DMA == 1) { if constexpr (I == 1) p4_piece<0>(c, ds); if constexpr (I == 5) p4_piece<1>(c, ds); if constexpr (I == 9) p4_piece<2>(c, ds); }
        if constexpr (DMA == 2) { if constexpr (I == 3) p4_piece<3>(c, ds); if constexpr (I == 7) p4_piece<4>(c, ds); }
        if constexpr (PUT >= 0) { if constexpr (B == 0 && I == 1) p4_put<PUT>(c, x, OFF_XG); if constexpr (B == 0 && I == 5) p4_put<PUT + 1>(c, x, OFF_XG); if constexpr (B == 1 && I == 3) p4_put<PUT + 2>(c, x, OFF_XG); }
        if constexpr (GET >= 0 && (I & 1) && GET + (I >> 1) < 9) p4_get<GET + (I >> 1)>(c, x, OFF_XG);
        SBAR();
    };
    (one(std::integral_constant<int, Is>{}), ...);
}
DI u64 poll_asm(gu64* flags, unsigned idx) { u64 v; asm volatile("global_load_dwordx2 %0, %1, off sc1" : "=v"(v) : "v"(flags + idx * 8) : "memory"); return v; }
template <int WAITN, bool RA, bool RB, bool LAST, int EV, int PUT, int GETA, int GETB> DI void p4_step(P4Ctx& c, const Sync& y, P4Regs& r, P4Xch& x, f32x4 (&acc)[10], unsigned& sb, unsigned& sp, int t, unsigned fidx, u64& seen) {
    constexpr auto seq = std::make_integer_sequence<int, 10>{};
    const unsigned sn = sb == 3 * P4STG ? 0 : sb + P4STG;
    p4_half<0, true, RA ? 2 : 0, PUT, GETA>(c, r, x, acc, sb, sp, seq);
    if constexpr (!LAST) {
        asm volatile("s_waitcnt vmcnt(%0) lgkmcnt(0)" :: "i"(WAITN) : "memory");
        if constexpr (EV == 1) { if (t == c.ng - 4 && c.w == 0) { asm volatile("s_waitcnt vmcnt(4)" : "+v"(seen) :: "memory"); check_flags(y.flags, fidx, seen, y.token); } }
        BARRIER();
        if constexpr (EV == 1) {
            if (c.w == 0) {
                if (t == 1) { if (lane_id() == 0) set_flag(y.flags, FL_CTX + y.j * 32 + y.h, y.token); }
                if (t == c.ng - 5) seen = poll_asm(y.flags, fidx);
            }
        }
        SBAR();
    }
    p4_half<1, !LAST, RB ? 1 : 0, PUT, GETB>(c, r, x, acc, sn, sb, seq);
    sp = sb; sb = sn;
}

__global__ __launch_bounds__(512, 2) void fused_kernel(const bf16_t* __restrict__ x, const bf16_t* __restrict__ ckv, const bf16_t* __restrict__ wq, const bf16_t* __restrict__ wuk,
                                                       const bf16_t* __restrict__ wuv, const bf16_t* __restrict__ wo, const bf16_t* __restrict__ w1, const bf16_t* __restrict__ b1,
                                                       const bf16_t* __restrict__ w2, bf16_t* __restrict__ out, char* __restrict__ ws) {
    __shared__ __attribute__((aligned(1024))) char smem[163840];
    const lptr lds = (lptr)smem;
    const int b = blockIdx.x, xcd = b & 7, bi = b >> 3, h = xcd * 4 + (bi >> 3), j = bi & 7;
    const int w = __builtin_amdgcn_readfirstlane(threadIdx.x >> 6);
    const rsrc_t sws = mk_rsrc(ws, WS_BYTES);
    STAMP(0);
    Sync y;
    y.flags = (gu64*)(ws + OFF_FLAGS); y.b = b; y.h = h; y.j = j; y.w = w;
    u64 epoch;
    asm volatile("s_load_dwordx2 %0, %1, 0x20" : "=s"(epoch) : "s"(ws) : "memory");
    u32x4 kv[16];
    P1Ctx c1;
    P2Ctx c2;
    const bool isv = (w & 3) >= 2;
    unsigned sink = 0;
    {
        const int l = lane_id(), g = l >> 4, q = (l & 15) >> 2;
        {
            const int nb = w >> 1;
            c1.w = w; c1.sws = sws; c1.wo = wo; c1.w2 = w2; c1.wq = wq; c1.w1 = w1; c1.lds = lds;
            c1.sx = mk_rsrc(ckv, 2048 * 512 * 2);
            c1.sw = mk_rsrc(nb < 2 ? wuk : wuv, 512 * 4096 * 2);
#pragma unroll
            for (int par = 0; par < 2; ++par) {
                c1.vx[par] = (l >> 3) * 1024 + (((l & 7) ^ (4 * par + (l >> 4))) * 16);
                c1.vw[par] = (l >> 3) * 8192 + (((l & 7) ^ ((((l >> 4) & 1) | (par << 1)) << 1)) * 16);
            }
            c1.sxo = (256 * j + 32 * w) * 1024;
            c1.swo = (32 * (w & 1)) * 8192 + (h * 128 + 64 * (nb & 1)) * 2;
            const int wn = w & 3, wm = w >> 2, f = (l & 15) >> 1, s = ((q >> 1) & 1) | ((g & 1) << 1);
            c1.xb[0] = lds + wm * 16384 + (l & 15) * 128 + ((g ^ f) * 16);
            c1.xb[1] = lds + wm * 16384 + (l & 15) * 128 + (((g ^ f) ^ 4) * 16);
#pragma unroll
            for (int nt = 0; nt < 4; ++nt) c1.wb[nt] = lds + 65536 + wn * 8192 + (8 * g + q) * 128 + ((nt ^ s) * 32) + (l & 3) * 8;
        }
        p1_dma(c1, 0, 0);
        u32x4 xo0, xo1;
        {
            const rsrc_t sxi = mk_rsrc(x, 2048 * 640 * 2);
            const unsigned vx = (l & 7) * 1280 + (l >> 3) * 16, so0 = b * 8 * 1280 + w * 128, so1 = b * 8 * 1280 + (8 + (w & 1)) * 128;
            asm volatile("s_nop 4\n\tbuffer_load_dwordx4 %0, %2, %3, %4 offen\n\tbuffer_load_dwordx4 %1, %2, %3, %5 offen" : "=&v"(xo0), "=&v"(xo1) : "v"(vx), "s"(sxi), "s"(so0), "s"(so1) : "memory");
        }
        p1_dma(c1, 1, 1);
        asm volatile("s_waitcnt lgkmcnt(0)" : "+s"(epoch) :: "memory");
        y.token = epoch + 1;
        STAMP(12);
        asm volatile("s_waitcnt vmcnt(8)" : "+v"(xo0), "+v"(xo1) :: "memory");
        STAMP(13);
        float v0[8], v1[8];
#pragma unroll
        for (int e = 0; e < 4; ++e) { v0[2 * e] = bflo(xo0[e]); v0[2 * e + 1] = bfhi(xo0[e]); v1[2 * e] = bflo(xo1[e]); v1[2 * e + 1] = bfhi(xo1[e]); }
        auto red8 = [](float t) { t += __uint_as_float(__builtin_amdgcn_update_dpp(0u, __float_as_uint(t), 0x128, 0xf, 0xf, false)); return xsum4(t); };
        {
            const float m0 = red8(((v0[0] + v0[1]) + (v0[2] + v0[3])) + ((v0[4] + v0[5]) + (v0[6] + v0[7]))) * (1.f / 64.f);
            const float m1 = red8(((v1[0] + v1[1]) + (v1[2] + v1[3])) + ((v1[4] + v1[5]) + (v1[6] + v1[7]))) * (1.f / 64.f);
            float q0 = 0.f, q1 = 0.f;
#pragma unroll
            for (int e = 0; e < 8; ++e) { const float d0 = v0[e] - m0, d1 = v1[e] - m1; q0 += d0 * d0; q1 += d1 * d1; }
            q0 = red8(q0); q1 = red8(q1);
            if (l < 8) { *(LAS f32x2*)(lds + 162816 + l * 80 + w * 8) = (f32x2){m0, q0}; if (w < 2) *(LAS f32x2*)(lds + 162816 + l * 80 + 64 + w * 8) = (f32x2){m1, q1}; }
        }
        asm volatile("s_waitcnt lgkmcnt(0)" ::: "memory"); BARRIER();
        STAMP(14);
        {
            f32x4 t[5];
#pragma unroll
            for (int i = 0; i < 5; ++i) t[i] = *(const LAS f32x4*)(lds + 162816 + (l & 7) * 80 + i * 16);
            float mean = 0.f;
#pragma unroll
            for (int i = 0; i < 5; ++i) mean += t[i][0] + t[i][2];
            mean *= 0.1f;
            float m2 = 0.f;
#pragma unroll
            for (int i = 0; i < 5; ++i) { const float d0 = t[i][0] - mean, d1 = t[i][2] - mean; m2 += (t[i][1] + t[i][3]) + 64.f * (d0 * d0 + d1 * d1); }
            const float rstd = 1.f / __builtin_sqrtf(m2 * (1.f / 640.f) + 1e-5f);
            const unsigned vo = (l >> 3) * 256 + (l & 7) * 16, xo = OFF_XN + (b >> 1) * 20480 + (b & 1) * 128;
            u32x4 o0, o1;
#pragma unroll
            for (int e = 0; e < 4; ++e) { o0[e] = cvtpk((v0[2 * e] - mean) * rstd, (v0[2 * e + 1] - mean) * rstd); o1[e] = cvtpk((v1[2 * e] - mean) * rstd, (v1[2 * e + 1] - mean) * rstd); }
            st16_sc1(sws, o0, vo, xo + w * 2048);
            if (w < 2) st16_sc1(sws, o1, vo, xo + (8 + w) * 2048);
        }
        {
            const char* pa = (const char*)ckv + (size_t)(256 * j + 64 * (h & 3) + 8 * w + (l >> 3)) * 1024 + (l & 7) * 128;
            const char* pw = (const char*)((w & 1) ? wuv : wuk) + (size_t)(64 * j + 16 * (w >> 1) + ((l & 31) >> 1)) * 8192 + h * 256 + (l & 1) * 128;
            asm volatile("global_load_dword %0, %1, off\n\tglobal_load_dword %0, %2, off" : "+v"(sink) : "v"(pa), "v"(pw) : "memory");
        }
        {
            c2.w = w; c2.sws = sws; c2.swq = mk_rsrc(wq, 640 * 4096 * 2); c2.sw1 = mk_rsrc(w1, 640 * 1728 * 2);
            c2.vl16 = l * 16;
            const int swz = ((l >> 4) & 1) | ((w & 1) << 1);
            c2.vwq = (l >> 3) * 8192 + (((l & 7) ^ (swz << 1)) * 16);
            c2.vw1 = (l >> 3) * 3456 + (((l & 7) ^ (swz << 1)) * 16);
            { const int mtl = 2 * w, mtg = mtl < 8 ? 8 * j + mtl : 8 * (15 - j) + (mtl - 8); c2.sxo = OFF_XN + mtg * 20480; }
            c2.swqo = (8 * w) * 8192 + h * 256;
            c2.sw1o = (8 * w) * 3456 + h * 128;
            { const int wn = w & 3, wm = w >> 2;
              c2.skv = isv ? OFF_V + (unsigned)(h * 32 + 4 * j + 2 * wm) * 16384 + (wn - 2) * 4096 : OFF_K + (unsigned)((h * 128 + 16 * j + 8 * wm) * 4 + 2 * wn) * 1024; }
        }
    }
    STAMP(1); CSTAMP(14);
    if (isv) p1_main<true>(c1, c2, y, sink, kv); else p1_main<false>(c1, c2, y, sink, kv);
    STAMP(2); CSTAMP(15);
    p2_kvst<0>(c2, kv, isv); p2_kvst<1>(c2, kv, isv); p2_kvst<2>(c2, kv, isv); p2_kvst<3>(c2, kv, isv);
    const bool hasf = h < 27;
    AttnCtx c3;
    AttnRegs r;
    {
        const int l = lane_id(), g = l >> 4, q = (l & 15) >> 2;
        const int wn = w & 1, wm = w >> 1;
        const int s = ((q >> 1) & 1) | ((g & 1) << 1);
        c2.xb = lds + wm * 8192 + l * 16;
#pragma unroll
        for (int nt = 0; nt < 4; ++nt) c2.wq[nt] = lds + 65536 + wn * 8192 + (8 * g + q) * 128 + ((nt ^ s) * 32) + (l & 3) * 8;
#pragma unroll
        for (int ft = 0; ft < 2; ++ft) c2.wf[ft] = lds + 65536 + 16384 + (8 * g + q) * 128 + (((2 * wn + ft) ^ s) * 32) + (l & 3) * 8;
        f32x4 acc[6][4];
#pragma unroll
        for (int a = 0; a < 6; ++a)
#pragma unroll
            for (int m = 0; m < 4; ++m) acc[a][m] = (f32x4){0.f, 0.f, 0.f, 0.f};
        STAMP(3);
        if (hasf) p2_main<true>(c2, y, acc, kv, isv, std::make_integer_sequence<int, 10>{}); else p2_main<false>(c2, y, acc, kv, isv, std::make_integer_sequence<int, 10>{});
        STAMP(4);
        constexpr float C2 = 0.08838834764831845f * 1.4426950408889634f;
#pragma unroll
        for (int mt = 0; mt < 4; ++mt)
#pragma unroll
            for (int a = 0; a < 2; ++a)
                *(LAS u32x4*)(lds + 98304 + ((4 * wm + mt) * 4 + 2 * wn + a) * 1024 + l * 16) = pack8(acc[2 * a][mt] * C2, acc[2 * a + 1][mt] * C2);
        u32x4 actf[4];
        if (hasf) {
            const int f0 = 64 * h + 32 * wn + 4 * g;
            const u32x2 bA = *(const u32x2*)(b1 + f0), bB = *(const u32x2*)(b1 + f0 + 16);
            const f32x4 biasA = {bflo(bA[0]), bfhi(bA[0]), bflo(bA[1]), bfhi(bA[1])}, biasB = {bflo(bB[0]), bfhi(bB[0]), bflo(bB[1]), bfhi(bB[1])};
#pragma unroll
            for (int mt = 0; mt < 4; ++mt) {
                f32x4 hA = acc[4][mt] + biasA, hB = acc[5][mt] + biasB;
#pragma unroll
                for (int i = 0; i < 4; ++i) { hA[i] = mish(hA[i]); hB[i] = mish(hB[i]); }
                actf[mt] = pack8(hA, hB);
            }
        }
        asm volatile("s_waitcnt vmcnt(0) lgkmcnt(0)" ::: "memory"); BARRIER();
        const int grp = w >> 2, qg = w & 3;
        c3.sws = sws; c3.vl16 = l * 16; c3.kbase = OFF_K + (unsigned)h * 524288; c3.vbase = OFF_V + (unsigned)h * 524288;
        c3.ctxl = OFF_CTX + ((8 * j + 2 * qg) * 128 + 4 * h) * 1024;
        c3.nL = 2 * j + 2; c3.grp = grp; c3.qg = qg; c3.j = j; c3.rowrel = (l & 15) - 4 * g; c3.kl = lds + l * 16; c3.vl = lds + 65536 + l * 16;
        c3.ql = lds + 131072 + qg * 8192 + l * 16;
        if (grp == 0) { a_dmaK(c3, 0); a_dmaV(c3, 0); } else { a_dmaK(c3, 1); }
        if (hasf) {
#pragma unroll
            for (int mt = 0; mt < 4; ++mt) { const int mtl = 4 * wm + mt, mtg = mtl < 8 ? 8 * j + mtl : 8 * (15 - j) + (mtl - 8);
                st16_sc1(sws, actf[mt], l * 16, OFF_ACT + (mtg * 54 + 2 * h + wn) * 1024); }
        }
#pragma unroll
        for (int qt = 0; qt < 2; ++qt)
#pragma unroll
            for (int u = 0; u < 4; ++u) r.q[qt][u] = ld128(lds + 98304 + ((8 * grp + 2 * qg + qt) * 4 + u) * 1024 + l * 16);
    }
    STAMP(5);
    P4Ctx c4;
    {
        const int l = lane_id();
        const int grp = c3.grp, qg = c3.qg;
        const unsigned vl16 = c3.vl16;
#pragma unroll
        for (int d = 0; d < 8; ++d) { r.o[d][0] = (f32x4){0.f, 0.f, 0.f, 0.f}; r.o[d][1] = (f32x4){0.f, 0.f, 0.f, 0.f}; }
        r.negm[0] = (f32x4){0.f, 0.f, 0.f, 0.f}; r.negm[1] = (f32x4){0.f, 0.f, 0.f, 0.f};
        AttnSm z; z.m0 = 0.f; z.m1 = 0.f; z.l0 = 0.f; z.l1 = 0.f; z.ll0 = 0.f; z.ll1 = 0.f;
        if (hasf) asm volatile("s_waitcnt vmcnt(4) lgkmcnt(0)" ::: "memory"); else asm volatile("s_waitcnt vmcnt(0) lgkmcnt(0)" ::: "memory");
        BARRIER();
        STAMP(6); CSTAMP(12);
        a_pre<16>(r, c3.vl, c3.kl, std::make_integer_sequence<int, PD>{});
        if (grp == 1) BARRIER();
        { Piece none; none.on = false; none.sws = sws; none.vl16 = 0; none.soff = 0; none.ldso = 0; a_run<16, 0>(r, c3.vl, c3.kl, none, std::make_integer_sequence<int, 16>{}); }
        BARRIER();
        c3.swap_s = grp == 0 ? c3.nL - 1 : 99; c3.first_s = 0;
        asm volatile("" : "+s"(c3.swap_s), "+s"(c3.first_s));
        {
            const int nL = c3.nL;
            auto lean_init = [&](int s) {
                Lean st;
                const bool eK = grp == 1 && s + 2 < nL;
                const int t0 = s < nL ? s : 2 * s - nL + grp, t1 = s + 1 < nL ? s + 1 : 2 * s + 2 - nL + grp, t2 = s + 2 < nL ? s + 2 : 2 * s + 4 - nL + grp;
                const int te = eK ? t2 : t1;
                st.e_soff = (eK ? c3.kbase : c3.vbase) + te * 16384 + qg * 4096; st.e_lds = (eK ? 0 : 65536) + (te & 3) * 16384 + qg * 4096;
                st.l_soff = c3.kbase + t2 * 16384 + qg * 4096; st.l_lds = (t2 & 3) * 16384 + qg * 4096;
                st.voff = (t0 & 3) * 16384; st.koff = (t1 & 3) * 16384;
                return st;
            };
            int s = 0, phase = 0;
#pragma nounroll
            for (;;) {
                const int gend = phase == 0 ? 1 : (phase == 1 ? (grp == 0 ? nL + 1 : nL - 1) : 17);
#pragma nounroll
                while (s < gend) { a_slotfull(c3, y, r, z, s PPASSG); ++s; }
                if (phase == 0) { if (w == 0 && l == 0) set_flag(y.flags, FL_ACT + j * 32 + h, y.token); }
                if (phase == 2 || s > 16) break;
                if (phase == 1 && s <= 14) {
                    Lean st = lean_init(s);
#pragma nounroll
                    do { a_slot_lean<true>(c3, r, z, st PPASS);
                         st.e_soff += 32768; st.e_lds ^= 32768; st.l_soff += 32768; st.l_lds ^= 32768; st.voff ^= 32768; st.koff ^= 32768; ++s; } while (s <= 14);
                }
                const int send = phase == 0 ? nL - 2 : 16;
                if (s < send) {
                    Lean st = lean_init(s);
#pragma nounroll
                    do { a_slot_lean<false>(c3, r, z, st PPASS);
                         st.e_soff += 16384; st.e_lds = (st.e_lds & ~0xC000u) | ((st.e_lds + 16384) & 0xC000u); st.voff = (st.voff + 16384) & 0xC000u; st.koff = (st.koff + 16384) & 0xC000u; ++s; } while (s < send);
                }
                ++phase;
            }
        }
        float m0 = z.m0, m1 = z.m1, l0 = z.l0, l1 = z.l1;
        if (grp == 0) BARRIER();
        STAMP(7); CSTAMP(13);
        u64 seenA[4];
#pragma unroll
        for (int i = 0; i < 4; ++i) seenA[i] = poll_flag(y.flags, FL_ACT + 64 * i + l);
        {
            const int q = xcd >> 1, rbk = (xcd & 1) * 8 + (bi >> 2), cb = bi & 3, wm = w >> 1, wn = w & 1;
            c4.sws = sws; c4.vl16 = vl16; c4.w = w; c4.q = q; c4.o = (q + 2 * wn) & 3; c4.ng = q == 3 ? 6 : 7; c4.ra = 0; c4.rw = 0;
            c4.ga = OFF_ACT + ((8 * rbk + w) * 54 + 14 * q) * 1024; c4.gas = OFF_CTX + ((8 * rbk + w) * 128 + 32 * q) * 1024;
            c4.gw = OFF_WP + ((128 + 14 * q) * 40 + 10 * cb) * 1024; c4.gws = OFF_WP + (32 * q * 40 + 10 * cb) * 1024;
            c4.dw0 = w * 1024; c4.dw1 = (w < 2 ? 8 + w : 38 + w) * 1024; c4.dw2 = (46 + w) * 1024; c4.lw = 16384 + w * 1024;
            c4.tag = (unsigned)y.token & P4TAG;
            const unsigned xb = (unsigned)(((xcd & 1) * 32 + bi) * 128 + w) * 3072;
#pragma unroll
            for (int d = 0; d < 3; ++d) { const int qq = (q + 1 + d) & 3;
                c4.so[d] = xb + qq * 98304 + q * 24576; c4.so2[d] = ((c4.o + 1 + d) & 3) < 2 ? c4.so[d] : P4OOB;
                c4.lo[d] = xb + q * 98304 + qq * 24576; c4.lo2[d] = c4.o < 2 ? c4.lo[d] : P4OOB; }
            c4.pa = lds + (4 * wm) * 1024 + vl16; c4.pw = lds + 16384 + (5 * wn) * 1024 + vl16;
        }
        l0 = xsum4(l0); l1 = xsum4(l1);
        {
            const lptr xo = lds + (qg * 2 + grp) * 9216;
#pragma unroll
            for (int d = 0; d < 4; ++d)
#pragma unroll
                for (int qt = 0; qt < 2; ++qt) *(LAS f32x4*)(xo + (d * 2 + qt) * 1024 + l * 16) = sel(grp == 0, r.o[4 + d][qt], r.o[d][qt]);
            *(LAS f32x4*)(xo + 8192 + l * 16) = (f32x4){m0, m1, l0, l1};
            asm volatile("s_waitcnt lgkmcnt(0)" ::: "memory"); BARRIER();
#pragma unroll
            for (int i = 0; i < 4; ++i) check_flags(y.flags, FL_ACT + 64 * i + l, seenA[i], y.token);
            p4_piece<0>(c4, 2 * P4STG); p4_piece<1>(c4, 2 * P4STG); p4_piece<2>(c4, 2 * P4STG); p4_piece<3>(c4, 2 * P4STG); p4_piece<4>(c4, 2 * P4STG);
            p4_piece<0>(c4, 3 * P4STG); p4_piece<1>(c4, 3 * P4STG); p4_piece<2>(c4, 3 * P4STG); p4_piece<3>(c4, 3 * P4STG); p4_piece<4>(c4, 3 * P4STG);
            const lcptr xi = lds + (qg * 2 + (grp ^ 1)) * 9216;
            const f32x4 st = *(const LAS f32x4*)(xi + 8192 + l * 16);
            const float M0 = __builtin_fmaxf(m0, st[0]), M1 = __builtin_fmaxf(m1, st[1]);
            const float wa0 = __builtin_amdgcn_exp2f(m0 - M0), wb0 = __builtin_amdgcn_exp2f(st[0] - M0), wa1 = __builtin_amdgcn_exp2f(m1 - M1), wb1 = __builtin_amdgcn_exp2f(st[1] - M1);
            const float i0 = __builtin_amdgcn_rcpf(wa0 * l0 + wb0 * st[2]), i1 = __builtin_amdgcn_rcpf(wa1 * l1 + wb1 * st[3]);
            const float ca0 = wa0 * i0, cb0 = wb0 * i0, ca1 = wa1 * i1, cb1 = wb1 * i1;
#pragma unroll
            for (int up = 0; up < 2; ++up)
#pragma unroll
                for (int qt = 0; qt < 2; ++qt) {
                    const f32x4 pa = *(const LAS f32x4*)(xi + ((2 * up) * 2 + qt) * 1024 + l * 16), pb = *(const LAS f32x4*)(xi + ((2 * up + 1) * 2 + qt) * 1024 + l * 16);
                    const f32x4 oa = sel(grp == 0, r.o[2 * up][qt], r.o[4 + 2 * up][qt]), ob = sel(grp == 0, r.o[2 * up + 1][qt], r.o[4 + 2 * up + 1][qt]);
                    const float ca = qt ? ca1 : ca0, cb = qt ? cb1 : cb0;
                    st16_sc1(sws, pack8(oa * ca + pa * cb, ob * ca + pb * cb), vl16, OFF_CTX + ((8 * (15 - j) + 2 * qg + qt) * 128 + 4 * h + 2 * grp + up) * 1024);
                }
        }
    }
    STAMP(8);
    {
        asm volatile("s_waitcnt lgkmcnt(0)" ::: "memory"); BARRIER();
        p4_piece<0>(c4, 0); p4_piece<1>(c4, 0); p4_piece<2>(c4, 0); p4_piece<3>(c4, 0); p4_piece<4>(c4, 0);
        p4_piece<0>(c4, P4STG); p4_piece<1>(c4, P4STG); p4_piece<2>(c4, P4STG);
        const int l = lane_id();
        const int rbk = (xcd & 1) * 8 + (bi >> 2), cb = bi & 3;
        const unsigned cidx = FL_CTX + (rbk < 8 ? rbk : 15 - rbk) * 32 + 8 * c4.q + (l & 7);
        f32x4 accg[10], accs[10], gt[3];
#pragma unroll
        for (int i = 0; i < 10; ++i) { accg[i] = (f32x4){0.f, 0.f, 0.f, 0.f}; accs[i] = (f32x4){0.f, 0.f, 0.f, 0.f}; PIN(accg[i]); PIN(accs[i]); }
        P4Regs r4;
        P4Xch xg;
        const int row0 = 128 * rbk + 32 * (w >> 1) + (l & 15), col0 = 160 * cb + 80 * (w & 1) + 4 * (l >> 4);
        u32x2 xv[3];
        {
            const rsrc_t sx = mk_rsrc(x, 2048 * 640 * 2);
#pragma unroll
            for (int k = 0; k < 3; ++k) { const int i = c4.o + 4 * k < 10 ? c4.o + 4 * k : c4.o;
                asm volatile("buffer_load_dwordx2 %0, %1, %2, 0 offen" : "=v"(xv[k]) : "v"((unsigned)((row0 + (i & 1) * 16) * 640 + col0 + (i >> 1) * 16) * 2), "s"(sx) : "memory"); }
        }
        u64 seen = 0;
        asm volatile("s_waitcnt vmcnt(18)" ::: "memory"); BARRIER();
        unsigned sb = 2 * P4STG, sp = P4STG;
        p4_rd<0, 0, 0>(c4, r4, sb); p4_rd<0, 0, 1>(c4, r4, sb); p4_rd<0, 0, 2>(c4, r4, sb); p4_rd<0, 0, 3>(c4, r4, sb); p4_rd<0, 0, 4>(c4, r4, sb); p4_rd<0, 0, 5>(c4, r4, sb); p4_rd<0, 0, 6>(c4, r4, sb);
        SBAR();
        STAMP(9);
        p4_step<15, true, true, false, 0, -1, -1, -1>(c4, y, r4, xg, accg, sb, sp, 0, cidx, seen);
#pragma nounroll
        for (int t = 1; t < c4.ng; ++t) p4_step<8, true, true, false, 1, -1, -1, -1>(c4, y, r4, xg, accg, sb, sp, t, cidx, seen);
        p4_split(c4, accg, xg);
        p4_step<10, true, true, false, 0, 0, -1, -1>(c4, y, r4, xg, accs, sb, sp, 0, 0, seen);
        p4_step<13, true, true, false, 0, 3, -1, -1>(c4, y, r4, xg, accs, sb, sp, 0, 0, seen);
        p4_step<15, true, true, false, 0, 6, -1, -1>(c4, y, r4, xg, accs, sb, sp, 0, 0, seen);
        p4_step<13, true, true, false, 0, -1, -1, -1>(c4, y, r4, xg, accs, sb, sp, 0, 0, seen);
        p4_step<10, true, true, false, 0, -1, -1, -1>(c4, y, r4, xg, accs, sb, sp, 0, 0, seen);
#pragma nounroll
        for (int t = 5; t < 12; ++t) p4_step<8, true, true, false, 0, -1, -1, -1>(c4, y, r4, xg, accs, sb, sp, t, 0, seen);
        p4_step<8, true, false, false, 0, -1, -1, 0>(c4, y, r4, xg, accs, sb, sp, 0, 0, seen);
        p4_step<13, false, false, false, 0, -1, 5, -1>(c4, y, r4, xg, accs, sb, sp, 0, 0, seen);
        p4_step<9, false, false, false, 0, -1, -1, -1>(c4, y, r4, xg, accs, sb, sp, 0, 0, seen);
        p4_step<0, false, false, true, 0, -1, -1, -1>(c4, y, r4, xg, accs, sb, sp, 0, 0, seen);
        asm volatile("s_waitcnt vmcnt(0)" : "+v"(xg.rcv[0]), "+v"(xg.rcv[1]), "+v"(xg.rcv[2]), "+v"(xg.rcv[3]), "+v"(xg.rcv[4]), "+v"(xg.rcv[5]), "+v"(xg.rcv[6]), "+v"(xg.rcv[7]), "+v"(xg.rcv[8]),
                     "+v"(xv[0]), "+v"(xv[1]), "+v"(xv[2]) :: "memory");
        constexpr auto nine = std::make_integer_sequence<int, 9>{};
        P4Xch xs;
        p4_split(c4, accs, xs);
        p4_put<0>(c4, xs, OFF_XS); p4_put<1>(c4, xs, OFF_XS); p4_put<2>(c4, xs, OFF_XS); p4_put<3>(c4, xs, OFF_XS); p4_put<4>(c4, xs, OFF_XS);
        p4_put<5>(c4, xs, OFF_XS); p4_put<6>(c4, xs, OFF_XS); p4_put<7>(c4, xs, OFF_XS); p4_put<8>(c4, xs, OFF_XS);
        TS(0);
        if (!p4_tagged(c4, xg)) p4_refetch(c4, xg, OFF_XG, nine);
#pragma unroll
        for (int k = 0; k < 3; ++k) {
            gt[k] = p4_total(xg, k);
#pragma unroll
            for (int e = 0; e < 4; ++e) gt[k][e] = gt[k][e] * __builtin_amdgcn_rcpf(1.f + __builtin_amdgcn_exp2f(-1.4426950408889634f * gt[k][e]));
            PIN(gt[k]);
        }
        TS(1);
        p4_refetch(c4, xs, OFF_XS, nine);
        TS(2);
#pragma unroll
        for (int k = 0; k < 3; ++k) {
            if (k < 2 || c4.o < 2) { const int i = c4.o + 4 * k;
                const f32x4 sv = p4_total(xs, k);
                const f32x4 o = {bflo(xv[k][0]) + sv[0] * gt[k][0], bfhi(xv[k][0]) + sv[1] * gt[k][1], bflo(xv[k][1]) + sv[2] * gt[k][2], bfhi(xv[k][1]) + sv[3] * gt[k][3]};
                *(u32x2*)(out + (row0 + (i & 1) * 16) * 640 + col0 + (i >> 1) * 16) = (u32x2){cvtpk(o[0], o[1]), cvtpk(o[2], o[3])}; }
        }
        if (b == 0 && w == 0 && l == 0) __hip_atomic_store(y.flags + 4, y.token, __ATOMIC_RELAXED, __HIP_MEMORY_SCOPE_AGENT);
    }
    STAMP(11);
}

extern "C" void kernel_launch(void* const* d_in, const int* in_sizes, int n_in, void* d_out, int out_size, void* d_ws, size_t ws_size, hipStream_t stream) {
    (void)in_sizes; (void)n_in; (void)out_size; (void)ws_size;
    fused_kernel<<<256, 512, 0, stream>>>((const bf16_t*)d_in[0], (const bf16_t*)d_in[1], (const bf16_t*)d_in[2], (const bf16_t*)d_in[3], (const bf16_t*)d_in[4],
                                          (const bf16_t*)d_in[5], (const bf16_t*)d_in[7], (const bf16_t*)d_in[8], (const bf16_t*)d_in[9], (bf16_t*)d_out, (char*)d_ws);
}
```

```cpp
#include <hip/hip_runtime.h>
#include <stdint.h>
#include <utility>

typedef unsigned short bf16_t;
typedef short bf16x8 __attribute__((ext_vector_type(8)));
typedef short s16x4 __attribute__((ext_vector_type(4)));
typedef short v4i16_t __attribute__((ext_vector_type(4)));
typedef float f32x4 __attribute__((ext_vector_type(4)));
typedef float f32x2 __attribute__((ext_vector_type(2)));
typedef unsigned u32x4 __attribute__((ext_vector_type(4)));
typedef unsigned u32x2 __attribute__((ext_vector_type(2)));
typedef __bf16 bf16x2_t __attribute__((ext_vector_type(2)));
typedef __amdgpu_buffer_rsrc_t rsrc_t;
typedef unsigned long long u64;
#define LAS __attribute__((address_space(3)))
#define GAS __attribute__((address_space(1)))
typedef LAS char* lptr;
typedef LAS const char* lcptr;
typedef GAS u64 gu64;
#define DI __device__ __forceinline__
#define SBAR() __builtin_amdgcn_sched_barrier(0)
#define PIN(x) asm volatile("" : "+v"(x))
#define MF16(a, b, c) __builtin_amdgcn_mfma_f32_16x16x32_bf16(a, b, c, 0, 0, 0)
#define WAITV(n) asm volatile("s_waitcnt vmcnt(" #n ")" ::: "memory")
#define WAITL(n) asm volatile("s_waitcnt lgkmcnt(" #n ")" ::: "memory")
#define BARRIER() do { asm volatile("" ::: "memory"); __builtin_amdgcn_s_barrier(); asm volatile("" ::: "memory"); } while (0)

#define UPTO 4
#define STAG 1
#define SPREAD 1
#define SPOS 1
#define XST 0
#define STAMPS 0

constexpr unsigned OFF_FLAGS = 0;
constexpr unsigned OFF_XN = 65536;
constexpr unsigned OFF_K = OFF_XN + 2621440;
constexpr unsigned OFF_V = OFF_K + 16777216;
constexpr unsigned OFF_ACT = OFF_V + 16777216 + 8388608;
constexpr unsigned OFF_CTX = OFF_ACT + 7077888;
constexpr unsigned OFF_WP = OFF_CTX + 16777216;
constexpr unsigned OFF_ST = OFF_WP + 182 * 40 * 1024;
constexpr unsigned OFF_XG = OFF_ST + 65536;
constexpr unsigned OFF_XS = OFF_XG + 25165824;
constexpr unsigned WS_BYTES = OFF_XS + 25165824;
constexpr unsigned FL_P0 = 0, FL_KV = 256, FL_ACT = 512, FL_CTX = 768;
constexpr int SC1 = 16;

DI unsigned cvtpk(float lo, float hi) { f32x2 v = {lo, hi}; bf16x2_t b = __builtin_convertvector(v, bf16x2_t); return __builtin_bit_cast(unsigned, b); }
DI u32x4 pack8(f32x4 a, f32x4 b) { u32x4 w = {cvtpk(a[0], a[1]), cvtpk(a[2], a[3]), cvtpk(b[0], b[1]), cvtpk(b[2], b[3])}; return w; }
DI float bflo(unsigned u) { return __uint_as_float(u << 16); }
DI float bfhi(unsigned u) { return __uint_as_float(u & 0xffff0000u); }
DI s16x4 vtr(lcptr p) { return __builtin_bit_cast(s16x4, __builtin_amdgcn_ds_read_tr16_b64_v4i16((LAS v4i16_t*)p)); }
DI bf16x8 cat(s16x4 a, s16x4 b) { return (bf16x8){a[0], a[1], a[2], a[3], b[0], b[1], b[2], b[3]}; }
DI bf16x8 ld128(lcptr p) { return *(const LAS bf16x8*)p; }
DI bf16x8 asbf(u32x4 v) { return __builtin_bit_cast(bf16x8, v); }
DI rsrc_t mk_rsrc(const void* p, unsigned bytes) { return __builtin_amdgcn_make_buffer_rsrc((void*)p, (short)0, (int)bytes, 0x00020000); }
DI void dma16(rsrc_t r, unsigned voff, unsigned soff, unsigned ldsoff) {
    asm volatile("s_mov_b32 m0, %0\n\ts_nop 3\n\tbuffer_load_dwordx4 %1, %2, %3 offen lds" ::"s"(ldsoff), "v"(voff), "s"(r), "s"(soff) : "memory");
}
DI void dma16x4(rsrc_t r, unsigned voff, unsigned soff, unsigned ldsoff) {
    asm volatile("s_mov_b32 m0, %0\n\ts_nop 3\n\tbuffer_load_dwordx4 %1, %2, %3 offen lds\n\tbuffer_load_dwordx4 %1, %2, %3 offen offset:1024 lds\n\t"
                 "buffer_load_dwordx4 %1, %2, %3 offen offset:2048 lds\n\tbuffer_load_dwordx4 %1, %2, %3 offen offset:3072 lds" ::"s"(ldsoff), "v"(voff), "s"(r), "s"(soff) : "memory");
}
DI void st16_sc1(rsrc_t r, u32x4 v, unsigned voff, unsigned soff) { asm volatile("s_nop 4\n\tbuffer_store_dwordx4 %0, %1, %2, %3 offen sc1\n\ts_nop 1" ::"v"(v), "v"(voff), "s"(r), "s"(soff) : "memory"); }
template <int OFF> DI void st16_sc1o(rsrc_t r, u32x4 v, unsigned voff, unsigned soff) { asm volatile("s_nop 4\n\tbuffer_store_dwordx4 %0, %1, %2, %3 offen offset:%4 sc1\n\ts_nop 1" ::"v"(v), "v"(voff), "s"(r), "s"(soff), "i"(OFF) : "memory"); }
DI u32x4 ld16(rsrc_t r, unsigned voff, unsigned soff) { return __builtin_amdgcn_raw_buffer_load_b128(r, voff, soff, 0); }
#define P4AUX SC1
DI u32x4 ld16_sc1(rsrc_t r, unsigned voff, unsigned soff) { return __builtin_amdgcn_raw_buffer_load_b128(r, voff, soff, P4AUX); }
template <class T> DI T sel(bool c, T a, T b) { return c ? a : b; }
DI int lane_id() { int l; asm volatile("v_mbcnt_lo_u32_b32 %0, -1, 0\n\tv_mbcnt_hi_u32_b32 %0, -1, %0" : "=v"(l)); return l; }
DI void set_flag(gu64* flags, unsigned idx, u64 token) { __hip_atomic_store(flags + idx * 8, token, __ATOMIC_RELAXED, __HIP_MEMORY_SCOPE_AGENT); }
DI u64 poll_flag(gu64* flags, unsigned idx) { return __hip_atomic_load(flags + idx * 8, __ATOMIC_RELAXED, __HIP_MEMORY_SCOPE_AGENT); }
DI void spin_flags(gu64* flags, unsigned idx, u64 token) {
    const unsigned long long t0 = __builtin_amdgcn_s_memrealtime();
    for (;;) {
        __builtin_amdgcn_s_sleep(1);
        const u64 v = poll_flag(flags, idx);
        if (__all(v == token)) break;
        if (__builtin_amdgcn_s_memrealtime() - t0 > 2000000ull) break;
    }
}
DI u64 poll_asm(gu64* flags, unsigned idx) { u64 v; asm volatile("global_load_dwordx2 %0, %1, off sc1" : "=v"(v) : "v"(flags + idx * 8) : "memory"); return v; }
DI void check_flags(gu64* flags, unsigned idx, u64 seen, u64 token) { if (!__all(seen == token)) spin_flags(flags, idx, token); }
DI void acquire_issue() { asm volatile("buffer_inv sc1" ::: "memory"); }
DI u64 uni64(u64 v) { return ((u64)(unsigned)__builtin_amdgcn_readfirstlane((unsigned)(v >> 32)) << 32) | (unsigned)__builtin_amdgcn_readfirstlane((unsigned)v); }
#define SCLK __builtin_amdgcn_s_memrealtime
#define STAMP(i) do { } while (0)
#define STAMPW(i, wv) do { } while (0)
#define CSTAMP(i) do { } while (0)

#define TS(i) do { } while (0)
#define TSARG
#define TSPASS
struct Sync { gu64* flags; u64 token; int b, h, j, w; };
#define YSTAMP(i) do { } while (0)

struct P2Ctx { rsrc_t sws, swq, sw1; unsigned vl16, vwq, vw1; unsigned sxo, swqo, sw1o, skv; int w; lcptr xb; lcptr wq[4]; lcptr wf[2]; };
DI void p2_dma1(const P2Ctx& c, int st, int ks, int i) {
    if (i < 4) dma16(c.sws, c.vl16, c.sxo + (i >> 1) * 20480 + (i & 1) * 1024 + ks * 2048, st * 32768 + (4 * c.w + i) * 1024);
    else if (i < 6) dma16(c.swq, c.vwq, c.swqo + (i - 4) * 128 + ks * 524288, 65536 + st * 24576 + (i - 4) * 8192 + c.w * 1024);
    else dma16(c.sw1, c.vw1, c.sw1o + ks * (64 * 3456), 65536 + st * 24576 + 16384 + c.w * 1024);
}

struct P1Ctx { rsrc_t sx, sw, sws; unsigned vx[2], vw[2]; unsigned sxo, swo; int w; lcptr xb[2]; lcptr wb[4]; const bf16_t *wo, *w2, *wq, *w1; lcptr lds; };
DI void pack_fetch(const P1Ctx& c, int b) {
    const int l = lane_id(), gw = b * 8 + c.w, pu = gw / 10, pq = gw - pu * 10;
    const rsrc_t sp = mk_rsrc(pu < 128 ? c.wo : c.w2, pu < 128 ? 4096 * 640 * 2 : 1728 * 640 * 2);
    const unsigned so = gw < 1820 ? (unsigned)((pu < 128 ? pu : pu - 128) * 32 * 1280 + pq * 128) : 0xF0000000u;
#pragma unroll
    for (int i = 0; i < 4; ++i) dma16(sp, (l >> 3) * 1280 + (l & 7) * 16, so + i * 8 * 1280, 131072 + c.w * 4096 + i * 1024);
}
DI void pack_store(const P1Ctx& c, int b) {
    const int l = lane_id(), g = l >> 4, q = (l & 15) >> 2, gw = b * 8 + c.w, pu = gw / 10, pq = gw - pu * 10;
    lcptr p = c.lds + 131072 + c.w * 4096 + (4 * g + q) * 128 + (l & 3) * 8;
    bf16x8 fr[4];
#pragma unroll
    for (int i = 0; i < 4; ++i) fr[i] = cat(vtr(p + i * 32), vtr(p + i * 32 + 16 * 128));
    if (gw < 1820) {
#pragma unroll
        for (int i = 0; i < 4; ++i) st16_sc1(c.sws, __builtin_bit_cast(u32x4, fr[i]), l * 16, OFF_WP + (pu * 40 + pq * 4 + i) * 1024);
    }
}
DI void p1_dma1(const P1Ctx& c, int st, int ks, int i) {
    if (i < 4) dma16(c.sx, c.vx[i & 1], c.sxo + i * 8192 + ks * 128, st * 32768 + (4 * c.w + i) * 1024);
    else dma16(c.sw, c.vw[i & 1], c.swo + (i - 4) * 65536 + ks * 524288, 65536 + st * 32768 + (4 * c.w + (i - 4)) * 1024);
}
DI void p1_dma(const P1Ctx& c, int st, int ks) {
#pragma unroll
    for (int i = 0; i < 8; ++i) p1_dma1(c, st, ks, i);
}
template <int ST, int UP, int I> DI void p1_rdw(const P1Ctx& c, bf16x8 (&wf)[4], s16x4 (&wl)[4]) {
    constexpr int nt = I >> 1, rd = I & 1;
    const s16x4 v = vtr(c.wb[nt] + ST * 32768 + UP * 4096 + rd * 512);
    if constexpr (rd == 0) wl[nt] = v; else wf[nt] = cat(wl[nt], v);
}
template <int ST, int G> DI bf16x8 p1_rdx(const P1Ctx& c) { return ld128(c.xb[G >> 3] + ST * 32768 + (G & 7) * 2048); }
template <int ST, int KSC, int K> DI bool p1_is(int ks) { if constexpr (KSC >= 0) return KSC == K; else if constexpr ((K & 1) != ST || K > 5) return false; else return ks == K; }
template <bool ISV, int ST, int KSC, int G> DI void p1_group(const P1Ctx& c, const P2Ctx& c2, const Sync& y, const int ks, u64& seen, unsigned& sink, f32x4 (&acc)[4][8], bf16x8 (&wA)[4], bf16x8 (&wB)[4], s16x4 (&wl)[4], bf16x8 (&xr)[4]) {
    constexpr int up = G >> 3, mt = G & 7;
    constexpr bool NEXT = KSC != 7;
    if constexpr (G == 13) {
        if (p1_is<ST, KSC, 0>(ks)) asm volatile("s_waitcnt vmcnt(2) lgkmcnt(0)" ::: "memory");
        else if (p1_is<ST, KSC, 1>(ks) || p1_is<ST, KSC, 3>(ks)) asm volatile("s_waitcnt vmcnt(1) lgkmcnt(0)" ::: "memory");
        else if (p1_is<ST, KSC, 5>(ks) || p1_is<ST, KSC, 7>(ks)) asm volatile("s_waitcnt vmcnt(4) lgkmcnt(0)" ::: "memory");
        else asm volatile("s_waitcnt vmcnt(0) lgkmcnt(0)" ::: "memory");
        if constexpr (ST == 0) asm volatile("" :: "v"(sink));
        BARRIER();
        if constexpr (!ISV) {
            if (p1_is<ST, KSC, 0>(ks)) { if (y.w == 0 && lane_id() == 0) set_flag(y.flags, FL_P0 + y.b, y.token); }
            if (p1_is<ST, KSC, 4>(ks)) { if (y.w == 0) { const int l = lane_id() & 31; check_flags(y.flags, FL_P0 + (l < 16 ? 16 * y.j + l : 16 * (15 - y.j) + (l - 16)), seen, y.token); } }
        }
        SBAR();
    }
    const bf16x8 x = xr[G & 3];
#pragma unroll
    for (int nt = 0; nt < 4; ++nt) {
        const bf16x8 wv = up == 0 ? wA[nt] : wB[nt];
        if constexpr (ISV) acc[nt][mt] = MF16(x, wv, acc[nt][mt]); else acc[nt][mt] = MF16(wv, x, acc[nt][mt]);
        if (nt == 0) {
            if constexpr (G + 3 < 16) xr[(G + 3) & 3] = p1_rdx<ST, G + 3>(c);
            else if constexpr (NEXT) xr[(G + 3) & 3] = p1_rdx<ST ^ 1, G + 3 - 16>(c);
        }
        if constexpr (G >= 2 && G < 6) { if (nt == 1) p1_rdw<ST, 1, 2 * (G - 2)>(c, wB, wl); if (nt == 2) p1_rdw<ST, 1, 2 * (G - 2) + 1>(c, wB, wl); }
        if constexpr (G >= 13 && NEXT) {
            if (nt == 1) p1_rdw<ST ^ 1, 0, 3 * (G - 13)>(c, wA, wl);
            if (nt == 2) p1_rdw<ST ^ 1, 0, 3 * (G - 13) + 1>(c, wA, wl);
            if constexpr (G < 15) { if (nt == 3) p1_rdw<ST ^ 1, 0, 3 * (G - 13) + 2>(c, wA, wl); }
        }
        if (nt == SPOS) {
            if constexpr (G >= 13) { constexpr int p = G - 13;
                if constexpr (KSC < 0) p1_dma1(c, ST, ks + 2, p);
                else if constexpr (KSC == 6) p2_dma1(c2, 0, 0, p);
                else p2_dma1(c2, 1, 1, p);
            } else if constexpr (G <= 4) { constexpr int p = G + 3;
                if constexpr (KSC == 7) { if constexpr (p < 7) p2_dma1(c2, 0, 0, p); }
                else { if (!p1_is<ST, KSC, 0>(ks)) p1_dma1(c, ST ^ 1, ks + 1, p); }
            }
        }
        SBAR();
    }
    if constexpr (G == 6) {
        if (p1_is<ST, KSC, 1>(ks)) {
            const int l = lane_id(), L = (c.w & 3) * 64 + l;
            const char* pb = (L >= 160 && L < 240 && y.h < 27) ? (const char*)c.w1 + (size_t)(80 * y.j + L - 160) * 3456 + y.h * 128
                                                               : (const char*)c.wq + (size_t)(80 * y.j + ((L < 160 ? L : 0) >> 1)) * 8192 + y.h * 256 + (L & 1) * 128;
            asm volatile("global_load_dword %0, %1, off" : "+v"(sink) : "v"(pb) : "memory");
        }
        if (p1_is<ST, KSC, 3>(ks)) {
            const int l = lane_id(), gw = y.b * 8 + c.w < 1820 ? y.b * 8 + c.w : 0, pu = gw / 10, pq = gw - pu * 10;
            const char* pc = (const char*)(pu < 128 ? c.wo : c.w2) + (size_t)((pu < 128 ? pu : pu - 128) * 32 + (l & 31)) * 1280 + pq * 128;
            asm volatile("global_load_dword %0, %1, off" : "+v"(sink) : "v"(pc) : "memory");
        }
        if constexpr (!ISV) { if (p1_is<ST, KSC, 4>(ks)) { if (y.w == 0) { const int l = lane_id() & 31; seen = poll_flag(y.flags, FL_P0 + (l < 16 ? 16 * y.j + l : 16 * (15 - y.j) + (l - 16))); } } }
        if (p1_is<ST, KSC, 5>(ks)) { pack_fetch(c, y.b); if constexpr (!ISV) { if (y.w == 0) acquire_issue(); } }
        if constexpr (KSC == 7) pack_store(c, y.b);
        SBAR();
    }
}
template <bool ISV, int ST, int KSC, int... Gs> DI void p1_step(const P1Ctx& c, const P2Ctx& c2, const Sync& y, const int ks, u64& seen, unsigned& sink, f32x4 (&acc)[4][8], bf16x8 (&wA)[4], bf16x8 (&wB)[4], s16x4 (&wl)[4], bf16x8 (&xr)[4], std::integer_sequence<int, Gs...>) {
    (p1_group<ISV, ST, KSC, Gs>(c, c2, y, ks, seen, sink, acc, wA, wB, wl, xr), ...);
}
template <bool ISV> DI void p1_main(const P1Ctx& c, const P2Ctx& c2, const Sync& y, unsigned& sink, u32x4 (&kv)[16]) {
    f32x4 acc[4][8];
#pragma unroll
    for (int a = 0; a < 4; ++a)
#pragma unroll
        for (int b = 0; b < 8; ++b) acc[a][b] = (f32x4){0.f, 0.f, 0.f, 0.f};
    bf16x8 wA[4], wB[4], xr[4]; s16x4 wl[4];
    u64 seen = 0;
    asm volatile("" ::: "memory");
    p1_rdw<0, 0, 0>(c, wA, wl); p1_rdw<0, 0, 1>(c, wA, wl); p1_rdw<0, 0, 2>(c, wA, wl); p1_rdw<0, 0, 3>(c, wA, wl);
    p1_rdw<0, 0, 4>(c, wA, wl); p1_rdw<0, 0, 5>(c, wA, wl); p1_rdw<0, 0, 6>(c, wA, wl); p1_rdw<0, 0, 7>(c, wA, wl);
    xr[0] = p1_rdx<0, 0>(c); xr[1] = p1_rdx<0, 1>(c); xr[2] = p1_rdx<0, 2>(c);
    SBAR();
    constexpr auto seq = std::make_integer_sequence<int, 16>{};
#pragma nounroll
    for (int ks = 0; ks < 6; ks += 2) {
        p1_step<ISV, 0, -1>(c, c2, y, ks, seen, sink, acc, wA, wB, wl, xr, seq);
        p1_step<ISV, 1, -1>(c, c2, y, ks + 1, seen, sink, acc, wA, wB, wl, xr, seq);
    }
    p1_step<ISV, 0, 6>(c, c2, y, 6, seen, sink, acc, wA, wB, wl, xr, seq);
    p1_step<ISV, 1, 7>(c, c2, y, 7, seen, sink, acc, wA, wB, wl, xr, seq);
#pragma unroll
    for (int a = 0; a < 8; ++a)
#pragma unroll
        for (int b = 0; b < 2; ++b) {
            if constexpr (ISV) kv[a * 2 + b] = pack8(acc[(a & 1) * 2 + b][(a >> 1) * 2], acc[(a & 1) * 2 + b][(a >> 1) * 2 + 1]);
            else kv[a * 2 + b] = pack8(acc[2 * b][a], acc[2 * b + 1][a]);
        }
}

template <bool HASF, int ST, int I> DI bf16x8 p2_rdw(const P2Ctx& c) {
    constexpr int NW = HASF ? 6 : 4, up = I / NW, n = I % NW;
    lcptr p = (n < 4 ? c.wq[n & 3] : c.wf[n & 1]) + ST * 24576 + up * 4096;
    return cat(vtr(p), vtr(p + 512));
}
template <int ST, int UP, int MT> DI bf16x8 p2_rdx(const P2Ctx& c) { return ld128(c.xb + ST * 32768 + MT * 2048 + UP * 1024); }
template <int I> DI void p2_kvst(const P2Ctx& c, const u32x4 (&kv)[16], bool isv) {
    st16_sc1(c.sws, kv[I], c.vl16, c.skv + (isv ? (unsigned)(((I >> 3) * 16 + ((I >> 2) & 1) * 8 + (I & 3)) * 1024) : (unsigned)((I >> 1) * 4096 + (I & 1) * 1024)));
}
template <bool HASF, int IV> DI void p2_extras(const P2Ctx& c, const u32x4 (&kv)[16], bool isv) {
    if constexpr (IV < 6) { p2_kvst<4 + 2 * IV>(c, kv, isv); p2_kvst<5 + 2 * IV>(c, kv, isv); }
}
template <bool HASF, int KS, int G> DI void p2_group(const P2Ctx& c, const Sync& y, u64& seen, f32x4 (&acc)[6][4], bf16x8 (&wr)[4], bf16x8 (&x0)[4], bf16x8 (&x1)[4], const u32x4 (&kv)[16], bool isv) {
    constexpr int NW = HASF ? 6 : 4, NG = 2 * NW, ST = KS & 1, up = G / NW, n = G % NW;
    constexpr int NP = HASF ? 7 : 6, BD0 = HASF ? 2 : 0;
    constexpr bool LAST = (KS == 9);
    if constexpr (G == NG - 3) {
        if constexpr (KS == 0 && !SPREAD) asm volatile("s_waitcnt vmcnt(4) lgkmcnt(0)" ::: "memory");
        else if constexpr (KS == 0) asm volatile("s_waitcnt vmcnt(0) lgkmcnt(0)" ::: "memory");
        else if constexpr (KS <= 6) asm volatile("s_waitcnt vmcnt(2) lgkmcnt(0)" ::: "memory");
        else asm volatile("s_waitcnt vmcnt(0) lgkmcnt(0)" ::: "memory");
        BARRIER();
        if constexpr (KS == 7) { if (y.w == 0 && lane_id() == 0) set_flag(y.flags, FL_KV + y.h * 8 + y.j, y.token); }
        if constexpr (KS == 8) { if (y.w == 0) seen = poll_flag(y.flags, FL_KV + y.h * 8 + (lane_id() & 7)); }
        if constexpr (KS == 9) { if (y.w == 0) { check_flags(y.flags, FL_KV + y.h * 8 + (lane_id() & 7), seen, y.token); acquire_issue(); } }
        SBAR();
    }
    const bf16x8 wcur = wr[G & 3];
#pragma unroll
    for (int mt = 0; mt < 4; ++mt) {
        acc[n][mt] = MF16(wcur, up == 0 ? x0[mt] : x1[mt], acc[n][mt]);
        if (mt == 0) {
            if constexpr (G + 3 < NG) wr[(G + 3) & 3] = p2_rdw<HASF, ST, G + 3>(c);
            else if constexpr (!LAST) wr[(G + 3) & 3] = p2_rdw<HASF, ST ^ 1, G + 3 - NG>(c);
        }
        if (mt == 1) {
            if constexpr (G < 4) x1[G] = p2_rdx<ST, 1, G>(c);
            else if constexpr (G >= NG - 3 && !LAST) x0[G - (NG - 3)] = p2_rdx<ST ^ 1, 0, G - (NG - 3)>(c);
        }
        if (mt == 2) { if constexpr (G == NG - 1 && !LAST) x0[3] = p2_rdx<ST ^ 1, 0, 3>(c); }
        if (mt == SPOS) {
            if constexpr (G >= NG - 3) { if constexpr (KS + 2 < 10) p2_dma1(c, ST, KS + 2, G - (NG - 3)); }
            else if constexpr (G + 3 < NP) { if constexpr (KS + 1 < 10) p2_dma1(c, ST ^ 1, KS + 1, G + 3); }
        }
        SBAR();
    }
    if constexpr (G == NP - 3 && KS >= 1) { p2_extras<HASF, KS - 1>(c, kv, isv); SBAR(); }
}
template <bool HASF, int KS, int... Gs> DI void p2_step(const P2Ctx& c, const Sync& y, u64& seen, f32x4 (&acc)[6][4], bf16x8 (&wr)[4], bf16x8 (&x0)[4], bf16x8 (&x1)[4], const u32x4 (&kv)[16], bool isv, std::integer_sequence<int, Gs...>) {
    (p2_group<HASF, KS, Gs>(c, y, seen, acc, wr, x0, x1, kv, isv), ...);
}
template <bool HASF, int... KSs> DI void p2_main(const P2Ctx& c, const Sync& y, f32x4 (&acc)[6][4], const u32x4 (&kv)[16], bool isv, std::integer_sequence<int, KSs...>) {
    bf16x8 wr[4], x0[4], x1[4];
    u64 seen = 0;
    asm volatile("" ::: "memory");
    wr[0] = p2_rdw<HASF, 0, 0>(c); wr[1] = p2_rdw<HASF, 0, 1>(c); wr[2] = p2_rdw<HASF, 0, 2>(c);
    x0[0] = p2_rdx<0, 0, 0>(c); x0[1] = p2_rdx<0, 0, 1>(c); x0[2] = p2_rdx<0, 0, 2>(c); x0[3] = p2_rdx<0, 0, 3>(c);
    SBAR();
    (p2_step<HASF, KSs>(c, y, seen, acc, wr, x0, x1, kv, isv, std::make_integer_sequence<int, (HASF ? 12 : 8)>{}), ...);
}
DI float mish(float h) {
    const float t = __builtin_amdgcn_exp2f(__builtin_fminf(h, 20.f) * 1.4426950408889634f);
    const float n = t * (t + 2.f);
    return h * n * __builtin_amdgcn_rcpf(n + 2.f);
}

constexpr int PD = 8;
#define PRIOM 1
#define PRIOV 0
#define ADDMODE 1
#define ABL_NOEARLY 0
#define APRE_EARLY 1
#define LATE_STEP 7
#define LATE_OFF 2
struct AttnRegs { f32x4 o[8][2]; f32x4 s[4][2]; bf16x8 q[2][4]; u32x4 pk[2][2]; f32x4 negm[2]; bf16x8 op[32]; };
template <int I> DI void a_read(AttnRegs& r, lcptr vp, lcptr kp) {
    if constexpr (I < 16) r.op[I] = ld128(vp + I * 1024);
    else if constexpr (I < 32) r.op[I] = ld128(kp + (((I - 16) & 3) * 4 + ((I - 16) >> 2)) * 1024);
}
struct Piece { rsrc_t sws; unsigned vl16, soff, ldso; bool on; };
template <int P, bool COND> DI void piece(const Piece& d) {
    if ((!COND && !ABL_NOEARLY) || (COND && d.on)) asm volatile("s_mov_b32 m0, %0\n\ts_nop 3\n\tbuffer_load_dwordx4 %1, %2, %3 offen offset:%4 lds" ::"s"(d.ldso), "v"(d.vl16), "s"(d.sws), "s"(d.soff), "i"(P * 1024) : "memory");
}
template <int I, int DMA> DI void a_slot(AttnRegs& r, lcptr vp, lcptr kp, const Piece& late) {
    if constexpr (I < 16) { constexpr int ks = I >> 3, dt = I & 7;
        r.o[dt][0] = MF16(r.op[I], asbf(r.pk[0][ks]), r.o[dt][0]); r.o[dt][1] = MF16(r.op[I], asbf(r.pk[1][ks]), r.o[dt][1]); }
    else { constexpr int u = (I - 16) >> 2, kt = (I - 16) & 3;
        r.s[kt][0] = MF16(r.op[I], r.q[0][u], u == 0 ? r.negm[0] : r.s[kt][0]); r.s[kt][1] = MF16(r.op[I], r.q[1][u], u == 0 ? r.negm[1] : r.s[kt][1]); }
    a_read<I + PD>(r, vp, kp);
    SBAR();
    if constexpr (DMA != 0 && (I % LATE_STEP) == LATE_OFF && I / LATE_STEP < 4) { piece<I / LATE_STEP, DMA == 2>(late); SBAR(); }
}
template <int B, int DMA, int... Is> DI void a_run(AttnRegs& r, lcptr vp, lcptr kp, const Piece& late, std::integer_sequence<int, Is...>) { (a_slot<B + Is, DMA>(r, vp, kp, late), ...); }
template <int B, int... Is> DI void a_pre(AttnRegs& r, lcptr vp, lcptr kp, std::integer_sequence<int, Is...>) { (a_read<B + Is>(r, vp, kp), ...); }

template <int Q> DI void a_exp_quarter(AttnRegs& r, float& a0, float& a1) {
    constexpr int ks = Q >> 1, qt = Q & 1;
    float one = 1.0f; asm("" : "+s"(one));
    float e[8];
#pragma unroll
    for (int i = 0; i < 4; ++i) { e[i] = __builtin_amdgcn_exp2f(r.s[2 * ks][qt][i]); e[4 + i] = __builtin_amdgcn_exp2f(r.s[2 * ks + 1][qt][i]); }
#pragma unroll
    for (int i = 0; i < 8; ++i) {
        if (qt == 0) a0 += e[i]; else a1 = __builtin_fmaf(e[i], one, a1);
    }
    r.pk[qt][ks] = (u32x4){cvtpk(e[0], e[1]), cvtpk(e[2], e[3]), cvtpk(e[4], e[5]), cvtpk(e[6], e[7])};
}
template <bool DMA> DI void a_exp_pack(AttnRegs& r, float& a0, float& a1, const Piece& early) {
    a0 = 0.f; a1 = 0.f;
    if constexpr (DMA) { piece<0, false>(early); SBAR(); }
    a_exp_quarter<0>(r, a0, a1);
    if constexpr (DMA) { SBAR(); piece<1, false>(early); SBAR(); }
    a_exp_quarter<1>(r, a0, a1);
    if constexpr (DMA) { SBAR(); piece<2, false>(early); SBAR(); }
    a_exp_quarter<2>(r, a0, a1);
    if constexpr (DMA) { SBAR(); piece<3, false>(early); SBAR(); }
    a_exp_quarter<3>(r, a0, a1);
}
DI float xmax4(float v) {
    const u32x2 a = __builtin_amdgcn_permlane16_swap(__float_as_uint(v), __float_as_uint(v), false, false);
    v = __builtin_fmaxf(__uint_as_float(a[0]), __uint_as_float(a[1]));
    const u32x2 c = __builtin_amdgcn_permlane32_swap(__float_as_uint(v), __float_as_uint(v), false, false);
    return __builtin_fmaxf(__uint_as_float(c[0]), __uint_as_float(c[1]));
}
DI float xsum4(float v) {
    const u32x2 a = __builtin_amdgcn_permlane16_swap(__float_as_uint(v), __float_as_uint(v), false, false);
    v = __uint_as_float(a[0]) + __uint_as_float(a[1]);
    const u32x2 c = __builtin_amdgcn_permlane32_swap(__float_as_uint(v), __float_as_uint(v), false, false);
    return __uint_as_float(c[0]) + __uint_as_float(c[1]);
}
constexpr float SUM_HI = 1.152921504606846976e18f, SUM_LO = 8.673617379884035e-19f;

struct AttnCtx { rsrc_t sws; unsigned vl16, kbase, vbase, ctxl; int nL, grp, qg, j, rowrel, swap_s, first_s; lcptr kl, vl, ql; };
struct AttnSm { float m0, m1, l0, l1, ll0, ll1; };
DI void a_dmaK(const AttnCtx& c, int n) { dma16x4(c.sws, c.vl16, c.kbase + n * 16384 + c.qg * 4096, (n & 3) * 16384 + c.qg * 4096); }
DI void a_dmaV(const AttnCtx& c, int n) { dma16x4(c.sws, c.vl16, c.vbase + n * 16384 + c.qg * 4096, 65536 + (n & 3) * 16384 + c.qg * 4096); }
#define PT(i) do { } while (0)
#define PARG
#define PPASS
#define PPASSG
DI void a_slotfull(const AttnCtx& c, const Sync& y, AttnRegs& r, AttnSm& z, const int s PARG) {
    const int nL = c.nL, grp = c.grp;
    const bool SWAP = (s == c.swap_s), FIN = (s == c.swap_s + 1), FIRST = (s == c.first_s) || FIN;
    const int tcur = s < nL ? s : 2 * s - nL + grp;
    Piece early, late;
    {
        const int s2 = s + 2, s1 = s + 1;
        const bool eK = grp == 1 && s2 < nL;
        const int te = eK ? s2 : (s1 < nL ? s1 : 2 * s1 - nL + grp), tl = 2 * s2 - nL + grp;
        early.sws = c.sws; early.vl16 = c.vl16; late.sws = c.sws; late.vl16 = c.vl16;
        early.on = eK || (s1 <= 16 && (grp == 0 || s1 >= nL));
        early.soff = (eK ? c.kbase : c.vbase) + te * 16384 + c.qg * 4096; early.ldso = (eK ? 0 : 65536) + (te & 3) * 16384 + c.qg * 4096;
        late.on = s2 <= 16 && s2 >= nL;
        late.soff = c.kbase + tl * 16384 + c.qg * 4096; late.ldso = (tl & 3) * 16384 + c.qg * 4096;
    }
    const int snext = s + 1, tnext = snext < nL ? snext : 2 * snext - nL + grp;
    const lcptr vp = c.vl + (tcur & 3) * 16384, kp = c.kl + (tnext & 3) * 16384;
    u64 seenA = 0;
    if (s == 16) seenA = poll_asm(y.flags, FL_ACT + 32 * y.w + (lane_id() & 31));
    a_pre<0>(r, vp, kp, std::make_integer_sequence<int, PD>{});
    SBAR();
    if (SWAP) {
        asm volatile("ds_read_b128 %0, %8\n\tds_read_b128 %1, %8 offset:1024\n\tds_read_b128 %2, %8 offset:2048\n\tds_read_b128 %3, %8 offset:3072\n\t"
                     "ds_read_b128 %4, %8 offset:4096\n\tds_read_b128 %5, %8 offset:5120\n\tds_read_b128 %6, %8 offset:6144\n\tds_read_b128 %7, %8 offset:7168\n\ts_waitcnt lgkmcnt(0)"
                     : "=&v"(r.q[0][0]), "=&v"(r.q[0][1]), "=&v"(r.q[0][2]), "=&v"(r.q[0][3]), "=&v"(r.q[1][0]), "=&v"(r.q[1][1]), "=&v"(r.q[1][2]), "=&v"(r.q[1][3])
                     : "v"(c.ql) : "memory");
    }
    if (FIN) {
        const float i0 = __builtin_amdgcn_rcpf(xsum4(z.ll0)), i1 = __builtin_amdgcn_rcpf(xsum4(z.ll1));
        const unsigned c1o = c.ctxl + 131072;
        st16_sc1o<0>(c.sws, pack8(r.o[0][0] * i0, r.o[1][0] * i0), c.vl16, c.ctxl); st16_sc1o<1024>(c.sws, pack8(r.o[2][0] * i0, r.o[3][0] * i0), c.vl16, c.ctxl);
        st16_sc1o<2048>(c.sws, pack8(r.o[4][0] * i0, r.o[5][0] * i0), c.vl16, c.ctxl); st16_sc1o<3072>(c.sws, pack8(r.o[6][0] * i0, r.o[7][0] * i0), c.vl16, c.ctxl);
        st16_sc1o<0>(c.sws, pack8(r.o[0][1] * i1, r.o[1][1] * i1), c.vl16, c1o); st16_sc1o<1024>(c.sws, pack8(r.o[2][1] * i1, r.o[3][1] * i1), c.vl16, c1o);
        st16_sc1o<2048>(c.sws, pack8(r.o[4][1] * i1, r.o[5][1] * i1), c.vl16, c1o); st16_sc1o<3072>(c.sws, pack8(r.o[6][1] * i1, r.o[7][1] * i1), c.vl16, c1o);
#pragma unroll
        for (int d = 0; d < 8; ++d) { r.o[d][0] = (f32x4){0.f, 0.f, 0.f, 0.f}; r.o[d][1] = (f32x4){0.f, 0.f, 0.f, 0.f}; }
    }
    {
        const bool heavy = (grp == 1) || (s >= nL);
        const int qb = heavy ? 15 - c.j : c.j, kb = 64 * tcur - 128 * qb - 32 * c.qg;
        if (kb >= -32) {
            const float NI = -__builtin_inff();
            const f32x4 ninf = {NI, NI, NI, NI};
            f32x4 s00 = r.s[0][0], s10 = r.s[1][0], s20 = r.s[2][0], s30 = r.s[3][0], s01 = r.s[0][1], s11 = r.s[1][1], s21 = r.s[2][1], s31 = r.s[3][1];
            const int dq = c.rowrel;
            if (kb > 0) { s00 = ninf; s10 = ninf; s20 = ninf; s30 = ninf; s01 = ninf; s11 = ninf; s21 = ninf; s31 = ninf; }
            else if (kb == 0) {
#pragma unroll
                for (int i = 0; i < 4; ++i) { s00[i] = sel(i > dq, NI, s00[i]); s11[i] = sel(i > dq, NI, s11[i]); }
                s10 = ninf; s20 = ninf; s30 = ninf; s21 = ninf; s31 = ninf;
            } else {
#pragma unroll
                for (int i = 0; i < 4; ++i) { s20[i] = sel(i > dq, NI, s20[i]); s31[i] = sel(i > dq, NI, s31[i]); }
                s30 = ninf;
            }
            r.s[0][0] = s00; r.s[1][0] = s10; r.s[2][0] = s20; r.s[3][0] = s30; r.s[0][1] = s01; r.s[1][1] = s11; r.s[2][1] = s21; r.s[3][1] = s31;
        }
    }
    float a0, a1;
    if (early.on) dma16x4(c.sws, c.vl16, early.soff, early.ldso);
    a_exp_pack<false>(r, a0, a1, early);
    const float lo = FIRST ? SUM_LO : 0.f;
    if (__builtin_expect(__any(!(a0 <= SUM_HI) || !(a1 <= SUM_HI) || a0 < lo || a1 < lo), 0)) {
        float x0 = r.s[0][0][0], x1 = r.s[0][1][0];
#pragma unroll
        for (int kt = 0; kt < 4; ++kt)
#pragma unroll
            for (int i = 0; i < 4; ++i) { x0 = __builtin_fmaxf(x0, r.s[kt][0][i]); x1 = __builtin_fmaxf(x1, r.s[kt][1][i]); }
        x0 = xmax4(x0); x1 = xmax4(x1);
        if (!FIRST) { x0 = __builtin_fmaxf(x0, 0.f); x1 = __builtin_fmaxf(x1, 0.f);
            const float f0 = __builtin_amdgcn_exp2f(-x0), f1 = __builtin_amdgcn_exp2f(-x1);
#pragma unroll
            for (int d = 0; d < 8; ++d) { r.o[d][0] *= f0; r.o[d][1] *= f1; }
            z.l0 *= f0; z.l1 *= f1; }
        z.m0 += x0; z.m1 += x1;
        r.negm[0] = (f32x4){-z.m0, -z.m0, -z.m0, -z.m0}; r.negm[1] = (f32x4){-z.m1, -z.m1, -z.m1, -z.m1};
#pragma unroll
        for (int kt = 0; kt < 4; ++kt) { r.s[kt][0] -= x0; r.s[kt][1] -= x1; }
        a_exp_pack<false>(r, a0, a1, early);
    }
    z.l0 += a0; z.l1 += a1;
    if (SWAP) {
        z.ll0 = z.l0; z.ll1 = z.l1; z.l0 = 0.f; z.l1 = 0.f; z.m0 = 0.f; z.m1 = 0.f;
        r.negm[0] = (f32x4){0.f, 0.f, 0.f, 0.f}; r.negm[1] = (f32x4){0.f, 0.f, 0.f, 0.f};
    }
    SBAR();
    PT(0);
    if (FIN) { if (early.on) WAITV(12); else WAITV(8); } else { if (early.on) WAITV(4); else WAITV(0); }
    if (s == 16) { asm volatile("" : "+v"(seenA)); check_flags(y.flags, FL_ACT + 32 * y.w + (lane_id() & 31), seenA, y.token); }
    PT(1);
    BARRIER(); SBAR();
    PT(2);
    __builtin_amdgcn_s_setprio(PRIOM);
    a_run<0, 2>(r, vp, kp, late, std::make_integer_sequence<int, 16>{});
    if (s < 16) a_run<16, 2>(r, vp, kp, late, std::make_integer_sequence<int, 16>{});
    __builtin_amdgcn_s_setprio(PRIOV);
    SBAR();
    PT(3);
    if (late.on) WAITV(4); else WAITV(0);
    PT(4);
    BARRIER(); SBAR();
    PT(5);
}

struct Lean { unsigned e_soff, e_lds, l_soff, l_lds, voff, koff; };
template <bool LATE> DI void a_slot_lean(const AttnCtx& c, AttnRegs& r, AttnSm& z, const Lean& st PARG) {
    const lcptr vp = c.vl + st.voff, kp = c.kl + st.koff;
    Piece early, late;
    early.sws = c.sws; early.vl16 = c.vl16; early.soff = st.e_soff; early.ldso = st.e_lds; early.on = true;
    late.sws = c.sws; late.vl16 = c.vl16; late.soff = st.l_soff; late.ldso = st.l_lds; late.on = true;
    a_pre<0>(r, vp, kp, std::make_integer_sequence<int, PD>{});
    SBAR();
    float a0, a1;
    a_exp_pack<true>(r, a0, a1, early);
    if (__builtin_expect(__any(!(a0 <= SUM_HI) || !(a1 <= SUM_HI)), 0)) {
        float x0 = 0.f, x1 = 0.f;
#pragma unroll
        for (int kt = 0; kt < 4; ++kt)
#pragma unroll
            for (int i = 0; i < 4; ++i) { x0 = __builtin_fmaxf(x0, r.s[kt][0][i]); x1 = __builtin_fmaxf(x1, r.s[kt][1][i]); }
        x0 = xmax4(x0); x1 = xmax4(x1);
        const float f0 = __builtin_amdgcn_exp2f(-x0), f1 = __builtin_amdgcn_exp2f(-x1);
#pragma unroll
        for (int d = 0; d < 8; ++d) { r.o[d][0] *= f0; r.o[d][1] *= f1; }
        z.l0 *= f0; z.l1 *= f1;
        z.m0 += x0; z.m1 += x1;
        r.negm[0] = (f32x4){-z.m0, -z.m0, -z.m0, -z.m0}; r.negm[1] = (f32x4){-z.m1, -z.m1, -z.m1, -z.m1};
#pragma unroll
        for (int kt = 0; kt < 4; ++kt) { r.s[kt][0] -= x0; r.s[kt][1] -= x1; }
        a_exp_pack<false>(r, a0, a1, early);
    }
    z.l0 += a0; z.l1 += a1;
    SBAR();
    PT(0);
    WAITV(4);
    PT(1);
    BARRIER(); SBAR();
    PT(2);
    __builtin_amdgcn_s_setprio(PRIOM);
    a_run<0, LATE ? 1 : 0>(r, vp, kp, late, std::make_integer_sequence<int, 32>{});
    __builtin_amdgcn_s_setprio(PRIOV);
    SBAR();
    PT(3);
    if constexpr (LATE) WAITV(4); else WAITV(0);
    PT(4);
    BARRIER(); SBAR();
    PT(5);
}

#define P4W0 11
constexpr unsigned P4STG = 36864, P4TAG = 0x3FFu, P4OOB = 0xF0000000u;
struct P4Ctx { rsrc_t sws; unsigned vl16, ga, gw, gas, gws, dw0, dw1, dw2, lw, tag, so[3], so2[3], lo[3], lo2[3]; int w, q, o, ng, ra, rw; lcptr pa, pw; };
struct P4Regs { bf16x8 xa[2][2], wa[2][5]; };
struct P4Xch { f32x4 own[3]; u32x4 snd[9], rcv[9]; };
DI void mfa(f32x4& acc, bf16x8 a, bf16x8 b) { asm volatile("v_mfma_f32_16x16x32_bf16 %0, %1, %2, %0" : "+v"(acc) : "v"(a), "v"(b)); }
template <int OFF> DI void dma_sc1(const P4Ctx& c, unsigned soff, unsigned ldso) {
    asm volatile("s_mov_b32 m0, %0\n\ts_nop 3\n\tbuffer_load_dwordx4 %1, %2, %3 offen offset:%4 sc1 lds" ::"s"(ldso), "v"(c.vl16), "s"(c.sws), "s"(soff), "i"(OFF) : "memory");
}
template <int P> DI void p4_piece(P4Ctx& c, unsigned st) {
    if constexpr (P == 0) dma_sc1<0>(c, c.ga, st + c.w * 2048);
    else if constexpr (P == 1) { dma_sc1<1024>(c, c.ga, st + c.w * 2048); ++c.ra; c.ga = c.ra == c.ng ? c.gas : c.ga + 2048; }
    else if constexpr (P == 2) dma_sc1<0>(c, c.gw + c.dw0, st + c.lw);
    else if constexpr (P == 3) dma_sc1<0>(c, c.gw + c.dw1, st + c.lw + 8192);
    else { if (c.w < 4) dma_sc1<0>(c, c.gw + c.dw2, st + c.lw + 16384); ++c.rw; c.gw = c.rw == c.ng ? c.gws : c.gw + 81920; }
}
template <int B, int UP, int I> DI void p4_rd(const P4Ctx& c, P4Regs& r, unsigned st) {
    if constexpr (I == 0) r.wa[B][0] = ld128(c.pw + st + UP * 10240);
    else if constexpr (I <= 2) r.xa[B][I - 1] = ld128(c.pa + st + (I - 1) * 2048 + UP * 1024);
    else if constexpr (I < 7) r.wa[B][I - 2] = ld128(c.pw + st + UP * 10240 + (I - 2) * 1024);
}
template <int N> DI void p4_put(const P4Ctx& c, const P4Xch& x, unsigned xoff) { st16_sc1o<(N % 3) * 1024>(c.sws, x.snd[N], c.vl16, (N % 3 == 2 ? c.so2[N / 3] : c.so[N / 3]) + xoff); }
template <int N> DI void p4_get(const P4Ctx& c, P4Xch& x, unsigned xoff) {
    asm volatile("buffer_load_dwordx4 %0, %1, %2, %3 offen offset:%4 sc1" : "=v"(x.rcv[N]) : "v"(c.vl16), "s"(c.sws), "s"((N % 3 == 2 ? c.lo2[N / 3] : c.lo[N / 3]) + xoff), "i"((N % 3) * 1024) : "memory");
}
template <int O> DI void p4_split(const f32x4 (&acc)[10], unsigned tag, P4Xch& x) {
#pragma unroll
    for (int k = 0; k < 3; ++k) x.own[k] = acc[O + 4 * k < 10 ? O + 4 * k : O];
#pragma unroll
    for (int n = 0; n < 9; ++n) { const int i = ((O + 1 + n / 3) & 3) + 4 * (n % 3);
        const u32x4 v = __builtin_bit_cast(u32x4, acc[i < 10 ? i : 0]);
#pragma unroll
        for (int e = 0; e < 4; ++e) x.snd[n][e] = (v[e] & ~P4TAG) | tag; }
}
DI void p4_split(const P4Ctx& c, f32x4 (&a)[10], P4Xch& x) {
    asm volatile("s_nop 7\n\ts_nop 7\n\ts_nop 7" : "+v"(a[0]), "+v"(a[1]), "+v"(a[2]), "+v"(a[3]), "+v"(a[4]), "+v"(a[5]), "+v"(a[6]), "+v"(a[7]), "+v"(a[8]), "+v"(a[9]) :: "memory");
    switch (c.o) { case 0: p4_split<0>(a, c.tag, x); break; case 1: p4_split<1>(a, c.tag, x); break; case 2: p4_split<2>(a, c.tag, x); break; default: p4_split<3>(a, c.tag, x); break; }
}
DI bool p4_tagged(const P4Ctx& c, const P4Xch& x) {
    unsigned a = 0, b = 0;
#pragma unroll
    for (int n = 0; n < 9; ++n)
#pragma unroll
        for (int e = 0; e < 4; ++e) { if (n % 3 == 2) b |= x.rcv[n][e] ^ c.tag; else a |= x.rcv[n][e] ^ c.tag; }
    return __all(((a | (c.o < 2 ? b : 0u)) & P4TAG) == 0);
}
template <int... Ns> DI void p4_refetch(const P4Ctx& c, P4Xch& x, unsigned xoff, std::integer_sequence<int, Ns...>) {
    const unsigned long long t0 = __builtin_amdgcn_s_memrealtime();
    for (;;) {
        (p4_get<Ns>(c, x, xoff), ...);
        asm volatile("s_waitcnt vmcnt(0)" : "+v"(x.rcv[0]), "+v"(x.rcv[1]), "+v"(x.rcv[2]), "+v"(x.rcv[3]), "+v"(x.rcv[4]), "+v"(x.rcv[5]), "+v"(x.rcv[6]), "+v"(x.rcv[7]), "+v"(x.rcv[8]) :: "memory");
        if (p4_tagged(c, x)) break;
        if (__builtin_amdgcn_s_memrealtime() - t0 > 2000000ull) break;
        __builtin_amdgcn_s_sleep(1);
    }
}
DI f32x4 p4_untag(u32x4 v) { return __builtin_bit_cast(f32x4, v & ~P4TAG); }
DI f32x4 p4_total(const P4Xch& x, int k) { return (x.own[k] + p4_untag(x.rcv[k])) + (p4_untag(x.rcv[3 + k]) + p4_untag(x.rcv[6 + k])); }
template <int B, bool RD, int DMA, int PUT, int GET, int... Is> DI void p4_half(P4Ctx& c, P4Regs& r, P4Xch& x, f32x4 (&acc)[10], unsigned rs, unsigned ds, std::integer_sequence<int, Is...>) {
    auto one = [&](auto ic) {
        constexpr int I = decltype(ic)::value;
        mfa(acc[I], r.wa[B][I >> 1], r.xa[B][I & 1]);
        if constexpr (RD) p4_rd<B ^ 1, B ^ 1, I>(c, r, rs);
        if constexpr (DMA == 1) { if constexpr (I == 1) p4_piece<0>(c, ds); if constexpr (I == 5) p4_piece<1>(c, ds); if constexpr (I == 9) p4_piece<2>(c, ds); }
        if constexpr (DMA == 2) { if constexpr (I == 3) p4_piece<3>(c, ds); if constexpr (I == 7) p4_piece<4>(c, ds); }
        if constexpr (PUT >= 0 && B == 0 && I == 5) p4_put<PUT>(c, x, OFF_XG);
        if constexpr (GET >= 0 && (I & 1) && GET + (I >> 1) < 9) p4_get<GET + (I >> 1)>(c, x, OFF_XG);
        SBAR();
    };
    (one(std::integral_constant<int, Is>{}), ...);
}
template <int WAITN, bool RA, bool RB, bool LAST, int EV, int PUT, int GETA, int GETB> DI void p4_step(P4Ctx& c, const Sync& y, P4Regs& r, P4Xch& x, f32x4 (&acc)[10], unsigned& sb, unsigned& sp, int t, unsigned fidx, u64& seen) {
    constexpr auto seq = std::make_integer_sequence<int, 10>{};
    const unsigned sn = sb == 3 * P4STG ? 0 : sb + P4STG;
    p4_half<0, true, RA ? 2 : 0, PUT, GETA>(c, r, x, acc, sb, sp, seq);
    if constexpr (!LAST) {
        asm volatile("s_waitcnt vmcnt(%0) lgkmcnt(0)" :: "i"(WAITN) : "memory");
        if constexpr (EV == 1) { if (t == c.ng - 4 && c.w == 0) { asm volatile("s_waitcnt vmcnt(4)" : "+v"(seen) :: "memory"); check_flags(y.flags, fidx, seen, y.token); } }
        BARRIER();
        if constexpr (EV == 1) {
            if (c.w == 0 && t == c.ng - 5) seen = poll_asm(y.flags, fidx);
        }
        if constexpr (EV == 2) { if (c.w == 0 && lane_id() == 0) set_flag(y.flags, FL_CTX + y.j * 32 + y.h, y.token); }
        SBAR();
    }
    p4_half<1, !LAST, RB ? 1 : 0, PUT, GETB>(c, r, x, acc, sn, sb, seq);
    sp = sb; sb = sn;
}

template <int... Is> DI void p4_ssteps(P4Ctx& c, const Sync& y, P4Regs& r, P4Xch& x, f32x4 (&acc)[10], unsigned& sb, unsigned& sp, u64& seen, std::integer_sequence<int, Is...>) {
    (p4_step<8 + (Is >= 2 && Is <= 10) + (Is >= 1 && Is <= 9) + (Is <= 8), true, true, false, 0, (Is <= 8 ? Is : -1), -1, -1>(c, y, r, x, acc, sb, sp, 0, 0, seen), ...);
}

__global__ __launch_bounds__(512, 2) void fused_kernel(const bf16_t* __restrict__ x, const bf16_t* __restrict__ ckv, const bf16_t* __restrict__ wq, const bf16_t* __restrict__ wuk,
                                                       const bf16_t* __restrict__ wuv, const bf16_t* __restrict__ wo, const bf16_t* __restrict__ w1, const bf16_t* __restrict__ b1,
                                                       const bf16_t* __restrict__ w2, bf16_t* __restrict__ out, char* __restrict__ ws) {
    __shared__ __attribute__((aligned(1024))) char smem[163840];
    const lptr lds = (lptr)smem;
    const int b = blockIdx.x, xcd = b & 7, bi = b >> 3, h = xcd * 4 + (bi >> 3), j = bi & 7;
    const int w = __builtin_amdgcn_readfirstlane(threadIdx.x >> 6);
    const rsrc_t sws = mk_rsrc(ws, WS_BYTES);
    STAMP(0);
    Sync y;
    y.flags = (gu64*)(ws + OFF_FLAGS); y.b = b; y.h = h; y.j = j; y.w = w;
    u64 epoch;
    asm volatile("s_load_dwordx2 %0, %1, 0x20" : "=s"(epoch) : "s"(ws) : "memory");
    u32x4 kv[16];
    P1Ctx c1;
    P2Ctx c2;
    const bool isv = (w & 3) >= 2;
    unsigned sink = 0;
    {
        const int l = lane_id(), g = l >> 4, q = (l & 15) >> 2;
        {
            const int nb = w >> 1;
            c1.w = w; c1.sws = sws; c1.wo = wo; c1.w2 = w2; c1.wq = wq; c1.w1 = w1; c1.lds = lds;
            c1.sx = mk_rsrc(ckv, 2048 * 512 * 2);
            c1.sw = mk_rsrc(nb < 2 ? wuk : wuv, 512 * 4096 * 2);
#pragma unroll
            for (int par = 0; par < 2; ++par) {
                c1.vx[par] = (l >> 3) * 1024 + (((l & 7) ^ (4 * par + (l >> 4))) * 16);
                c1.vw[par] = (l >> 3) * 8192 + (((l & 7) ^ ((((l >> 4) & 1) | (par << 1)) << 1)) * 16);
            }
            c1.sxo = (256 * j + 32 * w) * 1024;
            c1.swo = (32 * (w & 1)) * 8192 + (h * 128 + 64 * (nb & 1)) * 2;
            const int wn = w & 3, wm = w >> 2, f = (l & 15) >> 1, s = ((q >> 1) & 1) | ((g & 1) << 1);
            c1.xb[0] = lds + wm * 16384 + (l & 15) * 128 + ((g ^ f) * 16);
            c1.xb[1] = lds + wm * 16384 + (l & 15) * 128 + (((g ^ f) ^ 4) * 16);
#pragma unroll
            for (int nt = 0; nt < 4; ++nt) c1.wb[nt] = lds + 65536 + wn * 8192 + (8 * g + q) * 128 + ((nt ^ s) * 32) + (l & 3) * 8;
        }
        p1_dma(c1, 0, 0);
        u32x4 xo0, xo1;
        {
            const rsrc_t sxi = mk_rsrc(x, 2048 * 640 * 2);
            const unsigned vx = (l & 7) * 1280 + (l >> 3) * 16, so0 = b * 8 * 1280 + w * 128, so1 = b * 8 * 1280 + (8 + (w & 1)) * 128;
            asm volatile("s_nop 4\n\tbuffer_load_dwordx4 %0, %2, %3, %4 offen\n\tbuffer_load_dwordx4 %1, %2, %3, %5 offen" : "=&v"(xo0), "=&v"(xo1) : "v"(vx), "s"(sxi), "s"(so0), "s"(so1) : "memory");
        }
        p1_dma(c1, 1, 1);
        asm volatile("s_waitcnt lgkmcnt(0)" : "+s"(epoch) :: "memory");
        y.token = epoch + 1;
        STAMP(12);
        asm volatile("s_waitcnt vmcnt(8)" : "+v"(xo0), "+v"(xo1) :: "memory");
        STAMP(13);
        float v0[8], v1[8];
#pragma unroll
        for (int e = 0; e < 4; ++e) { v0[2 * e] = bflo(xo0[e]); v0[2 * e + 1] = bfhi(xo0[e]); v1[2 * e] = bflo(xo1[e]); v1[2 * e + 1] = bfhi(xo1[e]); }
        auto red8 = [](float t) { t += __uint_as_float(__builtin_amdgcn_update_dpp(0u, __float_as_uint(t), 0x128, 0xf, 0xf, false)); return xsum4(t); };
        {
            const float m0 = red8(((v0[0] + v0[1]) + (v0[2] + v0[3])) + ((v0[4] + v0[5]) + (v0[6] + v0[7]))) * (1.f / 64.f);
            const float m1 = red8(((v1[0] + v1[1]) + (v1[2] + v1[3])) + ((v1[4] + v1[5]) + (v1[6] + v1[7]))) * (1.f / 64.f);
            float q0 = 0.f, q1 = 0.f;
#pragma unroll
            for (int e = 0; e < 8; ++e) { const float d0 = v0[e] - m0, d1 = v1[e] - m1; q0 += d0 * d0; q1 += d1 * d1; }
            q0 = red8(q0); q1 = red8(q1);
            if (l < 8) { *(LAS f32x2*)(lds + 162816 + l * 80 + w * 8) = (f32x2){m0, q0}; if (w < 2) *(LAS f32x2*)(lds + 162816 + l * 80 + 64 + w * 8) = (f32x2){m1, q1}; }
        }
        asm volatile("s_waitcnt lgkmcnt(0)" ::: "memory"); BARRIER();
        STAMP(14);
        {
            f32x4 t[5];
#pragma unroll
            for (int i = 0; i < 5; ++i) t[i] = *(const LAS f32x4*)(lds + 162816 + (l & 7) * 80 + i * 16);
            float mean = 0.f;
#pragma unroll
            for (int i = 0; i < 5; ++i) mean += t[i][0] + t[i][2];
            mean *= 0.1f;
            float m2 = 0.f;
#pragma unroll
            for (int i = 0; i < 5; ++i) { const float d0 = t[i][0] - mean, d1 = t[i][2] - mean; m2 += (t[i][1] + t[i][3]) + 64.f * (d0 * d0 + d1 * d1); }
            const float rstd = 1.f / __builtin_sqrtf(m2 * (1.f / 640.f) + 1e-5f);
            const unsigned vo = (l >> 3) * 256 + (l & 7) * 16, xo = OFF_XN + (b >> 1) * 20480 + (b & 1) * 128;
            u32x4 o0, o1;
#pragma unroll
            for (int e = 0; e < 4; ++e) { o0[e] = cvtpk((v0[2 * e] - mean) * rstd, (v0[2 * e + 1] - mean) * rstd); o1[e] = cvtpk((v1[2 * e] - mean) * rstd, (v1[2 * e + 1] - mean) * rstd); }
            st16_sc1(sws, o0, vo, xo + w * 2048);
            if (w < 2) st16_sc1(sws, o1, vo, xo + (8 + w) * 2048);
        }
        {
            const char* pa = (const char*)ckv + (size_t)(256 * j + 64 * (h & 3) + 8 * w + (l >> 3)) * 1024 + (l & 7) * 128;
            const char* pw = (const char*)((w & 1) ? wuv : wuk) + (size_t)(64 * j + 16 * (w >> 1) + ((l & 31) >> 1)) * 8192 + h * 256 + (l & 1) * 128;
            asm volatile("global_load_dword %0, %1, off\n\tglobal_load_dword %0, %2, off" : "+v"(sink) : "v"(pa), "v"(pw) : "memory");
        }
        {
            c2.w = w; c2.sws = sws; c2.swq = mk_rsrc(wq, 640 * 4096 * 2); c2.sw1 = mk_rsrc(w1, 640 * 1728 * 2);
            c2.vl16 = l * 16;
            const int swz = ((l >> 4) & 1) | ((w & 1) << 1);
            c2.vwq = (l >> 3) * 8192 + (((l & 7) ^ (swz << 1)) * 16);
            c2.vw1 = (l >> 3) * 3456 + (((l & 7) ^ (swz << 1)) * 16);
            { const int mtl = 2 * w, mtg = mtl < 8 ? 8 * j + mtl : 8 * (15 - j) + (mtl - 8); c2.sxo = OFF_XN + mtg * 20480; }
            c2.swqo = (8 * w) * 8192 + h * 256;
            c2.sw1o = (8 * w) * 3456 + h * 128;
            { const int wn = w & 3, wm = w >> 2;
              c2.skv = isv ? OFF_V + (unsigned)(h * 32 + 4 * j + 2 * wm) * 16384 + (wn - 2) * 4096 : OFF_K + (unsigned)((h * 128 + 16 * j + 8 * wm) * 4 + 2 * wn) * 1024; }
        }
    }
    STAMP(1); CSTAMP(14);
    if (isv) p1_main<true>(c1, c2, y, sink, kv); else p1_main<false>(c1, c2, y, sink, kv);
    STAMP(2); CSTAMP(15);
    p2_kvst<0>(c2, kv, isv); p2_kvst<1>(c2, kv, isv); p2_kvst<2>(c2, kv, isv); p2_kvst<3>(c2, kv, isv);
    const bool hasf = h < 27;
    AttnCtx c3;
    AttnRegs r;
    {
        const int l = lane_id(), g = l >> 4, q = (l & 15) >> 2;
        const int wn = w & 1, wm = w >> 1;
        const int s = ((q >> 1) & 1) | ((g & 1) << 1);
        c2.xb = lds + wm * 8192 + l * 16;
#pragma unroll
        for (int nt = 0; nt < 4; ++nt) c2.wq[nt] = lds + 65536 + wn * 8192 + (8 * g + q) * 128 + ((nt ^ s) * 32) + (l & 3) * 8;
#pragma unroll
        for (int ft = 0; ft < 2; ++ft) c2.wf[ft] = lds + 65536 + 16384 + (8 * g + q) * 128 + (((2 * wn + ft) ^ s) * 32) + (l & 3) * 8;
        f32x4 acc[6][4];
#pragma unroll
        for (int a = 0; a < 6; ++a)
#pragma unroll
            for (int m = 0; m < 4; ++m) acc[a][m] = (f32x4){0.f, 0.f, 0.f, 0.f};
        STAMP(3);
        if (hasf) p2_main<true>(c2, y, acc, kv, isv, std::make_integer_sequence<int, 10>{}); else p2_main<false>(c2, y, acc, kv, isv, std::make_integer_sequence<int, 10>{});
        STAMP(4);
        constexpr float C2 = 0.08838834764831845f * 1.4426950408889634f;
#pragma unroll
        for (int mt = 0; mt < 4; ++mt)
#pragma unroll
            for (int a = 0; a < 2; ++a)
                *(LAS u32x4*)(lds + 98304 + ((4 * wm + mt) * 4 + 2 * wn + a) * 1024 + l * 16) = pack8(acc[2 * a][mt] * C2, acc[2 * a + 1][mt] * C2);
        u32x4 actf[4];
        if (hasf) {
            const int f0 = 64 * h + 32 * wn + 4 * g;
            const u32x2 bA = *(const u32x2*)(b1 + f0), bB = *(const u32x2*)(b1 + f0 + 16);
            const f32x4 biasA = {bflo(bA[0]), bfhi(bA[0]), bflo(bA[1]), bfhi(bA[1])}, biasB = {bflo(bB[0]), bfhi(bB[0]), bflo(bB[1]), bfhi(bB[1])};
#pragma unroll
            for (int mt = 0; mt < 4; ++mt) {
                f32x4 hA = acc[4][mt] + biasA, hB = acc[5][mt] + biasB;
#pragma unroll
                for (int i = 0; i < 4; ++i) { hA[i] = mish(hA[i]); hB[i] = mish(hB[i]); }
                actf[mt] = pack8(hA, hB);
            }
        }
        asm volatile("s_waitcnt vmcnt(0) lgkmcnt(0)" ::: "memory"); BARRIER();
        const int grp = w >> 2, qg = w & 3;
        c3.sws = sws; c3.vl16 = l * 16; c3.kbase = OFF_K + (unsigned)h * 524288; c3.vbase = OFF_V + (unsigned)h * 524288;
        c3.ctxl = OFF_CTX + ((8 * j + 2 * qg) * 128 + 4 * h) * 1024;
        c3.nL = 2 * j + 2; c3.grp = grp; c3.qg = qg; c3.j = j; c3.rowrel = (l & 15) - 4 * g; c3.kl = lds + l * 16; c3.vl = lds + 65536 + l * 16;
        c3.ql = lds + 131072 + qg * 8192 + l * 16;
        if (grp == 0) { a_dmaK(c3, 0); a_dmaV(c3, 0); } else { a_dmaK(c3, 1); }
        if (hasf) {
#pragma unroll
            for (int mt = 0; mt < 4; ++mt) { const int mtl = 4 * wm + mt, mtg = mtl < 8 ? 8 * j + mtl : 8 * (15 - j) + (mtl - 8);
                st16_sc1(sws, actf[mt], l * 16, OFF_ACT + (mtg * 54 + 2 * h + wn) * 1024); }
        }
#pragma unroll
        for (int qt = 0; qt < 2; ++qt)
#pragma unroll
            for (int u = 0; u < 4; ++u) r.q[qt][u] = ld128(lds + 98304 + ((8 * grp + 2 * qg + qt) * 4 + u) * 1024 + l * 16);
    }
    STAMP(5);
    P4Ctx c4;
    {
        const int l = lane_id();
        const int grp = c3.grp, qg = c3.qg;
        const unsigned vl16 = c3.vl16;
#pragma unroll
        for (int d = 0; d < 8; ++d) { r.o[d][0] = (f32x4){0.f, 0.f, 0.f, 0.f}; r.o[d][1] = (f32x4){0.f, 0.f, 0.f, 0.f}; }
        r.negm[0] = (f32x4){0.f, 0.f, 0.f, 0.f}; r.negm[1] = (f32x4){0.f, 0.f, 0.f, 0.f};
        AttnSm z; z.m0 = 0.f; z.m1 = 0.f; z.l0 = 0.f; z.l1 = 0.f; z.ll0 = 0.f; z.ll1 = 0.f;
        if (hasf) asm volatile("s_waitcnt vmcnt(4) lgkmcnt(0)" ::: "memory"); else asm volatile("s_waitcnt vmcnt(0) lgkmcnt(0)" ::: "memory");
        BARRIER();
        STAMP(6); CSTAMP(12);
        a_pre<16>(r, c3.vl, c3.kl, std::make_integer_sequence<int, PD>{});
        if (grp == 1) BARRIER();
        { Piece none; none.on = false; none.sws = sws; none.vl16 = 0; none.soff = 0; none.ldso = 0; a_run<16, 0>(r, c3.vl, c3.kl, none, std::make_integer_sequence<int, 16>{}); }
        BARRIER();
        c3.swap_s = grp == 0 ? c3.nL - 1 : 99; c3.first_s = 0;
        asm volatile("" : "+s"(c3.swap_s), "+s"(c3.first_s));
        {
            const int nL = c3.nL;
            auto lean_init = [&](int s) {
                Lean st;
                const bool eK = grp == 1 && s + 2 < nL;
                const int t0 = s < nL ? s : 2 * s - nL + grp, t1 = s + 1 < nL ? s + 1 : 2 * s + 2 - nL + grp, t2 = s + 2 < nL ? s + 2 : 2 * s + 4 - nL + grp;
                const int te = eK ? t2 : t1;
                st.e_soff = (eK ? c3.kbase : c3.vbase) + te * 16384 + qg * 4096; st.e_lds = (eK ? 0 : 65536) + (te & 3) * 16384 + qg * 4096;
                st.l_soff = c3.kbase + t2 * 16384 + qg * 4096; st.l_lds = (t2 & 3) * 16384 + qg * 4096;
                st.voff = (t0 & 3) * 16384; st.koff = (t1 & 3) * 16384;
                return st;
            };
            int s = 0, phase = 0;
#pragma nounroll
            for (;;) {
                const int gend = phase == 0 ? 1 : (phase == 1 ? (grp == 0 ? nL + 1 : nL - 1) : 17);
#pragma nounroll
                while (s < gend) { a_slotfull(c3, y, r, z, s PPASSG); ++s; }
                if (phase == 0) { if (w == 0 && l == 0) set_flag(y.flags, FL_ACT + j * 32 + h, y.token); }
                if (phase == 2 || s > 16) break;
                if (phase == 1 && s <= 14) {
                    Lean st = lean_init(s);
#pragma nounroll
                    do { a_slot_lean<true>(c3, r, z, st PPASS);
                         st.e_soff += 32768; st.e_lds ^= 32768; st.l_soff += 32768; st.l_lds ^= 32768; st.voff ^= 32768; st.koff ^= 32768; ++s; } while (s <= 14);
                }
                const int send = phase == 0 ? nL - 2 : 16;
                if (s < send) {
                    Lean st = lean_init(s);
#pragma nounroll
                    do { a_slot_lean<false>(c3, r, z, st PPASS);
                         st.e_soff += 16384; st.e_lds = (st.e_lds & ~0xC000u) | ((st.e_lds + 16384) & 0xC000u); st.voff = (st.voff + 16384) & 0xC000u; st.koff = (st.koff + 16384) & 0xC000u; ++s; } while (s < send);
                }
                ++phase;
            }
        }
        float m0 = z.m0, m1 = z.m1, l0 = z.l0, l1 = z.l1;
        if (grp == 0) BARRIER();
        STAMP(7); CSTAMP(13);
        {
            const int q = xcd >> 1, rbk = (xcd & 1) * 8 + (bi >> 2), cb = bi & 3, wm = w >> 1, wn = w & 1;
            c4.sws = sws; c4.vl16 = vl16; c4.w = w; c4.q = q; c4.o = (q + 2 * wn) & 3; c4.ng = q == 3 ? 6 : 7; c4.ra = 0; c4.rw = 0;
            c4.ga = OFF_ACT + ((8 * rbk + w) * 54 + 14 * q) * 1024; c4.gas = OFF_CTX + ((8 * rbk + w) * 128 + 32 * q) * 1024;
            c4.gw = OFF_WP + ((128 + 14 * q) * 40 + 10 * cb) * 1024; c4.gws = OFF_WP + (32 * q * 40 + 10 * cb) * 1024;
            c4.dw0 = w * 1024; c4.dw1 = (w < 2 ? 8 + w : 38 + w) * 1024; c4.dw2 = (46 + w) * 1024; c4.lw = 16384 + w * 1024;
            c4.tag = (unsigned)y.token & P4TAG;
            const unsigned xb = (unsigned)(((xcd & 1) * 32 + bi) * 128 + w) * 3072;
#pragma unroll
            for (int d = 0; d < 3; ++d) { const int qq = (q + 1 + d) & 3;
                c4.so[d] = xb + qq * 98304 + q * 24576; c4.so2[d] = ((c4.o + 1 + d) & 3) < 2 ? c4.so[d] : P4OOB;
                c4.lo[d] = xb + q * 98304 + qq * 24576; c4.lo2[d] = c4.o < 2 ? c4.lo[d] : P4OOB; }
            c4.pa = lds + (4 * wm) * 1024 + vl16; c4.pw = lds + 16384 + (5 * wn) * 1024 + vl16;
        }
        p4_piece<0>(c4, 2 * P4STG); p4_piece<1>(c4, 2 * P4STG); p4_piece<2>(c4, 2 * P4STG); p4_piece<3>(c4, 2 * P4STG); p4_piece<4>(c4, 2 * P4STG);
        p4_piece<0>(c4, 3 * P4STG); p4_piece<1>(c4, 3 * P4STG); p4_piece<2>(c4, 3 * P4STG); p4_piece<3>(c4, 3 * P4STG); p4_piece<4>(c4, 3 * P4STG);
        l0 = xsum4(l0); l1 = xsum4(l1);
        {
            const lptr xo = lds + (qg * 2 + grp) * 9216;
#pragma unroll
            for (int d = 0; d < 4; ++d)
#pragma unroll
                for (int qt = 0; qt < 2; ++qt) *(LAS f32x4*)(xo + (d * 2 + qt) * 1024 + l * 16) = sel(grp == 0, r.o[4 + d][qt], r.o[d][qt]);
            *(LAS f32x4*)(xo + 8192 + l * 16) = (f32x4){m0, m1, l0, l1};
            asm volatile("s_waitcnt lgkmcnt(0)" ::: "memory"); BARRIER();
            const lcptr xi = lds + (qg * 2 + (grp ^ 1)) * 9216;
            const f32x4 st = *(const LAS f32x4*)(xi + 8192 + l * 16);
            const float M0 = __builtin_fmaxf(m0, st[0]), M1 = __builtin_fmaxf(m1, st[1]);
            const float wa0 = __builtin_amdgcn_exp2f(m0 - M0), wb0 = __builtin_amdgcn_exp2f(st[0] - M0), wa1 = __builtin_amdgcn_exp2f(m1 - M1), wb1 = __builtin_amdgcn_exp2f(st[1] - M1);
            const float i0 = __builtin_amdgcn_rcpf(wa0 * l0 + wb0 * st[2]), i1 = __builtin_amdgcn_rcpf(wa1 * l1 + wb1 * st[3]);
            const float ca0 = wa0 * i0, cb0 = wb0 * i0, ca1 = wa1 * i1, cb1 = wb1 * i1;
#pragma unroll
            for (int up = 0; up < 2; ++up)
#pragma unroll
                for (int qt = 0; qt < 2; ++qt) {
                    const f32x4 pa = *(const LAS f32x4*)(xi + ((2 * up) * 2 + qt) * 1024 + l * 16), pb = *(const LAS f32x4*)(xi + ((2 * up + 1) * 2 + qt) * 1024 + l * 16);
                    const f32x4 oa = sel(grp == 0, r.o[2 * up][qt], r.o[4 + 2 * up][qt]), ob = sel(grp == 0, r.o[2 * up + 1][qt], r.o[4 + 2 * up + 1][qt]);
                    const float ca = qt ? ca1 : ca0, cb = qt ? cb1 : cb0;
                    st16_sc1(sws, pack8(oa * ca + pa * cb, ob * ca + pb * cb), vl16, OFF_CTX + ((8 * (15 - j) + 2 * qg + qt) * 128 + 4 * h + 2 * grp + up) * 1024);
                }
        }
    }
    STAMP(8);
    {
        asm volatile("s_waitcnt lgkmcnt(0)" ::: "memory"); BARRIER();
        p4_piece<0>(c4, 0); p4_piece<1>(c4, 0); p4_piece<2>(c4, 0); p4_piece<3>(c4, 0); p4_piece<4>(c4, 0);
        p4_piece<0>(c4, P4STG); p4_piece<1>(c4, P4STG); p4_piece<2>(c4, P4STG);
        const int l = lane_id();
        const int rbk = (xcd & 1) * 8 + (bi >> 2), cb = bi & 3;
        const unsigned cidx = FL_CTX + (rbk < 8 ? rbk : 15 - rbk) * 32 + 8 * c4.q + (l & 7);
        f32x4 accg[10], accs[10], gt[3];
#pragma unroll
        for (int i = 0; i < 10; ++i) { accg[i] = (f32x4){0.f, 0.f, 0.f, 0.f}; accs[i] = (f32x4){0.f, 0.f, 0.f, 0.f}; PIN(accg[i]); PIN(accs[i]); }
        P4Regs r4;
        P4Xch xg;
        const int row0 = 128 * rbk + 32 * (w >> 1) + (l & 15), col0 = 160 * cb + 80 * (w & 1) + 4 * (l >> 4);
        u32x2 xv[3];
        {
            const rsrc_t sx = mk_rsrc(x, 2048 * 640 * 2);
#pragma unroll
            for (int k = 0; k < 3; ++k) { const int i = c4.o + 4 * k < 10 ? c4.o + 4 * k : c4.o;
                asm volatile("buffer_load_dwordx2 %0, %1, %2, 0 offen" : "=v"(xv[k]) : "v"((unsigned)((row0 + (i & 1) * 16) * 640 + col0 + (i >> 1) * 16) * 2), "s"(sx) : "memory"); }
        }
        u64 seen = 0;
        asm volatile("s_waitcnt vmcnt(18)" ::: "memory"); BARRIER();
        unsigned sb = 2 * P4STG, sp = P4STG;
        p4_rd<0, 0, 0>(c4, r4, sb); p4_rd<0, 0, 1>(c4, r4, sb); p4_rd<0, 0, 2>(c4, r4, sb); p4_rd<0, 0, 3>(c4, r4, sb); p4_rd<0, 0, 4>(c4, r4, sb); p4_rd<0, 0, 5>(c4, r4, sb); p4_rd<0, 0, 6>(c4, r4, sb);
        SBAR();
        STAMP(9);
        p4_step<P4W0, true, true, false, 2, -1, -1, -1>(c4, y, r4, xg, accg, sb, sp, 0, cidx, seen);
#pragma nounroll
        for (int t = 1; t < c4.ng; ++t) p4_step<8, true, true, false, 1, -1, -1, -1>(c4, y, r4, xg, accg, sb, sp, t, cidx, seen);
        p4_split(c4, accg, xg);
        p4_ssteps(c4, y, r4, xg, accs, sb, sp, seen, std::make_integer_sequence<int, 12>{});
        p4_step<8, true, false, false, 0, -1, -1, 0>(c4, y, r4, xg, accs, sb, sp, 0, 0, seen);
        p4_step<13, false, false, false, 0, -1, 5, -1>(c4, y, r4, xg, accs, sb, sp, 0, 0, seen);
        p4_step<9, false, false, false, 0, -1, -1, -1>(c4, y, r4, xg, accs, sb, sp, 0, 0, seen);
        p4_step<0, false, false, true, 0, -1, -1, -1>(c4, y, r4, xg, accs, sb, sp, 0, 0, seen);
        asm volatile("s_waitcnt vmcnt(0)" : "+v"(xg.rcv[0]), "+v"(xg.rcv[1]), "+v"(xg.rcv[2]), "+v"(xg.rcv[3]), "+v"(xg.rcv[4]), "+v"(xg.rcv[5]), "+v"(xg.rcv[6]), "+v"(xg.rcv[7]), "+v"(xg.rcv[8]),
                     "+v"(xv[0]), "+v"(xv[1]), "+v"(xv[2]) :: "memory");
        constexpr auto nine = std::make_integer_sequence<int, 9>{};
        P4Xch xs;
        p4_split(c4, accs, xs);
        p4_put<0>(c4, xs, OFF_XS); p4_put<1>(c4, xs, OFF_XS); p4_put<2>(c4, xs, OFF_XS); p4_put<3>(c4, xs, OFF_XS); p4_put<4>(c4, xs, OFF_XS);
        p4_put<5>(c4, xs, OFF_XS); p4_put<6>(c4, xs, OFF_XS); p4_put<7>(c4, xs, OFF_XS); p4_put<8>(c4, xs, OFF_XS);
        TS(0);
        if (!p4_tagged(c4, xg)) p4_refetch(c4, xg, OFF_XG, nine);
#pragma unroll
        for (int k = 0; k < 3; ++k) {
            gt[k] = p4_total(xg, k);
#pragma unroll
            for (int e = 0; e < 4; ++e) gt[k][e] = gt[k][e] * __builtin_amdgcn_rcpf(1.f + __builtin_amdgcn_exp2f(-1.4426950408889634f * gt[k][e]));
            PIN(gt[k]);
        }
        TS(1);
        p4_refetch(c4, xs, OFF_XS, nine);
        TS(2);
#pragma unroll
        for (int k = 0; k < 3; ++k) {
            if (k < 2 || c4.o < 2) { const int i = c4.o + 4 * k;
                const f32x4 sv = p4_total(xs, k);
                const f32x4 o = {bflo(xv[k][0]) + sv[0] * gt[k][0], bfhi(xv[k][0]) + sv[1] * gt[k][1], bflo(xv[k][1]) + sv[2] * gt[k][2], bfhi(xv[k][1]) + sv[3] * gt[k][3]};
                const u32x2 ov = {cvtpk(o[0], o[1]), cvtpk(o[2], o[3])};
                asm volatile("global_store_dwordx2 %0, %1, off sc0 sc1\n\ts_nop 1" :: "v"(out + (row0 + (i & 1) * 16) * 640 + col0 + (i >> 1) * 16), "v"(ov) : "memory"); }
        }
        if (b == 0 && w == 0 && l == 0) __hip_atomic_store(y.flags + 4, y.token, __ATOMIC_RELAXED, __HIP_MEMORY_SCOPE_AGENT);
    }
    STAMP(11);
}

extern "C" void kernel_launch(void* const* d_in, const int* in_sizes, int n_in, void* d_out, int out_size, void* d_ws, size_t ws_size, hipStream_t stream) {
    (void)in_sizes; (void)n_in; (void)out_size; (void)ws_size;
    fused_kernel<<<256, 512, 0, stream>>>((const bf16_t*)d_in[0], (const bf16_t*)d_in[1], (const bf16_t*)d_in[2], (const bf16_t*)d_in[3], (const bf16_t*)d_in[4],
                                          (const bf16_t*)d_in[5], (const bf16_t*)d_in[7], (const bf16_t*)d_in[8], (const bf16_t*)d_in[9], (bf16_t*)d_out, (char*)d_ws);
}
```

```cpp
#include <hip/hip_runtime.h>
#include <stdint.h>
#include <utility>

typedef unsigned short bf16_t;
typedef short bf16x8 __attribute__((ext_vector_type(8)));
typedef short s16x4 __attribute__((ext_vector_type(4)));
typedef short v4i16_t __attribute__((ext_vector_type(4)));
typedef float f32x4 __attribute__((ext_vector_type(4)));
typedef float f32x2 __attribute__((ext_vector_type(2)));
typedef unsigned u32x4 __attribute__((ext_vector_type(4)));
typedef unsigned u32x2 __attribute__((ext_vector_type(2)));
typedef __bf16 bf16x2_t __attribute__((ext_vector_type(2)));
typedef unsigned short u16x2 __attribute__((ext_vector_type(2)));
typedef __amdgpu_buffer_rsrc_t rsrc_t;
typedef unsigned long long u64;
#define LAS __attribute__((address_space(3)))
#define GAS __attribute__((address_space(1)))
typedef LAS char* lptr;
typedef LAS const char* lcptr;
typedef GAS u64 gu64;
#define DI __device__ __forceinline__
#define SBAR() __builtin_amdgcn_sched_barrier(0)
#define PIN(x) asm volatile("" : "+v"(x))
#define MF16(a, b, c) __builtin_amdgcn_mfma_f32_16x16x32_bf16(a, b, c, 0, 0, 0)
#define WAITV(n) asm volatile("s_waitcnt vmcnt(" #n ")" ::: "memory")
#define WAITL(n) asm volatile("s_waitcnt lgkmcnt(" #n ")" ::: "memory")
#define BARRIER() do { asm volatile("" ::: "memory"); __builtin_amdgcn_s_barrier(); asm volatile("" ::: "memory"); } while (0)

#define UPTO 4
#define STAG 1
#define NOINV 1
#define SPREAD 1
#define SPOS 1
#define XST 0
#define STAMPS 0

constexpr unsigned OFF_FLAGS = 0;
constexpr unsigned OFF_XN = 65536;
constexpr unsigned OFF_K = OFF_XN + 2621440;
constexpr unsigned OFF_V = OFF_K + 16777216;
constexpr unsigned OFF_ACT = OFF_V + 16777216 + 8388608;
constexpr unsigned OFF_CTX = OFF_ACT + 7077888;
constexpr unsigned OFF_WP = OFF_CTX + 16777216;
constexpr unsigned OFF_ST = OFF_WP + 182 * 40 * 1024;
constexpr unsigned OFF_XG = OFF_ST + 65536;
constexpr unsigned OFF_XS = OFF_XG + 25165824;
constexpr unsigned WS_BYTES = OFF_XS + 25165824;
constexpr unsigned FL_P0 = 0, FL_KV = 256, FL_ACT = 512, FL_CTX = 768;
constexpr int SC1 = 16;

DI unsigned cvtpk(float lo, float hi) { f32x2 v = {lo, hi}; bf16x2_t b = __builtin_convertvector(v, bf16x2_t); return __builtin_bit_cast(unsigned, b); }
DI u32x4 pack8(f32x4 a, f32x4 b) { u32x4 w = {cvtpk(a[0], a[1]), cvtpk(a[2], a[3]), cvtpk(b[0], b[1]), cvtpk(b[2], b[3])}; return w; }
DI float bflo(unsigned u) { return __uint_as_float(u << 16); }
DI float bfhi(unsigned u) { return __uint_as_float(u & 0xffff0000u); }
DI s16x4 vtr(lcptr p) { return __builtin_bit_cast(s16x4, __builtin_amdgcn_ds_read_tr16_b64_v4i16((LAS v4i16_t*)p)); }
DI bf16x8 cat(s16x4 a, s16x4 b) { return (bf16x8){a[0], a[1], a[2], a[3], b[0], b[1], b[2], b[3]}; }
DI bf16x8 ld128(lcptr p) { return *(const LAS bf16x8*)p; }
DI bf16x8 asbf(u32x4 v) { return __builtin_bit_cast(bf16x8, v); }
DI rsrc_t mk_rsrc(const void* p, unsigned bytes) { return __builtin_amdgcn_make_buffer_rsrc((void*)p, (short)0, (int)bytes, 0x00020000); }
DI void dma16(rsrc_t r, unsigned voff, unsigned soff, unsigned ldsoff) {
    asm volatile("s_mov_b32 m0, %0\n\ts_nop 3\n\tbuffer_load_dwordx4 %1, %2, %3 offen lds" ::"s"(ldsoff), "v"(voff), "s"(r), "s"(soff) : "memory");
}
#define HS " sc1"
DI void dma16h(rsrc_t r, unsigned voff, unsigned soff, unsigned ldsoff) {
    asm volatile("s_mov_b32 m0, %0\n\ts_nop 3\n\tbuffer_load_dwordx4 %1, %2, %3 offen" HS " lds" ::"s"(ldsoff), "v"(voff), "s"(r), "s"(soff) : "memory");
}
DI void dma16x4(rsrc_t r, unsigned voff, unsigned soff, unsigned ldsoff) {
    asm volatile("s_mov_b32 m0, %0\n\ts_nop 3\n\tbuffer_load_dwordx4 %1, %2, %3 offen" HS " lds\n\tbuffer_load_dwordx4 %1, %2, %3 offen offset:1024" HS " lds\n\t"
                 "buffer_load_dwordx4 %1, %2, %3 offen offset:2048" HS " lds\n\tbuffer_load_dwordx4 %1, %2, %3 offen offset:3072" HS " lds" ::"s"(ldsoff), "v"(voff), "s"(r), "s"(soff) : "memory");
}
DI void st16_sc1(rsrc_t r, u32x4 v, unsigned voff, unsigned soff) { asm volatile("s_nop 4\n\tbuffer_store_dwordx4 %0, %1, %2, %3 offen sc1\n\ts_nop 1" ::"v"(v), "v"(voff), "s"(r), "s"(soff) : "memory"); }
template <int OFF> DI void st16_sc1o(rsrc_t r, u32x4 v, unsigned voff, unsigned soff) { asm volatile("s_nop 4\n\tbuffer_store_dwordx4 %0, %1, %2, %3 offen offset:%4 sc1\n\ts_nop 1" ::"v"(v), "v"(voff), "s"(r), "s"(soff), "i"(OFF) : "memory"); }
DI u32x4 ld16(rsrc_t r, unsigned voff, unsigned soff) { return __builtin_amdgcn_raw_buffer_load_b128(r, voff, soff, 0); }
#define P4AUX SC1
DI u32x4 ld16_sc1(rsrc_t r, unsigned voff, unsigned soff) { return __builtin_amdgcn_raw_buffer_load_b128(r, voff, soff, P4AUX); }
template <class T> DI T sel(bool c, T a, T b) { return c ? a : b; }
DI int lane_id() { int l; asm volatile("v_mbcnt_lo_u32_b32 %0, -1, 0\n\tv_mbcnt_hi_u32_b32 %0, -1, %0" : "=v"(l)); return l; }
DI void set_flag(gu64* flags, unsigned idx, u64 token) { __hip_atomic_store(flags + idx * 8, token, __ATOMIC_RELAXED, __HIP_MEMORY_SCOPE_AGENT); }
DI u64 poll_flag(gu64* flags, unsigned idx) { return __hip_atomic_load(flags + idx * 8, __ATOMIC_RELAXED, __HIP_MEMORY_SCOPE_AGENT); }
DI void spin_flags(gu64* flags, unsigned idx, u64 token) {
    const unsigned long long t0 = __builtin_amdgcn_s_memrealtime();
    for (;;) {
        __builtin_amdgcn_s_sleep(1);
        const u64 v = poll_flag(flags, idx);
        if (__all(v == token)) break;
        if (__builtin_amdgcn_s_memrealtime() - t0 > 2000000ull) break;
    }
}
DI u64 poll_asm(gu64* flags, unsigned idx) { u64 v; asm volatile("global_load_dwordx2 %0, %1, off sc1" : "=v"(v) : "v"(flags + idx * 8) : "memory"); return v; }
DI void check_flags(gu64* flags, unsigned idx, u64 seen, u64 token) { if (!__all(seen == token)) spin_flags(flags, idx, token); }
DI void acquire_issue() { asm volatile("buffer_inv sc1" ::: "memory"); }
DI u64 uni64(u64 v) { return ((u64)(unsigned)__builtin_amdgcn_readfirstlane((unsigned)(v >> 32)) << 32) | (unsigned)__builtin_amdgcn_readfirstlane((unsigned)v); }
#define SCLK __builtin_amdgcn_s_memrealtime
#define STAMP(i) do { } while (0)
#define STAMPW(i, wv) do { } while (0)
#define CSTAMP(i) do { } while (0)

#define TS(i) do { } while (0)
#define TSARG
#define TSPASS
struct Sync { gu64* flags; u64 token; int b, h, j, w; };
#define YSTAMP(i) do { } while (0)

struct P2Ctx { rsrc_t sws, swq, sw1; unsigned vl16, vwq, vw1; unsigned sxo, swqo, sw1o, skv; int w; lcptr xb; lcptr wq[4]; lcptr wf[2]; };
DI void p2_dma1(const P2Ctx& c, int st, int ks, int i) {
    if (i < 4) dma16h(c.sws, c.vl16, c.sxo + (i >> 1) * 20480 + (i & 1) * 1024 + ks * 2048, st * 32768 + (4 * c.w + i) * 1024);
    else if (i < 6) dma16(c.swq, c.vwq, c.swqo + (i - 4) * 128 + ks * 524288, 65536 + st * 24576 + (i - 4) * 8192 + c.w * 1024);
    else dma16(c.sw1, c.vw1, c.sw1o + ks * (64 * 3456), 65536 + st * 24576 + 16384 + c.w * 1024);
}

struct P1Ctx { rsrc_t sx, sw, sws; unsigned vx[2], vw[2]; unsigned sxo, swo; int w; lcptr xb[2]; lcptr wb[4]; const bf16_t *wo, *w2, *wq, *w1; lcptr lds; };
DI void pack_fetch(const P1Ctx& c, int b) {
    const int l = lane_id(), gw = b * 8 + c.w, pu = gw / 10, pq = gw - pu * 10;
    const rsrc_t sp = mk_rsrc(pu < 128 ? c.wo : c.w2, pu < 128 ? 4096 * 640 * 2 : 1728 * 640 * 2);
    const unsigned so = gw < 1820 ? (unsigned)((pu < 128 ? pu : pu - 128) * 32 * 1280 + pq * 128) : 0xF0000000u;
#pragma unroll
    for (int i = 0; i < 4; ++i) dma16(sp, (l >> 3) * 1280 + (l & 7) * 16, so + i * 8 * 1280, 131072 + c.w * 4096 + i * 1024);
}
DI void pack_store(const P1Ctx& c, int b) {
    const int l = lane_id(), g = l >> 4, q = (l & 15) >> 2, gw = b * 8 + c.w, pu = gw / 10, pq = gw - pu * 10;
    lcptr p = c.lds + 131072 + c.w * 4096 + (4 * g + q) * 128 + (l & 3) * 8;
    bf16x8 fr[4];
#pragma unroll
    for (int i = 0; i < 4; ++i) fr[i] = cat(vtr(p + i * 32), vtr(p + i * 32 + 16 * 128));
    if (gw < 1820) {
#pragma unroll
        for (int i = 0; i < 4; ++i) st16_sc1(c.sws, __builtin_bit_cast(u32x4, fr[i]), l * 16, OFF_WP + (pu * 40 + pq * 4 + i) * 1024);
    }
}
DI void p1_dma1(const P1Ctx& c, int st, int ks, int i) {
    if (i < 4) dma16(c.sx, c.vx[i & 1], c.sxo + i * 8192 + ks * 128, st * 32768 + (4 * c.w + i) * 1024);
    else dma16(c.sw, c.vw[i & 1], c.swo + (i - 4) * 65536 + ks * 524288, 65536 + st * 32768 + (4 * c.w + (i - 4)) * 1024);
}
DI void p1_dma(const P1Ctx& c, int st, int ks) {
#pragma unroll
    for (int i = 0; i < 8; ++i) p1_dma1(c, st, ks, i);
}
template <int ST, int UP, int I> DI void p1_rdw(const P1Ctx& c, bf16x8 (&wf)[4], s16x4 (&wl)[4]) {
    constexpr int nt = I >> 1, rd = I & 1;
    const s16x4 v = vtr(c.wb[nt] + ST * 32768 + UP * 4096 + rd * 512);
    if constexpr (rd == 0) wl[nt] = v; else wf[nt] = cat(wl[nt], v);
}
template <int ST, int G> DI bf16x8 p1_rdx(const P1Ctx& c) { return ld128(c.xb[G >> 3] + ST * 32768 + (G & 7) * 2048); }
template <int ST, int KSC, int K> DI bool p1_is(int ks) { if constexpr (KSC >= 0) return KSC == K; else if constexpr ((K & 1) != ST || K > 5) return false; else return ks == K; }
template <bool ISV, int ST, int KSC, int G> DI void p1_group(const P1Ctx& c, const P2Ctx& c2, const Sync& y, const int ks, u64& seen, unsigned& sink, f32x4 (&acc)[4][8], bf16x8 (&wA)[4], bf16x8 (&wB)[4], s16x4 (&wl)[4], bf16x8 (&xr)[4]) {
    constexpr int up = G >> 3, mt = G & 7;
    constexpr bool NEXT = KSC != 7;
    if constexpr (G == 13) {
        if (p1_is<ST, KSC, 0>(ks)) asm volatile("s_waitcnt vmcnt(2) lgkmcnt(0)" ::: "memory");
        else if (p1_is<ST, KSC, 1>(ks) || p1_is<ST, KSC, 3>(ks)) asm volatile("s_waitcnt vmcnt(1) lgkmcnt(0)" ::: "memory");
        else if (p1_is<ST, KSC, 5>(ks) || p1_is<ST, KSC, 7>(ks)) asm volatile("s_waitcnt vmcnt(4) lgkmcnt(0)" ::: "memory");
        else asm volatile("s_waitcnt vmcnt(0) lgkmcnt(0)" ::: "memory");
        if constexpr (ST == 0) asm volatile("" :: "v"(sink));
        BARRIER();
        if constexpr (!ISV) {
            if (p1_is<ST, KSC, 0>(ks)) { if (y.w == 0 && lane_id() == 0) set_flag(y.flags, FL_P0 + y.b, y.token); }
            if (p1_is<ST, KSC, 4>(ks)) { if (y.w == 0) { const int l = lane_id() & 31; check_flags(y.flags, FL_P0 + (l < 16 ? 16 * y.j + l : 16 * (15 - y.j) + (l - 16)), seen, y.token); } }
        }
        SBAR();
    }
    const bf16x8 x = xr[G & 3];
#pragma unroll
    for (int nt = 0; nt < 4; ++nt) {
        const bf16x8 wv = up == 0 ? wA[nt] : wB[nt];
        if constexpr (ISV) acc[nt][mt] = MF16(x, wv, acc[nt][mt]); else acc[nt][mt] = MF16(wv, x, acc[nt][mt]);
        if (nt == 0) {
            if constexpr (G + 3 < 16) xr[(G + 3) & 3] = p1_rdx<ST, G + 3>(c);
            else if constexpr (NEXT) xr[(G + 3) & 3] = p1_rdx<ST ^ 1, G + 3 - 16>(c);
        }
        if constexpr (G >= 2 && G < 6) { if (nt == 1) p1_rdw<ST, 1, 2 * (G - 2)>(c, wB, wl); if (nt == 2) p1_rdw<ST, 1, 2 * (G - 2) + 1>(c, wB, wl); }
        if constexpr (G >= 13 && NEXT) {
            if (nt == 1) p1_rdw<ST ^ 1, 0, 3 * (G - 13)>(c, wA, wl);
            if (nt == 2) p1_rdw<ST ^ 1, 0, 3 * (G - 13) + 1>(c, wA, wl);
            if constexpr (G < 15) { if (nt == 3) p1_rdw<ST ^ 1, 0, 3 * (G - 13) + 2>(c, wA, wl); }
        }
        if (nt == SPOS) {
            if constexpr (G >= 13) { constexpr int p = G - 13;
                if constexpr (KSC < 0) p1_dma1(c, ST, ks + 2, p);
                else if constexpr (KSC == 6) p2_dma1(c2, 0, 0, p);
                else p2_dma1(c2, 1, 1, p);
            } else if constexpr (G <= 4) { constexpr int p = G + 3;
                if constexpr (KSC == 7) { if constexpr (p < 7) p2_dma1(c2, 0, 0, p); }
                else { if (!p1_is<ST, KSC, 0>(ks)) p1_dma1(c, ST ^ 1, ks + 1, p); }
            }
        }
        SBAR();
    }
    if constexpr (G == 6) {
        if (p1_is<ST, KSC, 1>(ks)) {
            const int l = lane_id(), L = (c.w & 3) * 64 + l;
            const char* pb = (L >= 160 && L < 240 && y.h < 27) ? (const char*)c.w1 + (size_t)(80 * y.j + L - 160) * 3456 + y.h * 128
                                                               : (const char*)c.wq + (size_t)(80 * y.j + ((L < 160 ? L : 0) >> 1)) * 8192 + y.h * 256 + (L & 1) * 128;
            asm volatile("global_load_dword %0, %1, off" : "+v"(sink) : "v"(pb) : "memory");
        }
        if (p1_is<ST, KSC, 3>(ks)) {
            const int l = lane_id(), gw = y.b * 8 + c.w < 1820 ? y.b * 8 + c.w : 0, pu = gw / 10, pq = gw - pu * 10;
            const char* pc = (const char*)(pu < 128 ? c.wo : c.w2) + (size_t)((pu < 128 ? pu : pu - 128) * 32 + (l & 31)) * 1280 + pq * 128;
            asm volatile("global_load_dword %0, %1, off" : "+v"(sink) : "v"(pc) : "memory");
        }
        if constexpr (!ISV) { if (p1_is<ST, KSC, 4>(ks)) { if (y.w == 0) { const int l = lane_id() & 31; seen = poll_flag(y.flags, FL_P0 + (l < 16 ? 16 * y.j + l : 16 * (15 - y.j) + (l - 16))); } } }
        if (p1_is<ST, KSC, 5>(ks)) { pack_fetch(c, y.b); if constexpr (!ISV && !NOINV) { if (y.w == 0) acquire_issue(); } }
        if constexpr (KSC == 7) pack_store(c, y.b);
        SBAR();
    }
}
template <bool ISV, int ST, int KSC, int... Gs> DI void p1_step(const P1Ctx& c, const P2Ctx& c2, const Sync& y, const int ks, u64& seen, unsigned& sink, f32x4 (&acc)[4][8], bf16x8 (&wA)[4], bf16x8 (&wB)[4], s16x4 (&wl)[4], bf16x8 (&xr)[4], std::integer_sequence<int, Gs...>) {
    (p1_group<ISV, ST, KSC, Gs>(c, c2, y, ks, seen, sink, acc, wA, wB, wl, xr), ...);
}
template <bool ISV> DI void p1_main(const P1Ctx& c, const P2Ctx& c2, const Sync& y, unsigned& sink, u32x4 (&kv)[16]) {
    f32x4 acc[4][8];
#pragma unroll
    for (int a = 0; a < 4; ++a)
#pragma unroll
        for (int b = 0; b < 8; ++b) acc[a][b] = (f32x4){0.f, 0.f, 0.f, 0.f};
    bf16x8 wA[4], wB[4], xr[4]; s16x4 wl[4];
    u64 seen = 0;
    asm volatile("" ::: "memory");
    p1_rdw<0, 0, 0>(c, wA, wl); p1_rdw<0, 0, 1>(c, wA, wl); p1_rdw<0, 0, 2>(c, wA, wl); p1_rdw<0, 0, 3>(c, wA, wl);
    p1_rdw<0, 0, 4>(c, wA, wl); p1_rdw<0, 0, 5>(c, wA, wl); p1_rdw<0, 0, 6>(c, wA, wl); p1_rdw<0, 0, 7>(c, wA, wl);
    xr[0] = p1_rdx<0, 0>(c); xr[1] = p1_rdx<0, 1>(c); xr[2] = p1_rdx<0, 2>(c);
    SBAR();
    constexpr auto seq = std::make_integer_sequence<int, 16>{};
#pragma nounroll
    for (int ks = 0; ks < 6; ks += 2) {
        p1_step<ISV, 0, -1>(c, c2, y, ks, seen, sink, acc, wA, wB, wl, xr, seq);
        p1_step<ISV, 1, -1>(c, c2, y, ks + 1, seen, sink, acc, wA, wB, wl, xr, seq);
    }
    p1_step<ISV, 0, 6>(c, c2, y, 6, seen, sink, acc, wA, wB, wl, xr, seq);
    p1_step<ISV, 1, 7>(c, c2, y, 7, seen, sink, acc, wA, wB, wl, xr, seq);
#pragma unroll
    for (int a = 0; a < 8; ++a)
#pragma unroll
        for (int b = 0; b < 2; ++b) {
            if constexpr (ISV) kv[a * 2 + b] = pack8(acc[(a & 1) * 2 + b][(a >> 1) * 2], acc[(a & 1) * 2 + b][(a >> 1) * 2 + 1]);
            else kv[a * 2 + b] = pack8(acc[2 * b][a], acc[2 * b + 1][a]);
        }
}

template <bool HASF, int ST, int I> DI bf16x8 p2_rdw(const P2Ctx& c) {
    constexpr int NW = HASF ? 6 : 4, up = I / NW, n = I % NW;
    lcptr p = (n < 4 ? c.wq[n & 3] : c.wf[n & 1]) + ST * 24576 + up * 4096;
    return cat(vtr(p), vtr(p + 512));
}
template <int ST, int UP, int MT> DI bf16x8 p2_rdx(const P2Ctx& c) { return ld128(c.xb + ST * 32768 + MT * 2048 + UP * 1024); }
template <int I> DI void p2_kvst(const P2Ctx& c, const u32x4 (&kv)[16], bool isv) {
    st16_sc1(c.sws, kv[I], c.vl16, c.skv + (isv ? (unsigned)(((I >> 3) * 16 + ((I >> 2) & 1) * 8 + (I & 3)) * 1024) : (unsigned)((I >> 1) * 4096 + (I & 1) * 1024)));
}
template <bool HASF, int IV> DI void p2_extras(const P2Ctx& c, const u32x4 (&kv)[16], bool isv) {
    if constexpr (IV < 6) { p2_kvst<4 + 2 * IV>(c, kv, isv); p2_kvst<5 + 2 * IV>(c, kv, isv); }
}
template <bool HASF, int KS, int G> DI void p2_group(const P2Ctx& c, const Sync& y, u64& seen, f32x4 (&acc)[6][4], bf16x8 (&wr)[4], bf16x8 (&x0)[4], bf16x8 (&x1)[4], const u32x4 (&kv)[16], bool isv) {
    constexpr int NW = HASF ? 6 : 4, NG = 2 * NW, ST = KS & 1, up = G / NW, n = G % NW;
    constexpr int NP = HASF ? 7 : 6, BD0 = HASF ? 2 : 0;
    constexpr bool LAST = (KS == 9);
    if constexpr (G == NG - 3) {
        if constexpr (KS == 0 && !SPREAD) asm volatile("s_waitcnt vmcnt(4) lgkmcnt(0)" ::: "memory");
        else if constexpr (KS == 0) asm volatile("s_waitcnt vmcnt(0) lgkmcnt(0)" ::: "memory");
        else if constexpr (KS <= 6) asm volatile("s_waitcnt vmcnt(2) lgkmcnt(0)" ::: "memory");
        else asm volatile("s_waitcnt vmcnt(0) lgkmcnt(0)" ::: "memory");
        BARRIER();
        if constexpr (KS == 7) { if (y.w == 0 && lane_id() == 0) set_flag(y.flags, FL_KV + y.h * 8 + y.j, y.token); }
        if constexpr (KS == 8) { if (y.w == 0) seen = poll_flag(y.flags, FL_KV + y.h * 8 + (lane_id() & 7)); }
        if constexpr (KS == 9) { if (y.w == 0) { check_flags(y.flags, FL_KV + y.h * 8 + (lane_id() & 7), seen, y.token); if (!NOINV) acquire_issue(); } }
        SBAR();
    }
    const bf16x8 wcur = wr[G & 3];
#pragma unroll
    for (int mt = 0; mt < 4; ++mt) {
        acc[n][mt] = MF16(wcur, up == 0 ? x0[mt] : x1[mt], acc[n][mt]);
        if (mt == 0) {
            if constexpr (G + 3 < NG) wr[(G + 3) & 3] = p2_rdw<HASF, ST, G + 3>(c);
            else if constexpr (!LAST) wr[(G + 3) & 3] = p2_rdw<HASF, ST ^ 1, G + 3 - NG>(c);
        }
        if (mt == 1) {
            if constexpr (G < 4) x1[G] = p2_rdx<ST, 1, G>(c);
            else if constexpr (G >= NG - 3 && !LAST) x0[G - (NG - 3)] = p2_rdx<ST ^ 1, 0, G - (NG - 3)>(c);
        }
        if (mt == 2) { if constexpr (G == NG - 1 && !LAST) x0[3] = p2_rdx<ST ^ 1, 0, 3>(c); }
        if (mt == SPOS) {
            if constexpr (G >= NG - 3) { if constexpr (KS + 2 < 10) p2_dma1(c, ST, KS + 2, G - (NG - 3)); }
            else if constexpr (G + 3 < NP) { if constexpr (KS + 1 < 10) p2_dma1(c, ST ^ 1, KS + 1, G + 3); }
        }
        SBAR();
    }
    if constexpr (G == NP - 3 && KS >= 1) { p2_extras<HASF, KS - 1>(c, kv, isv); SBAR(); }
}
template <bool HASF, int KS, int... Gs> DI void p2_step(const P2Ctx& c, const Sync& y, u64& seen, f32x4 (&acc)[6][4], bf16x8 (&wr)[4], bf16x8 (&x0)[4], bf16x8 (&x1)[4], const u32x4 (&kv)[16], bool isv, std::integer_sequence<int, Gs...>) {
    (p2_group<HASF, KS, Gs>(c, y, seen, acc, wr, x0, x1, kv, isv), ...);
}
template <bool HASF, int... KSs> DI void p2_main(const P2Ctx& c, const Sync& y, f32x4 (&acc)[6][4], const u32x4 (&kv)[16], bool isv, std::integer_sequence<int, KSs...>) {
    bf16x8 wr[4], x0[4], x1[4];
    u64 seen = 0;
    asm volatile("" ::: "memory");
    wr[0] = p2_rdw<HASF, 0, 0>(c); wr[1] = p2_rdw<HASF, 0, 1>(c); wr[2] = p2_rdw<HASF, 0, 2>(c);
    x0[0] = p2_rdx<0, 0, 0>(c); x0[1] = p2_rdx<0, 0, 1>(c); x0[2] = p2_rdx<0, 0, 2>(c); x0[3] = p2_rdx<0, 0, 3>(c);
    SBAR();
    (p2_step<HASF, KSs>(c, y, seen, acc, wr, x0, x1, kv, isv, std::make_integer_sequence<int, (HASF ? 12 : 8)>{}), ...);
}
DI float mish(float h) {
    const float t = __builtin_amdgcn_exp2f(__builtin_fminf(h, 20.f) * 1.4426950408889634f);
    const float n = t * (t + 2.f);
    return h * n * __builtin_amdgcn_rcpf(n + 2.f);
}

constexpr int PD = 8;
#define PRIOM 1
#define PRIOV 0
#define ADDMODE 2
#define ABL_NOEARLY 0
#define APRE_EARLY 1
#define LATE_STEP 7
#define LATE_OFF 2
struct AttnRegs { f32x4 o[8][2]; f32x4 ls[2]; f32x4 s[4][2]; bf16x8 q[2][4]; u32x4 pk[2][2]; f32x4 negm[2]; bf16x8 ones; bf16x8 op[32]; };
template <int I> DI void a_read(AttnRegs& r, lcptr vp, lcptr kp) {
    if constexpr (I < 16) r.op[I] = ld128(vp + I * 1024);
    else if constexpr (I < 32) r.op[I] = ld128(kp + (((I - 16) & 3) * 4 + ((I - 16) >> 2)) * 1024);
}
struct Piece { rsrc_t sws; unsigned vl16, soff, ldso; bool on; };
template <int P, bool COND> DI void piece(const Piece& d) {
    if ((!COND && !ABL_NOEARLY) || (COND && d.on)) asm volatile("s_mov_b32 m0, %0\n\ts_nop 3\n\tbuffer_load_dwordx4 %1, %2, %3 offen offset:%4" HS " lds" ::"s"(d.ldso), "v"(d.vl16), "s"(d.sws), "s"(d.soff), "i"(P * 1024) : "memory");
}
template <int I, int DMA> DI void a_slot(AttnRegs& r, lcptr vp, lcptr kp, const Piece& late) {
    if constexpr (I < 16) { constexpr int ks = I >> 3, dt = I & 7;
        r.o[dt][0] = MF16(r.op[I], asbf(r.pk[0][ks]), r.o[dt][0]); r.o[dt][1] = MF16(r.op[I], asbf(r.pk[1][ks]), r.o[dt][1]);
        if constexpr (dt == 3) { r.ls[0] = MF16(r.ones, asbf(r.pk[0][ks]), r.ls[0]); r.ls[1] = MF16(r.ones, asbf(r.pk[1][ks]), r.ls[1]); } }
    else { constexpr int u = (I - 16) >> 2, kt = (I - 16) & 3;
        r.s[kt][0] = MF16(r.op[I], r.q[0][u], u == 0 ? r.negm[0] : r.s[kt][0]); r.s[kt][1] = MF16(r.op[I], r.q[1][u], u == 0 ? r.negm[1] : r.s[kt][1]); }
    a_read<I + PD>(r, vp, kp);
    SBAR();
    if constexpr (DMA != 0 && (I % LATE_STEP) == LATE_OFF && I / LATE_STEP < 4) { piece<I / LATE_STEP, DMA == 2>(late); SBAR(); }
}
template <int B, int DMA, int... Is> DI void a_run(AttnRegs& r, lcptr vp, lcptr kp, const Piece& late, std::integer_sequence<int, Is...>) { (a_slot<B + Is, DMA>(r, vp, kp, late), ...); }
template <int B, int... Is> DI void a_pre(AttnRegs& r, lcptr vp, lcptr kp, std::integer_sequence<int, Is...>) { (a_read<B + Is>(r, vp, kp), ...); }

DI unsigned pkmax(unsigned a, unsigned b) { return __builtin_bit_cast(unsigned, __builtin_elementwise_max(__builtin_bit_cast(u16x2, a), __builtin_bit_cast(u16x2, b))); }
DI unsigned hmax(unsigned v) { const unsigned lo = v & 0xFFFFu, hi = v >> 16; return lo > hi ? lo : hi; }
template <int Q> DI void a_exp_quarter(AttnRegs& r, unsigned& x0, unsigned& x1) {
    constexpr int ks = Q >> 1, qt = Q & 1;
    float e[8];
#pragma unroll
    for (int i = 0; i < 4; ++i) { e[i] = __builtin_amdgcn_exp2f(r.s[2 * ks][qt][i]); e[4 + i] = __builtin_amdgcn_exp2f(r.s[2 * ks + 1][qt][i]); }
    const u32x4 p = {cvtpk(e[0], e[1]), cvtpk(e[2], e[3]), cvtpk(e[4], e[5]), cvtpk(e[6], e[7])};
    r.pk[qt][ks] = p;
    const unsigned m = pkmax(pkmax(p[0], p[1]), pkmax(p[2], p[3]));
    if constexpr (Q == 0) x0 = m; else if constexpr (Q == 1) x1 = m; else if constexpr (qt == 0) x0 = pkmax(x0, m); else x1 = pkmax(x1, m);
}
template <bool DMA> DI void a_exp_pack(AttnRegs& r, unsigned& x0, unsigned& x1, const Piece& early) {
    if constexpr (DMA) { piece<0, false>(early); SBAR(); }
    a_exp_quarter<0>(r, x0, x1);
    if constexpr (DMA) { SBAR(); piece<1, false>(early); SBAR(); }
    a_exp_quarter<1>(r, x0, x1);
    if constexpr (DMA) { SBAR(); piece<2, false>(early); SBAR(); }
    a_exp_quarter<2>(r, x0, x1);
    if constexpr (DMA) { SBAR(); piece<3, false>(early); SBAR(); }
    a_exp_quarter<3>(r, x0, x1);
}
DI float xmax4(float v) {
    const u32x2 a = __builtin_amdgcn_permlane16_swap(__float_as_uint(v), __float_as_uint(v), false, false);
    v = __builtin_fmaxf(__uint_as_float(a[0]), __uint_as_float(a[1]));
    const u32x2 c = __builtin_amdgcn_permlane32_swap(__float_as_uint(v), __float_as_uint(v), false, false);
    return __builtin_fmaxf(__uint_as_float(c[0]), __uint_as_float(c[1]));
}
DI float xsum4(float v) {
    const u32x2 a = __builtin_amdgcn_permlane16_swap(__float_as_uint(v), __float_as_uint(v), false, false);
    v = __uint_as_float(a[0]) + __uint_as_float(a[1]);
    const u32x2 c = __builtin_amdgcn_permlane32_swap(__float_as_uint(v), __float_as_uint(v), false, false);
    return __uint_as_float(c[0]) + __uint_as_float(c[1]);
}
constexpr unsigned P_HI = 0x5D80u, P_LO = 0x2180u;

struct AttnCtx { rsrc_t sws; unsigned vl16, kbase, vbase, ctxl; int nL, grp, qg, j, rowrel, swap_s, first_s; lcptr kl, vl, ql; };
struct AttnSm { float m0, m1; };
DI void a_dmaK(const AttnCtx& c, int n) { dma16x4(c.sws, c.vl16, c.kbase + n * 16384 + c.qg * 4096, (n & 3) * 16384 + c.qg * 4096); }
DI void a_dmaV(const AttnCtx& c, int n) { dma16x4(c.sws, c.vl16, c.vbase + n * 16384 + c.qg * 4096, 65536 + (n & 3) * 16384 + c.qg * 4096); }
#define PT(i) do { } while (0)
#define PARG
#define PPASS
#define PPASSG
DI void a_slotfull(const AttnCtx& c, const Sync& y, AttnRegs& r, AttnSm& z, const int s PARG) {
    const int nL = c.nL, grp = c.grp;
    const bool SWAP = (s == c.swap_s), FIN = (s == c.swap_s + 1), FIRST = (s == c.first_s) || FIN;
    const int tcur = s < nL ? s : 2 * s - nL + grp;
    Piece early, late;
    {
        const int s2 = s + 2, s1 = s + 1;
        const bool eK = grp == 1 && s2 < nL;
        const int te = eK ? s2 : (s1 < nL ? s1 : 2 * s1 - nL + grp), tl = 2 * s2 - nL + grp;
        early.sws = c.sws; early.vl16 = c.vl16; late.sws = c.sws; late.vl16 = c.vl16;
        early.on = eK || (s1 <= 16 && (grp == 0 || s1 >= nL));
        early.soff = (eK ? c.kbase : c.vbase) + te * 16384 + c.qg * 4096; early.ldso = (eK ? 0 : 65536) + (te & 3) * 16384 + c.qg * 4096;
        late.on = s2 <= 16 && s2 >= nL;
        late.soff = c.kbase + tl * 16384 + c.qg * 4096; late.ldso = (tl & 3) * 16384 + c.qg * 4096;
    }
    const int snext = s + 1, tnext = snext < nL ? snext : 2 * snext - nL + grp;
    const lcptr vp = c.vl + (tcur & 3) * 16384, kp = c.kl + (tnext & 3) * 16384;
    u64 seenA = 0;
    if (s == 16) seenA = poll_asm(y.flags, FL_ACT + 32 * y.w + (lane_id() & 31));
    a_pre<0>(r, vp, kp, std::make_integer_sequence<int, PD>{});
    SBAR();
    if (SWAP) {
        asm volatile("ds_read_b128 %0, %8\n\tds_read_b128 %1, %8 offset:1024\n\tds_read_b128 %2, %8 offset:2048\n\tds_read_b128 %3, %8 offset:3072\n\t"
                     "ds_read_b128 %4, %8 offset:4096\n\tds_read_b128 %5, %8 offset:5120\n\tds_read_b128 %6, %8 offset:6144\n\tds_read_b128 %7, %8 offset:7168\n\ts_waitcnt lgkmcnt(0)"
                     : "=&v"(r.q[0][0]), "=&v"(r.q[0][1]), "=&v"(r.q[0][2]), "=&v"(r.q[0][3]), "=&v"(r.q[1][0]), "=&v"(r.q[1][1]), "=&v"(r.q[1][2]), "=&v"(r.q[1][3])
                     : "v"(c.ql) : "memory");
    }
    if (FIN) {
        const float i0 = __builtin_amdgcn_rcpf(r.ls[0][0]), i1 = __builtin_amdgcn_rcpf(r.ls[1][0]);
        const unsigned c1o = c.ctxl + 131072;
        st16_sc1o<0>(c.sws, pack8(r.o[0][0] * i0, r.o[1][0] * i0), c.vl16, c.ctxl); st16_sc1o<1024>(c.sws, pack8(r.o[2][0] * i0, r.o[3][0] * i0), c.vl16, c.ctxl);
        st16_sc1o<2048>(c.sws, pack8(r.o[4][0] * i0, r.o[5][0] * i0), c.vl16, c.ctxl); st16_sc1o<3072>(c.sws, pack8(r.o[6][0] * i0, r.o[7][0] * i0), c.vl16, c.ctxl);
        st16_sc1o<0>(c.sws, pack8(r.o[0][1] * i1, r.o[1][1] * i1), c.vl16, c1o); st16_sc1o<1024>(c.sws, pack8(r.o[2][1] * i1, r.o[3][1] * i1), c.vl16, c1o);
        st16_sc1o<2048>(c.sws, pack8(r.o[4][1] * i1, r.o[5][1] * i1), c.vl16, c1o); st16_sc1o<3072>(c.sws, pack8(r.o[6][1] * i1, r.o[7][1] * i1), c.vl16, c1o);
#pragma unroll
        for (int d = 0; d < 8; ++d) { r.o[d][0] = (f32x4){0.f, 0.f, 0.f, 0.f}; r.o[d][1] = (f32x4){0.f, 0.f, 0.f, 0.f}; }
        r.ls[0] = (f32x4){0.f, 0.f, 0.f, 0.f}; r.ls[1] = (f32x4){0.f, 0.f, 0.f, 0.f};
    }
    {
        const bool heavy = (grp == 1) || (s >= nL);
        const int qb = heavy ? 15 - c.j : c.j, kb = 64 * tcur - 128 * qb - 32 * c.qg;
        if (kb >= -32) {
            const float NI = -__builtin_inff();
            const f32x4 ninf = {NI, NI, NI, NI};
            f32x4 s00 = r.s[0][0], s10 = r.s[1][0], s20 = r.s[2][0], s30 = r.s[3][0], s01 = r.s[0][1], s11 = r.s[1][1], s21 = r.s[2][1], s31 = r.s[3][1];
            const int dq = c.rowrel;
            if (kb > 0) { s00 = ninf; s10 = ninf; s20 = ninf; s30 = ninf; s01 = ninf; s11 = ninf; s21 = ninf; s31 = ninf; }
            else if (kb == 0) {
#pragma unroll
                for (int i = 0; i < 4; ++i) { s00[i] = sel(i > dq, NI, s00[i]); s11[i] = sel(i > dq, NI, s11[i]); }
                s10 = ninf; s20 = ninf; s30 = ninf; s21 = ninf; s31 = ninf;
            } else {
#pragma unroll
                for (int i = 0; i < 4; ++i) { s20[i] = sel(i > dq, NI, s20[i]); s31[i] = sel(i > dq, NI, s31[i]); }
                s30 = ninf;
            }
            r.s[0][0] = s00; r.s[1][0] = s10; r.s[2][0] = s20; r.s[3][0] = s30; r.s[0][1] = s01; r.s[1][1] = s11; r.s[2][1] = s21; r.s[3][1] = s31;
        }
    }
    unsigned a0, a1;
    if (early.on) dma16x4(c.sws, c.vl16, early.soff, early.ldso);
    a_exp_pack<false>(r, a0, a1, early);
    a0 = hmax(a0); a1 = hmax(a1);
    const unsigned lo = FIRST ? P_LO : 0u;
    if (__builtin_expect(__any(a0 > P_HI || a1 > P_HI || a0 < lo || a1 < lo), 0)) {
        float x0 = r.s[0][0][0], x1 = r.s[0][1][0];
#pragma unroll
        for (int kt = 0; kt < 4; ++kt)
#pragma unroll
            for (int i = 0; i < 4; ++i) { x0 = __builtin_fmaxf(x0, r.s[kt][0][i]); x1 = __builtin_fmaxf(x1, r.s[kt][1][i]); }
        x0 = xmax4(x0); x1 = xmax4(x1);
        if (!FIRST) { x0 = __builtin_fmaxf(x0, 0.f); x1 = __builtin_fmaxf(x1, 0.f);
            const float f0 = __builtin_amdgcn_exp2f(-x0), f1 = __builtin_amdgcn_exp2f(-x1);
#pragma unroll
            for (int d = 0; d < 8; ++d) { r.o[d][0] *= f0; r.o[d][1] *= f1; }
            r.ls[0] *= f0; r.ls[1] *= f1; }
        z.m0 += x0; z.m1 += x1;
        r.negm[0] = (f32x4){-z.m0, -z.m0, -z.m0, -z.m0}; r.negm[1] = (f32x4){-z.m1, -z.m1, -z.m1, -z.m1};
#pragma unroll
        for (int kt = 0; kt < 4; ++kt) { r.s[kt][0] -= x0; r.s[kt][1] -= x1; }
        a_exp_pack<false>(r, a0, a1, early);
    }
    if (SWAP) {
        z.m0 = 0.f; z.m1 = 0.f;
        r.negm[0] = (f32x4){0.f, 0.f, 0.f, 0.f}; r.negm[1] = (f32x4){0.f, 0.f, 0.f, 0.f};
    }
    SBAR();
    PT(0);
    if (FIN) { if (early.on) WAITV(12); else WAITV(8); } else { if (early.on) WAITV(4); else WAITV(0); }
    if (s == 16) { asm volatile("" : "+v"(seenA)); check_flags(y.flags, FL_ACT + 32 * y.w + (lane_id() & 31), seenA, y.token); }
    PT(1);
    BARRIER(); SBAR();
    PT(2);
    __builtin_amdgcn_s_setprio(PRIOM);
    a_run<0, 2>(r, vp, kp, late, std::make_integer_sequence<int, 16>{});
    if (s < 16) a_run<16, 2>(r, vp, kp, late, std::make_integer_sequence<int, 16>{});
    __builtin_amdgcn_s_setprio(PRIOV);
    SBAR();
    PT(3);
    if (late.on) WAITV(4); else WAITV(0);
    PT(4);
    BARRIER(); SBAR();
    PT(5);
}

struct Lean { unsigned e_soff, e_lds, l_soff, l_lds, voff, koff; };
template <bool LATE> DI void a_slot_lean(const AttnCtx& c, AttnRegs& r, AttnSm& z, const Lean& st PARG) {
    const lcptr vp = c.vl + st.voff, kp = c.kl + st.koff;
    Piece early, late;
    early.sws = c.sws; early.vl16 = c.vl16; early.soff = st.e_soff; early.ldso = st.e_lds; early.on = true;
    late.sws = c.sws; late.vl16 = c.vl16; late.soff = st.l_soff; late.ldso = st.l_lds; late.on = true;
    a_pre<0>(r, vp, kp, std::make_integer_sequence<int, PD>{});
    SBAR();
    unsigned a0, a1;
    a_exp_pack<true>(r, a0, a1, early);
    if (__builtin_expect(__any(hmax(pkmax(a0, a1)) > P_HI), 0)) {
        float x0 = 0.f, x1 = 0.f;
#pragma unroll
        for (int kt = 0; kt < 4; ++kt)
#pragma unroll
            for (int i = 0; i < 4; ++i) { x0 = __builtin_fmaxf(x0, r.s[kt][0][i]); x1 = __builtin_fmaxf(x1, r.s[kt][1][i]); }
        x0 = xmax4(x0); x1 = xmax4(x1);
        const float f0 = __builtin_amdgcn_exp2f(-x0), f1 = __builtin_amdgcn_exp2f(-x1);
#pragma unroll
        for (int d = 0; d < 8; ++d) { r.o[d][0] *= f0; r.o[d][1] *= f1; }
        r.ls[0] *= f0; r.ls[1] *= f1;
        z.m0 += x0; z.m1 += x1;
        r.negm[0] = (f32x4){-z.m0, -z.m0, -z.m0, -z.m0}; r.negm[1] = (f32x4){-z.m1, -z.m1, -z.m1, -z.m1};
#pragma unroll
        for (int kt = 0; kt < 4; ++kt) { r.s[kt][0] -= x0; r.s[kt][1] -= x1; }
        a_exp_pack<false>(r, a0, a1, early);
    }
    SBAR();
    PT(0);
    WAITV(4);
    PT(1);
    BARRIER(); SBAR();
    PT(2);
    __builtin_amdgcn_s_setprio(PRIOM);
    a_run<0, LATE ? 1 : 0>(r, vp, kp, late, std::make_integer_sequence<int, 32>{});
    __builtin_amdgcn_s_setprio(PRIOV);
    SBAR();
    PT(3);
    if constexpr (LATE) WAITV(4); else WAITV(0);
    PT(4);
    BARRIER(); SBAR();
    PT(5);
}

#define P4W0 11
constexpr unsigned P4STG = 36864, P4TAG = 0x3FFu, P4OOB = 0xF0000000u;
struct P4Ctx { rsrc_t sws; unsigned vl16, ga, gw, gas, gws, dw0, dw1, dw2, lw, tag, so[3], so2[3], lo[3], lo2[3]; int w, q, o, ng, ra, rw; lcptr pa, pw; };
struct P4Regs { bf16x8 xa[2][2], wa[2][5]; };
struct P4Xch { f32x4 own[3]; u32x4 snd[9], rcv[9]; };
DI void mfa(f32x4& acc, bf16x8 a, bf16x8 b) { asm volatile("v_mfma_f32_16x16x32_bf16 %0, %1, %2, %0" : "+v"(acc) : "v"(a), "v"(b)); }
template <int OFF> DI void dma_sc1(const P4Ctx& c, unsigned soff, unsigned ldso) {
    asm volatile("s_mov_b32 m0, %0\n\ts_nop 3\n\tbuffer_load_dwordx4 %1, %2, %3 offen offset:%4 sc1 lds" ::"s"(ldso), "v"(c.vl16), "s"(c.sws), "s"(soff), "i"(OFF) : "memory");
}
template <int P> DI void p4_piece(P4Ctx& c, unsigned st) {
    if constexpr (P == 0) dma_sc1<0>(c, c.ga, st + c.w * 2048);
    else if constexpr (P == 1) { dma_sc1<1024>(c, c.ga, st + c.w * 2048); ++c.ra; c.ga = c.ra == c.ng ? c.gas : c.ga + 2048; }
    else if constexpr (P == 2) dma_sc1<0>(c, c.gw + c.dw0, st + c.lw);
    else if constexpr (P == 3) dma_sc1<0>(c, c.gw + c.dw1, st + c.lw + 8192);
    else { if (c.w < 4) dma_sc1<0>(c, c.gw + c.dw2, st + c.lw + 16384); ++c.rw; c.gw = c.rw == c.ng ? c.gws : c.gw + 81920; }
}
template <int B, int UP, int I> DI void p4_rd(const P4Ctx& c, P4Regs& r, unsigned st) {
    if constexpr (I == 0) r.wa[B][0] = ld128(c.pw + st + UP * 10240);
    else if constexpr (I <= 2) r.xa[B][I - 1] = ld128(c.pa + st + (I - 1) * 2048 + UP * 1024);
    else if constexpr (I < 7) r.wa[B][I - 2] = ld128(c.pw + st + UP * 10240 + (I - 2) * 1024);
}
template <int N> DI void p4_put(const P4Ctx& c, const P4Xch& x, unsigned xoff) { st16_sc1o<(N % 3) * 1024>(c.sws, x.snd[N], c.vl16, (N % 3 == 2 ? c.so2[N / 3] : c.so[N / 3]) + xoff); }
template <int N> DI void p4_get(const P4Ctx& c, P4Xch& x, unsigned xoff) {
    asm volatile("buffer_load_dwordx4 %0, %1, %2, %3 offen offset:%4 sc1" : "=v"(x.rcv[N]) : "v"(c.vl16), "s"(c.sws), "s"((N % 3 == 2 ? c.lo2[N / 3] : c.lo[N / 3]) + xoff), "i"((N % 3) * 1024) : "memory");
}
template <int O> DI void p4_split(const f32x4 (&acc)[10], unsigned tag, P4Xch& x) {
#pragma unroll
    for (int k = 0; k < 3; ++k) x.own[k] = acc[O + 4 * k < 10 ? O + 4 * k : O];
#pragma unroll
    for (int n = 0; n < 9; ++n) { const int i = ((O + 1 + n / 3) & 3) + 4 * (n % 3);
        const u32x4 v = __builtin_bit_cast(u32x4, acc[i < 10 ? i : 0]);
#pragma unroll
        for (int e = 0; e < 4; ++e) x.snd[n][e] = (v[e] & ~P4TAG) | tag; }
}
DI void p4_split(const P4Ctx& c, f32x4 (&a)[10], P4Xch& x) {
    asm volatile("s_nop 7\n\ts_nop 7\n\ts_nop 7" : "+v"(a[0]), "+v"(a[1]), "+v"(a[2]), "+v"(a[3]), "+v"(a[4]), "+v"(a[5]), "+v"(a[6]), "+v"(a[7]), "+v"(a[8]), "+v"(a[9]) :: "memory");
    switch (c.o) { case 0: p4_split<0>(a, c.tag, x); break; case 1: p4_split<1>(a, c.tag, x); break; case 2: p4_split<2>(a, c.tag, x); break; default: p4_split<3>(a, c.tag, x); break; }
}
DI bool p4_tagged(const P4Ctx& c, const P4Xch& x) {
    unsigned a = 0, b = 0;
#pragma unroll
    for (int n = 0; n < 9; ++n)
#pragma unroll
        for (int e = 0; e < 4; ++e) { if (n % 3 == 2) b |= x.rcv[n][e] ^ c.tag; else a |= x.rcv[n][e] ^ c.tag; }
    return __all(((a | (c.o < 2 ? b : 0u)) & P4TAG) == 0);
}
template <int... Ns> DI void p4_refetch(const P4Ctx& c, P4Xch& x, unsigned xoff, std::integer_sequence<int, Ns...>) {
    const unsigned long long t0 = __builtin_amdgcn_s_memrealtime();
    for (;;) {
        (p4_get<Ns>(c, x, xoff), ...);
        asm volatile("s_waitcnt vmcnt(0)" : "+v"(x.rcv[0]), "+v"(x.rcv[1]), "+v"(x.rcv[2]), "+v"(x.rcv[3]), "+v"(x.rcv[4]), "+v"(x.rcv[5]), "+v"(x.rcv[6]), "+v"(x.rcv[7]), "+v"(x.rcv[8]) :: "memory");
        if (p4_tagged(c, x)) break;
        if (__builtin_amdgcn_s_memrealtime() - t0 > 2000000ull) break;
        __builtin_amdgcn_s_sleep(1);
    }
}
DI f32x4 p4_untag(u32x4 v) { return __builtin_bit_cast(f32x4, v & ~P4TAG); }
DI f32x4 p4_total(const P4Xch& x, int k) { return (x.own[k] + p4_untag(x.rcv[k])) + (p4_untag(x.rcv[3 + k]) + p4_untag(x.rcv[6 + k])); }
template <int B, bool RD, int DMA, int PUT, int GET, int... Is> DI void p4_half(P4Ctx& c, P4Regs& r, P4Xch& x, f32x4 (&acc)[10], unsigned rs, unsigned ds, std::integer_sequence<int, Is...>) {
    auto one = [&](auto ic) {
        constexpr int I = decltype(ic)::value;
        mfa(acc[I], r.wa[B][I >> 1], r.xa[B][I & 1]);
        if constexpr (RD) p4_rd<B ^ 1, B ^ 1, I>(c, r, rs);
        if constexpr (DMA == 1) { if constexpr (I == 1) p4_piece<0>(c, ds); if constexpr (I == 5) p4_piece<1>(c, ds); if constexpr (I == 9) p4_piece<2>(c, ds); }
        if constexpr (DMA == 2) { if constexpr (I == 3) p4_piece<3>(c, ds); if constexpr (I == 7) p4_piece<4>(c, ds); }
        if constexpr (PUT >= 0 && B == 0 && I == 5) p4_put<PUT>(c, x, OFF_XG);
        if constexpr (GET >= 0 && (I & 1) && GET + (I >> 1) < 9) p4_get<GET + (I >> 1)>(c, x, OFF_XG);
        SBAR();
    };
    (one(std::integral_constant<int, Is>{}), ...);
}
template <int WAITN, bool RA, bool RB, bool LAST, int EV, int PUT, int GETA, int GETB> DI void p4_step(P4Ctx& c, const Sync& y, P4Regs& r, P4Xch& x, f32x4 (&acc)[10], unsigned& sb, unsigned& sp, int t, unsigned fidx, u64& seen) {
    constexpr auto seq = std::make_integer_sequence<int, 10>{};
    const unsigned sn = sb == 3 * P4STG ? 0 : sb + P4STG;
    p4_half<0, true, RA ? 2 : 0, PUT, GETA>(c, r, x, acc, sb, sp, seq);
    if constexpr (!LAST) {
        asm volatile("s_waitcnt vmcnt(%0) lgkmcnt(0)" :: "i"(WAITN) : "memory");
        if constexpr (EV == 1) { if (t == c.ng - 4 && c.w == 0) { asm volatile("s_waitcnt vmcnt(4)" : "+v"(seen) :: "memory"); check_flags(y.flags, fidx, seen, y.token); } }
        BARRIER();
        if constexpr (EV == 1) {
            if (c.w == 0 && t == c.ng - 5) seen = poll_asm(y.flags, fidx);
        }
        if constexpr (EV == 2) { if (c.w == 0 && lane_id() == 0) set_flag(y.flags, FL_CTX + y.j * 32 + y.h, y.token); }
        SBAR();
    }
    p4_half<1, !LAST, RB ? 1 : 0, PUT, GETB>(c, r, x, acc, sn, sb, seq);
    sp = sb; sb = sn;
}

template <int... Is> DI void p4_ssteps(P4Ctx& c, const Sync& y, P4Regs& r, P4Xch& x, f32x4 (&acc)[10], unsigned& sb, unsigned& sp, u64& seen, std::integer_sequence<int, Is...>) {
    (p4_step<8 + (Is >= 2 && Is <= 10) + (Is >= 1 && Is <= 9) + (Is <= 8), true, true, false, 0, (Is <= 8 ? Is : -1), -1, -1>(c, y, r, x, acc, sb, sp, 0, 0, seen), ...);
}

__global__ __launch_bounds__(512, 2) void fused_kernel(const bf16_t* __restrict__ x, const bf16_t* __restrict__ ckv, const bf16_t* __restrict__ wq, const bf16_t* __restrict__ wuk,
                                                       const bf16_t* __restrict__ wuv, const bf16_t* __restrict__ wo, const bf16_t* __restrict__ w1, const bf16_t* __restrict__ b1,
                                                       const bf16_t* __restrict__ w2, bf16_t* __restrict__ out, char* __restrict__ ws) {
    __shared__ __attribute__((aligned(1024))) char smem[163840];
    const lptr lds = (lptr)smem;
    const int b = blockIdx.x, xcd = b & 7, bi = b >> 3, h = xcd * 4 + (bi >> 3), j = bi & 7;
    const int w = __builtin_amdgcn_readfirstlane(threadIdx.x >> 6);
    const rsrc_t sws = mk_rsrc(ws, WS_BYTES);
    STAMP(0);
    Sync y;
    y.flags = (gu64*)(ws + OFF_FLAGS); y.b = b; y.h = h; y.j = j; y.w = w;
    u64 epoch;
    asm volatile("s_load_dwordx2 %0, %1, 0x20" : "=s"(epoch) : "s"(ws) : "memory");
    u32x4 kv[16];
    P1Ctx c1;
    P2Ctx c2;
    const bool isv = (w & 3) >= 2;
    unsigned sink = 0;
    {
        const int l = lane_id(), g = l >> 4, q = (l & 15) >> 2;
        {
            const int nb = w >> 1;
            c1.w = w; c1.sws = sws; c1.wo = wo; c1.w2 = w2; c1.wq = wq; c1.w1 = w1; c1.lds = lds;
            c1.sx = mk_rsrc(ckv, 2048 * 512 * 2);
            c1.sw = mk_rsrc(nb < 2 ? wuk : wuv, 512 * 4096 * 2);
#pragma unroll
            for (int par = 0; par < 2; ++par) {
                c1.vx[par] = (l >> 3) * 1024 + (((l & 7) ^ (4 * par + (l >> 4))) * 16);
                c1.vw[par] = (l >> 3) * 8192 + (((l & 7) ^ ((((l >> 4) & 1) | (par << 1)) << 1)) * 16);
            }
            c1.sxo = (256 * j + 32 * w) * 1024;
            c1.swo = (32 * (w & 1)) * 8192 + (h * 128 + 64 * (nb & 1)) * 2;
            const int wn = w & 3, wm = w >> 2, f = (l & 15) >> 1, s = ((q >> 1) & 1) | ((g & 1) << 1);
            c1.xb[0] = lds + wm * 16384 + (l & 15) * 128 + ((g ^ f) * 16);
            c1.xb[1] = lds + wm * 16384 + (l & 15) * 128 + (((g ^ f) ^ 4) * 16);
#pragma unroll
            for (int nt = 0; nt < 4; ++nt) c1.wb[nt] = lds + 65536 + wn * 8192 + (8 * g + q) * 128 + ((nt ^ s) * 32) + (l & 3) * 8;
        }
        p1_dma(c1, 0, 0);
        u32x4 xo0, xo1;
        {
            const rsrc_t sxi = mk_rsrc(x, 2048 * 640 * 2);
            const unsigned vx = (l & 7) * 1280 + (l >> 3) * 16, so0 = b * 8 * 1280 + w * 128, so1 = b * 8 * 1280 + (8 + (w & 1)) * 128;
            asm volatile("s_nop 4\n\tbuffer_load_dwordx4 %0, %2, %3, %4 offen\n\tbuffer_load_dwordx4 %1, %2, %3, %5 offen" : "=&v"(xo0), "=&v"(xo1) : "v"(vx), "s"(sxi), "s"(so0), "s"(so1) : "memory");
        }
        p1_dma(c1, 1, 1);
        asm volatile("s_waitcnt lgkmcnt(0)" : "+s"(epoch) :: "memory");
        y.token = epoch + 1;
        STAMP(12);
        asm volatile("s_waitcnt vmcnt(8)" : "+v"(xo0), "+v"(xo1) :: "memory");
        STAMP(13);
        float v0[8], v1[8];
#pragma unroll
        for (int e = 0; e < 4; ++e) { v0[2 * e] = bflo(xo0[e]); v0[2 * e + 1] = bfhi(xo0[e]); v1[2 * e] = bflo(xo1[e]); v1[2 * e + 1] = bfhi(xo1[e]); }
        auto red8 = [](float t) { t += __uint_as_float(__builtin_amdgcn_update_dpp(0u, __float_as_uint(t), 0x128, 0xf, 0xf, false)); return xsum4(t); };
        {
            const float m0 = red8(((v0[0] + v0[1]) + (v0[2] + v0[3])) + ((v0[4] + v0[5]) + (v0[6] + v0[7]))) * (1.f / 64.f);
            const float m1 = red8(((v1[0] + v1[1]) + (v1[2] + v1[3])) + ((v1[4] + v1[5]) + (v1[6] + v1[7]))) * (1.f / 64.f);
            float q0 = 0.f, q1 = 0.f;
#pragma unroll
            for (int e = 0; e < 8; ++e) { const float d0 = v0[e] - m0, d1 = v1[e] - m1; q0 += d0 * d0; q1 += d1 * d1; }
            q0 = red8(q0); q1 = red8(q1);
            if (l < 8) { *(LAS f32x2*)(lds + 162816 + l * 80 + w * 8) = (f32x2){m0, q0}; if (w < 2) *(LAS f32x2*)(lds + 162816 + l * 80 + 64 + w * 8) = (f32x2){m1, q1}; }
        }
        asm volatile("s_waitcnt lgkmcnt(0)" ::: "memory"); BARRIER();
        STAMP(14);
        {
            f32x4 t[5];
#pragma unroll
            for (int i = 0; i < 5; ++i) t[i] = *(const LAS f32x4*)(lds + 162816 + (l & 7) * 80 + i * 16);
            float mean = 0.f;
#pragma unroll
            for (int i = 0; i < 5; ++i) mean += t[i][0] + t[i][2];
            mean *= 0.1f;
            float m2 = 0.f;
#pragma unroll
            for (int i = 0; i < 5; ++i) { const float d0 = t[i][0] - mean, d1 = t[i][2] - mean; m2 += (t[i][1] + t[i][3]) + 64.f * (d0 * d0 + d1 * d1); }
            const float rstd = 1.f / __builtin_sqrtf(m2 * (1.f / 640.f) + 1e-5f);
            const unsigned vo = (l >> 3) * 256 + (l & 7) * 16, xo = OFF_XN + (b >> 1) * 20480 + (b & 1) * 128;
            u32x4 o0, o1;
#pragma unroll
            for (int e = 0; e < 4; ++e) { o0[e] = cvtpk((v0[2 * e] - mean) * rstd, (v0[2 * e + 1] - mean) * rstd); o1[e] = cvtpk((v1[2 * e] - mean) * rstd, (v1[2 * e + 1] - mean) * rstd); }
            st16_sc1(sws, o0, vo, xo + w * 2048);
            if (w < 2) st16_sc1(sws, o1, vo, xo + (8 + w) * 2048);
        }
        {
            const char* pa = (const char*)ckv + (size_t)(256 * j + 64 * (h & 3) + 8 * w + (l >> 3)) * 1024 + (l & 7) * 128;
            const char* pw = (const char*)((w & 1) ? wuv : wuk) + (size_t)(64 * j + 16 * (w >> 1) + ((l & 31) >> 1)) * 8192 + h * 256 + (l & 1) * 128;
            asm volatile("global_load_dword %0, %1, off\n\tglobal_load_dword %0, %2, off" : "+v"(sink) : "v"(pa), "v"(pw) : "memory");
        }
        {
            c2.w = w; c2.sws = sws; c2.swq = mk_rsrc(wq, 640 * 4096 * 2); c2.sw1 = mk_rsrc(w1, 640 * 1728 * 2);
            c2.vl16 = l * 16;
            const int swz = ((l >> 4) & 1) | ((w & 1) << 1);
            c2.vwq = (l >> 3) * 8192 + (((l & 7) ^ (swz << 1)) * 16);
            c2.vw1 = (l >> 3) * 3456 + (((l & 7) ^ (swz << 1)) * 16);
            { const int mtl = 2 * w, mtg = mtl < 8 ? 8 * j + mtl : 8 * (15 - j) + (mtl - 8); c2.sxo = OFF_XN + mtg * 20480; }
            c2.swqo = (8 * w) * 8192 + h * 256;
            c2.sw1o = (8 * w) * 3456 + h * 128;
            { const int wn = w & 3, wm = w >> 2;
              c2.skv = isv ? OFF_V + (unsigned)(h * 32 + 4 * j + 2 * wm) * 16384 + (wn - 2) * 4096 : OFF_K + (unsigned)((h * 128 + 16 * j + 8 * wm) * 4 + 2 * wn) * 1024; }
        }
    }
    STAMP(1); CSTAMP(14);
    if (isv) p1_main<true>(c1, c2, y, sink, kv); else p1_main<false>(c1, c2, y, sink, kv);
    STAMP(2); CSTAMP(15);
    p2_kvst<0>(c2, kv, isv); p2_kvst<1>(c2, kv, isv); p2_kvst<2>(c2, kv, isv); p2_kvst<3>(c2, kv, isv);
    const bool hasf = h < 27;
    AttnCtx c3;
    AttnRegs r;
    {
        const int l = lane_id(), g = l >> 4, q = (l & 15) >> 2;
        const int wn = w & 1, wm = w >> 1;
        const int s = ((q >> 1) & 1) | ((g & 1) << 1);
        c2.xb = lds + wm * 8192 + l * 16;
#pragma unroll
        for (int nt = 0; nt < 4; ++nt) c2.wq[nt] = lds + 65536 + wn * 8192 + (8 * g + q) * 128 + ((nt ^ s) * 32) + (l & 3) * 8;
#pragma unroll
        for (int ft = 0; ft < 2; ++ft) c2.wf[ft] = lds + 65536 + 16384 + (8 * g + q) * 128 + (((2 * wn + ft) ^ s) * 32) + (l & 3) * 8;
        f32x4 acc[6][4];
#pragma unroll
        for (int a = 0; a < 6; ++a)
#pragma unroll
            for (int m = 0; m < 4; ++m) acc[a][m] = (f32x4){0.f, 0.f, 0.f, 0.f};
        STAMP(3);
        if (hasf) p2_main<true>(c2, y, acc, kv, isv, std::make_integer_sequence<int, 10>{}); else p2_main<false>(c2, y, acc, kv, isv, std::make_integer_sequence<int, 10>{});
        STAMP(4);
        constexpr float C2 = 0.08838834764831845f * 1.4426950408889634f;
#pragma unroll
        for (int mt = 0; mt < 4; ++mt)
#pragma unroll
            for (int a = 0; a < 2; ++a)
                *(LAS u32x4*)(lds + 98304 + ((4 * wm + mt) * 4 + 2 * wn + a) * 1024 + l * 16) = pack8(acc[2 * a][mt] * C2, acc[2 * a + 1][mt] * C2);
        u32x4 actf[4];
        if (hasf) {
            const int f0 = 64 * h + 32 * wn + 4 * g;
            const u32x2 bA = *(const u32x2*)(b1 + f0), bB = *(const u32x2*)(b1 + f0 + 16);
            const f32x4 biasA = {bflo(bA[0]), bfhi(bA[0]), bflo(bA[1]), bfhi(bA[1])}, biasB = {bflo(bB[0]), bfhi(bB[0]), bflo(bB[1]), bfhi(bB[1])};
#pragma unroll
            for (int mt = 0; mt < 4; ++mt) {
                f32x4 hA = acc[4][mt] + biasA, hB = acc[5][mt] + biasB;
#pragma unroll
                for (int i = 0; i < 4; ++i) { hA[i] = mish(hA[i]); hB[i] = mish(hB[i]); }
                actf[mt] = pack8(hA, hB);
            }
        }
        asm volatile("s_waitcnt vmcnt(0) lgkmcnt(0)" ::: "memory"); BARRIER();
        const int grp = w >> 2, qg = w & 3;
        c3.sws = sws; c3.vl16 = l * 16; c3.kbase = OFF_K + (unsigned)h * 524288; c3.vbase = OFF_V + (unsigned)h * 524288;
        c3.ctxl = OFF_CTX + ((8 * j + 2 * qg) * 128 + 4 * h) * 1024;
        c3.nL = 2 * j + 2; c3.grp = grp; c3.qg = qg; c3.j = j; c3.rowrel = (l & 15) - 4 * g; c3.kl = lds + l * 16; c3.vl = lds + 65536 + l * 16;
        c3.ql = lds + 131072 + qg * 8192 + l * 16;
        if (grp == 0) { a_dmaK(c3, 0); a_dmaV(c3, 0); } else { a_dmaK(c3, 1); }
        if (hasf) {
#pragma unroll
            for (int mt = 0; mt < 4; ++mt) { const int mtl = 4 * wm + mt, mtg = mtl < 8 ? 8 * j + mtl : 8 * (15 - j) + (mtl - 8);
                st16_sc1(sws, actf[mt], l * 16, OFF_ACT + (mtg * 54 + 2 * h + wn) * 1024); }
        }
#pragma unroll
        for (int qt = 0; qt < 2; ++qt)
#pragma unroll
            for (int u = 0; u < 4; ++u) r.q[qt][u] = ld128(lds + 98304 + ((8 * grp + 2 * qg + qt) * 4 + u) * 1024 + l * 16);
    }
    STAMP(5);
    P4Ctx c4;
    {
        const int l = lane_id();
        const int grp = c3.grp, qg = c3.qg;
        const unsigned vl16 = c3.vl16;
#pragma unroll
        for (int d = 0; d < 8; ++d) { r.o[d][0] = (f32x4){0.f, 0.f, 0.f, 0.f}; r.o[d][1] = (f32x4){0.f, 0.f, 0.f, 0.f}; }
        r.negm[0] = (f32x4){0.f, 0.f, 0.f, 0.f}; r.negm[1] = (f32x4){0.f, 0.f, 0.f, 0.f};
        AttnSm z; z.m0 = 0.f; z.m1 = 0.f;
        r.ls[0] = (f32x4){0.f, 0.f, 0.f, 0.f}; r.ls[1] = (f32x4){0.f, 0.f, 0.f, 0.f};
        r.ones = (bf16x8){0x3F80, 0x3F80, 0x3F80, 0x3F80, 0x3F80, 0x3F80, 0x3F80, 0x3F80}; PIN(r.ones);
        if (hasf) asm volatile("s_waitcnt vmcnt(4) lgkmcnt(0)" ::: "memory"); else asm volatile("s_waitcnt vmcnt(0) lgkmcnt(0)" ::: "memory");
        BARRIER();
        STAMP(6); CSTAMP(12);
        a_pre<16>(r, c3.vl, c3.kl, std::make_integer_sequence<int, PD>{});
        if (grp == 1) BARRIER();
        { Piece none; none.on = false; none.sws = sws; none.vl16 = 0; none.soff = 0; none.ldso = 0; a_run<16, 0>(r, c3.vl, c3.kl, none, std::make_integer_sequence<int, 16>{}); }
        BARRIER();
        c3.swap_s = grp == 0 ? c3.nL - 1 : 99; c3.first_s = 0;
        asm volatile("" : "+s"(c3.swap_s), "+s"(c3.first_s));
        {
            const int nL = c3.nL;
            auto lean_init = [&](int s) {
                Lean st;
                const bool eK = grp == 1 && s + 2 < nL;
                const int t0 = s < nL ? s : 2 * s - nL + grp, t1 = s + 1 < nL ? s + 1 : 2 * s + 2 - nL + grp, t2 = s + 2 < nL ? s + 2 : 2 * s + 4 - nL + grp;
                const int te = eK ? t2 : t1;
                st.e_soff = (eK ? c3.kbase : c3.vbase) + te * 16384 + qg * 4096; st.e_lds = (eK ? 0 : 65536) + (te & 3) * 16384 + qg * 4096;
                st.l_soff = c3.kbase + t2 * 16384 + qg * 4096; st.l_lds = (t2 & 3) * 16384 + qg * 4096;
                st.voff = (t0 & 3) * 16384; st.koff = (t1 & 3) * 16384;
                return st;
            };
            int s = 0, phase = 0;
#pragma nounroll
            for (;;) {
                const int gend = phase == 0 ? 1 : (phase == 1 ? (grp == 0 ? nL + 1 : nL - 1) : 17);
#pragma nounroll
                while (s < gend) { a_slotfull(c3, y, r, z, s PPASSG); ++s; }
                if (phase == 0) { if (w == 0 && l == 0) set_flag(y.flags, FL_ACT + j * 32 + h, y.token); }
                if (phase == 2 || s > 16) break;
                if (phase == 1 && s <= 14) {
                    Lean st = lean_init(s);
#pragma nounroll
                    do { a_slot_lean<true>(c3, r, z, st PPASS);
                         st.e_soff += 32768; st.e_lds ^= 32768; st.l_soff += 32768; st.l_lds ^= 32768; st.voff ^= 32768; st.koff ^= 32768; ++s; } while (s <= 14);
                }
                const int send = phase == 0 ? nL - 2 : 16;
                if (s < send) {
                    Lean st = lean_init(s);
#pragma nounroll
                    do { a_slot_lean<false>(c3, r, z, st PPASS);
                         st.e_soff += 16384; st.e_lds = (st.e_lds & ~0xC000u) | ((st.e_lds + 16384) & 0xC000u); st.voff = (st.voff + 16384) & 0xC000u; st.koff = (st.koff + 16384) & 0xC000u; ++s; } while (s < send);
                }
                ++phase;
            }
        }
        const float m0 = z.m0, m1 = z.m1, l0 = r.ls[0][0], l1 = r.ls[1][0];
        if (grp == 0) BARRIER();
        STAMP(7); CSTAMP(13);
        {
            const int q = xcd >> 1, rbk = (xcd & 1) * 8 + (bi >> 2), cb = bi & 3, wm = w >> 1, wn = w & 1;
            c4.sws = sws; c4.vl16 = vl16; c4.w = w; c4.q = q; c4.o = (q + 2 * wn) & 3; c4.ng = q == 3 ? 6 : 7; c4.ra = 0; c4.rw = 0;
            c4.ga = OFF_ACT + ((8 * rbk + w) * 54 + 14 * q) * 1024; c4.gas = OFF_CTX + ((8 * rbk + w) * 128 + 32 * q) * 1024;
            c4.gw = OFF_WP + ((128 + 14 * q) * 40 + 10 * cb) * 1024; c4.gws = OFF_WP + (32 * q * 40 + 10 * cb) * 1024;
            c4.dw0 = w * 1024; c4.dw1 = (w < 2 ? 8 + w : 38 + w) * 1024; c4.dw2 = (46 + w) * 1024; c4.lw = 16384 + w * 1024;
            c4.tag = (unsigned)y.token & P4TAG;
            const unsigned xb = (unsigned)(((xcd & 1) * 32 + bi) * 128 + w) * 3072;
#pragma unroll
            for (int d = 0; d < 3; ++d) { const int qq = (q + 1 + d) & 3;
                c4.so[d] = xb + qq * 98304 + q * 24576; c4.so2[d] = ((c4.o + 1 + d) & 3) < 2 ? c4.so[d] : P4OOB;
                c4.lo[d] = xb + q * 98304 + qq * 24576; c4.lo2[d] = c4.o < 2 ? c4.lo[d] : P4OOB; }
            c4.pa = lds + (4 * wm) * 1024 + vl16; c4.pw = lds + 16384 + (5 * wn) * 1024 + vl16;
        }
        p4_piece<0>(c4, 2 * P4STG); p4_piece<1>(c4, 2 * P4STG); p4_piece<2>(c4, 2 * P4STG); p4_piece<3>(c4, 2 * P4STG); p4_piece<4>(c4, 2 * P4STG);
        p4_piece<0>(c4, 3 * P4STG); p4_piece<1>(c4, 3 * P4STG); p4_piece<2>(c4, 3 * P4STG); p4_piece<3>(c4, 3 * P4STG); p4_piece<4>(c4, 3 * P4STG);
        {
            const lptr xo = lds + (qg * 2 + grp) * 9216;
#pragma unroll
            for (int d = 0; d < 4; ++d)
#pragma unroll
                for (int qt = 0; qt < 2; ++qt) *(LAS f32x4*)(xo + (d * 2 + qt) * 1024 + l * 16) = sel(grp == 0, r.o[4 + d][qt], r.o[d][qt]);
            *(LAS f32x4*)(xo + 8192 + l * 16) = (f32x4){m0, m1, l0, l1};
            asm volatile("s_waitcnt lgkmcnt(0)" ::: "memory"); BARRIER();
            const lcptr xi = lds + (qg * 2 + (grp ^ 1)) * 9216;
            const f32x4 st = *(const LAS f32x4*)(xi + 8192 + l * 16);
            const float M0 = __builtin_fmaxf(m0, st[0]), M1 = __builtin_fmaxf(m1, st[1]);
            const float wa0 = __builtin_amdgcn_exp2f(m0 - M0), wb0 = __builtin_amdgcn_exp2f(st[0] - M0), wa1 = __builtin_amdgcn_exp2f(m1 - M1), wb1 = __builtin_amdgcn_exp2f(st[1] - M1);
            const float i0 = __builtin_amdgcn_rcpf(wa0 * l0 + wb0 * st[2]), i1 = __builtin_amdgcn_rcpf(wa1 * l1 + wb1 * st[3]);
            const float ca0 = wa0 * i0, cb0 = wb0 * i0, ca1 = wa1 * i1, cb1 = wb1 * i1;
#pragma unroll
            for (int up = 0; up < 2; ++up)
#pragma unroll
                for (int qt = 0; qt < 2; ++qt) {
                    const f32x4 pa = *(const LAS f32x4*)(xi + ((2 * up) * 2 + qt) * 1024 + l * 16), pb = *(const LAS f32x4*)(xi + ((2 * up + 1) * 2 + qt) * 1024 + l * 16);
                    const f32x4 oa = sel(grp == 0, r.o[2 * up][qt], r.o[4 + 2 * up][qt]), ob = sel(grp == 0, r.o[2 * up + 1][qt], r.o[4 + 2 * up + 1][qt]);
                    const float ca = qt ? ca1 : ca0, cb = qt ? cb1 : cb0;
                    st16_sc1(sws, pack8(oa * ca + pa * cb, ob * ca + pb * cb), vl16, OFF_CTX + ((8 * (15 - j) + 2 * qg + qt) * 128 + 4 * h + 2 * grp + up) * 1024);
                }
        }
    }
    STAMP(8);
    {
        asm volatile("s_waitcnt lgkmcnt(0)" ::: "memory"); BARRIER();
        p4_piece<0>(c4, 0); p4_piece<1>(c4, 0); p4_piece<2>(c4, 0); p4_piece<3>(c4, 0); p4_piece<4>(c4, 0);
        p4_piece<0>(c4, P4STG); p4_piece<1>(c4, P4STG); p4_piece<2>(c4, P4STG);
        const int l = lane_id();
        const int rbk = (xcd & 1) * 8 + (bi >> 2), cb = bi & 3;
        const unsigned cidx = FL_CTX + (rbk < 8 ? rbk : 15 - rbk) * 32 + 8 * c4.q + (l & 7);
        f32x4 accg[10], accs[10], gt[3];
#pragma unroll
        for (int i = 0; i < 10; ++i) { accg[i] = (f32x4){0.f, 0.f, 0.f, 0.f}; accs[i] = (f32x4){0.f, 0.f, 0.f, 0.f}; PIN(accg[i]); PIN(accs[i]); }
        P4Regs r4;
        P4Xch xg;
        const int row0 = 128 * rbk + 32 * (w >> 1) + (l & 15), col0 = 160 * cb + 80 * (w & 1) + 4 * (l >> 4);
        u32x2 xv[3];
        {
            const rsrc_t sx = mk_rsrc(x, 2048 * 640 * 2);
#pragma unroll
            for (int k = 0; k < 3; ++k) { const int i = c4.o + 4 * k < 10 ? c4.o + 4 * k : c4.o;
                asm volatile("buffer_load_dwordx2 %0, %1, %2, 0 offen" : "=v"(xv[k]) : "v"((unsigned)((row0 + (i & 1) * 16) * 640 + col0 + (i >> 1) * 16) * 2), "s"(sx) : "memory"); }
        }
        u64 seen = 0;
        asm volatile("s_waitcnt vmcnt(18)" ::: "memory"); BARRIER();
        unsigned sb = 2 * P4STG, sp = P4STG;
        p4_rd<0, 0, 0>(c4, r4, sb); p4_rd<0, 0, 1>(c4, r4, sb); p4_rd<0, 0, 2>(c4, r4, sb); p4_rd<0, 0, 3>(c4, r4, sb); p4_rd<0, 0, 4>(c4, r4, sb); p4_rd<0, 0, 5>(c4, r4, sb); p4_rd<0, 0, 6>(c4, r4, sb);
        SBAR();
        STAMP(9);
        p4_step<P4W0, true, true, false, 2, -1, -1, -1>(c4, y, r4, xg, accg, sb, sp, 0, cidx, seen);
#pragma nounroll
        for (int t = 1; t < c4.ng; ++t) p4_step<8, true, true, false, 1, -1, -1, -1>(c4, y, r4, xg, accg, sb, sp, t, cidx, seen);
        p4_split(c4, accg, xg);
        p4_ssteps(c4, y, r4, xg, accs, sb, sp, seen, std::make_integer_sequence<int, 12>{});
        p4_step<8, true, false, false, 0, -1, -1, 0>(c4, y, r4, xg, accs, sb, sp, 0, 0, seen);
        p4_step<13, false, false, false, 0, -1, 5, -1>(c4, y, r4, xg, accs, sb, sp, 0, 0, seen);
        p4_step<9, false, false, false, 0, -1, -1, -1>(c4, y, r4, xg, accs, sb, sp, 0, 0, seen);
        p4_step<0, false, false, true, 0, -1, -1, -1>(c4, y, r4, xg, accs, sb, sp, 0, 0, seen);
        asm volatile("s_waitcnt vmcnt(0)" : "+v"(xg.rcv[0]), "+v"(xg.rcv[1]), "+v"(xg.rcv[2]), "+v"(xg.rcv[3]), "+v"(xg.rcv[4]), "+v"(xg.rcv[5]), "+v"(xg.rcv[6]), "+v"(xg.rcv[7]), "+v"(xg.rcv[8]),
                     "+v"(xv[0]), "+v"(xv[1]), "+v"(xv[2]) :: "memory");
        constexpr auto nine = std::make_integer_sequence<int, 9>{};
        P4Xch xs;
        p4_split(c4, accs, xs);
        p4_put<0>(c4, xs, OFF_XS); p4_put<1>(c4, xs, OFF_XS); p4_put<2>(c4, xs, OFF_XS); p4_put<3>(c4, xs, OFF_XS); p4_put<4>(c4, xs, OFF_XS);
        p4_put<5>(c4, xs, OFF_XS); p4_put<6>(c4, xs, OFF_XS); p4_put<7>(c4, xs, OFF_XS); p4_put<8>(c4, xs, OFF_XS);
        TS(0);
        if (!p4_tagged(c4, xg)) p4_refetch(c4, xg, OFF_XG, nine);
#pragma unroll
        for (int k = 0; k < 3; ++k) {
            gt[k] = p4_total(xg, k);
#pragma unroll
            for (int e = 0; e < 4; ++e) gt[k][e] = gt[k][e] * __builtin_amdgcn_rcpf(1.f + __builtin_amdgcn_exp2f(-1.4426950408889634f * gt[k][e]));
            PIN(gt[k]);
        }
        TS(1);
        p4_refetch(c4, xs, OFF_XS, nine);
        TS(2);
#pragma unroll
        for (int k = 0; k < 3; ++k) {
            if (k < 2 || c4.o < 2) { const int i = c4.o + 4 * k;
                const f32x4 sv = p4_total(xs, k);
                const f32x4 o = {bflo(xv[k][0]) + sv[0] * gt[k][0], bfhi(xv[k][0]) + sv[1] * gt[k][1], bflo(xv[k][1]) + sv[2] * gt[k][2], bfhi(xv[k][1]) + sv[3] * gt[k][3]};
                const u32x2 ov = {cvtpk(o[0], o[1]), cvtpk(o[2], o[3])};
                asm volatile("global_store_dwordx2 %0, %1, off sc0 sc1\n\ts_nop 1" :: "v"(out + (row0 + (i & 1) * 16) * 640 + col0 + (i >> 1) * 16), "v"(ov) : "memory"); }
        }
        if (b == 0 && w == 0 && l == 0) __hip_atomic_store(y.flags + 4, y.token, __ATOMIC_RELAXED, __HIP_MEMORY_SCOPE_AGENT);
    }
    STAMP(11);
}

extern "C" void kernel_launch(void* const* d_in, const int* in_sizes, int n_in, void* d_out, int out_size, void* d_ws, size_t ws_size, hipStream_t stream) {
    (void)in_sizes; (void)n_in; (void)out_size; (void)ws_size;
    fused_kernel<<<256, 512, 0, stream>>>((const bf16_t*)d_in[0], (const bf16_t*)d_in[1], (const bf16_t*)d_in[2], (const bf16_t*)d_in[3], (const bf16_t*)d_in[4],
                                          (const bf16_t*)d_in[5], (const bf16_t*)d_in[7], (const bf16_t*)d_in[8], (const bf16_t*)d_in[9], (bf16_t*)d_out, (char*)d_ws);
}
```

```cpp
#include <hip/hip_runtime.h>
#include <stdint.h>
#include <utility>

typedef unsigned short bf16_t;
typedef short bf16x8 __attribute__((ext_vector_type(8)));
typedef short s16x4 __attribute__((ext_vector_type(4)));
typedef short v4i16_t __attribute__((ext_vector_type(4)));
typedef float f32x4 __attribute__((ext_vector_type(4)));
typedef float f32x2 __attribute__((ext_vector_type(2)));
typedef unsigned u32x4 __attribute__((ext_vector_type(4)));
typedef unsigned u32x2 __attribute__((ext_vector_type(2)));
typedef __bf16 bf16x2_t __attribute__((ext_vector_type(2)));
typedef unsigned short u16x2 __attribute__((ext_vector_type(2)));
typedef __amdgpu_buffer_rsrc_t rsrc_t;
typedef unsigned long long u64;
#define LAS __attribute__((address_space(3)))
#define GAS __attribute__((address_space(1)))
typedef LAS char* lptr;
typedef LAS const char* lcptr;
typedef GAS u64 gu64;
#define DI __device__ __forceinline__
#define SBAR() __builtin_amdgcn_sched_barrier(0)
#define PIN(x) asm volatile("" : "+v"(x))
#define MF16(a, b, c) __builtin_amdgcn_mfma_f32_16x16x32_bf16(a, b, c, 0, 0, 0)
#define WAITV(n) asm volatile("s_waitcnt vmcnt(" #n ")" ::: "memory")
#define WAITL(n) asm volatile("s_waitcnt lgkmcnt(" #n ")" ::: "memory")
#define BARRIER() do { asm volatile("" ::: "memory"); __builtin_amdgcn_s_barrier(); asm volatile("" ::: "memory"); } while (0)

#define UPTO 4
#define STAG 1
#define NOINV 1
#define P0EB 1
#define KVR 2
#define SPREAD 1
#define SPOS 1
#define XST 0
#define STAMPS 0

constexpr unsigned OFF_FLAGS = 0;
constexpr unsigned OFF_XN = 65536;
constexpr unsigned OFF_K = OFF_XN + 2621440;
constexpr unsigned OFF_V = OFF_K + 16777216;
constexpr unsigned OFF_ACT = OFF_V + 16777216 + 8388608;
constexpr unsigned OFF_CTX = OFF_ACT + 7077888;
constexpr unsigned OFF_WP = OFF_CTX + 16777216;
constexpr unsigned OFF_ST = OFF_WP + 182 * 40 * 1024;
constexpr unsigned OFF_XG = OFF_ST + 65536;
constexpr unsigned OFF_XS = OFF_XG + 25165824;
constexpr unsigned WS_BYTES = OFF_XS + 25165824;
constexpr unsigned FL_P0 = 0, FL_KV = 256, FL_ACT = 512, FL_CTX = 768;
constexpr int SC1 = 16;

DI unsigned cvtpk(float lo, float hi) { f32x2 v = {lo, hi}; bf16x2_t b = __builtin_convertvector(v, bf16x2_t); return __builtin_bit_cast(unsigned, b); }
DI u32x4 pack8(f32x4 a, f32x4 b) { u32x4 w = {cvtpk(a[0], a[1]), cvtpk(a[2], a[3]), cvtpk(b[0], b[1]), cvtpk(b[2], b[3])}; return w; }
DI float bflo(unsigned u) { return __uint_as_float(u << 16); }
DI float bfhi(unsigned u) { return __uint_as_float(u & 0xffff0000u); }
DI s16x4 vtr(lcptr p) { return __builtin_bit_cast(s16x4, __builtin_amdgcn_ds_read_tr16_b64_v4i16((LAS v4i16_t*)p)); }
DI bf16x8 cat(s16x4 a, s16x4 b) { return (bf16x8){a[0], a[1], a[2], a[3], b[0], b[1], b[2], b[3]}; }
DI bf16x8 ld128(lcptr p) { return *(const LAS bf16x8*)p; }
DI bf16x8 asbf(u32x4 v) { return __builtin_bit_cast(bf16x8, v); }
DI rsrc_t mk_rsrc(const void* p, unsigned bytes) { return __builtin_amdgcn_make_buffer_rsrc((void*)p, (short)0, (int)bytes, 0x00020000); }
DI void dma16(rsrc_t r, unsigned voff, unsigned soff, unsigned ldsoff) {
    asm volatile("s_mov_b32 m0, %0\n\ts_nop 3\n\tbuffer_load_dwordx4 %1, %2, %3 offen lds" ::"s"(ldsoff), "v"(voff), "s"(r), "s"(soff) : "memory");
}
#define HS " sc1"
DI void dma16h(rsrc_t r, unsigned voff, unsigned soff, unsigned ldsoff) {
    asm volatile("s_mov_b32 m0, %0\n\ts_nop 3\n\tbuffer_load_dwordx4 %1, %2, %3 offen" HS " lds" ::"s"(ldsoff), "v"(voff), "s"(r), "s"(soff) : "memory");
}
DI void dma16x4(rsrc_t r, unsigned voff, unsigned soff, unsigned ldsoff) {
    asm volatile("s_mov_b32 m0, %0\n\ts_nop 3\n\tbuffer_load_dwordx4 %1, %2, %3 offen" HS " lds\n\tbuffer_load_dwordx4 %1, %2, %3 offen offset:1024" HS " lds\n\t"
                 "buffer_load_dwordx4 %1, %2, %3 offen offset:2048" HS " lds\n\tbuffer_load_dwordx4 %1, %2, %3 offen offset:3072" HS " lds" ::"s"(ldsoff), "v"(voff), "s"(r), "s"(soff) : "memory");
}
DI void touch(const void* p, unsigned ldsoff) { asm volatile("s_mov_b32 m0, %0\n\ts_nop 3\n\tglobal_load_lds_dword %1, off" ::"s"(ldsoff), "v"(p) : "memory"); }
DI void st16_sc1(rsrc_t r, u32x4 v, unsigned voff, unsigned soff) { asm volatile("s_nop 4\n\tbuffer_store_dwordx4 %0, %1, %2, %3 offen sc1\n\ts_nop 1" ::"v"(v), "v"(voff), "s"(r), "s"(soff) : "memory"); }
template <int OFF> DI void st16_sc1o(rsrc_t r, u32x4 v, unsigned voff, unsigned soff) { asm volatile("s_nop 4\n\tbuffer_store_dwordx4 %0, %1, %2, %3 offen offset:%4 sc1\n\ts_nop 1" ::"v"(v), "v"(voff), "s"(r), "s"(soff), "i"(OFF) : "memory"); }
DI u32x4 ld16(rsrc_t r, unsigned voff, unsigned soff) { return __builtin_amdgcn_raw_buffer_load_b128(r, voff, soff, 0); }
#define P4AUX SC1
DI u32x4 ld16_sc1(rsrc_t r, unsigned voff, unsigned soff) { return __builtin_amdgcn_raw_buffer_load_b128(r, voff, soff, P4AUX); }
template <class T> DI T sel(bool c, T a, T b) { return c ? a : b; }
DI int lane_id() { int l; asm volatile("v_mbcnt_lo_u32_b32 %0, -1, 0\n\tv_mbcnt_hi_u32_b32 %0, -1, %0" : "=v"(l)); return l; }
DI void set_flag(gu64* flags, unsigned idx, u64 token) { __hip_atomic_store(flags + idx * 8, token, __ATOMIC_RELAXED, __HIP_MEMORY_SCOPE_AGENT); }
DI u64 poll_flag(gu64* flags, unsigned idx) { return __hip_atomic_load(flags + idx * 8, __ATOMIC_RELAXED, __HIP_MEMORY_SCOPE_AGENT); }
DI void spin_flags(gu64* flags, unsigned idx, u64 token) {
    const unsigned long long t0 = __builtin_amdgcn_s_memrealtime();
    for (;;) {
        __builtin_amdgcn_s_sleep(1);
        const u64 v = poll_flag(flags, idx);
        if (__all(v == token)) break;
        if (__builtin_amdgcn_s_memrealtime() - t0 > 2000000ull) break;
    }
}
DI u64 poll_asm(gu64* flags, unsigned idx) { u64 v; asm volatile("global_load_dwordx2 %0, %1, off sc1" : "=v"(v) : "v"(flags + idx * 8) : "memory"); return v; }
DI void check_flags(gu64* flags, unsigned idx, u64 seen, u64 token) { if (!__all(seen == token)) spin_flags(flags, idx, token); }
DI void acquire_issue() { asm volatile("buffer_inv sc1" ::: "memory"); }
DI u64 uni64(u64 v) { return ((u64)(unsigned)__builtin_amdgcn_readfirstlane((unsigned)(v >> 32)) << 32) | (unsigned)__builtin_amdgcn_readfirstlane((unsigned)v); }
#define SCLK __builtin_amdgcn_s_memrealtime
#define STAMP(i) do { } while (0)
#define STAMPW(i, wv) do { } while (0)
#define CSTAMP(i) do { } while (0)

#define TS(i) do { } while (0)
#define TSARG
#define TSPASS
struct Sync { gu64* flags; u64 token; int b, h, j, w; };
#define YSTAMP(i) do { } while (0)

struct P2Ctx { rsrc_t sws, swq, sw1; unsigned vl16, vwq, vw1; unsigned sxo, swqo, sw1o, skv, t0s, t0l, t1s; int w; lcptr xb; lcptr wq[4]; lcptr wf[2]; };
DI void p2_dma1(const P2Ctx& c, int st, int ks, int i) {
    if (i < 4) dma16h(c.sws, c.vl16, c.sxo + (i >> 1) * 20480 + (i & 1) * 1024 + ks * 2048, st * 32768 + (4 * c.w + i) * 1024);
    else if (i < 6) dma16(c.swq, c.vwq, c.swqo + (i - 4) * 128 + ks * 524288, 65536 + st * 24576 + (i - 4) * 8192 + c.w * 1024);
    else dma16(c.sw1, c.vw1, c.sw1o + ks * (64 * 3456), 65536 + st * 24576 + 16384 + c.w * 1024);
}

struct P1Ctx { rsrc_t sx, sw, sws; unsigned vx[2], vw[2]; unsigned sxo, swo; int w; lcptr xb[2]; lcptr wb[4]; const bf16_t *wo, *w2, *wq, *w1; lcptr lds; };
DI void pack_fetch(const P1Ctx& c, int b) {
    const int l = lane_id(), gw = b * 8 + c.w, pu = gw / 10, pq = gw - pu * 10;
    const rsrc_t sp = mk_rsrc(pu < 128 ? c.wo : c.w2, pu < 128 ? 4096 * 640 * 2 : 1728 * 640 * 2);
    const unsigned so = gw < 1820 ? (unsigned)((pu < 128 ? pu : pu - 128) * 32 * 1280 + pq * 128) : 0xF0000000u;
#pragma unroll
    for (int i = 0; i < 4; ++i) dma16(sp, (l >> 3) * 1280 + (l & 7) * 16, so + i * 8 * 1280, 131072 + c.w * 4096 + i * 1024);
}
DI void pack_store(const P1Ctx& c, int b) {
    const int l = lane_id(), g = l >> 4, q = (l & 15) >> 2, gw = b * 8 + c.w, pu = gw / 10, pq = gw - pu * 10;
    lcptr p = c.lds + 131072 + c.w * 4096 + (4 * g + q) * 128 + (l & 3) * 8;
    bf16x8 fr[4];
#pragma unroll
    for (int i = 0; i < 4; ++i) fr[i] = cat(vtr(p + i * 32), vtr(p + i * 32 + 16 * 128));
    if (gw < 1820) {
#pragma unroll
        for (int i = 0; i < 4; ++i) st16_sc1(c.sws, __builtin_bit_cast(u32x4, fr[i]), l * 16, OFF_WP + (pu * 40 + pq * 4 + i) * 1024);
    }
}
DI void p1_dma1(const P1Ctx& c, int st, int ks, int i) {
    if (i < 4) dma16(c.sx, c.vx[i & 1], c.sxo + i * 8192 + ks * 128, st * 32768 + (4 * c.w + i) * 1024);
    else dma16(c.sw, c.vw[i & 1], c.swo + (i - 4) * 65536 + ks * 524288, 65536 + st * 32768 + (4 * c.w + (i - 4)) * 1024);
}
DI void p1_dma(const P1Ctx& c, int st, int ks) {
#pragma unroll
    for (int i = 0; i < 8; ++i) p1_dma1(c, st, ks, i);
}
template <int ST, int UP, int I> DI void p1_rdw(const P1Ctx& c, bf16x8 (&wf)[4], s16x4 (&wl)[4]) {
    constexpr int nt = I >> 1, rd = I & 1;
    const s16x4 v = vtr(c.wb[nt] + ST * 32768 + UP * 4096 + rd * 512);
    if constexpr (rd == 0) wl[nt] = v; else wf[nt] = cat(wl[nt], v);
}
template <int ST, int G> DI bf16x8 p1_rdx(const P1Ctx& c) { return ld128(c.xb[G >> 3] + ST * 32768 + (G & 7) * 2048); }
template <int ST, int KSC, int K> DI bool p1_is(int ks) { if constexpr (KSC >= 0) return KSC == K; else if constexpr ((K & 1) != ST || K > 5) return false; else return ks == K; }
template <bool ISV, int ST, int KSC, int G> DI void p1_group(const P1Ctx& c, const P2Ctx& c2, const Sync& y, const int ks, u64& seen, f32x4 (&acc)[4][8], bf16x8 (&wA)[4], bf16x8 (&wB)[4], s16x4 (&wl)[4], bf16x8 (&xr)[4]) {
    constexpr int up = G >> 3, mt = G & 7;
    constexpr bool NEXT = KSC != 7;
    if constexpr (G == 13) {
        if (p1_is<ST, KSC, 0>(ks)) asm volatile("s_waitcnt vmcnt(3) lgkmcnt(0)" ::: "memory");
        else if (p1_is<ST, KSC, 1>(ks) || p1_is<ST, KSC, 3>(ks)) asm volatile("s_waitcnt vmcnt(1) lgkmcnt(0)" ::: "memory");
        else if (p1_is<ST, KSC, 5>(ks) || p1_is<ST, KSC, 7>(ks)) asm volatile("s_waitcnt vmcnt(4) lgkmcnt(0)" ::: "memory");
        else asm volatile("s_waitcnt vmcnt(0) lgkmcnt(0)" ::: "memory");
        if constexpr (!ISV && KSC == 6) { if (y.w == 0) { const int l = lane_id() & 31; check_flags(y.flags, FL_P0 + (l < 16 ? 16 * y.j + l : 16 * (15 - y.j) + (l - 16)), seen, y.token); } }
        BARRIER();
        if constexpr (!ISV) {
            if (p1_is<ST, KSC, 1>(ks)) { if (y.w == 0 && lane_id() == 0) set_flag(y.flags, FL_P0 + y.b, y.token); }
        }
        SBAR();
    }
    const bf16x8 x = xr[G & 3];
#pragma unroll
    for (int nt = 0; nt < 4; ++nt) {
        const bf16x8 wv = up == 0 ? wA[nt] : wB[nt];
        if constexpr (ISV) acc[nt][mt] = MF16(x, wv, acc[nt][mt]); else acc[nt][mt] = MF16(wv, x, acc[nt][mt]);
        if (nt == 0) {
            if constexpr (G + 3 < 16) xr[(G + 3) & 3] = p1_rdx<ST, G + 3>(c);
            else if constexpr (NEXT) xr[(G + 3) & 3] = p1_rdx<ST ^ 1, G + 3 - 16>(c);
        }
        if constexpr (G >= 2 && G < 6) { if (nt == 1) p1_rdw<ST, 1, 2 * (G - 2)>(c, wB, wl); if (nt == 2) p1_rdw<ST, 1, 2 * (G - 2) + 1>(c, wB, wl); }
        if constexpr (G >= 13 && NEXT) {
            if (nt == 1) p1_rdw<ST ^ 1, 0, 3 * (G - 13)>(c, wA, wl);
            if (nt == 2) p1_rdw<ST ^ 1, 0, 3 * (G - 13) + 1>(c, wA, wl);
            if constexpr (G < 15) { if (nt == 3) p1_rdw<ST ^ 1, 0, 3 * (G - 13) + 2>(c, wA, wl); }
        }
        if (nt == SPOS) {
            if constexpr (G >= 13) { constexpr int p = G - 13;
                if constexpr (KSC < 0) p1_dma1(c, ST, ks + 2, p);
                else if constexpr (KSC == 6) p2_dma1(c2, 0, 0, p);
                else p2_dma1(c2, 1, 1, p);
            } else if constexpr (G <= 4) { constexpr int p = G + 3;
                if constexpr (KSC == 7) { if constexpr (p < 7) p2_dma1(c2, 0, 0, p); }
                else { if (!p1_is<ST, KSC, 0>(ks)) p1_dma1(c, ST ^ 1, ks + 1, p); }
            }
        }
        SBAR();
    }
    if constexpr (G == 6) {
        if (p1_is<ST, KSC, 1>(ks)) {
            const int l = lane_id(), L = (c.w & 3) * 64 + l;
            const char* pb = (L >= 160 && L < 240 && y.h < 27) ? (const char*)c.w1 + (size_t)(80 * y.j + L - 160) * 3456 + y.h * 128
                                                               : (const char*)c.wq + (size_t)(80 * y.j + ((L < 160 ? L : 0) >> 1)) * 8192 + y.h * 256 + (L & 1) * 128;
            touch(pb, 131072 + c.w * 256);
        }
        if (p1_is<ST, KSC, 3>(ks)) {
            const int l = lane_id(), gw = y.b * 8 + c.w < 1820 ? y.b * 8 + c.w : 0, pu = gw / 10, pq = gw - pu * 10;
            const char* pc = (const char*)(pu < 128 ? c.wo : c.w2) + (size_t)((pu < 128 ? pu : pu - 128) * 32 + (l & 31)) * 1280 + pq * 128;
            touch(pc, 131072 + c.w * 256);
        }
        if constexpr (!ISV && KSC == 6) { if (y.w == 0) { const int l = lane_id() & 31; seen = poll_flag(y.flags, FL_P0 + (l < 16 ? 16 * y.j + l : 16 * (15 - y.j) + (l - 16))); } }
        if (p1_is<ST, KSC, 5>(ks)) { pack_fetch(c, y.b); if constexpr (!ISV && !NOINV) { if (y.w == 0) acquire_issue(); } }
        if constexpr (KSC == 7) pack_store(c, y.b);
        SBAR();
    }
}
template <bool ISV, int ST, int KSC, int... Gs> DI void p1_step(const P1Ctx& c, const P2Ctx& c2, const Sync& y, const int ks, u64& seen, f32x4 (&acc)[4][8], bf16x8 (&wA)[4], bf16x8 (&wB)[4], s16x4 (&wl)[4], bf16x8 (&xr)[4], std::integer_sequence<int, Gs...>) {
    (p1_group<ISV, ST, KSC, Gs>(c, c2, y, ks, seen, acc, wA, wB, wl, xr), ...);
}
template <bool ISV> DI void p1_main(const P1Ctx& c, const P2Ctx& c2, const Sync& y, u32x4 (&kv)[16]) {
    f32x4 acc[4][8];
#pragma unroll
    for (int a = 0; a < 4; ++a)
#pragma unroll
        for (int b = 0; b < 8; ++b) acc[a][b] = (f32x4){0.f, 0.f, 0.f, 0.f};
    bf16x8 wA[4], wB[4], xr[4]; s16x4 wl[4];
    u64 seen = 0;
    asm volatile("" ::: "memory");
    p1_rdw<0, 0, 0>(c, wA, wl); p1_rdw<0, 0, 1>(c, wA, wl); p1_rdw<0, 0, 2>(c, wA, wl); p1_rdw<0, 0, 3>(c, wA, wl);
    p1_rdw<0, 0, 4>(c, wA, wl); p1_rdw<0, 0, 5>(c, wA, wl); p1_rdw<0, 0, 6>(c, wA, wl); p1_rdw<0, 0, 7>(c, wA, wl);
    xr[0] = p1_rdx<0, 0>(c); xr[1] = p1_rdx<0, 1>(c); xr[2] = p1_rdx<0, 2>(c);
    SBAR();
    constexpr auto seq = std::make_integer_sequence<int, 16>{};
#pragma nounroll
    for (int ks = 0; ks < 6; ks += 2) {
        p1_step<ISV, 0, -1>(c, c2, y, ks, seen, acc, wA, wB, wl, xr, seq);
        p1_step<ISV, 1, -1>(c, c2, y, ks + 1, seen, acc, wA, wB, wl, xr, seq);
    }
    p1_step<ISV, 0, 6>(c, c2, y, 6, seen, acc, wA, wB, wl, xr, seq);
    p1_step<ISV, 1, 7>(c, c2, y, 7, seen, acc, wA, wB, wl, xr, seq);
#pragma unroll
    for (int a = 0; a < 8; ++a)
#pragma unroll
        for (int b = 0; b < 2; ++b) {
            if constexpr (ISV) kv[a * 2 + b] = pack8(acc[(a & 1) * 2 + b][(a >> 1) * 2], acc[(a & 1) * 2 + b][(a >> 1) * 2 + 1]);
            else kv[a * 2 + b] = pack8(acc[2 * b][a], acc[2 * b + 1][a]);
        }
}

template <bool HASF, int ST, int I> DI bf16x8 p2_rdw(const P2Ctx& c) {
    constexpr int NW = HASF ? 6 : 4, up = I / NW, n = I % NW;
    lcptr p = (n < 4 ? c.wq[n & 3] : c.wf[n & 1]) + ST * 24576 + up * 4096;
    return cat(vtr(p), vtr(p + 512));
}
template <int ST, int UP, int MT> DI bf16x8 p2_rdx(const P2Ctx& c) { return ld128(c.xb + ST * 32768 + MT * 2048 + UP * 1024); }
template <int I> DI void p2_kvst(const P2Ctx& c, const u32x4 (&kv)[16], bool isv) {
    st16_sc1(c.sws, kv[I], c.vl16, c.skv + (isv ? (unsigned)(((I >> 3) * 16 + ((I >> 2) & 1) * 8 + (I & 3)) * 1024) : (unsigned)((I >> 1) * 4096 + (I & 1) * 1024)));
}
template <bool HASF, int IV> DI void p2_extras(const P2Ctx& c, const u32x4 (&kv)[16], bool isv) {
    if constexpr (IV < 6) { p2_kvst<4 + 2 * IV>(c, kv, isv); p2_kvst<5 + 2 * IV>(c, kv, isv); }
}
struct P2Bias { u32x2 a, b; const bf16_t* p; };
template <bool HASF, int KS, int G> DI void p2_group(const P2Ctx& c, const Sync& y, P2Bias& bs, u64& seen, f32x4 (&acc)[6][4], bf16x8 (&wr)[4], bf16x8 (&x0)[4], bf16x8 (&x1)[4], const u32x4 (&kv)[16], bool isv) {
    constexpr int NW = HASF ? 6 : 4, NG = 2 * NW, ST = KS & 1, up = G / NW, n = G % NW;
    constexpr int NP = HASF ? 7 : 6, BD0 = HASF ? 2 : 0;
    constexpr bool LAST = (KS == 9);
    if constexpr (G == NG - 3) {
        if constexpr (KS == 0 && !SPREAD) asm volatile("s_waitcnt vmcnt(4) lgkmcnt(0)" ::: "memory");
        else if constexpr (KS == 0) asm volatile("s_waitcnt vmcnt(0) lgkmcnt(0)" ::: "memory");
        else if constexpr (HASF && KS == 6) asm volatile("s_waitcnt vmcnt(4) lgkmcnt(0)" ::: "memory");
        else if constexpr (KS <= 6) asm volatile("s_waitcnt vmcnt(2) lgkmcnt(0)" ::: "memory");
        else asm volatile("s_waitcnt vmcnt(0) lgkmcnt(0)" ::: "memory");
        if constexpr (KS == 9) { if (y.w == 0) check_flags(y.flags, FL_KV + y.h * 8 + (lane_id() & 7), seen, y.token); }
        BARRIER();
        if constexpr (KS == (KVR == 3 ? 5 : 7)) { if (y.w == 0 && lane_id() == 0) set_flag(y.flags, FL_KV + y.h * 8 + y.j, y.token); }
        if constexpr (KS == 8) { if (y.w == 0) seen = poll_flag(y.flags, FL_KV + y.h * 8 + (lane_id() & 7)); }
        SBAR();
    }
    const bf16x8 wcur = wr[G & 3];
#pragma unroll
    for (int mt = 0; mt < 4; ++mt) {
        acc[n][mt] = MF16(wcur, up == 0 ? x0[mt] : x1[mt], acc[n][mt]);
        if (mt == 0) {
            if constexpr (G + 3 < NG) wr[(G + 3) & 3] = p2_rdw<HASF, ST, G + 3>(c);
            else if constexpr (!LAST) wr[(G + 3) & 3] = p2_rdw<HASF, ST ^ 1, G + 3 - NG>(c);
        }
        if (mt == 1) {
            if constexpr (G < 4) x1[G] = p2_rdx<ST, 1, G>(c);
            else if constexpr (G >= NG - 3 && !LAST) x0[G - (NG - 3)] = p2_rdx<ST ^ 1, 0, G - (NG - 3)>(c);
        }
        if (mt == 2) { if constexpr (G == NG - 1 && !LAST) x0[3] = p2_rdx<ST ^ 1, 0, 3>(c); }
        if (mt == SPOS) {
            if constexpr (G >= NG - 3) { if constexpr (KS + 2 < 10) p2_dma1(c, ST, KS + 2, G - (NG - 3)); }
            else if constexpr (G + 3 < NP) { if constexpr (KS + 1 < 10) p2_dma1(c, ST ^ 1, KS + 1, G + 3); }
        }
        if constexpr (LAST && G >= NG - 3) {
            constexpr int p0 = (G - (NG - 3)) * 4;
            if (p0 + mt < 4) dma16h(c.sws, c.vl16, c.t0s + (p0 + mt) * 1024, c.t0l + (p0 + mt) * 1024);
            else if (p0 + mt < 8) { if (c.w < 4) dma16h(c.sws, c.vl16, c.t1s + (p0 + mt - 4) * 1024, 65536 + c.t0l + (p0 + mt - 4) * 1024); }
        }
        SBAR();
    }
    if constexpr (G == NP - 3 && KS >= 1) { p2_extras<HASF, KS - 1>(c, kv, isv); SBAR(); }
    if constexpr (HASF && KS == 6 && G == 5) { asm volatile("global_load_dwordx2 %0, %2, off\n\tglobal_load_dwordx2 %1, %2, off offset:32" : "=&v"(bs.a), "=&v"(bs.b) : "v"(bs.p) : "memory"); SBAR(); }
}
template <bool HASF, int KS, int... Gs> DI void p2_step(const P2Ctx& c, const Sync& y, P2Bias& bs, u64& seen, f32x4 (&acc)[6][4], bf16x8 (&wr)[4], bf16x8 (&x0)[4], bf16x8 (&x1)[4], const u32x4 (&kv)[16], bool isv, std::integer_sequence<int, Gs...>) {
    (p2_group<HASF, KS, Gs>(c, y, bs, seen, acc, wr, x0, x1, kv, isv), ...);
}
template <bool HASF, int... KSs> DI void p2_main(const P2Ctx& c, const Sync& y, P2Bias& bs, f32x4 (&acc)[6][4], const u32x4 (&kv)[16], bool isv, std::integer_sequence<int, KSs...>) {
    bf16x8 wr[4], x0[4], x1[4];
    u64 seen = 0;
    asm volatile("" ::: "memory");
    wr[0] = p2_rdw<HASF, 0, 0>(c); wr[1] = p2_rdw<HASF, 0, 1>(c); wr[2] = p2_rdw<HASF, 0, 2>(c);
    x0[0] = p2_rdx<0, 0, 0>(c); x0[1] = p2_rdx<0, 0, 1>(c); x0[2] = p2_rdx<0, 0, 2>(c); x0[3] = p2_rdx<0, 0, 3>(c);
    SBAR();
    (p2_step<HASF, KSs>(c, y, bs, seen, acc, wr, x0, x1, kv, isv, std::make_integer_sequence<int, (HASF ? 12 : 8)>{}), ...);
}
DI float mish(float h) {
    const float t = __builtin_amdgcn_exp2f(__builtin_fminf(h, 20.f) * 1.4426950408889634f);
    const float n = t * (t + 2.f);
    return h * n * __builtin_amdgcn_rcpf(n + 2.f);
}

constexpr int PD = 8;
#define PRIOM 1
#define PRIOV 0
#define ADDMODE 2
#define ABL_NOEARLY 0
#define APRE_EARLY 1
#define LATE_STEP 7
#define LATE_OFF 2
struct AttnRegs { f32x4 o[8][2]; f32x4 ls[2]; f32x4 s[4][2]; bf16x8 q[2][4]; u32x4 pk[2][2]; f32x4 negm[2]; bf16x8 ones; bf16x8 op[32]; };
template <int I> DI void a_read(AttnRegs& r, lcptr vp, lcptr kp) {
    if constexpr (I < 16) r.op[I] = ld128(vp + I * 1024);
    else if constexpr (I < 32) r.op[I] = ld128(kp + (((I - 16) & 3) * 4 + ((I - 16) >> 2)) * 1024);
}
struct Piece { rsrc_t sws; unsigned vl16, soff, ldso; bool on; };
template <int P, bool COND> DI void piece(const Piece& d) {
    if ((!COND && !ABL_NOEARLY) || (COND && d.on)) asm volatile("s_mov_b32 m0, %0\n\ts_nop 3\n\tbuffer_load_dwordx4 %1, %2, %3 offen offset:%4" HS " lds" ::"s"(d.ldso), "v"(d.vl16), "s"(d.sws), "s"(d.soff), "i"(P * 1024) : "memory");
}
template <int I, int DMA> DI void a_slot(AttnRegs& r, lcptr vp, lcptr kp, const Piece& late) {
    if constexpr (I < 16) { constexpr int ks = I >> 3, dt = I & 7;
        r.o[dt][0] = MF16(r.op[I], asbf(r.pk[0][ks]), r.o[dt][0]); r.o[dt][1] = MF16(r.op[I], asbf(r.pk[1][ks]), r.o[dt][1]);
        if constexpr (dt == 3) { r.ls[0] = MF16(r.ones, asbf(r.pk[0][ks]), r.ls[0]); r.ls[1] = MF16(r.ones, asbf(r.pk[1][ks]), r.ls[1]); } }
    else { constexpr int u = (I - 16) >> 2, kt = (I - 16) & 3;
        r.s[kt][0] = MF16(r.op[I], r.q[0][u], u == 0 ? r.negm[0] : r.s[kt][0]); r.s[kt][1] = MF16(r.op[I], r.q[1][u], u == 0 ? r.negm[1] : r.s[kt][1]); }
    a_read<I + PD>(r, vp, kp);
    SBAR();
    if constexpr (DMA != 0 && (I % LATE_STEP) == LATE_OFF && I / LATE_STEP < 4) { piece<I / LATE_STEP, DMA == 2>(late); SBAR(); }
}
template <int B, int DMA, int... Is> DI void a_run(AttnRegs& r, lcptr vp, lcptr kp, const Piece& late, std::integer_sequence<int, Is...>) { (a_slot<B + Is, DMA>(r, vp, kp, late), ...); }
template <int B, int... Is> DI void a_pre(AttnRegs& r, lcptr vp, lcptr kp, std::integer_sequence<int, Is...>) { (a_read<B + Is>(r, vp, kp), ...); }

DI unsigned pkmax(unsigned a, unsigned b) { return __builtin_bit_cast(unsigned, __builtin_elementwise_max(__builtin_bit_cast(u16x2, a), __builtin_bit_cast(u16x2, b))); }
DI unsigned hmax(unsigned v) { const unsigned lo = v & 0xFFFFu, hi = v >> 16; return lo > hi ? lo : hi; }
template <int Q> DI void a_exp_quarter(AttnRegs& r, unsigned& x0, unsigned& x1) {
    constexpr int ks = Q >> 1, qt = Q & 1;
    float e[8];
#pragma unroll
    for (int i = 0; i < 4; ++i) { e[i] = __builtin_amdgcn_exp2f(r.s[2 * ks][qt][i]); e[4 + i] = __builtin_amdgcn_exp2f(r.s[2 * ks + 1][qt][i]); }
    const u32x4 p = {cvtpk(e[0], e[1]), cvtpk(e[2], e[3]), cvtpk(e[4], e[5]), cvtpk(e[6], e[7])};
    r.pk[qt][ks] = p;
    const unsigned m = pkmax(pkmax(p[0], p[1]), pkmax(p[2], p[3]));
    if constexpr (Q == 0) x0 = m; else if constexpr (Q == 1) x1 = m; else if constexpr (qt == 0) x0 = pkmax(x0, m); else x1 = pkmax(x1, m);
}
template <bool DMA> DI void a_exp_pack(AttnRegs& r, unsigned& x0, unsigned& x1, const Piece& early) {
    if constexpr (DMA) { piece<0, false>(early); SBAR(); }
    a_exp_quarter<0>(r, x0, x1);
    if constexpr (DMA) { SBAR(); piece<1, false>(early); SBAR(); }
    a_exp_quarter<1>(r, x0, x1);
    if constexpr (DMA) { SBAR(); piece<2, false>(early); SBAR(); }
    a_exp_quarter<2>(r, x0, x1);
    if constexpr (DMA) { SBAR(); piece<3, false>(early); SBAR(); }
    a_exp_quarter<3>(r, x0, x1);
}
DI float xmax4(float v) {
    const u32x2 a = __builtin_amdgcn_permlane16_swap(__float_as_uint(v), __float_as_uint(v), false, false);
    v = __builtin_fmaxf(__uint_as_float(a[0]), __uint_as_float(a[1]));
    const u32x2 c = __builtin_amdgcn_permlane32_swap(__float_as_uint(v), __float_as_uint(v), false, false);
    return __builtin_fmaxf(__uint_as_float(c[0]), __uint_as_float(c[1]));
}
DI float xsum4(float v) {
    const u32x2 a = __builtin_amdgcn_permlane16_swap(__float_as_uint(v), __float_as_uint(v), false, false);
    v = __uint_as_float(a[0]) + __uint_as_float(a[1]);
    const u32x2 c = __builtin_amdgcn_permlane32_swap(__float_as_uint(v), __float_as_uint(v), false, false);
    return __uint_as_float(c[0]) + __uint_as_float(c[1]);
}
constexpr unsigned P_HI = 0x5D80u, P_LO = 0x2180u;

struct AttnCtx { rsrc_t sws; unsigned vl16, kbase, vbase, ctxl; int nL, grp, qg, j, rowrel, swap_s, first_s; lcptr kl, vl, ql; };
struct AttnSm { float m0, m1; };
DI void a_dmaK(const AttnCtx& c, int n) { dma16x4(c.sws, c.vl16, c.kbase + n * 16384 + c.qg * 4096, (n & 3) * 16384 + c.qg * 4096); }
DI void a_dmaV(const AttnCtx& c, int n) { dma16x4(c.sws, c.vl16, c.vbase + n * 16384 + c.qg * 4096, 65536 + (n & 3) * 16384 + c.qg * 4096); }
#define PT(i) do { } while (0)
#define PARG
#define PPASS
#define PPASSG
DI void a_slotfull(const AttnCtx& c, const Sync& y, AttnRegs& r, AttnSm& z, const int s PARG) {
    const int nL = c.nL, grp = c.grp;
    const bool SWAP = (s == c.swap_s), FIN = (s == c.swap_s + 1), FIRST = (s == c.first_s) || FIN;
    const int tcur = s < nL ? s : 2 * s - nL + grp;
    Piece early, late;
    {
        const int s2 = s + 2, s1 = s + 1;
        const bool eK = grp == 1 && s2 < nL;
        const int te = eK ? s2 : (s1 < nL ? s1 : 2 * s1 - nL + grp), tl = 2 * s2 - nL + grp;
        early.sws = c.sws; early.vl16 = c.vl16; late.sws = c.sws; late.vl16 = c.vl16;
        early.on = eK || (s1 <= 16 && (grp == 0 || s1 >= nL));
        early.soff = (eK ? c.kbase : c.vbase) + te * 16384 + c.qg * 4096; early.ldso = (eK ? 0 : 65536) + (te & 3) * 16384 + c.qg * 4096;
        late.on = s2 <= 16 && s2 >= nL;
        late.soff = c.kbase + tl * 16384 + c.qg * 4096; late.ldso = (tl & 3) * 16384 + c.qg * 4096;
    }
    const int snext = s + 1, tnext = snext < nL ? snext : 2 * snext - nL + grp;
    const lcptr vp = c.vl + (tcur & 3) * 16384, kp = c.kl + (tnext & 3) * 16384;
    u64 seenA = 0;
    if (s == 16) seenA = poll_asm(y.flags, FL_ACT + 32 * y.w + (lane_id() & 31));
    a_pre<0>(r, vp, kp, std::make_integer_sequence<int, PD>{});
    SBAR();
    if (SWAP) {
        asm volatile("ds_read_b128 %0, %8\n\tds_read_b128 %1, %8 offset:1024\n\tds_read_b128 %2, %8 offset:2048\n\tds_read_b128 %3, %8 offset:3072\n\t"
                     "ds_read_b128 %4, %8 offset:4096\n\tds_read_b128 %5, %8 offset:5120\n\tds_read_b128 %6, %8 offset:6144\n\tds_read_b128 %7, %8 offset:7168\n\ts_waitcnt lgkmcnt(0)"
                     : "=&v"(r.q[0][0]), "=&v"(r.q[0][1]), "=&v"(r.q[0][2]), "=&v"(r.q[0][3]), "=&v"(r.q[1][0]), "=&v"(r.q[1][1]), "=&v"(r.q[1][2]), "=&v"(r.q[1][3])
                     : "v"(c.ql) : "memory");
    }
    if (FIN) {
        const float i0 = __builtin_amdgcn_rcpf(r.ls[0][0]), i1 = __builtin_amdgcn_rcpf(r.ls[1][0]);
        const unsigned c1o = c.ctxl + 131072;
        st16_sc1o<0>(c.sws, pack8(r.o[0][0] * i0, r.o[1][0] * i0), c.vl16, c.ctxl); st16_sc1o<1024>(c.sws, pack8(r.o[2][0] * i0, r.o[3][0] * i0), c.vl16, c.ctxl);
        st16_sc1o<2048>(c.sws, pack8(r.o[4][0] * i0, r.o[5][0] * i0), c.vl16, c.ctxl); st16_sc1o<3072>(c.sws, pack8(r.o[6][0] * i0, r.o[7][0] * i0), c.vl16, c.ctxl);
        st16_sc1o<0>(c.sws, pack8(r.o[0][1] * i1, r.o[1][1] * i1), c.vl16, c1o); st16_sc1o<1024>(c.sws, pack8(r.o[2][1] * i1, r.o[3][1] * i1), c.vl16, c1o);
        st16_sc1o<2048>(c.sws, pack8(r.o[4][1] * i1, r.o[5][1] * i1), c.vl16, c1o); st16_sc1o<3072>(c.sws, pack8(r.o[6][1] * i1, r.o[7][1] * i1), c.vl16, c1o);
#pragma unroll
        for (int d = 0; d < 8; ++d) { r.o[d][0] = (f32x4){0.f, 0.f, 0.f, 0.f}; r.o[d][1] = (f32x4){0.f, 0.f, 0.f, 0.f}; }
        r.ls[0] = (f32x4){0.f, 0.f, 0.f, 0.f}; r.ls[1] = (f32x4){0.f, 0.f, 0.f, 0.f};
    }
    {
        const bool heavy = (grp == 1) || (s >= nL);
        const int qb = heavy ? 15 - c.j : c.j, kb = 64 * tcur - 128 * qb - 32 * c.qg;
        if (kb >= -32) {
            const float NI = -__builtin_inff();
            const f32x4 ninf = {NI, NI, NI, NI};
            f32x4 s00 = r.s[0][0], s10 = r.s[1][0], s20 = r.s[2][0], s30 = r.s[3][0], s01 = r.s[0][1], s11 = r.s[1][1], s21 = r.s[2][1], s31 = r.s[3][1];
            const int dq = c.rowrel;
            if (kb > 0) { s00 = ninf; s10 = ninf; s20 = ninf; s30 = ninf; s01 = ninf; s11 = ninf; s21 = ninf; s31 = ninf; }
            else if (kb == 0) {
#pragma unroll
                for (int i = 0; i < 4; ++i) { s00[i] = sel(i > dq, NI, s00[i]); s11[i] = sel(i > dq, NI, s11[i]); }
                s10 = ninf; s20 = ninf; s30 = ninf; s21 = ninf; s31 = ninf;
            } else {
#pragma unroll
                for (int i = 0; i < 4; ++i) { s20[i] = sel(i > dq, NI, s20[i]); s31[i] = sel(i > dq, NI, s31[i]); }
                s30 = ninf;
            }
            r.s[0][0] = s00; r.s[1][0] = s10; r.s[2][0] = s20; r.s[3][0] = s30; r.s[0][1] = s01; r.s[1][1] = s11; r.s[2][1] = s21; r.s[3][1] = s31;
        }
    }
    unsigned a0, a1;
    if (early.on) dma16x4(c.sws, c.vl16, early.soff, early.ldso);
    a_exp_pack<false>(r, a0, a1, early);
    a0 = hmax(a0); a1 = hmax(a1);
    const unsigned lo = FIRST ? P_LO : 0u;
    if (__builtin_expect(__any(a0 > P_HI || a1 > P_HI || a0 < lo || a1 < lo), 0)) {
        float x0 = r.s[0][0][0], x1 = r.s[0][1][0];
#pragma unroll
        for (int kt = 0; kt < 4; ++kt)
#pragma unroll
            for (int i = 0; i < 4; ++i) { x0 = __builtin_fmaxf(x0, r.s[kt][0][i]); x1 = __builtin_fmaxf(x1, r.s[kt][1][i]); }
        x0 = xmax4(x0); x1 = xmax4(x1);
        if (!FIRST) { x0 = __builtin_fmaxf(x0, 0.f); x1 = __builtin_fmaxf(x1, 0.f);
            const float f0 = __builtin_amdgcn_exp2f(-x0), f1 = __builtin_amdgcn_exp2f(-x1);
#pragma unroll
            for (int d = 0; d < 8; ++d) { r.o[d][0] *= f0; r.o[d][1] *= f1; }
            r.ls[0] *= f0; r.ls[1] *= f1; }
        z.m0 += x0; z.m1 += x1;
        r.negm[0] = (f32x4){-z.m0, -z.m0, -z.m0, -z.m0}; r.negm[1] = (f32x4){-z.m1, -z.m1, -z.m1, -z.m1};
#pragma unroll
        for (int kt = 0; kt < 4; ++kt) { r.s[kt][0] -= x0; r.s[kt][1] -= x1; }
        a_exp_pack<false>(r, a0, a1, early);
    }
    if (SWAP) {
        z.m0 = 0.f; z.m1 = 0.f;
        r.negm[0] = (f32x4){0.f, 0.f, 0.f, 0.f}; r.negm[1] = (f32x4){0.f, 0.f, 0.f, 0.f};
    }
    SBAR();
    PT(0);
    if (FIN) { if (early.on) WAITV(12); else WAITV(8); } else { if (early.on) WAITV(4); else WAITV(0); }
    if (s == 16) { asm volatile("" : "+v"(seenA)); check_flags(y.flags, FL_ACT + 32 * y.w + (lane_id() & 31), seenA, y.token); }
    PT(1);
    BARRIER(); SBAR();
    PT(2);
    __builtin_amdgcn_s_setprio(PRIOM);
    a_run<0, 2>(r, vp, kp, late, std::make_integer_sequence<int, 16>{});
    if (s < 16) a_run<16, 2>(r, vp, kp, late, std::make_integer_sequence<int, 16>{});
    __builtin_amdgcn_s_setprio(PRIOV);
    SBAR();
    PT(3);
    if (late.on) WAITV(4); else WAITV(0);
    PT(4);
    BARRIER(); SBAR();
    PT(5);
}

struct Lean { unsigned e_soff, e_lds, l_soff, l_lds, voff, koff; };
template <bool LATE> DI void a_slot_lean(const AttnCtx& c, AttnRegs& r, AttnSm& z, const Lean& st PARG) {
    const lcptr vp = c.vl + st.voff, kp = c.kl + st.koff;
    Piece early, late;
    early.sws = c.sws; early.vl16 = c.vl16; early.soff = st.e_soff; early.ldso = st.e_lds; early.on = true;
    late.sws = c.sws; late.vl16 = c.vl16; late.soff = st.l_soff; late.ldso = st.l_lds; late.on = true;
    a_pre<0>(r, vp, kp, std::make_integer_sequence<int, PD>{});
    SBAR();
    unsigned a0, a1;
    a_exp_pack<true>(r, a0, a1, early);
    if (__builtin_expect(__any(hmax(pkmax(a0, a1)) > P_HI), 0)) {
        float x0 = 0.f, x1 = 0.f;
#pragma unroll
        for (int kt = 0; kt < 4; ++kt)
#pragma unroll
            for (int i = 0; i < 4; ++i) { x0 = __builtin_fmaxf(x0, r.s[kt][0][i]); x1 = __builtin_fmaxf(x1, r.s[kt][1][i]); }
        x0 = xmax4(x0); x1 = xmax4(x1);
        const float f0 = __builtin_amdgcn_exp2f(-x0), f1 = __builtin_amdgcn_exp2f(-x1);
#pragma unroll
        for (int d = 0; d < 8; ++d) { r.o[d][0] *= f0; r.o[d][1] *= f1; }
        r.ls[0] *= f0; r.ls[1] *= f1;
        z.m0 += x0; z.m1 += x1;
        r.negm[0] = (f32x4){-z.m0, -z.m0, -z.m0, -z.m0}; r.negm[1] = (f32x4){-z.m1, -z.m1, -z.m1, -z.m1};
#pragma unroll
        for (int kt = 0; kt < 4; ++kt) { r.s[kt][0] -= x0; r.s[kt][1] -= x1; }
        a_exp_pack<false>(r, a0, a1, early);
    }
    SBAR();
    PT(0);
    WAITV(4);
    PT(1);
    BARRIER(); SBAR();
    PT(2);
    __builtin_amdgcn_s_setprio(PRIOM);
    a_run<0, LATE ? 1 : 0>(r, vp, kp, late, std::make_integer_sequence<int, 32>{});
    __builtin_amdgcn_s_setprio(PRIOV);
    SBAR();
    PT(3);
    if constexpr (LATE) WAITV(4); else WAITV(0);
    PT(4);
    BARRIER(); SBAR();
    PT(5);
}

#define P4W0 11
constexpr unsigned P4STG = 36864, P4TAG = 0x3FFu, P4OOB = 0xF0000000u;
struct P4Ctx { rsrc_t sws; unsigned vl16, ga, gw, gas, gws, dw0, dw1, dw2, lw, tag, so[3], so2[3], lo[3], lo2[3]; int w, q, o, ng, ra, rw; lcptr pa, pw; };
struct P4Regs { bf16x8 xa[2][2], wa[2][5]; };
struct P4Xch { f32x4 own[3]; u32x4 snd[9], rcv[9]; };
DI void mfa(f32x4& acc, bf16x8 a, bf16x8 b) { asm volatile("v_mfma_f32_16x16x32_bf16 %0, %1, %2, %0" : "+v"(acc) : "v"(a), "v"(b)); }
DI void mfz(f32x4& acc, bf16x8 a, bf16x8 b) { asm volatile("v_mfma_f32_16x16x32_bf16 %0, %1, %2, 0" : "=&v"(acc) : "v"(a), "v"(b)); }
template <int OFF> DI void dma_sc1(const P4Ctx& c, unsigned soff, unsigned ldso) {
    asm volatile("s_mov_b32 m0, %0\n\ts_nop 3\n\tbuffer_load_dwordx4 %1, %2, %3 offen offset:%4 sc1 lds" ::"s"(ldso), "v"(c.vl16), "s"(c.sws), "s"(soff), "i"(OFF) : "memory");
}
template <int P> DI void p4_piece(P4Ctx& c, unsigned st) {
    if constexpr (P == 0) dma_sc1<0>(c, c.ga, st + c.w * 2048);
    else if constexpr (P == 1) { dma_sc1<1024>(c, c.ga, st + c.w * 2048); ++c.ra; c.ga = c.ra == c.ng ? c.gas : c.ga + 2048; }
    else if constexpr (P == 2) dma_sc1<0>(c, c.gw + c.dw0, st + c.lw);
    else if constexpr (P == 3) dma_sc1<0>(c, c.gw + c.dw1, st + c.lw + 8192);
    else { if (c.w < 4) dma_sc1<0>(c, c.gw + c.dw2, st + c.lw + 16384); ++c.rw; c.gw = c.rw == c.ng ? c.gws : c.gw + 81920; }
}
template <int B, int UP, int I> DI void p4_rd(const P4Ctx& c, P4Regs& r, unsigned st) {
    if constexpr (I == 0) r.wa[B][0] = ld128(c.pw + st + UP * 10240);
    else if constexpr (I <= 2) r.xa[B][I - 1] = ld128(c.pa + st + (I - 1) * 2048 + UP * 1024);
    else if constexpr (I < 7) r.wa[B][I - 2] = ld128(c.pw + st + UP * 10240 + (I - 2) * 1024);
}
template <int N> DI void p4_put(const P4Ctx& c, const P4Xch& x, unsigned xoff) { st16_sc1o<(N % 3) * 1024>(c.sws, x.snd[N], c.vl16, (N % 3 == 2 ? c.so2[N / 3] : c.so[N / 3]) + xoff); }
template <int N> DI void p4_get(const P4Ctx& c, P4Xch& x, unsigned xoff) {
    asm volatile("buffer_load_dwordx4 %0, %1, %2, %3 offen offset:%4 sc1" : "=v"(x.rcv[N]) : "v"(c.vl16), "s"(c.sws), "s"((N % 3 == 2 ? c.lo2[N / 3] : c.lo[N / 3]) + xoff), "i"((N % 3) * 1024) : "memory");
}
template <int O> DI void p4_split(const f32x4 (&acc)[10], unsigned tag, P4Xch& x) {
#pragma unroll
    for (int k = 0; k < 3; ++k) x.own[k] = acc[O + 4 * k < 10 ? O + 4 * k : O];
#pragma unroll
    for (int n = 0; n < 9; ++n) { const int i = ((O + 1 + n / 3) & 3) + 4 * (n % 3);
        const u32x4 v = __builtin_bit_cast(u32x4, acc[i < 10 ? i : 0]);
#pragma unroll
        for (int e = 0; e < 4; ++e) x.snd[n][e] = (v[e] & ~P4TAG) | tag; }
}
DI void p4_split(const P4Ctx& c, f32x4 (&a)[10], P4Xch& x) {
    asm volatile("s_nop 7\n\ts_nop 7\n\ts_nop 7" : "+v"(a[0]), "+v"(a[1]), "+v"(a[2]), "+v"(a[3]), "+v"(a[4]), "+v"(a[5]), "+v"(a[6]), "+v"(a[7]), "+v"(a[8]), "+v"(a[9]) :: "memory");
    switch (c.o) { case 0: p4_split<0>(a, c.tag, x); break; case 1: p4_split<1>(a, c.tag, x); break; case 2: p4_split<2>(a, c.tag, x); break; default: p4_split<3>(a, c.tag, x); break; }
}
DI bool p4_tagged(const P4Ctx& c, const P4Xch& x) {
    unsigned a = 0, b = 0;
#pragma unroll
    for (int n = 0; n < 9; ++n)
#pragma unroll
        for (int e = 0; e < 4; ++e) { if (n % 3 == 2) b |= x.rcv[n][e] ^ c.tag; else a |= x.rcv[n][e] ^ c.tag; }
    return __all(((a | (c.o < 2 ? b : 0u)) & P4TAG) == 0);
}
template <int... Ns> DI void p4_refetch(const P4Ctx& c, P4Xch& x, unsigned xoff, std::integer_sequence<int, Ns...>) {
    const unsigned long long t0 = __builtin_amdgcn_s_memrealtime();
    for (;;) {
        (p4_get<Ns>(c, x, xoff), ...);
        asm volatile("s_waitcnt vmcnt(0)" : "+v"(x.rcv[0]), "+v"(x.rcv[1]), "+v"(x.rcv[2]), "+v"(x.rcv[3]), "+v"(x.rcv[4]), "+v"(x.rcv[5]), "+v"(x.rcv[6]), "+v"(x.rcv[7]), "+v"(x.rcv[8]) :: "memory");
        if (p4_tagged(c, x)) break;
        if (__builtin_amdgcn_s_memrealtime() - t0 > 2000000ull) break;
        __builtin_amdgcn_s_sleep(1);
    }
}
DI f32x4 p4_untag(u32x4 v) { return __builtin_bit_cast(f32x4, v & ~P4TAG); }
DI f32x4 p4_total(const P4Xch& x, int k) { return (x.own[k] + p4_untag(x.rcv[k])) + (p4_untag(x.rcv[3 + k]) + p4_untag(x.rcv[6 + k])); }
template <int B, bool ZC, bool RD, int DMA, int PUT, int GET, int... Is> DI void p4_half(P4Ctx& c, P4Regs& r, P4Xch& x, f32x4 (&acc)[10], unsigned rs, unsigned ds, std::integer_sequence<int, Is...>) {
    auto one = [&](auto ic) {
        constexpr int I = decltype(ic)::value;
        if constexpr (ZC) mfz(acc[I], r.wa[B][I >> 1], r.xa[B][I & 1]); else mfa(acc[I], r.wa[B][I >> 1], r.xa[B][I & 1]);
        if constexpr (RD) p4_rd<B ^ 1, B ^ 1, I>(c, r, rs);
        if constexpr (DMA == 1) { if constexpr (I == 1) p4_piece<0>(c, ds); if constexpr (I == 5) p4_piece<1>(c, ds); if constexpr (I == 9) p4_piece<2>(c, ds); }
        if constexpr (DMA == 2) { if constexpr (I == 3) p4_piece<3>(c, ds); if constexpr (I == 7) p4_piece<4>(c, ds); }
        if constexpr (PUT >= 0 && B == 0 && I == 5) p4_put<PUT>(c, x, OFF_XG);
        if constexpr (GET >= 0 && (I & 1) && GET + (I >> 1) < 9) p4_get<GET + (I >> 1)>(c, x, OFF_XG);
        SBAR();
    };
    (one(std::integral_constant<int, Is>{}), ...);
}
template <int WAITN, bool RA, bool RB, bool LAST, int EV, int PUT, int GETA, int GETB, bool ZC = false> DI void p4_step(P4Ctx& c, const Sync& y, P4Regs& r, P4Xch& x, f32x4 (&acc)[10], unsigned& sb, unsigned& sp, int t, unsigned fidx, u64& seen) {
    constexpr auto seq = std::make_integer_sequence<int, 10>{};
    const unsigned sn = sb == 3 * P4STG ? 0 : sb + P4STG;
    p4_half<0, ZC, true, RA ? 2 : 0, PUT, GETA>(c, r, x, acc, sb, sp, seq);
    if constexpr (!LAST) {
        asm volatile("s_waitcnt vmcnt(%0) lgkmcnt(0)" :: "i"(WAITN) : "memory");
        if constexpr (EV == 1) { if (t == c.ng - 4 && c.w == 0) { asm volatile("s_waitcnt vmcnt(4)" : "+v"(seen) :: "memory"); check_flags(y.flags, fidx, seen, y.token); } }
        BARRIER();
        if constexpr (EV == 1) {
            if (c.w == 0 && t == c.ng - 5) seen = poll_asm(y.flags, fidx);
        }
        if constexpr (EV == 2) { if (c.w == 0 && lane_id() == 0) set_flag(y.flags, FL_CTX + y.j * 32 + y.h, y.token); }
        SBAR();
    }
    p4_half<1, false, !LAST, RB ? 1 : 0, PUT, GETB>(c, r, x, acc, sn, sb, seq);
    sp = sb; sb = sn;
}

template <int... Is> DI void p4_ssteps(P4Ctx& c, const Sync& y, P4Regs& r, P4Xch& x, f32x4 (&acc)[10], unsigned& sb, unsigned& sp, u64& seen, std::integer_sequence<int, Is...>) {
    (p4_step<8 + (Is >= 2 && Is <= 10) + (Is >= 1 && Is <= 9) + (Is <= 8), true, true, false, 0, (Is <= 8 ? Is : -1), -1, -1, Is == 0>(c, y, r, x, acc, sb, sp, 0, 0, seen), ...);
}

__global__ __launch_bounds__(512, 2) void fused_kernel(const bf16_t* __restrict__ x, const bf16_t* __restrict__ ckv, const bf16_t* __restrict__ wq, const bf16_t* __restrict__ wuk,
                                                       const bf16_t* __restrict__ wuv, const bf16_t* __restrict__ wo, const bf16_t* __restrict__ w1, const bf16_t* __restrict__ b1,
                                                       const bf16_t* __restrict__ w2, bf16_t* __restrict__ out, char* __restrict__ ws) {
    __shared__ __attribute__((aligned(1024))) char smem[163840];
    const lptr lds = (lptr)smem;
    const int b = blockIdx.x, xcd = b & 7, bi = b >> 3, h = xcd * 4 + (bi >> 3), j = bi & 7;
    const int w = __builtin_amdgcn_readfirstlane(threadIdx.x >> 6);
    const rsrc_t sws = mk_rsrc(ws, WS_BYTES);
    STAMP(0);
    Sync y;
    y.flags = (gu64*)(ws + OFF_FLAGS); y.b = b; y.h = h; y.j = j; y.w = w;
    u64 epoch;
    asm volatile("s_load_dwordx2 %0, %1, 0x20" : "=s"(epoch) : "s"(ws) : "memory");
    u32x4 kv[16];
    P1Ctx c1;
    P2Ctx c2;
    const bool isv = (w & 3) >= 2;
    {
        const int l = lane_id(), g = l >> 4, q = (l & 15) >> 2;
        {
            const int nb = w >> 1;
            c1.w = w; c1.sws = sws; c1.wo = wo; c1.w2 = w2; c1.wq = wq; c1.w1 = w1; c1.lds = lds;
            c1.sx = mk_rsrc(ckv, 2048 * 512 * 2);
            c1.sw = mk_rsrc(nb < 2 ? wuk : wuv, 512 * 4096 * 2);
#pragma unroll
            for (int par = 0; par < 2; ++par) {
                c1.vx[par] = (l >> 3) * 1024 + (((l & 7) ^ (4 * par + (l >> 4))) * 16);
                c1.vw[par] = (l >> 3) * 8192 + (((l & 7) ^ ((((l >> 4) & 1) | (par << 1)) << 1)) * 16);
            }
            c1.sxo = (256 * j + 32 * w) * 1024;
            c1.swo = (32 * (w & 1)) * 8192 + (h * 128 + 64 * (nb & 1)) * 2;
            const int wn = w & 3, wm = w >> 2, f = (l & 15) >> 1, s = ((q >> 1) & 1) | ((g & 1) << 1);
            c1.xb[0] = lds + wm * 16384 + (l & 15) * 128 + ((g ^ f) * 16);
            c1.xb[1] = lds + wm * 16384 + (l & 15) * 128 + (((g ^ f) ^ 4) * 16);
#pragma unroll
            for (int nt = 0; nt < 4; ++nt) c1.wb[nt] = lds + 65536 + wn * 8192 + (8 * g + q) * 128 + ((nt ^ s) * 32) + (l & 3) * 8;
        }
        p1_dma(c1, 0, 0);
        u32x4 xo0, xo1;
        {
            const rsrc_t sxi = mk_rsrc(x, 2048 * 640 * 2);
            const unsigned vx = (l & 7) * 1280 + (l >> 3) * 16, so0 = b * 8 * 1280 + w * 128, so1 = b * 8 * 1280 + (8 + (w & 1)) * 128;
            asm volatile("s_nop 4\n\tbuffer_load_dwordx4 %0, %2, %3, %4 offen\n\tbuffer_load_dwordx4 %1, %2, %3, %5 offen" : "=&v"(xo0), "=&v"(xo1) : "v"(vx), "s"(sxi), "s"(so0), "s"(so1) : "memory");
        }
        BARRIER();
        p1_dma(c1, 1, 1);
        asm volatile("s_waitcnt lgkmcnt(0)" : "+s"(epoch) :: "memory");
        y.token = epoch + 1;
        STAMP(12);
        asm volatile("s_waitcnt vmcnt(8)" : "+v"(xo0), "+v"(xo1) :: "memory");
        STAMP(13);
        float v0[8], v1[8];
#pragma unroll
        for (int e = 0; e < 4; ++e) { v0[2 * e] = bflo(xo0[e]); v0[2 * e + 1] = bfhi(xo0[e]); v1[2 * e] = bflo(xo1[e]); v1[2 * e + 1] = bfhi(xo1[e]); }
        auto red8 = [](float t) { t += __uint_as_float(__builtin_amdgcn_update_dpp(0u, __float_as_uint(t), 0x128, 0xf, 0xf, false)); return xsum4(t); };
        {
            const float m0 = red8(((v0[0] + v0[1]) + (v0[2] + v0[3])) + ((v0[4] + v0[5]) + (v0[6] + v0[7]))) * (1.f / 64.f);
            const float m1 = red8(((v1[0] + v1[1]) + (v1[2] + v1[3])) + ((v1[4] + v1[5]) + (v1[6] + v1[7]))) * (1.f / 64.f);
            float q0 = 0.f, q1 = 0.f;
#pragma unroll
            for (int e = 0; e < 8; ++e) { const float d0 = v0[e] - m0, d1 = v1[e] - m1; q0 += d0 * d0; q1 += d1 * d1; }
            q0 = red8(q0); q1 = red8(q1);
            if (l < 8) { *(LAS f32x2*)(lds + 162816 + l * 80 + w * 8) = (f32x2){m0, q0}; if (w < 2) *(LAS f32x2*)(lds + 162816 + l * 80 + 64 + w * 8) = (f32x2){m1, q1}; }
        }
        asm volatile("s_waitcnt lgkmcnt(0)" ::: "memory"); BARRIER();
        STAMP(14);
        {
            f32x4 t[5];
#pragma unroll
            for (int i = 0; i < 5; ++i) t[i] = *(const LAS f32x4*)(lds + 162816 + (l & 7) * 80 + i * 16);
            float mean = 0.f;
#pragma unroll
            for (int i = 0; i < 5; ++i) mean += t[i][0] + t[i][2];
            mean *= 0.1f;
            float m2 = 0.f;
#pragma unroll
            for (int i = 0; i < 5; ++i) { const float d0 = t[i][0] - mean, d1 = t[i][2] - mean; m2 += (t[i][1] + t[i][3]) + 64.f * (d0 * d0 + d1 * d1); }
            const float rstd = 1.f / __builtin_sqrtf(m2 * (1.f / 640.f) + 1e-5f);
            const unsigned vo = (l >> 3) * 256 + (l & 7) * 16, xo = OFF_XN + (b >> 1) * 20480 + (b & 1) * 128;
            u32x4 o0, o1;
#pragma unroll
            for (int e = 0; e < 4; ++e) { o0[e] = cvtpk((v0[2 * e] - mean) * rstd, (v0[2 * e + 1] - mean) * rstd); o1[e] = cvtpk((v1[2 * e] - mean) * rstd, (v1[2 * e + 1] - mean) * rstd); }
            st16_sc1(sws, o0, vo, xo + w * 2048);
            if (w < 2) st16_sc1(sws, o1, vo, xo + (8 + w) * 2048);
        }
        {
            const char* pa = (const char*)ckv + (size_t)(256 * j + 64 * (h & 3) + 8 * w + (l >> 3)) * 1024 + (l & 7) * 128;
            const char* pw = (const char*)((w & 1) ? wuv : wuk) + (size_t)(64 * j + 16 * (w >> 1) + ((l & 31) >> 1)) * 8192 + h * 256 + (l & 1) * 128;
            touch(pa, 131072 + w * 256); touch(pw, 131072 + w * 256);
        }
        {
            c2.w = w; c2.sws = sws; c2.swq = mk_rsrc(wq, 640 * 4096 * 2); c2.sw1 = mk_rsrc(w1, 640 * 1728 * 2);
            c2.vl16 = l * 16;
            const int swz = ((l >> 4) & 1) | ((w & 1) << 1);
            c2.vwq = (l >> 3) * 8192 + (((l & 7) ^ (swz << 1)) * 16);
            c2.vw1 = (l >> 3) * 3456 + (((l & 7) ^ (swz << 1)) * 16);
            { const int mtl = 2 * w, mtg = mtl < 8 ? 8 * j + mtl : 8 * (15 - j) + (mtl - 8); c2.sxo = OFF_XN + mtg * 20480; }
            c2.swqo = (8 * w) * 8192 + h * 256;
            c2.sw1o = (8 * w) * 3456 + h * 128;
            c2.t0l = (w >> 2) * 16384 + (w & 3) * 4096; c2.t0s = OFF_K + (unsigned)h * 524288 + c2.t0l; c2.t1s = OFF_V + (unsigned)h * 524288 + (w & 3) * 4096;
            { const int wn = w & 3, wm = w >> 2;
              c2.skv = isv ? OFF_V + (unsigned)(h * 32 + 4 * j + 2 * wm) * 16384 + (wn - 2) * 4096 : OFF_K + (unsigned)((h * 128 + 16 * j + 8 * wm) * 4 + 2 * wn) * 1024; }
        }
    }
    STAMP(1); CSTAMP(14);
    if (isv) p1_main<true>(c1, c2, y, kv); else p1_main<false>(c1, c2, y, kv);
    STAMP(2); CSTAMP(15);
    p2_kvst<0>(c2, kv, isv); p2_kvst<1>(c2, kv, isv); p2_kvst<2>(c2, kv, isv); p2_kvst<3>(c2, kv, isv);
    const bool hasf = h < 27;
    AttnCtx c3;
    AttnRegs r;
    {
        const int l = lane_id(), g = l >> 4, q = (l & 15) >> 2;
        const int wn = w & 1, wm = w >> 1;
        const int s = ((q >> 1) & 1) | ((g & 1) << 1);
        c2.xb = lds + wm * 8192 + l * 16;
#pragma unroll
        for (int nt = 0; nt < 4; ++nt) c2.wq[nt] = lds + 65536 + wn * 8192 + (8 * g + q) * 128 + ((nt ^ s) * 32) + (l & 3) * 8;
#pragma unroll
        for (int ft = 0; ft < 2; ++ft) c2.wf[ft] = lds + 65536 + 16384 + (8 * g + q) * 128 + (((2 * wn + ft) ^ s) * 32) + (l & 3) * 8;
        f32x4 acc[6][4];
#pragma unroll
        for (int a = 0; a < 6; ++a)
#pragma unroll
            for (int m = 0; m < 4; ++m) acc[a][m] = (f32x4){0.f, 0.f, 0.f, 0.f};
        STAMP(3);
        P2Bias bs; bs.p = b1 + (64 * h + 32 * wn + 4 * g); bs.a = (u32x2){0u, 0u}; bs.b = (u32x2){0u, 0u};
        if (hasf) p2_main<true>(c2, y, bs, acc, kv, isv, std::make_integer_sequence<int, 10>{}); else p2_main<false>(c2, y, bs, acc, kv, isv, std::make_integer_sequence<int, 10>{});
        STAMP(4);
        constexpr float C2 = 0.08838834764831845f * 1.4426950408889634f;
#pragma unroll
        for (int mt = 0; mt < 4; ++mt)
#pragma unroll
            for (int a = 0; a < 2; ++a)
                *(LAS u32x4*)(lds + 98304 + ((4 * wm + mt) * 4 + 2 * wn + a) * 1024 + l * 16) = pack8(acc[2 * a][mt] * C2, acc[2 * a + 1][mt] * C2);
        u32x4 actf[4];
        if (hasf) {
            asm volatile("" : "+v"(bs.a), "+v"(bs.b));
            const u32x2 bA = bs.a, bB = bs.b;
            const f32x4 biasA = {bflo(bA[0]), bfhi(bA[0]), bflo(bA[1]), bfhi(bA[1])}, biasB = {bflo(bB[0]), bfhi(bB[0]), bflo(bB[1]), bfhi(bB[1])};
#pragma unroll
            for (int mt = 0; mt < 4; ++mt) {
                f32x4 hA = acc[4][mt] + biasA, hB = acc[5][mt] + biasB;
#pragma unroll
                for (int i = 0; i < 4; ++i) { hA[i] = mish(hA[i]); hB[i] = mish(hB[i]); }
                actf[mt] = pack8(hA, hB);
            }
        }
        asm volatile("s_waitcnt vmcnt(0) lgkmcnt(0)" ::: "memory"); BARRIER();
        const int grp = w >> 2, qg = w & 3;
        c3.sws = sws; c3.vl16 = l * 16; c3.kbase = OFF_K + (unsigned)h * 524288; c3.vbase = OFF_V + (unsigned)h * 524288;
        c3.ctxl = OFF_CTX + ((8 * j + 2 * qg) * 128 + 4 * h) * 1024;
        c3.nL = 2 * j + 2; c3.grp = grp; c3.qg = qg; c3.j = j; c3.rowrel = (l & 15) - 4 * g; c3.kl = lds + l * 16; c3.vl = lds + 65536 + l * 16;
        c3.ql = lds + 131072 + qg * 8192 + l * 16;
        if (hasf) {
#pragma unroll
            for (int mt = 0; mt < 4; ++mt) { const int mtl = 4 * wm + mt, mtg = mtl < 8 ? 8 * j + mtl : 8 * (15 - j) + (mtl - 8);
                st16_sc1(sws, actf[mt], l * 16, OFF_ACT + (mtg * 54 + 2 * h + wn) * 1024); }
        }
#pragma unroll
        for (int qt = 0; qt < 2; ++qt)
#pragma unroll
            for (int u = 0; u < 4; ++u) r.q[qt][u] = ld128(lds + 98304 + ((8 * grp + 2 * qg + qt) * 4 + u) * 1024 + l * 16);
    }
    STAMP(5);
    P4Ctx c4;
    {
        const int l = lane_id();
        const int grp = c3.grp, qg = c3.qg;
        const unsigned vl16 = c3.vl16;
#pragma unroll
        for (int d = 0; d < 8; ++d) { r.o[d][0] = (f32x4){0.f, 0.f, 0.f, 0.f}; r.o[d][1] = (f32x4){0.f, 0.f, 0.f, 0.f}; }
        r.negm[0] = (f32x4){0.f, 0.f, 0.f, 0.f}; r.negm[1] = (f32x4){0.f, 0.f, 0.f, 0.f};
        AttnSm z; z.m0 = 0.f; z.m1 = 0.f;
        r.ls[0] = (f32x4){0.f, 0.f, 0.f, 0.f}; r.ls[1] = (f32x4){0.f, 0.f, 0.f, 0.f};
        r.ones = (bf16x8){0x3F80, 0x3F80, 0x3F80, 0x3F80, 0x3F80, 0x3F80, 0x3F80, 0x3F80}; PIN(r.ones);
        asm volatile("s_waitcnt lgkmcnt(0)" ::: "memory");
        BARRIER();
        STAMP(6); CSTAMP(12);
        a_pre<16>(r, c3.vl, c3.kl, std::make_integer_sequence<int, PD>{});
        if (grp == 1) BARRIER();
        { Piece none; none.on = false; none.sws = sws; none.vl16 = 0; none.soff = 0; none.ldso = 0; a_run<16, 0>(r, c3.vl, c3.kl, none, std::make_integer_sequence<int, 16>{}); }
        BARRIER();
        c3.swap_s = grp == 0 ? c3.nL - 1 : 99; c3.first_s = 0;
        asm volatile("" : "+s"(c3.swap_s), "+s"(c3.first_s));
        {
            const int nL = c3.nL;
            auto lean_init = [&](int s) {
                Lean st;
                const bool eK = grp == 1 && s + 2 < nL;
                const int t0 = s < nL ? s : 2 * s - nL + grp, t1 = s + 1 < nL ? s + 1 : 2 * s + 2 - nL + grp, t2 = s + 2 < nL ? s + 2 : 2 * s + 4 - nL + grp;
                const int te = eK ? t2 : t1;
                st.e_soff = (eK ? c3.kbase : c3.vbase) + te * 16384 + qg * 4096; st.e_lds = (eK ? 0 : 65536) + (te & 3) * 16384 + qg * 4096;
                st.l_soff = c3.kbase + t2 * 16384 + qg * 4096; st.l_lds = (t2 & 3) * 16384 + qg * 4096;
                st.voff = (t0 & 3) * 16384; st.koff = (t1 & 3) * 16384;
                return st;
            };
            int s = 0, phase = 0;
#pragma nounroll
            for (;;) {
                const int gend = phase == 0 ? 1 : (phase == 1 ? (grp == 0 ? nL + 1 : nL - 1) : 17);
#pragma nounroll
                while (s < gend) { a_slotfull(c3, y, r, z, s PPASSG); ++s; }
                if (phase == 0) { if (w == 0 && l == 0) set_flag(y.flags, FL_ACT + j * 32 + h, y.token); }
                if (phase == 2 || s > 16) break;
                if (phase == 1 && s <= 14) {
                    Lean st = lean_init(s);
#pragma nounroll
                    do { a_slot_lean<true>(c3, r, z, st PPASS);
                         st.e_soff += 32768; st.e_lds ^= 32768; st.l_soff += 32768; st.l_lds ^= 32768; st.voff ^= 32768; st.koff ^= 32768; ++s; } while (s <= 14);
                }
                const int send = phase == 0 ? nL - 2 : 16;
                if (s < send) {
                    Lean st = lean_init(s);
#pragma nounroll
                    do { a_slot_lean<false>(c3, r, z, st PPASS);
                         st.e_soff += 16384; st.e_lds = (st.e_lds & ~0xC000u) | ((st.e_lds + 16384) & 0xC000u); st.voff = (st.voff + 16384) & 0xC000u; st.koff = (st.koff + 16384) & 0xC000u; ++s; } while (s < send);
                }
                ++phase;
            }
        }
        const float m0 = z.m0, m1 = z.m1, l0 = r.ls[0][0], l1 = r.ls[1][0];
        if (grp == 0) BARRIER();
        STAMP(7); CSTAMP(13);
        {
            const int q = xcd >> 1, rbk = (xcd & 1) * 8 + (bi >> 2), cb = bi & 3, wm = w >> 1, wn = w & 1;
            c4.sws = sws; c4.vl16 = vl16; c4.w = w; c4.q = q; c4.o = (q + 2 * wn) & 3; c4.ng = q == 3 ? 6 : 7; c4.ra = 0; c4.rw = 0;
            c4.ga = OFF_ACT + ((8 * rbk + w) * 54 + 14 * q) * 1024; c4.gas = OFF_CTX + ((8 * rbk + w) * 128 + 32 * q) * 1024;
            c4.gw = OFF_WP + ((128 + 14 * q) * 40 + 10 * cb) * 1024; c4.gws = OFF_WP + (32 * q * 40 + 10 * cb) * 1024;
            c4.dw0 = w * 1024; c4.dw1 = (w < 2 ? 8 + w : 38 + w) * 1024; c4.dw2 = (46 + w) * 1024; c4.lw = 16384 + w * 1024;
            c4.tag = (unsigned)y.token & P4TAG;
            const unsigned xb = (unsigned)(((xcd & 1) * 32 + bi) * 128 + w) * 3072;
#pragma unroll
            for (int d = 0; d < 3; ++d) { const int qq = (q + 1 + d) & 3;
                c4.so[d] = xb + qq * 98304 + q * 24576; c4.so2[d] = ((c4.o + 1 + d) & 3) < 2 ? c4.so[d] : P4OOB;
                c4.lo[d] = xb + q * 98304 + qq * 24576; c4.lo2[d] = c4.o < 2 ? c4.lo[d] : P4OOB; }
            c4.pa = lds + (4 * wm) * 1024 + vl16; c4.pw = lds + 16384 + (5 * wn) * 1024 + vl16;
        }
        p4_piece<0>(c4, 2 * P4STG); p4_piece<1>(c4, 2 * P4STG); p4_piece<2>(c4, 2 * P4STG); p4_piece<3>(c4, 2 * P4STG); p4_piece<4>(c4, 2 * P4STG);
        p4_piece<0>(c4, 3 * P4STG); p4_piece<1>(c4, 3 * P4STG); p4_piece<2>(c4, 3 * P4STG); p4_piece<3>(c4, 3 * P4STG); p4_piece<4>(c4, 3 * P4STG);
        {
            const lptr xo = lds + (qg * 2 + grp) * 9216;
#pragma unroll
            for (int d = 0; d < 4; ++d)
#pragma unroll
                for (int qt = 0; qt < 2; ++qt) *(LAS f32x4*)(xo + (d * 2 + qt) * 1024 + l * 16) = sel(grp == 0, r.o[4 + d][qt], r.o[d][qt]);
            *(LAS f32x4*)(xo + 8192 + l * 16) = (f32x4){m0, m1, l0, l1};
            asm volatile("s_waitcnt lgkmcnt(0)" ::: "memory"); BARRIER();
            const lcptr xi = lds + (qg * 2 + (grp ^ 1)) * 9216;
            const f32x4 st = *(const LAS f32x4*)(xi + 8192 + l * 16);
            const float M0 = __builtin_fmaxf(m0, st[0]), M1 = __builtin_fmaxf(m1, st[1]);
            const float wa0 = __builtin_amdgcn_exp2f(m0 - M0), wb0 = __builtin_amdgcn_exp2f(st[0] - M0), wa1 = __builtin_amdgcn_exp2f(m1 - M1), wb1 = __builtin_amdgcn_exp2f(st[1] - M1);
            const float i0 = __builtin_amdgcn_rcpf(wa0 * l0 + wb0 * st[2]), i1 = __builtin_amdgcn_rcpf(wa1 * l1 + wb1 * st[3]);
            const float ca0 = wa0 * i0, cb0 = wb0 * i0, ca1 = wa1 * i1, cb1 = wb1 * i1;
#pragma unroll
            for (int up = 0; up < 2; ++up)
#pragma unroll
                for (int qt = 0; qt < 2; ++qt) {
                    const f32x4 pa = *(const LAS f32x4*)(xi + ((2 * up) * 2 + qt) * 1024 + l * 16), pb = *(const LAS f32x4*)(xi + ((2 * up + 1) * 2 + qt) * 1024 + l * 16);
                    const f32x4 oa = sel(grp == 0, r.o[2 * up][qt], r.o[4 + 2 * up][qt]), ob = sel(grp == 0, r.o[2 * up + 1][qt], r.o[4 + 2 * up + 1][qt]);
                    const float ca = qt ? ca1 : ca0, cb = qt ? cb1 : cb0;
                    st16_sc1(sws, pack8(oa * ca + pa * cb, ob * ca + pb * cb), vl16, OFF_CTX + ((8 * (15 - j) + 2 * qg + qt) * 128 + 4 * h + 2 * grp + up) * 1024);
                }
        }
    }
    STAMP(8);
    {
        asm volatile("s_waitcnt vmcnt(8) lgkmcnt(0)" ::: "memory"); BARRIER();
        P4Regs r4;
        unsigned sb = 2 * P4STG, sp = P4STG;
        p4_rd<0, 0, 0>(c4, r4, sb); p4_rd<0, 0, 1>(c4, r4, sb); p4_rd<0, 0, 2>(c4, r4, sb); p4_rd<0, 0, 3>(c4, r4, sb); p4_rd<0, 0, 4>(c4, r4, sb); p4_rd<0, 0, 5>(c4, r4, sb); p4_rd<0, 0, 6>(c4, r4, sb);
        SBAR();
        p4_piece<0>(c4, 0); p4_piece<1>(c4, 0); p4_piece<2>(c4, 0); p4_piece<3>(c4, 0); p4_piece<4>(c4, 0);
        p4_piece<0>(c4, P4STG); p4_piece<1>(c4, P4STG); p4_piece<2>(c4, P4STG);
        const int l = lane_id();
        const int rbk = (xcd & 1) * 8 + (bi >> 2), cb = bi & 3;
        const unsigned cidx = FL_CTX + (rbk < 8 ? rbk : 15 - rbk) * 32 + 8 * c4.q + (l & 7);
        f32x4 accg[10], accs[10], gt[3];
        P4Xch xg;
        const int row0 = 128 * rbk + 32 * (w >> 1) + (l & 15), col0 = 160 * cb + 80 * (w & 1) + 4 * (l >> 4);
        u32x2 xv[3];
        {
            const rsrc_t sx = mk_rsrc(x, 2048 * 640 * 2);
#pragma unroll
            for (int k = 0; k < 3; ++k) { const int i = c4.o + 4 * k < 10 ? c4.o + 4 * k : c4.o;
                asm volatile("buffer_load_dwordx2 %0, %1, %2, 0 offen" : "=v"(xv[k]) : "v"((unsigned)((row0 + (i & 1) * 16) * 640 + col0 + (i >> 1) * 16) * 2), "s"(sx) : "memory"); }
        }
        u64 seen = 0;
        SBAR();
        STAMP(9);
        p4_step<P4W0, true, true, false, 2, -1, -1, -1, true>(c4, y, r4, xg, accg, sb, sp, 0, cidx, seen);
#pragma nounroll
        for (int t = 1; t < c4.ng; ++t) p4_step<8, true, true, false, 1, -1, -1, -1>(c4, y, r4, xg, accg, sb, sp, t, cidx, seen);
        p4_split(c4, accg, xg);
        p4_ssteps(c4, y, r4, xg, accs, sb, sp, seen, std::make_integer_sequence<int, 12>{});
        p4_step<8, true, false, false, 0, -1, -1, 0>(c4, y, r4, xg, accs, sb, sp, 0, 0, seen);
        p4_step<13, false, false, false, 0, -1, 5, -1>(c4, y, r4, xg, accs, sb, sp, 0, 0, seen);
        p4_step<9, false, false, false, 0, -1, -1, -1>(c4, y, r4, xg, accs, sb, sp, 0, 0, seen);
        p4_step<0, false, false, true, 0, -1, -1, -1>(c4, y, r4, xg, accs, sb, sp, 0, 0, seen);
        asm volatile("s_waitcnt vmcnt(0)" : "+v"(xg.rcv[0]), "+v"(xg.rcv[1]), "+v"(xg.rcv[2]), "+v"(xg.rcv[3]), "+v"(xg.rcv[4]), "+v"(xg.rcv[5]), "+v"(xg.rcv[6]), "+v"(xg.rcv[7]), "+v"(xg.rcv[8]),
                     "+v"(xv[0]), "+v"(xv[1]), "+v"(xv[2]) :: "memory");
        constexpr auto nine = std::make_integer_sequence<int, 9>{};
        P4Xch xs;
        p4_split(c4, accs, xs);
        p4_put<0>(c4, xs, OFF_XS); p4_put<1>(c4, xs, OFF_XS); p4_put<2>(c4, xs, OFF_XS); p4_put<3>(c4, xs, OFF_XS); p4_put<4>(c4, xs, OFF_XS);
        p4_put<5>(c4, xs, OFF_XS); p4_put<6>(c4, xs, OFF_XS); p4_put<7>(c4, xs, OFF_XS); p4_put<8>(c4, xs, OFF_XS);
        TS(0);
        if (!p4_tagged(c4, xg)) p4_refetch(c4, xg, OFF_XG, nine);
#pragma unroll
        for (int k = 0; k < 3; ++k) {
            gt[k] = p4_total(xg, k);
#pragma unroll
            for (int e = 0; e < 4; ++e) gt[k][e] = gt[k][e] * __builtin_amdgcn_rcpf(1.f + __builtin_amdgcn_exp2f(-1.4426950408889634f * gt[k][e]));
            PIN(gt[k]);
        }
        TS(1);
        p4_refetch(c4, xs, OFF_XS, nine);
        TS(2);
#pragma unroll
        for (int k = 0; k < 3; ++k) {
            if (k < 2 || c4.o < 2) { const int i = c4.o + 4 * k;
                const f32x4 sv = p4_total(xs, k);
                const f32x4 o = {bflo(xv[k][0]) + sv[0] * gt[k][0], bfhi(xv[k][0]) + sv[1] * gt[k][1], bflo(xv[k][1]) + sv[2] * gt[k][2], bfhi(xv[k][1]) + sv[3] * gt[k][3]};
                const u32x2 ov = {cvtpk(o[0], o[1]), cvtpk(o[2], o[3])};
                asm volatile("global_store_dwordx2 %0, %1, off sc0 sc1\n\ts_nop 1" :: "v"(out + (row0 + (i & 1) * 16) * 640 + col0 + (i >> 1) * 16), "v"(ov) : "memory"); }
        }
        if (b == 0 && w == 0 && l == 0) __hip_atomic_store(y.flags + 4, y.token, __ATOMIC_RELAXED, __HIP_MEMORY_SCOPE_AGENT);
    }
    STAMP(11);
}

extern "C" void kernel_launch(void* const* d_in, const int* in_sizes, int n_in, void* d_out, int out_size, void* d_ws, size_t ws_size, hipStream_t stream) {
    (void)in_sizes; (void)n_in; (void)out_size; (void)ws_size;
    fused_kernel<<<256, 512, 0, stream>>>((const bf16_t*)d_in[0], (const bf16_t*)d_in[1], (const bf16_t*)d_in[2], (const bf16_t*)d_in[3], (const bf16_t*)d_in[4],
                                          (const bf16_t*)d_in[5], (const bf16_t*)d_in[7], (const bf16_t*)d_in[8], (const bf16_t*)d_in[9], (bf16_t*)d_out, (char*)d_ws);
}
```

```cpp
#include <hip/hip_runtime.h>
#include <stdint.h>
#include <utility>

typedef unsigned short bf16_t;
typedef short bf16x8 __attribute__((ext_vector_type(8)));
typedef short s16x4 __attribute__((ext_vector_type(4)));
typedef short v4i16_t __attribute__((ext_vector_type(4)));
typedef float f32x4 __attribute__((ext_vector_type(4)));
typedef float f32x2 __attribute__((ext_vector_type(2)));
typedef unsigned u32x4 __attribute__((ext_vector_type(4)));
typedef unsigned u32x2 __attribute__((ext_vector_type(2)));
typedef __bf16 bf16x2_t __attribute__((ext_vector_type(2)));
typedef unsigned short u16x2 __attribute__((ext_vector_type(2)));
typedef __amdgpu_buffer_rsrc_t rsrc_t;
typedef unsigned long long u64;
#define LAS __attribute__((address_space(3)))
#define GAS __attribute__((address_space(1)))
typedef LAS char* lptr;
typedef LAS const char* lcptr;
typedef GAS u64 gu64;
#define DI __device__ __forceinline__
#define SBAR() __builtin_amdgcn_sched_barrier(0)
#define PIN(x) asm volatile("" : "+v"(x))
#define MF16(a, b, c) __builtin_amdgcn_mfma_f32_16x16x32_bf16(a, b, c, 0, 0, 0)
#define WAITV(n) asm volatile("s_waitcnt vmcnt(" #n ")" ::: "memory")
#define WAITL(n) asm volatile("s_waitcnt lgkmcnt(" #n ")" ::: "memory")
#define BARRIER() do { asm volatile("" ::: "memory"); __builtin_amdgcn_s_barrier(); asm volatile("" ::: "memory"); } while (0)

#define UPTO 4
#define STAG 1
#define NOINV 1
#define P0EB 1
#define KVR 2
#define CODET 1
#define SPREAD 1
#define SPOS 1
#define XST 0
#define STAMPS 0

constexpr unsigned OFF_FLAGS = 0;
constexpr unsigned OFF_XN = 65536;
constexpr unsigned OFF_K = OFF_XN + 2621440;
constexpr unsigned OFF_V = OFF_K + 16777216;
constexpr unsigned OFF_ACT = OFF_V + 16777216 + 8388608;
constexpr unsigned OFF_CTX = OFF_ACT + 7077888;
constexpr unsigned OFF_WP = OFF_CTX + 16777216;
constexpr unsigned OFF_ST = OFF_WP + 182 * 40 * 1024;
constexpr unsigned OFF_XG = OFF_ST + 65536;
constexpr unsigned OFF_XS = OFF_XG + 25165824;
constexpr unsigned WS_BYTES = OFF_XS + 25165824;
constexpr unsigned FL_P0 = 0, FL_KV = 256, FL_ACT = 512, FL_CTX = 768;
constexpr int SC1 = 16;

DI unsigned cvtpk(float lo, float hi) { f32x2 v = {lo, hi}; bf16x2_t b = __builtin_convertvector(v, bf16x2_t); return __builtin_bit_cast(unsigned, b); }
DI u32x4 pack8(f32x4 a, f32x4 b) { u32x4 w = {cvtpk(a[0], a[1]), cvtpk(a[2], a[3]), cvtpk(b[0], b[1]), cvtpk(b[2], b[3])}; return w; }
DI float bflo(unsigned u) { return __uint_as_float(u << 16); }
DI float bfhi(unsigned u) { return __uint_as_float(u & 0xffff0000u); }
DI s16x4 vtr(lcptr p) { return __builtin_bit_cast(s16x4, __builtin_amdgcn_ds_read_tr16_b64_v4i16((LAS v4i16_t*)p)); }
DI bf16x8 cat(s16x4 a, s16x4 b) { return (bf16x8){a[0], a[1], a[2], a[3], b[0], b[1], b[2], b[3]}; }
DI bf16x8 ld128(lcptr p) { return *(const LAS bf16x8*)p; }
DI bf16x8 asbf(u32x4 v) { return __builtin_bit_cast(bf16x8, v); }
DI rsrc_t mk_rsrc(const void* p, unsigned bytes) { return __builtin_amdgcn_make_buffer_rsrc((void*)p, (short)0, (int)bytes, 0x00020000); }
DI void dma16(rsrc_t r, unsigned voff, unsigned soff, unsigned ldsoff) {
    asm volatile("s_mov_b32 m0, %0\n\ts_nop 3\n\tbuffer_load_dwordx4 %1, %2, %3 offen lds" ::"s"(ldsoff), "v"(voff), "s"(r), "s"(soff) : "memory");
}
#define HS " sc1"
DI void dma16h(rsrc_t r, unsigned voff, unsigned soff, unsigned ldsoff) {
    asm volatile("s_mov_b32 m0, %0\n\ts_nop 3\n\tbuffer_load_dwordx4 %1, %2, %3 offen" HS " lds" ::"s"(ldsoff), "v"(voff), "s"(r), "s"(soff) : "memory");
}
DI void dma16x4(rsrc_t r, unsigned voff, unsigned soff, unsigned ldsoff) {
    asm volatile("s_mov_b32 m0, %0\n\ts_nop 3\n\tbuffer_load_dwordx4 %1, %2, %3 offen" HS " lds\n\tbuffer_load_dwordx4 %1, %2, %3 offen offset:1024" HS " lds\n\t"
                 "buffer_load_dwordx4 %1, %2, %3 offen offset:2048" HS " lds\n\tbuffer_load_dwordx4 %1, %2, %3 offen offset:3072" HS " lds" ::"s"(ldsoff), "v"(voff), "s"(r), "s"(soff) : "memory");
}
DI void touch(const void* p, unsigned ldsoff) { asm volatile("s_mov_b32 m0, %0\n\ts_nop 3\n\tglobal_load_lds_dword %1, off" ::"s"(ldsoff), "v"(p) : "memory"); }
DI void st16_sc1(rsrc_t r, u32x4 v, unsigned voff, unsigned soff) { asm volatile("s_nop 4\n\tbuffer_store_dwordx4 %0, %1, %2, %3 offen sc1\n\ts_nop 1" ::"v"(v), "v"(voff), "s"(r), "s"(soff) : "memory"); }
template <int OFF> DI void st16_sc1o(rsrc_t r, u32x4 v, unsigned voff, unsigned soff) { asm volatile("s_nop 4\n\tbuffer_store_dwordx4 %0, %1, %2, %3 offen offset:%4 sc1\n\ts_nop 1" ::"v"(v), "v"(voff), "s"(r), "s"(soff), "i"(OFF) : "memory"); }
DI u32x4 ld16(rsrc_t r, unsigned voff, unsigned soff) { return __builtin_amdgcn_raw_buffer_load_b128(r, voff, soff, 0); }
#define P4AUX SC1
DI u32x4 ld16_sc1(rsrc_t r, unsigned voff, unsigned soff) { return __builtin_amdgcn_raw_buffer_load_b128(r, voff, soff, P4AUX); }
template <class T> DI T sel(bool c, T a, T b) { return c ? a : b; }
DI int lane_id() { int l; asm volatile("v_mbcnt_lo_u32_b32 %0, -1, 0\n\tv_mbcnt_hi_u32_b32 %0, -1, %0" : "=v"(l)); return l; }
DI void set_flag(gu64* flags, unsigned idx, u64 token) { __hip_atomic_store(flags + idx * 8, token, __ATOMIC_RELAXED, __HIP_MEMORY_SCOPE_AGENT); }
DI u64 poll_flag(gu64* flags, unsigned idx) { return __hip_atomic_load(flags + idx * 8, __ATOMIC_RELAXED, __HIP_MEMORY_SCOPE_AGENT); }
DI void spin_flags(gu64* flags, unsigned idx, u64 token) {
    const unsigned long long t0 = __builtin_amdgcn_s_memrealtime();
    for (;;) {
        __builtin_amdgcn_s_sleep(1);
        const u64 v = poll_flag(flags, idx);
        if (__all(v == token)) break;
        if (__builtin_amdgcn_s_memrealtime() - t0 > 2000000ull) break;
    }
}
DI u64 poll_asm(gu64* flags, unsigned idx) { u64 v; asm volatile("global_load_dwordx2 %0, %1, off sc1" : "=v"(v) : "v"(flags + idx * 8) : "memory"); return v; }
DI void check_flags(gu64* flags, unsigned idx, u64 seen, u64 token) { if (!__all(seen == token)) spin_flags(flags, idx, token); }
DI void acquire_issue() { asm volatile("buffer_inv sc1" ::: "memory"); }
DI u64 uni64(u64 v) { return ((u64)(unsigned)__builtin_amdgcn_readfirstlane((unsigned)(v >> 32)) << 32) | (unsigned)__builtin_amdgcn_readfirstlane((unsigned)v); }
#define SCLK __builtin_amdgcn_s_memrealtime
#define STAMP(i) do { } while (0)
#define STAMPW(i, wv) do { } while (0)
#define CSTAMP(i) do { } while (0)

#define TS(i) do { } while (0)
#define TSARG
#define TSPASS
struct Sync { gu64* flags; u64 token; int b, h, j, w; };
#define YSTAMP(i) do { } while (0)

struct P2Ctx { rsrc_t sws, swq, sw1; unsigned vl16, vwq, vw1; unsigned sxo, swqo, sw1o, skv, t0s, t0l, t1s; int w; lcptr xb; lcptr wq[4]; lcptr wf[2]; };
DI void p2_dma1(const P2Ctx& c, int st, int ks, int i) {
    if (i < 4) dma16h(c.sws, c.vl16, c.sxo + (i >> 1) * 20480 + (i & 1) * 1024 + ks * 2048, st * 32768 + (4 * c.w + i) * 1024);
    else if (i < 6) dma16(c.swq, c.vwq, c.swqo + (i - 4) * 128 + ks * 524288, 65536 + st * 24576 + (i - 4) * 8192 + c.w * 1024);
    else dma16(c.sw1, c.vw1, c.sw1o + ks * (64 * 3456), 65536 + st * 24576 + 16384 + c.w * 1024);
}

struct P1Ctx { rsrc_t sx, sw, sws; unsigned vx[2], vw[2]; unsigned sxo, swo; int w; lcptr xb[2]; lcptr wb[4]; const bf16_t *wo, *w2, *wq, *w1; lcptr lds; };
DI void pack_fetch(const P1Ctx& c, int b) {
    const int l = lane_id(), gw = b * 8 + c.w, pu = gw / 10, pq = gw - pu * 10;
    const rsrc_t sp = mk_rsrc(pu < 128 ? c.wo : c.w2, pu < 128 ? 4096 * 640 * 2 : 1728 * 640 * 2);
    const unsigned so = gw < 1820 ? (unsigned)((pu < 128 ? pu : pu - 128) * 32 * 1280 + pq * 128) : 0xF0000000u;
#pragma unroll
    for (int i = 0; i < 4; ++i) dma16(sp, (l >> 3) * 1280 + (l & 7) * 16, so + i * 8 * 1280, 131072 + c.w * 4096 + i * 1024);
}
DI void pack_store(const P1Ctx& c, int b) {
    const int l = lane_id(), g = l >> 4, q = (l & 15) >> 2, gw = b * 8 + c.w, pu = gw / 10, pq = gw - pu * 10;
    lcptr p = c.lds + 131072 + c.w * 4096 + (4 * g + q) * 128 + (l & 3) * 8;
    bf16x8 fr[4];
#pragma unroll
    for (int i = 0; i < 4; ++i) fr[i] = cat(vtr(p + i * 32), vtr(p + i * 32 + 16 * 128));
    if (gw < 1820) {
#pragma unroll
        for (int i = 0; i < 4; ++i) st16_sc1(c.sws, __builtin_bit_cast(u32x4, fr[i]), l * 16, OFF_WP + (pu * 40 + pq * 4 + i) * 1024);
    }
}
DI void p1_dma1(const P1Ctx& c, int st, int ks, int i) {
    if (i < 4) dma16(c.sx, c.vx[i & 1], c.sxo + i * 8192 + ks * 128, st * 32768 + (4 * c.w + i) * 1024);
    else dma16(c.sw, c.vw[i & 1], c.swo + (i - 4) * 65536 + ks * 524288, 65536 + st * 32768 + (4 * c.w + (i - 4)) * 1024);
}
DI void p1_dma(const P1Ctx& c, int st, int ks) {
#pragma unroll
    for (int i = 0; i < 8; ++i) p1_dma1(c, st, ks, i);
}
template <int ST, int UP, int I> DI void p1_rdw(const P1Ctx& c, bf16x8 (&wf)[4], s16x4 (&wl)[4]) {
    constexpr int nt = I >> 1, rd = I & 1;
    const s16x4 v = vtr(c.wb[nt] + ST * 32768 + UP * 4096 + rd * 512);
    if constexpr (rd == 0) wl[nt] = v; else wf[nt] = cat(wl[nt], v);
}
template <int ST, int G> DI bf16x8 p1_rdx(const P1Ctx& c) { return ld128(c.xb[G >> 3] + ST * 32768 + (G & 7) * 2048); }
template <int ST, int KSC, int K> DI bool p1_is(int ks) { if constexpr (KSC >= 0) return KSC == K; else if constexpr ((K & 1) != ST || K > 5) return false; else return ks == K; }
template <bool ISV, int ST, int KSC, int G> DI void p1_group(const P1Ctx& c, const P2Ctx& c2, const Sync& y, const int ks, u64& seen, f32x4 (&acc)[4][8], bf16x8 (&wA)[4], bf16x8 (&wB)[4], s16x4 (&wl)[4], bf16x8 (&xr)[4]) {
    constexpr int up = G >> 3, mt = G & 7;
    constexpr bool NEXT = KSC != 7;
    if constexpr (G == 13) {
        if (p1_is<ST, KSC, 0>(ks)) asm volatile("s_waitcnt vmcnt(4) lgkmcnt(0)" ::: "memory");
        else if (p1_is<ST, KSC, 1>(ks) || p1_is<ST, KSC, 3>(ks)) asm volatile("s_waitcnt vmcnt(1) lgkmcnt(0)" ::: "memory");
        else if (p1_is<ST, KSC, 5>(ks) || p1_is<ST, KSC, 7>(ks)) asm volatile("s_waitcnt vmcnt(4) lgkmcnt(0)" ::: "memory");
        else asm volatile("s_waitcnt vmcnt(0) lgkmcnt(0)" ::: "memory");
        if constexpr (!ISV && KSC == 6) { if (y.w == 0) { const int l = lane_id() & 31; check_flags(y.flags, FL_P0 + (l < 16 ? 16 * y.j + l : 16 * (15 - y.j) + (l - 16)), seen, y.token); } }
        BARRIER();
        if constexpr (!ISV) {
            if (p1_is<ST, KSC, 1>(ks)) { if (y.w == 0 && lane_id() == 0) set_flag(y.flags, FL_P0 + y.b, y.token); }
        }
        SBAR();
    }
    const bf16x8 x = xr[G & 3];
#pragma unroll
    for (int nt = 0; nt < 4; ++nt) {
        const bf16x8 wv = up == 0 ? wA[nt] : wB[nt];
        if constexpr (ISV) acc[nt][mt] = MF16(x, wv, acc[nt][mt]); else acc[nt][mt] = MF16(wv, x, acc[nt][mt]);
        if (nt == 0) {
            if constexpr (G + 3 < 16) xr[(G + 3) & 3] = p1_rdx<ST, G + 3>(c);
            else if constexpr (NEXT) xr[(G + 3) & 3] = p1_rdx<ST ^ 1, G + 3 - 16>(c);
        }
        if constexpr (G >= 2 && G < 6) { if (nt == 1) p1_rdw<ST, 1, 2 * (G - 2)>(c, wB, wl); if (nt == 2) p1_rdw<ST, 1, 2 * (G - 2) + 1>(c, wB, wl); }
        if constexpr (G >= 13 && NEXT) {
            if (nt == 1) p1_rdw<ST ^ 1, 0, 3 * (G - 13)>(c, wA, wl);
            if (nt == 2) p1_rdw<ST ^ 1, 0, 3 * (G - 13) + 1>(c, wA, wl);
            if constexpr (G < 15) { if (nt == 3) p1_rdw<ST ^ 1, 0, 3 * (G - 13) + 2>(c, wA, wl); }
        }
        if (nt == SPOS) {
            if constexpr (G >= 13) { constexpr int p = G - 13;
                if constexpr (KSC < 0) p1_dma1(c, ST, ks + 2, p);
                else if constexpr (KSC == 6) p2_dma1(c2, 0, 0, p);
                else p2_dma1(c2, 1, 1, p);
            } else if constexpr (G <= 4) { constexpr int p = G + 3;
                if constexpr (KSC == 7) { if constexpr (p < 7) p2_dma1(c2, 0, 0, p); }
                else { if (!p1_is<ST, KSC, 0>(ks)) p1_dma1(c, ST ^ 1, ks + 1, p); }
            }
        }
        SBAR();
    }
    if constexpr (G == 6) {
        if (p1_is<ST, KSC, 1>(ks)) {
            const int l = lane_id(), L = (c.w & 3) * 64 + l;
            const char* pb = (L >= 160 && L < 240 && y.h < 27) ? (const char*)c.w1 + (size_t)(80 * y.j + L - 160) * 3456 + y.h * 128
                                                               : (const char*)c.wq + (size_t)(80 * y.j + ((L < 160 ? L : 0) >> 1)) * 8192 + y.h * 256 + (L & 1) * 128;
            touch(pb, 131072 + c.w * 256);
        }
        if (p1_is<ST, KSC, 3>(ks)) {
            const int l = lane_id(), gw = y.b * 8 + c.w < 1820 ? y.b * 8 + c.w : 0, pu = gw / 10, pq = gw - pu * 10;
            const char* pc = (const char*)(pu < 128 ? c.wo : c.w2) + (size_t)((pu < 128 ? pu : pu - 128) * 32 + (l & 31)) * 1280 + pq * 128;
            touch(pc, 131072 + c.w * 256);
        }
        if constexpr (!ISV && KSC == 6) { if (y.w == 0) { const int l = lane_id() & 31; seen = poll_flag(y.flags, FL_P0 + (l < 16 ? 16 * y.j + l : 16 * (15 - y.j) + (l - 16))); } }
        if (p1_is<ST, KSC, 5>(ks)) { pack_fetch(c, y.b); if constexpr (!ISV && !NOINV) { if (y.w == 0) acquire_issue(); } }
        if constexpr (KSC == 7) pack_store(c, y.b);
        SBAR();
    }
}
template <bool ISV, int ST, int KSC, int... Gs> DI void p1_step(const P1Ctx& c, const P2Ctx& c2, const Sync& y, const int ks, u64& seen, f32x4 (&acc)[4][8], bf16x8 (&wA)[4], bf16x8 (&wB)[4], s16x4 (&wl)[4], bf16x8 (&xr)[4], std::integer_sequence<int, Gs...>) {
    (p1_group<ISV, ST, KSC, Gs>(c, c2, y, ks, seen, acc, wA, wB, wl, xr), ...);
}
template <bool ISV> DI void p1_main(const P1Ctx& c, const P2Ctx& c2, const Sync& y, u32x4 (&kv)[16]) {
    f32x4 acc[4][8];
#pragma unroll
    for (int a = 0; a < 4; ++a)
#pragma unroll
        for (int b = 0; b < 8; ++b) acc[a][b] = (f32x4){0.f, 0.f, 0.f, 0.f};
    bf16x8 wA[4], wB[4], xr[4]; s16x4 wl[4];
    u64 seen = 0;
    asm volatile("" ::: "memory");
    p1_rdw<0, 0, 0>(c, wA, wl); p1_rdw<0, 0, 1>(c, wA, wl); p1_rdw<0, 0, 2>(c, wA, wl); p1_rdw<0, 0, 3>(c, wA, wl);
    p1_rdw<0, 0, 4>(c, wA, wl); p1_rdw<0, 0, 5>(c, wA, wl); p1_rdw<0, 0, 6>(c, wA, wl); p1_rdw<0, 0, 7>(c, wA, wl);
    xr[0] = p1_rdx<0, 0>(c); xr[1] = p1_rdx<0, 1>(c); xr[2] = p1_rdx<0, 2>(c);
    SBAR();
    constexpr auto seq = std::make_integer_sequence<int, 16>{};
#pragma nounroll
    for (int ks = 0; ks < 6; ks += 2) {
        p1_step<ISV, 0, -1>(c, c2, y, ks, seen, acc, wA, wB, wl, xr, seq);
        p1_step<ISV, 1, -1>(c, c2, y, ks + 1, seen, acc, wA, wB, wl, xr, seq);
    }
    p1_step<ISV, 0, 6>(c, c2, y, 6, seen, acc, wA, wB, wl, xr, seq);
    p1_step<ISV, 1, 7>(c, c2, y, 7, seen, acc, wA, wB, wl, xr, seq);
#pragma unroll
    for (int a = 0; a < 8; ++a)
#pragma unroll
        for (int b = 0; b < 2; ++b) {
            if constexpr (ISV) kv[a * 2 + b] = pack8(acc[(a & 1) * 2 + b][(a >> 1) * 2], acc[(a & 1) * 2 + b][(a >> 1) * 2 + 1]);
            else kv[a * 2 + b] = pack8(acc[2 * b][a], acc[2 * b + 1][a]);
        }
}

template <bool HASF, int ST, int I> DI bf16x8 p2_rdw(const P2Ctx& c) {
    constexpr int NW = HASF ? 6 : 4, up = I / NW, n = I % NW;
    lcptr p = (n < 4 ? c.wq[n & 3] : c.wf[n & 1]) + ST * 24576 + up * 4096;
    return cat(vtr(p), vtr(p + 512));
}
template <int ST, int UP, int MT> DI bf16x8 p2_rdx(const P2Ctx& c) { return ld128(c.xb + ST * 32768 + MT * 2048 + UP * 1024); }
template <int I> DI void p2_kvst(const P2Ctx& c, const u32x4 (&kv)[16], bool isv) {
    st16_sc1(c.sws, kv[I], c.vl16, c.skv + (isv ? (unsigned)(((I >> 3) * 16 + ((I >> 2) & 1) * 8 + (I & 3)) * 1024) : (unsigned)((I >> 1) * 4096 + (I & 1) * 1024)));
}
template <bool HASF, int IV> DI void p2_extras(const P2Ctx& c, const u32x4 (&kv)[16], bool isv) {
    if constexpr (IV < 6) { p2_kvst<4 + 2 * IV>(c, kv, isv); p2_kvst<5 + 2 * IV>(c, kv, isv); }
}
struct P2Bias { u32x2 a, b; const bf16_t* p; };
template <bool HASF, int KS, int G> DI void p2_group(const P2Ctx& c, const Sync& y, P2Bias& bs, u64& seen, f32x4 (&acc)[6][4], bf16x8 (&wr)[4], bf16x8 (&x0)[4], bf16x8 (&x1)[4], const u32x4 (&kv)[16], bool isv) {
    constexpr int NW = HASF ? 6 : 4, NG = 2 * NW, ST = KS & 1, up = G / NW, n = G % NW;
    constexpr int NP = HASF ? 7 : 6, BD0 = HASF ? 2 : 0;
    constexpr bool LAST = (KS == 9);
    if constexpr (G == NG - 3) {
        if constexpr (KS == 0 && !SPREAD) asm volatile("s_waitcnt vmcnt(4) lgkmcnt(0)" ::: "memory");
        else if constexpr (KS == 0) asm volatile("s_waitcnt vmcnt(0) lgkmcnt(0)" ::: "memory");
        else if constexpr (HASF && KS == 6) asm volatile("s_waitcnt vmcnt(4) lgkmcnt(0)" ::: "memory");
        else if constexpr (KS <= 6) asm volatile("s_waitcnt vmcnt(2) lgkmcnt(0)" ::: "memory");
        else asm volatile("s_waitcnt vmcnt(0) lgkmcnt(0)" ::: "memory");
        if constexpr (KS == 9) { if (y.w == 0) check_flags(y.flags, FL_KV + y.h * 8 + (lane_id() & 7), seen, y.token); }
        BARRIER();
        if constexpr (KS == (KVR == 3 ? 5 : 7)) { if (y.w == 0 && lane_id() == 0) set_flag(y.flags, FL_KV + y.h * 8 + y.j, y.token); }
        if constexpr (KS == 8) { if (y.w == 0) seen = poll_flag(y.flags, FL_KV + y.h * 8 + (lane_id() & 7)); }
        SBAR();
    }
    const bf16x8 wcur = wr[G & 3];
#pragma unroll
    for (int mt = 0; mt < 4; ++mt) {
        acc[n][mt] = MF16(wcur, up == 0 ? x0[mt] : x1[mt], acc[n][mt]);
        if (mt == 0) {
            if constexpr (G + 3 < NG) wr[(G + 3) & 3] = p2_rdw<HASF, ST, G + 3>(c);
            else if constexpr (!LAST) wr[(G + 3) & 3] = p2_rdw<HASF, ST ^ 1, G + 3 - NG>(c);
        }
        if (mt == 1) {
            if constexpr (G < 4) x1[G] = p2_rdx<ST, 1, G>(c);
            else if constexpr (G >= NG - 3 && !LAST) x0[G - (NG - 3)] = p2_rdx<ST ^ 1, 0, G - (NG - 3)>(c);
        }
        if (mt == 2) { if constexpr (G == NG - 1 && !LAST) x0[3] = p2_rdx<ST ^ 1, 0, 3>(c); }
        if (mt == SPOS) {
            if constexpr (G >= NG - 3) { if constexpr (KS + 2 < 10) p2_dma1(c, ST, KS + 2, G - (NG - 3)); }
            else if constexpr (G + 3 < NP) { if constexpr (KS + 1 < 10) p2_dma1(c, ST ^ 1, KS + 1, G + 3); }
        }
        if constexpr (LAST && G >= NG - 3) {
            constexpr int p0 = (G - (NG - 3)) * 4;
            if (p0 + mt < 4) dma16h(c.sws, c.vl16, c.t0s + (p0 + mt) * 1024, c.t0l + (p0 + mt) * 1024);
            else if (p0 + mt < 8) { if (c.w < 4) dma16h(c.sws, c.vl16, c.t1s + (p0 + mt - 4) * 1024, 65536 + c.t0l + (p0 + mt - 4) * 1024); }
        }
        SBAR();
    }
    if constexpr (G == NP - 3 && KS >= 1) { p2_extras<HASF, KS - 1>(c, kv, isv); SBAR(); }
    if constexpr (HASF && KS == 6 && G == 5) { asm volatile("global_load_dwordx2 %0, %2, off\n\tglobal_load_dwordx2 %1, %2, off offset:32" : "=&v"(bs.a), "=&v"(bs.b) : "v"(bs.p) : "memory"); SBAR(); }
}
template <bool HASF, int KS, int... Gs> DI void p2_step(const P2Ctx& c, const Sync& y, P2Bias& bs, u64& seen, f32x4 (&acc)[6][4], bf16x8 (&wr)[4], bf16x8 (&x0)[4], bf16x8 (&x1)[4], const u32x4 (&kv)[16], bool isv, std::integer_sequence<int, Gs...>) {
    (p2_group<HASF, KS, Gs>(c, y, bs, seen, acc, wr, x0, x1, kv, isv), ...);
}
template <bool HASF, int... KSs> DI void p2_main(const P2Ctx& c, const Sync& y, P2Bias& bs, f32x4 (&acc)[6][4], const u32x4 (&kv)[16], bool isv, std::integer_sequence<int, KSs...>) {
    bf16x8 wr[4], x0[4], x1[4];
    u64 seen = 0;
    asm volatile("" ::: "memory");
    wr[0] = p2_rdw<HASF, 0, 0>(c); wr[1] = p2_rdw<HASF, 0, 1>(c); wr[2] = p2_rdw<HASF, 0, 2>(c);
    x0[0] = p2_rdx<0, 0, 0>(c); x0[1] = p2_rdx<0, 0, 1>(c); x0[2] = p2_rdx<0, 0, 2>(c); x0[3] = p2_rdx<0, 0, 3>(c);
    SBAR();
    (p2_step<HASF, KSs>(c, y, bs, seen, acc, wr, x0, x1, kv, isv, std::make_integer_sequence<int, (HASF ? 12 : 8)>{}), ...);
}
DI float mish(float h) {
    const float t = __builtin_amdgcn_exp2f(__builtin_fminf(h, 20.f) * 1.4426950408889634f);
    const float n = t * (t + 2.f);
    return h * n * __builtin_amdgcn_rcpf(n + 2.f);
}

constexpr int PD = 8;
#define PRIOM 1
#define PRIOV 0
#define ADDMODE 2
#define ABL_NOEARLY 0
#define APRE_EARLY 1
#define LATE_STEP 7
#define LATE_OFF 2
struct AttnRegs { f32x4 o[8][2]; f32x4 ls[2]; f32x4 s[4][2]; bf16x8 q[2][4]; u32x4 pk[2][2]; f32x4 negm[2]; bf16x8 ones; bf16x8 op[32]; };
template <int I> DI void a_read(AttnRegs& r, lcptr vp, lcptr kp) {
    if constexpr (I < 16) r.op[I] = ld128(vp + I * 1024);
    else if constexpr (I < 32) r.op[I] = ld128(kp + (((I - 16) & 3) * 4 + ((I - 16) >> 2)) * 1024);
}
struct Piece { rsrc_t sws; unsigned vl16, soff, ldso; bool on; };
template <int P, bool COND> DI void piece(const Piece& d) {
    if ((!COND && !ABL_NOEARLY) || (COND && d.on)) asm volatile("s_mov_b32 m0, %0\n\ts_nop 3\n\tbuffer_load_dwordx4 %1, %2, %3 offen offset:%4" HS " lds" ::"s"(d.ldso), "v"(d.vl16), "s"(d.sws), "s"(d.soff), "i"(P * 1024) : "memory");
}
template <int I, int DMA> DI void a_slot(AttnRegs& r, lcptr vp, lcptr kp, const Piece& late) {
    if constexpr (I < 16) { constexpr int ks = I >> 3, dt = I & 7;
        r.o[dt][0] = MF16(r.op[I], asbf(r.pk[0][ks]), r.o[dt][0]); r.o[dt][1] = MF16(r.op[I], asbf(r.pk[1][ks]), r.o[dt][1]);
        if constexpr (dt == 3) { r.ls[0] = MF16(r.ones, asbf(r.pk[0][ks]), r.ls[0]); r.ls[1] = MF16(r.ones, asbf(r.pk[1][ks]), r.ls[1]); } }
    else { constexpr int u = (I - 16) >> 2, kt = (I - 16) & 3;
        r.s[kt][0] = MF16(r.op[I], r.q[0][u], u == 0 ? r.negm[0] : r.s[kt][0]); r.s[kt][1] = MF16(r.op[I], r.q[1][u], u == 0 ? r.negm[1] : r.s[kt][1]); }
    a_read<I + PD>(r, vp, kp);
    SBAR();
    if constexpr (DMA != 0 && (I % LATE_STEP) == LATE_OFF && I / LATE_STEP < 4) { piece<I / LATE_STEP, DMA == 2>(late); SBAR(); }
}
template <int B, int DMA, int... Is> DI void a_run(AttnRegs& r, lcptr vp, lcptr kp, const Piece& late, std::integer_sequence<int, Is...>) { (a_slot<B + Is, DMA>(r, vp, kp, late), ...); }
template <int B, int... Is> DI void a_pre(AttnRegs& r, lcptr vp, lcptr kp, std::integer_sequence<int, Is...>) { (a_read<B + Is>(r, vp, kp), ...); }

DI unsigned pkmax(unsigned a, unsigned b) { return __builtin_bit_cast(unsigned, __builtin_elementwise_max(__builtin_bit_cast(u16x2, a), __builtin_bit_cast(u16x2, b))); }
DI unsigned hmax(unsigned v) { const unsigned lo = v & 0xFFFFu, hi = v >> 16; return lo > hi ? lo : hi; }
template <int Q> DI void a_exp_quarter(AttnRegs& r, unsigned& x0, unsigned& x1) {
    constexpr int ks = Q >> 1, qt = Q & 1;
    float e[8];
#pragma unroll
    for (int i = 0; i < 4; ++i) { e[i] = __builtin_amdgcn_exp2f(r.s[2 * ks][qt][i]); e[4 + i] = __builtin_amdgcn_exp2f(r.s[2 * ks + 1][qt][i]); }
    const u32x4 p = {cvtpk(e[0], e[1]), cvtpk(e[2], e[3]), cvtpk(e[4], e[5]), cvtpk(e[6], e[7])};
    r.pk[qt][ks] = p;
    const unsigned m = pkmax(pkmax(p[0], p[1]), pkmax(p[2], p[3]));
    if constexpr (Q == 0) x0 = m; else if constexpr (Q == 1) x1 = m; else if constexpr (qt == 0) x0 = pkmax(x0, m); else x1 = pkmax(x1, m);
}
template <bool DMA> DI void a_exp_pack(AttnRegs& r, unsigned& x0, unsigned& x1, const Piece& early) {
    if constexpr (DMA) { piece<0, false>(early); SBAR(); }
    a_exp_quarter<0>(r, x0, x1);
    if constexpr (DMA) { SBAR(); piece<1, false>(early); SBAR(); }
    a_exp_quarter<1>(r, x0, x1);
    if constexpr (DMA) { SBAR(); piece<2, false>(early); SBAR(); }
    a_exp_quarter<2>(r, x0, x1);
    if constexpr (DMA) { SBAR(); piece<3, false>(early); SBAR(); }
    a_exp_quarter<3>(r, x0, x1);
}
DI float xmax4(float v) {
    const u32x2 a = __builtin_amdgcn_permlane16_swap(__float_as_uint(v), __float_as_uint(v), false, false);
    v = __builtin_fmaxf(__uint_as_float(a[0]), __uint_as_float(a[1]));
    const u32x2 c = __builtin_amdgcn_permlane32_swap(__float_as_uint(v), __float_as_uint(v), false, false);
    return __builtin_fmaxf(__uint_as_float(c[0]), __uint_as_float(c[1]));
}
DI float xsum4(float v) {
    const u32x2 a = __builtin_amdgcn_permlane16_swap(__float_as_uint(v), __float_as_uint(v), false, false);
    v = __uint_as_float(a[0]) + __uint_as_float(a[1]);
    const u32x2 c = __builtin_amdgcn_permlane32_swap(__float_as_uint(v), __float_as_uint(v), false, false);
    return __uint_as_float(c[0]) + __uint_as_float(c[1]);
}
constexpr unsigned P_HI = 0x5D80u, P_LO = 0x2180u;

struct AttnCtx { rsrc_t sws; unsigned vl16, kbase, vbase, ctxl; int nL, grp, qg, j, rowrel, swap_s, first_s; lcptr kl, vl, ql; };
struct AttnSm { float m0, m1; };
DI void a_dmaK(const AttnCtx& c, int n) { dma16x4(c.sws, c.vl16, c.kbase + n * 16384 + c.qg * 4096, (n & 3) * 16384 + c.qg * 4096); }
DI void a_dmaV(const AttnCtx& c, int n) { dma16x4(c.sws, c.vl16, c.vbase + n * 16384 + c.qg * 4096, 65536 + (n & 3) * 16384 + c.qg * 4096); }
#define PT(i) do { } while (0)
#define PARG
#define PPASS
#define PPASSG
DI void a_slotfull(const AttnCtx& c, const Sync& y, AttnRegs& r, AttnSm& z, const int s PARG) {
    const int nL = c.nL, grp = c.grp;
    const bool SWAP = (s == c.swap_s), FIN = (s == c.swap_s + 1), FIRST = (s == c.first_s) || FIN;
    const int tcur = s < nL ? s : 2 * s - nL + grp;
    Piece early, late;
    {
        const int s2 = s + 2, s1 = s + 1;
        const bool eK = grp == 1 && s2 < nL;
        const int te = eK ? s2 : (s1 < nL ? s1 : 2 * s1 - nL + grp), tl = 2 * s2 - nL + grp;
        early.sws = c.sws; early.vl16 = c.vl16; late.sws = c.sws; late.vl16 = c.vl16;
        early.on = eK || (s1 <= 16 && (grp == 0 || s1 >= nL));
        early.soff = (eK ? c.kbase : c.vbase) + te * 16384 + c.qg * 4096; early.ldso = (eK ? 0 : 65536) + (te & 3) * 16384 + c.qg * 4096;
        late.on = s2 <= 16 && s2 >= nL;
        late.soff = c.kbase + tl * 16384 + c.qg * 4096; late.ldso = (tl & 3) * 16384 + c.qg * 4096;
    }
    const int snext = s + 1, tnext = snext < nL ? snext : 2 * snext - nL + grp;
    const lcptr vp = c.vl + (tcur & 3) * 16384, kp = c.kl + (tnext & 3) * 16384;
    u64 seenA = 0;
    if (s == 16) seenA = poll_asm(y.flags, FL_ACT + 32 * y.w + (lane_id() & 31));
    a_pre<0>(r, vp, kp, std::make_integer_sequence<int, PD>{});
    SBAR();
    if (SWAP) {
        asm volatile("ds_read_b128 %0, %8\n\tds_read_b128 %1, %8 offset:1024\n\tds_read_b128 %2, %8 offset:2048\n\tds_read_b128 %3, %8 offset:3072\n\t"
                     "ds_read_b128 %4, %8 offset:4096\n\tds_read_b128 %5, %8 offset:5120\n\tds_read_b128 %6, %8 offset:6144\n\tds_read_b128 %7, %8 offset:7168\n\ts_waitcnt lgkmcnt(0)"
                     : "=&v"(r.q[0][0]), "=&v"(r.q[0][1]), "=&v"(r.q[0][2]), "=&v"(r.q[0][3]), "=&v"(r.q[1][0]), "=&v"(r.q[1][1]), "=&v"(r.q[1][2]), "=&v"(r.q[1][3])
                     : "v"(c.ql) : "memory");
    }
    if (FIN) {
        const float i0 = __builtin_amdgcn_rcpf(r.ls[0][0]), i1 = __builtin_amdgcn_rcpf(r.ls[1][0]);
        const unsigned c1o = c.ctxl + 131072;
        st16_sc1o<0>(c.sws, pack8(r.o[0][0] * i0, r.o[1][0] * i0), c.vl16, c.ctxl); st16_sc1o<1024>(c.sws, pack8(r.o[2][0] * i0, r.o[3][0] * i0), c.vl16, c.ctxl);
        st16_sc1o<2048>(c.sws, pack8(r.o[4][0] * i0, r.o[5][0] * i0), c.vl16, c.ctxl); st16_sc1o<3072>(c.sws, pack8(r.o[6][0] * i0, r.o[7][0] * i0), c.vl16, c.ctxl);
        st16_sc1o<0>(c.sws, pack8(r.o[0][1] * i1, r.o[1][1] * i1), c.vl16, c1o); st16_sc1o<1024>(c.sws, pack8(r.o[2][1] * i1, r.o[3][1] * i1), c.vl16, c1o);
        st16_sc1o<2048>(c.sws, pack8(r.o[4][1] * i1, r.o[5][1] * i1), c.vl16, c1o); st16_sc1o<3072>(c.sws, pack8(r.o[6][1] * i1, r.o[7][1] * i1), c.vl16, c1o);
#pragma unroll
        for (int d = 0; d < 8; ++d) { r.o[d][0] = (f32x4){0.f, 0.f, 0.f, 0.f}; r.o[d][1] = (f32x4){0.f, 0.f, 0.f, 0.f}; }
        r.ls[0] = (f32x4){0.f, 0.f, 0.f, 0.f}; r.ls[1] = (f32x4){0.f, 0.f, 0.f, 0.f};
    }
    {
        const bool heavy = (grp == 1) || (s >= nL);
        const int qb = heavy ? 15 - c.j : c.j, kb = 64 * tcur - 128 * qb - 32 * c.qg;
        if (kb >= -32) {
            const float NI = -__builtin_inff();
            const f32x4 ninf = {NI, NI, NI, NI};
            f32x4 s00 = r.s[0][0], s10 = r.s[1][0], s20 = r.s[2][0], s30 = r.s[3][0], s01 = r.s[0][1], s11 = r.s[1][1], s21 = r.s[2][1], s31 = r.s[3][1];
            const int dq = c.rowrel;
            if (kb > 0) { s00 = ninf; s10 = ninf; s20 = ninf; s30 = ninf; s01 = ninf; s11 = ninf; s21 = ninf; s31 = ninf; }
            else if (kb == 0) {
#pragma unroll
                for (int i = 0; i < 4; ++i) { s00[i] = sel(i > dq, NI, s00[i]); s11[i] = sel(i > dq, NI, s11[i]); }
                s10 = ninf; s20 = ninf; s30 = ninf; s21 = ninf; s31 = ninf;
            } else {
#pragma unroll
                for (int i = 0; i < 4; ++i) { s20[i] = sel(i > dq, NI, s20[i]); s31[i] = sel(i > dq, NI, s31[i]); }
                s30 = ninf;
            }
            r.s[0][0] = s00; r.s[1][0] = s10; r.s[2][0] = s20; r.s[3][0] = s30; r.s[0][1] = s01; r.s[1][1] = s11; r.s[2][1] = s21; r.s[3][1] = s31;
        }
    }
    unsigned a0, a1;
    if (early.on) dma16x4(c.sws, c.vl16, early.soff, early.ldso);
    a_exp_pack<false>(r, a0, a1, early);
    a0 = hmax(a0); a1 = hmax(a1);
    const unsigned lo = FIRST ? P_LO : 0u;
    if (__builtin_expect(__any(a0 > P_HI || a1 > P_HI || a0 < lo || a1 < lo), 0)) {
        float x0 = r.s[0][0][0], x1 = r.s[0][1][0];
#pragma unroll
        for (int kt = 0; kt < 4; ++kt)
#pragma unroll
            for (int i = 0; i < 4; ++i) { x0 = __builtin_fmaxf(x0, r.s[kt][0][i]); x1 = __builtin_fmaxf(x1, r.s[kt][1][i]); }
        x0 = xmax4(x0); x1 = xmax4(x1);
        if (!FIRST) { x0 = __builtin_fmaxf(x0, 0.f); x1 = __builtin_fmaxf(x1, 0.f);
            const float f0 = __builtin_amdgcn_exp2f(-x0), f1 = __builtin_amdgcn_exp2f(-x1);
#pragma unroll
            for (int d = 0; d < 8; ++d) { r.o[d][0] *= f0; r.o[d][1] *= f1; }
            r.ls[0] *= f0; r.ls[1] *= f1; }
        z.m0 += x0; z.m1 += x1;
        r.negm[0] = (f32x4){-z.m0, -z.m0, -z.m0, -z.m0}; r.negm[1] = (f32x4){-z.m1, -z.m1, -z.m1, -z.m1};
#pragma unroll
        for (int kt = 0; kt < 4; ++kt) { r.s[kt][0] -= x0; r.s[kt][1] -= x1; }
        a_exp_pack<false>(r, a0, a1, early);
    }
    if (SWAP) {
        z.m0 = 0.f; z.m1 = 0.f;
        r.negm[0] = (f32x4){0.f, 0.f, 0.f, 0.f}; r.negm[1] = (f32x4){0.f, 0.f, 0.f, 0.f};
    }
    SBAR();
    PT(0);
    if (FIN) { if (early.on) WAITV(12); else WAITV(8); } else { if (early.on) WAITV(4); else WAITV(0); }
    if (s == 16) { asm volatile("" : "+v"(seenA)); check_flags(y.flags, FL_ACT + 32 * y.w + (lane_id() & 31), seenA, y.token); }
    PT(1);
    BARRIER(); SBAR();
    PT(2);
    __builtin_amdgcn_s_setprio(PRIOM);
    a_run<0, 2>(r, vp, kp, late, std::make_integer_sequence<int, 16>{});
    if (s < 16) a_run<16, 2>(r, vp, kp, late, std::make_integer_sequence<int, 16>{});
    __builtin_amdgcn_s_setprio(PRIOV);
    SBAR();
    PT(3);
    if (late.on) WAITV(4); else WAITV(0);
    PT(4);
    BARRIER(); SBAR();
    PT(5);
}

struct Lean { unsigned e_soff, e_lds, l_soff, l_lds, voff, koff; };
template <bool LATE> DI void a_slot_lean(const AttnCtx& c, AttnRegs& r, AttnSm& z, const Lean& st PARG) {
    const lcptr vp = c.vl + st.voff, kp = c.kl + st.koff;
    Piece early, late;
    early.sws = c.sws; early.vl16 = c.vl16; early.soff = st.e_soff; early.ldso = st.e_lds; early.on = true;
    late.sws = c.sws; late.vl16 = c.vl16; late.soff = st.l_soff; late.ldso = st.l_lds; late.on = true;
    a_pre<0>(r, vp, kp, std::make_integer_sequence<int, PD>{});
    SBAR();
    unsigned a0, a1;
    a_exp_pack<true>(r, a0, a1, early);
    if (__builtin_expect(__any(hmax(pkmax(a0, a1)) > P_HI), 0)) {
        float x0 = 0.f, x1 = 0.f;
#pragma unroll
        for (int kt = 0; kt < 4; ++kt)
#pragma unroll
            for (int i = 0; i < 4; ++i) { x0 = __builtin_fmaxf(x0, r.s[kt][0][i]); x1 = __builtin_fmaxf(x1, r.s[kt][1][i]); }
        x0 = xmax4(x0); x1 = xmax4(x1);
        const float f0 = __builtin_amdgcn_exp2f(-x0), f1 = __builtin_amdgcn_exp2f(-x1);
#pragma unroll
        for (int d = 0; d < 8; ++d) { r.o[d][0] *= f0; r.o[d][1] *= f1; }
        r.ls[0] *= f0; r.ls[1] *= f1;
        z.m0 += x0; z.m1 += x1;
        r.negm[0] = (f32x4){-z.m0, -z.m0, -z.m0, -z.m0}; r.negm[1] = (f32x4){-z.m1, -z.m1, -z.m1, -z.m1};
#pragma unroll
        for (int kt = 0; kt < 4; ++kt) { r.s[kt][0] -= x0; r.s[kt][1] -= x1; }
        a_exp_pack<false>(r, a0, a1, early);
    }
    SBAR();
    PT(0);
    WAITV(4);
    PT(1);
    BARRIER(); SBAR();
    PT(2);
    __builtin_amdgcn_s_setprio(PRIOM);
    a_run<0, LATE ? 1 : 0>(r, vp, kp, late, std::make_integer_sequence<int, 32>{});
    __builtin_amdgcn_s_setprio(PRIOV);
    SBAR();
    PT(3);
    if constexpr (LATE) WAITV(4); else WAITV(0);
    PT(4);
    BARRIER(); SBAR();
    PT(5);
}

#define P4W0 11
constexpr unsigned P4STG = 36864, P4TAG = 0x3FFu, P4OOB = 0xF0000000u;
struct P4Ctx { rsrc_t sws; unsigned vl16, ga, gw, gas, gws, dw0, dw1, dw2, lw, tag, so[3], so2[3], lo[3], lo2[3]; int w, q, o, ng, ra, rw; lcptr pa, pw; };
struct P4Regs { bf16x8 xa[2][2], wa[2][5]; };
struct P4Xch { f32x4 own[3]; u32x4 snd[9], rcv[9]; };
DI void mfa(f32x4& acc, bf16x8 a, bf16x8 b) { asm volatile("v_mfma_f32_16x16x32_bf16 %0, %1, %2, %0" : "+v"(acc) : "v"(a), "v"(b)); }
DI void mfz(f32x4& acc, bf16x8 a, bf16x8 b) { asm volatile("v_mfma_f32_16x16x32_bf16 %0, %1, %2, 0" : "=&v"(acc) : "v"(a), "v"(b)); }
template <int OFF> DI void dma_sc1(const P4Ctx& c, unsigned soff, unsigned ldso) {
    asm volatile("s_mov_b32 m0, %0\n\ts_nop 3\n\tbuffer_load_dwordx4 %1, %2, %3 offen offset:%4 sc1 lds" ::"s"(ldso), "v"(c.vl16), "s"(c.sws), "s"(soff), "i"(OFF) : "memory");
}
template <int P> DI void p4_piece(P4Ctx& c, unsigned st) {
    if constexpr (P == 0) dma_sc1<0>(c, c.ga, st + c.w * 2048);
    else if constexpr (P == 1) { dma_sc1<1024>(c, c.ga, st + c.w * 2048); ++c.ra; c.ga = c.ra == c.ng ? c.gas : c.ga + 2048; }
    else if constexpr (P == 2) dma_sc1<0>(c, c.gw + c.dw0, st + c.lw);
    else if constexpr (P == 3) dma_sc1<0>(c, c.gw + c.dw1, st + c.lw + 8192);
    else { if (c.w < 4) dma_sc1<0>(c, c.gw + c.dw2, st + c.lw + 16384); ++c.rw; c.gw = c.rw == c.ng ? c.gws : c.gw + 81920; }
}
template <int B, int UP, int I> DI void p4_rd(const P4Ctx& c, P4Regs& r, unsigned st) {
    if constexpr (I == 0) r.wa[B][0] = ld128(c.pw + st + UP * 10240);
    else if constexpr (I <= 2) r.xa[B][I - 1] = ld128(c.pa + st + (I - 1) * 2048 + UP * 1024);
    else if constexpr (I < 7) r.wa[B][I - 2] = ld128(c.pw + st + UP * 10240 + (I - 2) * 1024);
}
template <int N> DI void p4_put(const P4Ctx& c, const P4Xch& x, unsigned xoff) { st16_sc1o<(N % 3) * 1024>(c.sws, x.snd[N], c.vl16, (N % 3 == 2 ? c.so2[N / 3] : c.so[N / 3]) + xoff); }
template <int N> DI void p4_get(const P4Ctx& c, P4Xch& x, unsigned xoff) {
    asm volatile("buffer_load_dwordx4 %0, %1, %2, %3 offen offset:%4 sc1" : "=v"(x.rcv[N]) : "v"(c.vl16), "s"(c.sws), "s"((N % 3 == 2 ? c.lo2[N / 3] : c.lo[N / 3]) + xoff), "i"((N % 3) * 1024) : "memory");
}
template <int O> DI void p4_split(const f32x4 (&acc)[10], unsigned tag, P4Xch& x) {
#pragma unroll
    for (int k = 0; k < 3; ++k) x.own[k] = acc[O + 4 * k < 10 ? O + 4 * k : O];
#pragma unroll
    for (int n = 0; n < 9; ++n) { const int i = ((O + 1 + n / 3) & 3) + 4 * (n % 3);
        const u32x4 v = __builtin_bit_cast(u32x4, acc[i < 10 ? i : 0]);
#pragma unroll
        for (int e = 0; e < 4; ++e) x.snd[n][e] = (v[e] & ~P4TAG) | tag; }
}
DI void p4_split(const P4Ctx& c, f32x4 (&a)[10], P4Xch& x) {
    asm volatile("s_nop 7\n\ts_nop 7\n\ts_nop 7" : "+v"(a[0]), "+v"(a[1]), "+v"(a[2]), "+v"(a[3]), "+v"(a[4]), "+v"(a[5]), "+v"(a[6]), "+v"(a[7]), "+v"(a[8]), "+v"(a[9]) :: "memory");
    switch (c.o) { case 0: p4_split<0>(a, c.tag, x); break; case 1: p4_split<1>(a, c.tag, x); break; case 2: p4_split<2>(a, c.tag, x); break; default: p4_split<3>(a, c.tag, x); break; }
}
DI bool p4_tagged(const P4Ctx& c, const P4Xch& x) {
    unsigned a = 0, b = 0;
#pragma unroll
    for (int n = 0; n < 9; ++n)
#pragma unroll
        for (int e = 0; e < 4; ++e) { if (n % 3 == 2) b |= x.rcv[n][e] ^ c.tag; else a |= x.rcv[n][e] ^ c.tag; }
    return __all(((a | (c.o < 2 ? b : 0u)) & P4TAG) == 0);
}
template <int... Ns> DI void p4_refetch(const P4Ctx& c, P4Xch& x, unsigned xoff, std::integer_sequence<int, Ns...>) {
    const unsigned long long t0 = __builtin_amdgcn_s_memrealtime();
    for (;;) {
        (p4_get<Ns>(c, x, xoff), ...);
        asm volatile("s_waitcnt vmcnt(0)" : "+v"(x.rcv[0]), "+v"(x.rcv[1]), "+v"(x.rcv[2]), "+v"(x.rcv[3]), "+v"(x.rcv[4]), "+v"(x.rcv[5]), "+v"(x.rcv[6]), "+v"(x.rcv[7]), "+v"(x.rcv[8]) :: "memory");
        if (p4_tagged(c, x)) break;
        if (__builtin_amdgcn_s_memrealtime() - t0 > 2000000ull) break;
        __builtin_amdgcn_s_sleep(1);
    }
}
DI f32x4 p4_untag(u32x4 v) { return __builtin_bit_cast(f32x4, v & ~P4TAG); }
DI f32x4 p4_total(const P4Xch& x, int k) { return (x.own[k] + p4_untag(x.rcv[k])) + (p4_untag(x.rcv[3 + k]) + p4_untag(x.rcv[6 + k])); }
template <int B, bool ZC, bool RD, int DMA, int PUT, int GET, int... Is> DI void p4_half(P4Ctx& c, P4Regs& r, P4Xch& x, f32x4 (&acc)[10], unsigned rs, unsigned ds, std::integer_sequence<int, Is...>) {
    auto one = [&](auto ic) {
        constexpr int I = decltype(ic)::value;
        if constexpr (ZC) mfz(acc[I], r.wa[B][I >> 1], r.xa[B][I & 1]); else mfa(acc[I], r.wa[B][I >> 1], r.xa[B][I & 1]);
        if constexpr (RD) p4_rd<B ^ 1, B ^ 1, I>(c, r, rs);
        if constexpr (DMA == 1) { if constexpr (I == 1) p4_piece<0>(c, ds); if constexpr (I == 5) p4_piece<1>(c, ds); if constexpr (I == 9) p4_piece<2>(c, ds); }
        if constexpr (DMA == 2) { if constexpr (I == 3) p4_piece<3>(c, ds); if constexpr (I == 7) p4_piece<4>(c, ds); }
        if constexpr (PUT >= 0 && B == 0 && I == 5) p4_put<PUT>(c, x, OFF_XG);
        if constexpr (GET >= 0 && (I & 1) && GET + (I >> 1) < 9) p4_get<GET + (I >> 1)>(c, x, OFF_XG);
        SBAR();
    };
    (one(std::integral_constant<int, Is>{}), ...);
}
template <int WAITN, bool RA, bool RB, bool LAST, int EV, int PUT, int GETA, int GETB, bool ZC = false> DI void p4_step(P4Ctx& c, const Sync& y, P4Regs& r, P4Xch& x, f32x4 (&acc)[10], unsigned& sb, unsigned& sp, int t, unsigned fidx, u64& seen) {
    constexpr auto seq = std::make_integer_sequence<int, 10>{};
    const unsigned sn = sb == 3 * P4STG ? 0 : sb + P4STG;
    p4_half<0, ZC, true, RA ? 2 : 0, PUT, GETA>(c, r, x, acc, sb, sp, seq);
    if constexpr (!LAST) {
        asm volatile("s_waitcnt vmcnt(%0) lgkmcnt(0)" :: "i"(WAITN) : "memory");
        if constexpr (EV == 1) { if (t == c.ng - 4 && c.w == 0) { asm volatile("s_waitcnt vmcnt(4)" : "+v"(seen) :: "memory"); check_flags(y.flags, fidx, seen, y.token); } }
        BARRIER();
        if constexpr (EV == 1) {
            if (c.w == 0 && t == c.ng - 5) seen = poll_asm(y.flags, fidx);
        }
        if constexpr (EV == 2) { if (c.w == 0 && lane_id() == 0) set_flag(y.flags, FL_CTX + y.j * 32 + y.h, y.token); }
        SBAR();
    }
    p4_half<1, false, !LAST, RB ? 1 : 0, PUT, GETB>(c, r, x, acc, sn, sb, seq);
    sp = sb; sb = sn;
}

template <int... Is> DI void p4_ssteps(P4Ctx& c, const Sync& y, P4Regs& r, P4Xch& x, f32x4 (&acc)[10], unsigned& sb, unsigned& sp, u64& seen, std::integer_sequence<int, Is...>) {
    (p4_step<8 + (Is >= 2 && Is <= 10) + (Is >= 1 && Is <= 9) + (Is <= 8), true, true, false, 0, (Is <= 8 ? Is : -1), -1, -1, Is == 0>(c, y, r, x, acc, sb, sp, 0, 0, seen), ...);
}

__global__ __launch_bounds__(512, 2) void fused_kernel(const bf16_t* __restrict__ x, const bf16_t* __restrict__ ckv, const bf16_t* __restrict__ wq, const bf16_t* __restrict__ wuk,
                                                       const bf16_t* __restrict__ wuv, const bf16_t* __restrict__ wo, const bf16_t* __restrict__ w1, const bf16_t* __restrict__ b1,
                                                       const bf16_t* __restrict__ w2, bf16_t* __restrict__ out, char* __restrict__ ws) {
    __shared__ __attribute__((aligned(1024))) char smem[163840];
    const lptr lds = (lptr)smem;
    const int b = blockIdx.x, xcd = b & 7, bi = b >> 3, h = xcd * 4 + (bi >> 3), j = bi & 7;
    const int w = __builtin_amdgcn_readfirstlane(threadIdx.x >> 6);
    const rsrc_t sws = mk_rsrc(ws, WS_BYTES);
    STAMP(0);
    Sync y;
    y.flags = (gu64*)(ws + OFF_FLAGS); y.b = b; y.h = h; y.j = j; y.w = w;
    u64 epoch;
    asm volatile("s_load_dwordx2 %0, %1, 0x20" : "=s"(epoch) : "s"(ws) : "memory");
    u32x4 kv[16];
    P1Ctx c1;
    P2Ctx c2;
    const bool isv = (w & 3) >= 2;
    {
        const int l = lane_id(), g = l >> 4, q = (l & 15) >> 2;
        {
            const int nb = w >> 1;
            c1.w = w; c1.sws = sws; c1.wo = wo; c1.w2 = w2; c1.wq = wq; c1.w1 = w1; c1.lds = lds;
            c1.sx = mk_rsrc(ckv, 2048 * 512 * 2);
            c1.sw = mk_rsrc(nb < 2 ? wuk : wuv, 512 * 4096 * 2);
#pragma unroll
            for (int par = 0; par < 2; ++par) {
                c1.vx[par] = (l >> 3) * 1024 + (((l & 7) ^ (4 * par + (l >> 4))) * 16);
                c1.vw[par] = (l >> 3) * 8192 + (((l & 7) ^ ((((l >> 4) & 1) | (par << 1)) << 1)) * 16);
            }
            c1.sxo = (256 * j + 32 * w) * 1024;
            c1.swo = (32 * (w & 1)) * 8192 + (h * 128 + 64 * (nb & 1)) * 2;
            const int wn = w & 3, wm = w >> 2, f = (l & 15) >> 1, s = ((q >> 1) & 1) | ((g & 1) << 1);
            c1.xb[0] = lds + wm * 16384 + (l & 15) * 128 + ((g ^ f) * 16);
            c1.xb[1] = lds + wm * 16384 + (l & 15) * 128 + (((g ^ f) ^ 4) * 16);
#pragma unroll
            for (int nt = 0; nt < 4; ++nt) c1.wb[nt] = lds + 65536 + wn * 8192 + (8 * g + q) * 128 + ((nt ^ s) * 32) + (l & 3) * 8;
        }
        p1_dma(c1, 0, 0);
        u32x4 xo0, xo1;
        {
            const rsrc_t sxi = mk_rsrc(x, 2048 * 640 * 2);
            const unsigned vx = (l & 7) * 1280 + (l >> 3) * 16, so0 = b * 8 * 1280 + w * 128, so1 = b * 8 * 1280 + (8 + (w & 1)) * 128;
            asm volatile("s_nop 4\n\tbuffer_load_dwordx4 %0, %2, %3, %4 offen\n\tbuffer_load_dwordx4 %1, %2, %3, %5 offen" : "=&v"(xo0), "=&v"(xo1) : "v"(vx), "s"(sxi), "s"(so0), "s"(so1) : "memory");
        }
        BARRIER();
        p1_dma(c1, 1, 1);
        asm volatile("s_waitcnt lgkmcnt(0)" : "+s"(epoch) :: "memory");
        y.token = epoch + 1;
        STAMP(12);
        asm volatile("s_waitcnt vmcnt(8)" : "+v"(xo0), "+v"(xo1) :: "memory");
        STAMP(13);
        float v0[8], v1[8];
#pragma unroll
        for (int e = 0; e < 4; ++e) { v0[2 * e] = bflo(xo0[e]); v0[2 * e + 1] = bfhi(xo0[e]); v1[2 * e] = bflo(xo1[e]); v1[2 * e + 1] = bfhi(xo1[e]); }
        auto red8 = [](float t) { t += __uint_as_float(__builtin_amdgcn_update_dpp(0u, __float_as_uint(t), 0x128, 0xf, 0xf, false)); return xsum4(t); };
        {
            const float m0 = red8(((v0[0] + v0[1]) + (v0[2] + v0[3])) + ((v0[4] + v0[5]) + (v0[6] + v0[7]))) * (1.f / 64.f);
            const float m1 = red8(((v1[0] + v1[1]) + (v1[2] + v1[3])) + ((v1[4] + v1[5]) + (v1[6] + v1[7]))) * (1.f / 64.f);
            float q0 = 0.f, q1 = 0.f;
#pragma unroll
            for (int e = 0; e < 8; ++e) { const float d0 = v0[e] - m0, d1 = v1[e] - m1; q0 += d0 * d0; q1 += d1 * d1; }
            q0 = red8(q0); q1 = red8(q1);
            if (l < 8) { *(LAS f32x2*)(lds + 162816 + l * 80 + w * 8) = (f32x2){m0, q0}; if (w < 2) *(LAS f32x2*)(lds + 162816 + l * 80 + 64 + w * 8) = (f32x2){m1, q1}; }
        }
        asm volatile("s_waitcnt lgkmcnt(0)" ::: "memory"); BARRIER();
        STAMP(14);
        {
            f32x4 t[5];
#pragma unroll
            for (int i = 0; i < 5; ++i) t[i] = *(const LAS f32x4*)(lds + 162816 + (l & 7) * 80 + i * 16);
            float mean = 0.f;
#pragma unroll
            for (int i = 0; i < 5; ++i) mean += t[i][0] + t[i][2];
            mean *= 0.1f;
            float m2 = 0.f;
#pragma unroll
            for (int i = 0; i < 5; ++i) { const float d0 = t[i][0] - mean, d1 = t[i][2] - mean; m2 += (t[i][1] + t[i][3]) + 64.f * (d0 * d0 + d1 * d1); }
            const float rstd = 1.f / __builtin_sqrtf(m2 * (1.f / 640.f) + 1e-5f);
            const unsigned vo = (l >> 3) * 256 + (l & 7) * 16, xo = OFF_XN + (b >> 1) * 20480 + (b & 1) * 128;
            u32x4 o0, o1;
#pragma unroll
            for (int e = 0; e < 4; ++e) { o0[e] = cvtpk((v0[2 * e] - mean) * rstd, (v0[2 * e + 1] - mean) * rstd); o1[e] = cvtpk((v1[2 * e] - mean) * rstd, (v1[2 * e + 1] - mean) * rstd); }
            st16_sc1(sws, o0, vo, xo + w * 2048);
            if (w < 2) st16_sc1(sws, o1, vo, xo + (8 + w) * 2048);
        }
        {
            const char* pa = (const char*)ckv + (size_t)(256 * j + 64 * (h & 3) + 8 * w + (l >> 3)) * 1024 + (l & 7) * 128;
            const char* pw = (const char*)((w & 1) ? wuv : wuk) + (size_t)(64 * j + 16 * (w >> 1) + ((l & 31) >> 1)) * 8192 + h * 256 + (l & 1) * 128;
            touch(pa, 131072 + w * 256); touch(pw, 131072 + w * 256);
            {
                u64 pc; unsigned len;
                asm volatile("s_getpc_b64 %0\n.Lcode_a:\n\ts_mov_b32 %1, .Lcode_z-.Lcode_a" : "=s"(pc), "=s"(len));
                const unsigned off = (unsigned)(bi * 64 + w * 8 + (l & 7)) * 64;
                touch((const char*)pc + ((int)len > 0 && off < len ? off : 0u), 131072 + w * 256);
            }
        }
        {
            c2.w = w; c2.sws = sws; c2.swq = mk_rsrc(wq, 640 * 4096 * 2); c2.sw1 = mk_rsrc(w1, 640 * 1728 * 2);
            c2.vl16 = l * 16;
            const int swz = ((l >> 4) & 1) | ((w & 1) << 1);
            c2.vwq = (l >> 3) * 8192 + (((l & 7) ^ (swz << 1)) * 16);
            c2.vw1 = (l >> 3) * 3456 + (((l & 7) ^ (swz << 1)) * 16);
            { const int mtl = 2 * w, mtg = mtl < 8 ? 8 * j + mtl : 8 * (15 - j) + (mtl - 8); c2.sxo = OFF_XN + mtg * 20480; }
            c2.swqo = (8 * w) * 8192 + h * 256;
            c2.sw1o = (8 * w) * 3456 + h * 128;
            c2.t0l = (w >> 2) * 16384 + (w & 3) * 4096; c2.t0s = OFF_K + (unsigned)h * 524288 + c2.t0l; c2.t1s = OFF_V + (unsigned)h * 524288 + (w & 3) * 4096;
            { const int wn = w & 3, wm = w >> 2;
              c2.skv = isv ? OFF_V + (unsigned)(h * 32 + 4 * j + 2 * wm) * 16384 + (wn - 2) * 4096 : OFF_K + (unsigned)((h * 128 + 16 * j + 8 * wm) * 4 + 2 * wn) * 1024; }
        }
    }
    STAMP(1); CSTAMP(14);
    if (isv) p1_main<true>(c1, c2, y, kv); else p1_main<false>(c1, c2, y, kv);
    STAMP(2); CSTAMP(15);
    p2_kvst<0>(c2, kv, isv); p2_kvst<1>(c2, kv, isv); p2_kvst<2>(c2, kv, isv); p2_kvst<3>(c2, kv, isv);
    const bool hasf = h < 27;
    AttnCtx c3;
    AttnRegs r;
    {
        const int l = lane_id(), g = l >> 4, q = (l & 15) >> 2;
        const int wn = w & 1, wm = w >> 1;
        const int s = ((q >> 1) & 1) | ((g & 1) << 1);
        c2.xb = lds + wm * 8192 + l * 16;
#pragma unroll
        for (int nt = 0; nt < 4; ++nt) c2.wq[nt] = lds + 65536 + wn * 8192 + (8 * g + q) * 128 + ((nt ^ s) * 32) + (l & 3) * 8;
#pragma unroll
        for (int ft = 0; ft < 2; ++ft) c2.wf[ft] = lds + 65536 + 16384 + (8 * g + q) * 128 + (((2 * wn + ft) ^ s) * 32) + (l & 3) * 8;
        f32x4 acc[6][4];
#pragma unroll
        for (int a = 0; a < 6; ++a)
#pragma unroll
            for (int m = 0; m < 4; ++m) acc[a][m] = (f32x4){0.f, 0.f, 0.f, 0.f};
        STAMP(3);
        P2Bias bs; bs.p = b1 + (64 * h + 32 * wn + 4 * g); bs.a = (u32x2){0u, 0u}; bs.b = (u32x2){0u, 0u};
        if (hasf) p2_main<true>(c2, y, bs, acc, kv, isv, std::make_integer_sequence<int, 10>{}); else p2_main<false>(c2, y, bs, acc, kv, isv, std::make_integer_sequence<int, 10>{});
        STAMP(4);
        constexpr float C2 = 0.08838834764831845f * 1.4426950408889634f;
#pragma unroll
        for (int mt = 0; mt < 4; ++mt)
#pragma unroll
            for (int a = 0; a < 2; ++a)
                *(LAS u32x4*)(lds + 98304 + ((4 * wm + mt) * 4 + 2 * wn + a) * 1024 + l * 16) = pack8(acc[2 * a][mt] * C2, acc[2 * a + 1][mt] * C2);
        u32x4 actf[4];
        if (hasf) {
            asm volatile("" : "+v"(bs.a), "+v"(bs.b));
            const u32x2 bA = bs.a, bB = bs.b;
            const f32x4 biasA = {bflo(bA[0]), bfhi(bA[0]), bflo(bA[1]), bfhi(bA[1])}, biasB = {bflo(bB[0]), bfhi(bB[0]), bflo(bB[1]), bfhi(bB[1])};
#pragma unroll
            for (int mt = 0; mt < 4; ++mt) {
                f32x4 hA = acc[4][mt] + biasA, hB = acc[5][mt] + biasB;
#pragma unroll
                for (int i = 0; i < 4; ++i) { hA[i] = mish(hA[i]); hB[i] = mish(hB[i]); }
                actf[mt] = pack8(hA, hB);
            }
        }
        asm volatile("s_waitcnt vmcnt(0) lgkmcnt(0)" ::: "memory"); BARRIER();
        const int grp = w >> 2, qg = w & 3;
        c3.sws = sws; c3.vl16 = l * 16; c3.kbase = OFF_K + (unsigned)h * 524288; c3.vbase = OFF_V + (unsigned)h * 524288;
        c3.ctxl = OFF_CTX + ((8 * j + 2 * qg) * 128 + 4 * h) * 1024;
        c3.nL = 2 * j + 2; c3.grp = grp; c3.qg = qg; c3.j = j; c3.rowrel = (l & 15) - 4 * g; c3.kl = lds + l * 16; c3.vl = lds + 65536 + l * 16;
        c3.ql = lds + 131072 + qg * 8192 + l * 16;
        if (hasf) {
#pragma unroll
            for (int mt = 0; mt < 4; ++mt) { const int mtl = 4 * wm + mt, mtg = mtl < 8 ? 8 * j + mtl : 8 * (15 - j) + (mtl - 8);
                st16_sc1(sws, actf[mt], l * 16, OFF_ACT + (mtg * 54 + 2 * h + wn) * 1024); }
        }
#pragma unroll
        for (int qt = 0; qt < 2; ++qt)
#pragma unroll
            for (int u = 0; u < 4; ++u) r.q[qt][u] = ld128(lds + 98304 + ((8 * grp + 2 * qg + qt) * 4 + u) * 1024 + l * 16);
    }
    STAMP(5);
    P4Ctx c4;
    {
        const int l = lane_id();
        const int grp = c3.grp, qg = c3.qg;
        const unsigned vl16 = c3.vl16;
#pragma unroll
        for (int d = 0; d < 8; ++d) { r.o[d][0] = (f32x4){0.f, 0.f, 0.f, 0.f}; r.o[d][1] = (f32x4){0.f, 0.f, 0.f, 0.f}; }
        r.negm[0] = (f32x4){0.f, 0.f, 0.f, 0.f}; r.negm[1] = (f32x4){0.f, 0.f, 0.f, 0.f};
        AttnSm z; z.m0 = 0.f; z.m1 = 0.f;
        r.ls[0] = (f32x4){0.f, 0.f, 0.f, 0.f}; r.ls[1] = (f32x4){0.f, 0.f, 0.f, 0.f};
        r.ones = (bf16x8){0x3F80, 0x3F80, 0x3F80, 0x3F80, 0x3F80, 0x3F80, 0x3F80, 0x3F80}; PIN(r.ones);
        asm volatile("s_waitcnt lgkmcnt(0)" ::: "memory");
        BARRIER();
        STAMP(6); CSTAMP(12);
        a_pre<16>(r, c3.vl, c3.kl, std::make_integer_sequence<int, PD>{});
        if (grp == 1) BARRIER();
        { Piece none; none.on = false; none.sws = sws; none.vl16 = 0; none.soff = 0; none.ldso = 0; a_run<16, 0>(r, c3.vl, c3.kl, none, std::make_integer_sequence<int, 16>{}); }
        BARRIER();
        c3.swap_s = grp == 0 ? c3.nL - 1 : 99; c3.first_s = 0;
        asm volatile("" : "+s"(c3.swap_s), "+s"(c3.first_s));
        {
            const int nL = c3.nL;
            auto lean_init = [&](int s) {
                Lean st;
                const bool eK = grp == 1 && s + 2 < nL;
                const int t0 = s < nL ? s : 2 * s - nL + grp, t1 = s + 1 < nL ? s + 1 : 2 * s + 2 - nL + grp, t2 = s + 2 < nL ? s + 2 : 2 * s + 4 - nL + grp;
                const int te = eK ? t2 : t1;
                st.e_soff = (eK ? c3.kbase : c3.vbase) + te * 16384 + qg * 4096; st.e_lds = (eK ? 0 : 65536) + (te & 3) * 16384 + qg * 4096;
                st.l_soff = c3.kbase + t2 * 16384 + qg * 4096; st.l_lds = (t2 & 3) * 16384 + qg * 4096;
                st.voff = (t0 & 3) * 16384; st.koff = (t1 & 3) * 16384;
                return st;
            };
            int s = 0, phase = 0;
#pragma nounroll
            for (;;) {
                const int gend = phase == 0 ? 1 : (phase == 1 ? (grp == 0 ? nL + 1 : nL - 1) : 17);
#pragma nounroll
                while (s < gend) { a_slotfull(c3, y, r, z, s PPASSG); ++s; }
                if (phase == 0) { if (w == 0 && l == 0) set_flag(y.flags, FL_ACT + j * 32 + h, y.token); }
                if (phase == 2 || s > 16) break;
                if (phase == 1 && s <= 14) {
                    Lean st = lean_init(s);
#pragma nounroll
                    do { a_slot_lean<true>(c3, r, z, st PPASS);
                         st.e_soff += 32768; st.e_lds ^= 32768; st.l_soff += 32768; st.l_lds ^= 32768; st.voff ^= 32768; st.koff ^= 32768; ++s; } while (s <= 14);
                }
                const int send = phase == 0 ? nL - 2 : 16;
                if (s < send) {
                    Lean st = lean_init(s);
#pragma nounroll
                    do { a_slot_lean<false>(c3, r, z, st PPASS);
                         st.e_soff += 16384; st.e_lds = (st.e_lds & ~0xC000u) | ((st.e_lds + 16384) & 0xC000u); st.voff = (st.voff + 16384) & 0xC000u; st.koff = (st.koff + 16384) & 0xC000u; ++s; } while (s < send);
                }
                ++phase;
            }
        }
        const float m0 = z.m0, m1 = z.m1, l0 = r.ls[0][0], l1 = r.ls[1][0];
        if (grp == 0) BARRIER();
        STAMP(7); CSTAMP(13);
        {
            const int q = xcd >> 1, rbk = (xcd & 1) * 8 + (bi >> 2), cb = bi & 3, wm = w >> 1, wn = w & 1;
            c4.sws = sws; c4.vl16 = vl16; c4.w = w; c4.q = q; c4.o = (q + 2 * wn) & 3; c4.ng = q == 3 ? 6 : 7; c4.ra = 0; c4.rw = 0;
            c4.ga = OFF_ACT + ((8 * rbk + w) * 54 + 14 * q) * 1024; c4.gas = OFF_CTX + ((8 * rbk + w) * 128 + 32 * q) * 1024;
            c4.gw = OFF_WP + ((128 + 14 * q) * 40 + 10 * cb) * 1024; c4.gws = OFF_WP + (32 * q * 40 + 10 * cb) * 1024;
            c4.dw0 = w * 1024; c4.dw1 = (w < 2 ? 8 + w : 38 + w) * 1024; c4.dw2 = (46 + w) * 1024; c4.lw = 16384 + w * 1024;
            c4.tag = (unsigned)y.token & P4TAG;
            const unsigned xb = (unsigned)(((xcd & 1) * 32 + bi) * 128 + w) * 3072;
#pragma unroll
            for (int d = 0; d < 3; ++d) { const int qq = (q + 1 + d) & 3;
                c4.so[d] = xb + qq * 98304 + q * 24576; c4.so2[d] = ((c4.o + 1 + d) & 3) < 2 ? c4.so[d] : P4OOB;
                c4.lo[d] = xb + q * 98304 + qq * 24576; c4.lo2[d] = c4.o < 2 ? c4.lo[d] : P4OOB; }
            c4.pa = lds + (4 * wm) * 1024 + vl16; c4.pw = lds + 16384 + (5 * wn) * 1024 + vl16;
        }
        p4_piece<0>(c4, 2 * P4STG); p4_piece<1>(c4, 2 * P4STG); p4_piece<2>(c4, 2 * P4STG); p4_piece<3>(c4, 2 * P4STG); p4_piece<4>(c4, 2 * P4STG);
        p4_piece<0>(c4, 3 * P4STG); p4_piece<1>(c4, 3 * P4STG); p4_piece<2>(c4, 3 * P4STG); p4_piece<3>(c4, 3 * P4STG); p4_piece<4>(c4, 3 * P4STG);
        {
            const lptr xo = lds + (qg * 2 + grp) * 9216;
#pragma unroll
            for (int d = 0; d < 4; ++d)
#pragma unroll
                for (int qt = 0; qt < 2; ++qt) *(LAS f32x4*)(xo + (d * 2 + qt) * 1024 + l * 16) = sel(grp == 0, r.o[4 + d][qt], r.o[d][qt]);
            *(LAS f32x4*)(xo + 8192 + l * 16) = (f32x4){m0, m1, l0, l1};
            asm volatile("s_waitcnt lgkmcnt(0)" ::: "memory"); BARRIER();
            const lcptr xi = lds + (qg * 2 + (grp ^ 1)) * 9216;
            const f32x4 st = *(const LAS f32x4*)(xi + 8192 + l * 16);
            const float M0 = __builtin_fmaxf(m0, st[0]), M1 = __builtin_fmaxf(m1, st[1]);
            const float wa0 = __builtin_amdgcn_exp2f(m0 - M0), wb0 = __builtin_amdgcn_exp2f(st[0] - M0), wa1 = __builtin_amdgcn_exp2f(m1 - M1), wb1 = __builtin_amdgcn_exp2f(st[1] - M1);
            const float i0 = __builtin_amdgcn_rcpf(wa0 * l0 + wb0 * st[2]), i1 = __builtin_amdgcn_rcpf(wa1 * l1 + wb1 * st[3]);
            const float ca0 = wa0 * i0, cb0 = wb0 * i0, ca1 = wa1 * i1, cb1 = wb1 * i1;
#pragma unroll
            for (int up = 0; up < 2; ++up)
#pragma unroll
                for (int qt = 0; qt < 2; ++qt) {
                    const f32x4 pa = *(const LAS f32x4*)(xi + ((2 * up) * 2 + qt) * 1024 + l * 16), pb = *(const LAS f32x4*)(xi + ((2 * up + 1) * 2 + qt) * 1024 + l * 16);
                    const f32x4 oa = sel(grp == 0, r.o[2 * up][qt], r.o[4 + 2 * up][qt]), ob = sel(grp == 0, r.o[2 * up + 1][qt], r.o[4 + 2 * up + 1][qt]);
                    const float ca = qt ? ca1 : ca0, cb = qt ? cb1 : cb0;
                    st16_sc1(sws, pack8(oa * ca + pa * cb, ob * ca + pb * cb), vl16, OFF_CTX + ((8 * (15 - j) + 2 * qg + qt) * 128 + 4 * h + 2 * grp + up) * 1024);
                }
        }
    }
    STAMP(8);
    {
        asm volatile("s_waitcnt vmcnt(8) lgkmcnt(0)" ::: "memory"); BARRIER();
        P4Regs r4;
        unsigned sb = 2 * P4STG, sp = P4STG;
        p4_rd<0, 0, 0>(c4, r4, sb); p4_rd<0, 0, 1>(c4, r4, sb); p4_rd<0, 0, 2>(c4, r4, sb); p4_rd<0, 0, 3>(c4, r4, sb); p4_rd<0, 0, 4>(c4, r4, sb); p4_rd<0, 0, 5>(c4, r4, sb); p4_rd<0, 0, 6>(c4, r4, sb);
        SBAR();
        p4_piece<0>(c4, 0); p4_piece<1>(c4, 0); p4_piece<2>(c4, 0); p4_piece<3>(c4, 0); p4_piece<4>(c4, 0);
        p4_piece<0>(c4, P4STG); p4_piece<1>(c4, P4STG); p4_piece<2>(c4, P4STG);
        const int l = lane_id();
        const int rbk = (xcd & 1) * 8 + (bi >> 2), cb = bi & 3;
        const unsigned cidx = FL_CTX + (rbk < 8 ? rbk : 15 - rbk) * 32 + 8 * c4.q + (l & 7);
        f32x4 accg[10], accs[10], gt[3];
        P4Xch xg;
        const int row0 = 128 * rbk + 32 * (w >> 1) + (l & 15), col0 = 160 * cb + 80 * (w & 1) + 4 * (l >> 4);
        u32x2 xv[3];
        {
            const rsrc_t sx = mk_rsrc(x, 2048 * 640 * 2);
#pragma unroll
            for (int k = 0; k < 3; ++k) { const int i = c4.o + 4 * k < 10 ? c4.o + 4 * k : c4.o;
                asm volatile("buffer_load_dwordx2 %0, %1, %2, 0 offen" : "=v"(xv[k]) : "v"((unsigned)((row0 + (i & 1) * 16) * 640 + col0 + (i >> 1) * 16) * 2), "s"(sx) : "memory"); }
        }
        u64 seen = 0;
        SBAR();
        STAMP(9);
        p4_step<P4W0, true, true, false, 2, -1, -1, -1, true>(c4, y, r4, xg, accg, sb, sp, 0, cidx, seen);
#pragma nounroll
        for (int t = 1; t < c4.ng; ++t) p4_step<8, true, true, false, 1, -1, -1, -1>(c4, y, r4, xg, accg, sb, sp, t, cidx, seen);
        p4_split(c4, accg, xg);
        p4_ssteps(c4, y, r4, xg, accs, sb, sp, seen, std::make_integer_sequence<int, 12>{});
        p4_step<8, true, false, false, 0, -1, -1, 0>(c4, y, r4, xg, accs, sb, sp, 0, 0, seen);
        p4_step<13, false, false, false, 0, -1, 5, -1>(c4, y, r4, xg, accs, sb, sp, 0, 0, seen);
        p4_step<9, false, false, false, 0, -1, -1, -1>(c4, y, r4, xg, accs, sb, sp, 0, 0, seen);
        p4_step<0, false, false, true, 0, -1, -1, -1>(c4, y, r4, xg, accs, sb, sp, 0, 0, seen);
        asm volatile("s_waitcnt vmcnt(0)" : "+v"(xg.rcv[0]), "+v"(xg.rcv[1]), "+v"(xg.rcv[2]), "+v"(xg.rcv[3]), "+v"(xg.rcv[4]), "+v"(xg.rcv[5]), "+v"(xg.rcv[6]), "+v"(xg.rcv[7]), "+v"(xg.rcv[8]),
                     "+v"(xv[0]), "+v"(xv[1]), "+v"(xv[2]) :: "memory");
        constexpr auto nine = std::make_integer_sequence<int, 9>{};
        P4Xch xs;
        p4_split(c4, accs, xs);
        p4_put<0>(c4, xs, OFF_XS); p4_put<1>(c4, xs, OFF_XS); p4_put<2>(c4, xs, OFF_XS); p4_put<3>(c4, xs, OFF_XS); p4_put<4>(c4, xs, OFF_XS);
        p4_put<5>(c4, xs, OFF_XS); p4_put<6>(c4, xs, OFF_XS); p4_put<7>(c4, xs, OFF_XS); p4_put<8>(c4, xs, OFF_XS);
        TS(0);
        if (!p4_tagged(c4, xg)) p4_refetch(c4, xg, OFF_XG, nine);
#pragma unroll
        for (int k = 0; k < 3; ++k) {
            gt[k] = p4_total(xg, k);
#pragma unroll
            for (int e = 0; e < 4; ++e) gt[k][e] = gt[k][e] * __builtin_amdgcn_rcpf(1.f + __builtin_amdgcn_exp2f(-1.4426950408889634f * gt[k][e]));
            PIN(gt[k]);
        }
        TS(1);
        p4_refetch(c4, xs, OFF_XS, nine);
        TS(2);
#pragma unroll
        for (int k = 0; k < 3; ++k) {
            if (k < 2 || c4.o < 2) { const int i = c4.o + 4 * k;
                const f32x4 sv = p4_total(xs, k);
                const f32x4 o = {bflo(xv[k][0]) + sv[0] * gt[k][0], bfhi(xv[k][0]) + sv[1] * gt[k][1], bflo(xv[k][1]) + sv[2] * gt[k][2], bfhi(xv[k][1]) + sv[3] * gt[k][3]};
                const u32x2 ov = {cvtpk(o[0], o[1]), cvtpk(o[2], o[3])};
                asm volatile("global_store_dwordx2 %0, %1, off sc0 sc1\n\ts_nop 1" :: "v"(out + (row0 + (i & 1) * 16) * 640 + col0 + (i >> 1) * 16), "v"(ov) : "memory"); }
        }
        asm volatile(".Lcode_z:");
        if (b == 0 && w == 0 && l == 0) __hip_atomic_store(y.flags + 4, y.token, __ATOMIC_RELAXED, __HIP_MEMORY_SCOPE_AGENT);
    }
    STAMP(11);
}

extern "C" void kernel_launch(void* const* d_in, const int* in_sizes, int n_in, void* d_out, int out_size, void* d_ws, size_t ws_size, hipStream_t stream) {
    (void)in_sizes; (void)n_in; (void)out_size; (void)ws_size;
    fused_kernel<<<256, 512, 0, stream>>>((const bf16_t*)d_in[0], (const bf16_t*)d_in[1], (const bf16_t*)d_in[2], (const bf16_t*)d_in[3], (const bf16_t*)d_in[4],
                                          (const bf16_t*)d_in[5], (const bf16_t*)d_in[7], (const bf16_t*)d_in[8], (const bf16_t*)d_in[9], (bf16_t*)d_out, (char*)d_ws);
}
```

```cpp
#include <hip/hip_runtime.h>
#include <stdint.h>
#include <utility>

typedef unsigned short bf16_t;
typedef short bf16x8 __attribute__((ext_vector_type(8)));
typedef short s16x4 __attribute__((ext_vector_type(4)));
typedef short v4i16_t __attribute__((ext_vector_type(4)));
typedef float f32x4 __attribute__((ext_vector_type(4)));
typedef float f32x2 __attribute__((ext_vector_type(2)));
typedef unsigned u32x4 __attribute__((ext_vector_type(4)));
typedef unsigned u32x2 __attribute__((ext_vector_type(2)));
typedef __bf16 bf16x2_t __attribute__((ext_vector_type(2)));
typedef unsigned short u16x2 __attribute__((ext_vector_type(2)));
typedef __amdgpu_buffer_rsrc_t rsrc_t;
typedef unsigned long long u64;
#define LAS __attribute__((address_space(3)))
#define GAS __attribute__((address_space(1)))
typedef LAS char* lptr;
typedef LAS const char* lcptr;
typedef GAS u64 gu64;
#define DI __device__ __forceinline__
#define SBAR() __builtin_amdgcn_sched_barrier(0)
#define PIN(x) asm volatile("" : "+v"(x))
#define MF16(a, b, c) __builtin_amdgcn_mfma_f32_16x16x32_bf16(a, b, c, 0, 0, 0)
#define WAITV(n) asm volatile("s_waitcnt vmcnt(" #n ")" ::: "memory")
#define WAITL(n) asm volatile("s_waitcnt lgkmcnt(" #n ")" ::: "memory")
#define BARRIER() do { asm volatile("" ::: "memory"); __builtin_amdgcn_s_barrier(); asm volatile("" ::: "memory"); } while (0)

#define UPTO 4
#define STAG 1
#define NOINV 1
#define P0EB 1
#define KVR 2
#define SPREAD 1
#define SPOS 1
#define XST 0
#define STAMPS 0

constexpr unsigned OFF_FLAGS = 0;
constexpr unsigned OFF_XN = 65536;
constexpr unsigned OFF_K = OFF_XN + 2621440;
constexpr unsigned OFF_V = OFF_K + 16777216;
constexpr unsigned OFF_ACT = OFF_V + 16777216 + 8388608;
constexpr unsigned OFF_CTX = OFF_ACT + 7077888;
constexpr unsigned OFF_WP = OFF_CTX + 16777216;
constexpr unsigned OFF_ST = OFF_WP + 182 * 40 * 1024;
constexpr unsigned OFF_XG = OFF_ST + 65536;
constexpr unsigned OFF_XS = OFF_XG + 25165824;
constexpr unsigned WS_BYTES = OFF_XS + 25165824;
constexpr unsigned FL_P0 = 0, FL_KV = 256, FL_ACT = 512, FL_CTX = 768;
constexpr int SC1 = 16;

DI unsigned cvtpk(float lo, float hi) { f32x2 v = {lo, hi}; bf16x2_t b = __builtin_convertvector(v, bf16x2_t); return __builtin_bit_cast(unsigned, b); }
DI u32x4 pack8(f32x4 a, f32x4 b) { u32x4 w = {cvtpk(a[0], a[1]), cvtpk(a[2], a[3]), cvtpk(b[0], b[1]), cvtpk(b[2], b[3])}; return w; }
DI float bflo(unsigned u) { return __uint_as_float(u << 16); }
DI float bfhi(unsigned u) { return __uint_as_float(u & 0xffff0000u); }
DI s16x4 vtr(lcptr p) { return __builtin_bit_cast(s16x4, __builtin_amdgcn_ds_read_tr16_b64_v4i16((LAS v4i16_t*)p)); }
DI bf16x8 cat(s16x4 a, s16x4 b) { return (bf16x8){a[0], a[1], a[2], a[3], b[0], b[1], b[2], b[3]}; }
DI bf16x8 ld128(lcptr p) { return *(const LAS bf16x8*)p; }
DI bf16x8 asbf(u32x4 v) { return __builtin_bit_cast(bf16x8, v); }
DI rsrc_t mk_rsrc(const void* p, unsigned bytes) { return __builtin_amdgcn_make_buffer_rsrc((void*)p, (short)0, (int)bytes, 0x00020000); }
DI void dma16(rsrc_t r, unsigned voff, unsigned soff, unsigned ldsoff) {
    asm volatile("s_mov_b32 m0, %0\n\ts_nop 3\n\tbuffer_load_dwordx4 %1, %2, %3 offen lds" ::"s"(ldsoff), "v"(voff), "s"(r), "s"(soff) : "memory");
}
#define HS " sc1"
DI void dma16h(rsrc_t r, unsigned voff, unsigned soff, unsigned ldsoff) {
    asm volatile("s_mov_b32 m0, %0\n\ts_nop 3\n\tbuffer_load_dwordx4 %1, %2, %3 offen" HS " lds" ::"s"(ldsoff), "v"(voff), "s"(r), "s"(soff) : "memory");
}
DI void dma16x4(rsrc_t r, unsigned voff, unsigned soff, unsigned ldsoff) {
    asm volatile("s_mov_b32 m0, %0\n\ts_nop 3\n\tbuffer_load_dwordx4 %1, %2, %3 offen" HS " lds\n\tbuffer_load_dwordx4 %1, %2, %3 offen offset:1024" HS " lds\n\t"
                 "buffer_load_dwordx4 %1, %2, %3 offen offset:2048" HS " lds\n\tbuffer_load_dwordx4 %1, %2, %3 offen offset:3072" HS " lds" ::"s"(ldsoff), "v"(voff), "s"(r), "s"(soff) : "memory");
}
DI void touch(const void* p, unsigned ldsoff) { asm volatile("s_mov_b32 m0, %0\n\ts_nop 3\n\tglobal_load_lds_dword %1, off" ::"s"(ldsoff), "v"(p) : "memory"); }
DI void st16_sc1(rsrc_t r, u32x4 v, unsigned voff, unsigned soff) { asm volatile("s_nop 4\n\tbuffer_store_dwordx4 %0, %1, %2, %3 offen sc1\n\ts_nop 1" ::"v"(v), "v"(voff), "s"(r), "s"(soff) : "memory"); }
template <int OFF> DI void st16_sc1o(rsrc_t r, u32x4 v, unsigned voff, unsigned soff) { asm volatile("s_nop 4\n\tbuffer_store_dwordx4 %0, %1, %2, %3 offen offset:%4 sc1\n\ts_nop 1" ::"v"(v), "v"(voff), "s"(r), "s"(soff), "i"(OFF) : "memory"); }
DI u32x4 ld16(rsrc_t r, unsigned voff, unsigned soff) { return __builtin_amdgcn_raw_buffer_load_b128(r, voff, soff, 0); }
#define P4AUX SC1
DI u32x4 ld16_sc1(rsrc_t r, unsigned voff, unsigned soff) { return __builtin_amdgcn_raw_buffer_load_b128(r, voff, soff, P4AUX); }
template <class T> DI T sel(bool c, T a, T b) { return c ? a : b; }
DI int lane_id() { int l; asm volatile("v_mbcnt_lo_u32_b32 %0, -1, 0\n\tv_mbcnt_hi_u32_b32 %0, -1, %0" : "=v"(l)); return l; }
DI void set_flag(gu64* flags, unsigned idx, u64 token) { __hip_atomic_store(flags + idx * 8, token, __ATOMIC_RELAXED, __HIP_MEMORY_SCOPE_AGENT); }
DI u64 poll_flag(gu64* flags, unsigned idx) { return __hip_atomic_load(flags + idx * 8, __ATOMIC_RELAXED, __HIP_MEMORY_SCOPE_AGENT); }
DI void spin_flags(gu64* flags, unsigned idx, u64 token) {
    const unsigned long long t0 = __builtin_amdgcn_s_memrealtime();
    for (;;) {
        __builtin_amdgcn_s_sleep(1);
        const u64 v = poll_flag(flags, idx);
        if (__all(v == token)) break;
        if (__builtin_amdgcn_s_memrealtime() - t0 > 2000000ull) break;
    }
}
DI u64 poll_asm(gu64* flags, unsigned idx) { u64 v; asm volatile("global_load_dwordx2 %0, %1, off sc1" : "=v"(v) : "v"(flags + idx * 8) : "memory"); return v; }
DI void check_flags(gu64* flags, unsigned idx, u64 seen, u64 token) { if (!__all(seen == token)) spin_flags(flags, idx, token); }
DI void acquire_issue() { asm volatile("buffer_inv sc1" ::: "memory"); }
DI u64 uni64(u64 v) { return ((u64)(unsigned)__builtin_amdgcn_readfirstlane((unsigned)(v >> 32)) << 32) | (unsigned)__builtin_amdgcn_readfirstlane((unsigned)v); }
#define SCLK __builtin_amdgcn_s_memrealtime
#define STAMP(i) do { } while (0)
#define STAMPW(i, wv) do { } while (0)
#define CSTAMP(i) do { } while (0)

#define TS(i) do { } while (0)
#define TSARG
#define TSPASS
struct Sync { gu64* flags; u64 token; int b, h, j, w; };
#define YSTAMP(i) do { } while (0)

struct P2Ctx { rsrc_t sws, swq, sw1; unsigned vl16, vwq, vw1; unsigned sxo, swqo, sw1o, skv, t0s, t0l, t1s; int w; lcptr xb; lcptr wq[4]; lcptr wf[2]; };
DI void p2_dma1(const P2Ctx& c, int st, int ks, int i) {
    if (i < 4) dma16h(c.sws, c.vl16, c.sxo + (i >> 1) * 20480 + (i & 1) * 1024 + ks * 2048, st * 32768 + (4 * c.w + i) * 1024);
    else if (i < 6) dma16(c.swq, c.vwq, c.swqo + (i - 4) * 128 + ks * 524288, 65536 + st * 24576 + (i - 4) * 8192 + c.w * 1024);
    else dma16(c.sw1, c.vw1, c.sw1o + ks * (64 * 3456), 65536 + st * 24576 + 16384 + c.w * 1024);
}

struct P1Ctx { rsrc_t sx, sw, sws; unsigned vx[2], vw[2]; unsigned sxo, swo; int w; lcptr xb[2]; lcptr wb[4]; const bf16_t *wo, *w2, *wq, *w1; lcptr lds; };
DI void pack_fetch(const P1Ctx& c, int b) {
    const int l = lane_id(), gw = b * 8 + c.w, pu = gw / 10, pq = gw - pu * 10;
    const rsrc_t sp = mk_rsrc(pu < 128 ? c.wo : c.w2, pu < 128 ? 4096 * 640 * 2 : 1728 * 640 * 2);
    const unsigned so = gw < 1820 ? (unsigned)((pu < 128 ? pu : pu - 128) * 32 * 1280 + pq * 128) : 0xF0000000u;
#pragma unroll
    for (int i = 0; i < 4; ++i) dma16(sp, (l >> 3) * 1280 + (l & 7) * 16, so + i * 8 * 1280, 131072 + c.w * 4096 + i * 1024);
}
DI void pack_store(const P1Ctx& c, int b) {
    const int l = lane_id(), g = l >> 4, q = (l & 15) >> 2, gw = b * 8 + c.w, pu = gw / 10, pq = gw - pu * 10;
    lcptr p = c.lds + 131072 + c.w * 4096 + (4 * g + q) * 128 + (l & 3) * 8;
    bf16x8 fr[4];
#pragma unroll
    for (int i = 0; i < 4; ++i) fr[i] = cat(vtr(p + i * 32), vtr(p + i * 32 + 16 * 128));
    if (gw < 1820) {
#pragma unroll
        for (int i = 0; i < 4; ++i) st16_sc1(c.sws, __builtin_bit_cast(u32x4, fr[i]), l * 16, OFF_WP + (pu * 40 + pq * 4 + i) * 1024);
    }
}
DI void p1_dma1(const P1Ctx& c, int st, int ks, int i) {
    if (i < 4) dma16(c.sx, c.vx[i & 1], c.sxo + i * 8192 + ks * 128, st * 32768 + (4 * c.w + i) * 1024);
    else dma16(c.sw, c.vw[i & 1], c.swo + (i - 4) * 65536 + ks * 524288, 65536 + st * 32768 + (4 * c.w + (i - 4)) * 1024);
}
DI void p1_dma(const P1Ctx& c, int st, int ks) {
#pragma unroll
    for (int i = 0; i < 8; ++i) p1_dma1(c, st, ks, i);
}
template <int ST, int UP, int I> DI void p1_rdw(const P1Ctx& c, bf16x8 (&wf)[4], s16x4 (&wl)[4]) {
    constexpr int nt = I >> 1, rd = I & 1;
    const s16x4 v = vtr(c.wb[nt] + ST * 32768 + UP * 4096 + rd * 512);
    if constexpr (rd == 0) wl[nt] = v; else wf[nt] = cat(wl[nt], v);
}
template <int ST, int G> DI bf16x8 p1_rdx(const P1Ctx& c) { return ld128(c.xb[G >> 3] + ST * 32768 + (G & 7) * 2048); }
template <int ST, int KSC, int K> DI bool p1_is(int ks) { if constexpr (KSC >= 0) return KSC == K; else if constexpr ((K & 1) != ST || K > 5) return false; else return ks == K; }
template <bool ISV, int ST, int KSC, int G> DI void p1_group(const P1Ctx& c, const P2Ctx& c2, const Sync& y, const int ks, u64& seen, f32x4 (&acc)[4][8], bf16x8 (&wA)[4], bf16x8 (&wB)[4], s16x4 (&wl)[4], bf16x8 (&xr)[4]) {
    constexpr int up = G >> 3, mt = G & 7;
    constexpr bool NEXT = KSC != 7;
    if constexpr (G == 13) {
        if (p1_is<ST, KSC, 0>(ks)) asm volatile("s_waitcnt vmcnt(3) lgkmcnt(0)" ::: "memory");
        else if (p1_is<ST, KSC, 1>(ks) || p1_is<ST, KSC, 3>(ks)) asm volatile("s_waitcnt vmcnt(1) lgkmcnt(0)" ::: "memory");
        else if (p1_is<ST, KSC, 5>(ks) || p1_is<ST, KSC, 7>(ks)) asm volatile("s_waitcnt vmcnt(4) lgkmcnt(0)" ::: "memory");
        else asm volatile("s_waitcnt vmcnt(0) lgkmcnt(0)" ::: "memory");
        if constexpr (!ISV && KSC == 6) { if (y.w == 0) { const int l = lane_id() & 31; check_flags(y.flags, FL_P0 + (l < 16 ? 16 * y.j + l : 16 * (15 - y.j) + (l - 16)), seen, y.token); } }
        BARRIER();
        if constexpr (!ISV) {
            if (p1_is<ST, KSC, 1>(ks)) { if (y.w == 0 && lane_id() == 0) set_flag(y.flags, FL_P0 + y.b, y.token); }
        }
        SBAR();
    }
    const bf16x8 x = xr[G & 3];
#pragma unroll
    for (int nt = 0; nt < 4; ++nt) {
        const bf16x8 wv = up == 0 ? wA[nt] : wB[nt];
        if constexpr (ISV) acc[nt][mt] = MF16(x, wv, acc[nt][mt]); else acc[nt][mt] = MF16(wv, x, acc[nt][mt]);
        if (nt == 0) {
            if constexpr (G + 3 < 16) xr[(G + 3) & 3] = p1_rdx<ST, G + 3>(c);
            else if constexpr (NEXT) xr[(G + 3) & 3] = p1_rdx<ST ^ 1, G + 3 - 16>(c);
        }
        if constexpr (G >= 2 && G < 6) { if (nt == 1) p1_rdw<ST, 1, 2 * (G - 2)>(c, wB, wl); if (nt == 2) p1_rdw<ST, 1, 2 * (G - 2) + 1>(c, wB, wl); }
        if constexpr (G >= 13 && NEXT) {
            if (nt == 1) p1_rdw<ST ^ 1, 0, 3 * (G - 13)>(c, wA, wl);
            if (nt == 2) p1_rdw<ST ^ 1, 0, 3 * (G - 13) + 1>(c, wA, wl);
            if constexpr (G < 15) { if (nt == 3) p1_rdw<ST ^ 1, 0, 3 * (G - 13) + 2>(c, wA, wl); }
        }
        if (nt == SPOS) {
            if constexpr (G >= 13) { constexpr int p = G - 13;
                if constexpr (KSC < 0) p1_dma1(c, ST, ks + 2, p);
                else if constexpr (KSC == 6) p2_dma1(c2, 0, 0, p);
                else p2_dma1(c2, 1, 1, p);
            } else if constexpr (G <= 4) { constexpr int p = G + 3;
                if constexpr (KSC == 7) { if constexpr (p < 7) p2_dma1(c2, 0, 0, p); }
                else { if (!p1_is<ST, KSC, 0>(ks)) p1_dma1(c, ST ^ 1, ks + 1, p); }
            }
        }
        SBAR();
    }
    if constexpr (G == 6) {
        if (p1_is<ST, KSC, 1>(ks)) {
            const int l = lane_id(), L = (c.w & 3) * 64 + l;
            const char* pb = (L >= 160 && L < 240 && y.h < 27) ? (const char*)c.w1 + (size_t)(80 * y.j + L - 160) * 3456 + y.h * 128
                                                               : (const char*)c.wq + (size_t)(80 * y.j + ((L < 160 ? L : 0) >> 1)) * 8192 + y.h * 256 + (L & 1) * 128;
            touch(pb, 131072 + c.w * 256);
        }
        if (p1_is<ST, KSC, 3>(ks)) {
            const int l = lane_id(), gw = y.b * 8 + c.w < 1820 ? y.b * 8 + c.w : 0, pu = gw / 10, pq = gw - pu * 10;
            const char* pc = (const char*)(pu < 128 ? c.wo : c.w2) + (size_t)((pu < 128 ? pu : pu - 128) * 32 + (l & 31)) * 1280 + pq * 128;
            touch(pc, 131072 + c.w * 256);
        }
        if constexpr (!ISV && KSC == 6) { if (y.w == 0) { const int l = lane_id() & 31; seen = poll_flag(y.flags, FL_P0 + (l < 16 ? 16 * y.j + l : 16 * (15 - y.j) + (l - 16))); } }
        if (p1_is<ST, KSC, 5>(ks)) { pack_fetch(c, y.b); if constexpr (!ISV && !NOINV) { if (y.w == 0) acquire_issue(); } }
        if constexpr (KSC == 7) pack_store(c, y.b);
        SBAR();
    }
}
template <bool ISV, int ST, int KSC, int... Gs> DI void p1_step(const P1Ctx& c, const P2Ctx& c2, const Sync& y, const int ks, u64& seen, f32x4 (&acc)[4][8], bf16x8 (&wA)[4], bf16x8 (&wB)[4], s16x4 (&wl)[4], bf16x8 (&xr)[4], std::integer_sequence<int, Gs...>) {
    (p1_group<ISV, ST, KSC, Gs>(c, c2, y, ks, seen, acc, wA, wB, wl, xr), ...);
}
template <bool ISV> DI void p1_main(const P1Ctx& c, const P2Ctx& c2, const Sync& y, u32x4 (&kv)[16]) {
    f32x4 acc[4][8];
#pragma unroll
    for (int a = 0; a < 4; ++a)
#pragma unroll
        for (int b = 0; b < 8; ++b) acc[a][b] = (f32x4){0.f, 0.f, 0.f, 0.f};
    bf16x8 wA[4], wB[4], xr[4]; s16x4 wl[4];
    u64 seen = 0;
    asm volatile("" ::: "memory");
    p1_rdw<0, 0, 0>(c, wA, wl); p1_rdw<0, 0, 1>(c, wA, wl); p1_rdw<0, 0, 2>(c, wA, wl); p1_rdw<0, 0, 3>(c, wA, wl);
    p1_rdw<0, 0, 4>(c, wA, wl); p1_rdw<0, 0, 5>(c, wA, wl); p1_rdw<0, 0, 6>(c, wA, wl); p1_rdw<0, 0, 7>(c, wA, wl);
    xr[0] = p1_rdx<0, 0>(c); xr[1] = p1_rdx<0, 1>(c); xr[2] = p1_rdx<0, 2>(c);
    SBAR();
    constexpr auto seq = std::make_integer_sequence<int, 16>{};
#pragma nounroll
    for (int ks = 0; ks < 6; ks += 2) {
        p1_step<ISV, 0, -1>(c, c2, y, ks, seen, acc, wA, wB, wl, xr, seq);
        p1_step<ISV, 1, -1>(c, c2, y, ks + 1, seen, acc, wA, wB, wl, xr, seq);
    }
    p1_step<ISV, 0, 6>(c, c2, y, 6, seen, acc, wA, wB, wl, xr, seq);
    p1_step<ISV, 1, 7>(c, c2, y, 7, seen, acc, wA, wB, wl, xr, seq);
#pragma unroll
    for (int a = 0; a < 8; ++a)
#pragma unroll
        for (int b = 0; b < 2; ++b) {
            if constexpr (ISV) kv[a * 2 + b] = pack8(acc[(a & 1) * 2 + b][(a >> 1) * 2], acc[(a & 1) * 2 + b][(a >> 1) * 2 + 1]);
            else kv[a * 2 + b] = pack8(acc[2 * b][a], acc[2 * b + 1][a]);
        }
}

template <bool HASF, int ST, int I> DI bf16x8 p2_rdw(const P2Ctx& c) {
    constexpr int NW = HASF ? 6 : 4, up = I / NW, n = I % NW;
    lcptr p = (n < 4 ? c.wq[n & 3] : c.wf[n & 1]) + ST * 24576 + up * 4096;
    return cat(vtr(p), vtr(p + 512));
}
template <int ST, int UP, int MT> DI bf16x8 p2_rdx(const P2Ctx& c) { return ld128(c.xb + ST * 32768 + MT * 2048 + UP * 1024); }
template <int I> DI void p2_kvst(const P2Ctx& c, const u32x4 (&kv)[16], bool isv) {
    st16_sc1(c.sws, kv[I], c.vl16, c.skv + (isv ? (unsigned)(((I >> 3) * 16 + ((I >> 2) & 1) * 8 + (I & 3)) * 1024) : (unsigned)((I >> 1) * 4096 + (I & 1) * 1024)));
}
template <bool HASF, int IV> DI void p2_extras(const P2Ctx& c, const u32x4 (&kv)[16], bool isv) {
    if constexpr (IV < 6) { p2_kvst<4 + 2 * IV>(c, kv, isv); p2_kvst<5 + 2 * IV>(c, kv, isv); }
}
struct P2Bias { u32x2 a, b; const bf16_t* p; };
template <bool HASF, int KS, int G> DI void p2_group(const P2Ctx& c, const Sync& y, P2Bias& bs, u64& seen, f32x4 (&acc)[6][4], bf16x8 (&wr)[4], bf16x8 (&x0)[4], bf16x8 (&x1)[4], const u32x4 (&kv)[16], bool isv) {
    constexpr int NW = HASF ? 6 : 4, NG = 2 * NW, ST = KS & 1, up = G / NW, n = G % NW;
    constexpr int NP = HASF ? 7 : 6, BD0 = HASF ? 2 : 0;
    constexpr bool LAST = (KS == 9);
    if constexpr (G == NG - 3) {
        if constexpr (KS == 0 && !SPREAD) asm volatile("s_waitcnt vmcnt(4) lgkmcnt(0)" ::: "memory");
        else if constexpr (KS == 0) asm volatile("s_waitcnt vmcnt(0) lgkmcnt(0)" ::: "memory");
        else if constexpr (HASF && KS == 6) asm volatile("s_waitcnt vmcnt(4) lgkmcnt(0)" ::: "memory");
        else if constexpr (KS <= 6) asm volatile("s_waitcnt vmcnt(2) lgkmcnt(0)" ::: "memory");
        else asm volatile("s_waitcnt vmcnt(0) lgkmcnt(0)" ::: "memory");
        if constexpr (KS == 9) { if (y.w == 0) check_flags(y.flags, FL_KV + y.h * 8 + (lane_id() & 7), seen, y.token); }
        BARRIER();
        if constexpr (KS == (KVR == 3 ? 5 : 7)) { if (y.w == 0 && lane_id() == 0) set_flag(y.flags, FL_KV + y.h * 8 + y.j, y.token); }
        if constexpr (KS == 8) { if (y.w == 0) seen = poll_flag(y.flags, FL_KV + y.h * 8 + (lane_id() & 7)); }
        SBAR();
    }
    const bf16x8 wcur = wr[G & 3];
#pragma unroll
    for (int mt = 0; mt < 4; ++mt) {
        acc[n][mt] = MF16(wcur, up == 0 ? x0[mt] : x1[mt], acc[n][mt]);
        if (mt == 0) {
            if constexpr (G + 3 < NG) wr[(G + 3) & 3] = p2_rdw<HASF, ST, G + 3>(c);
            else if constexpr (!LAST) wr[(G + 3) & 3] = p2_rdw<HASF, ST ^ 1, G + 3 - NG>(c);
        }
        if (mt == 1) {
            if constexpr (G < 4) x1[G] = p2_rdx<ST, 1, G>(c);
            else if constexpr (G >= NG - 3 && !LAST) x0[G - (NG - 3)] = p2_rdx<ST ^ 1, 0, G - (NG - 3)>(c);
        }
        if (mt == 2) { if constexpr (G == NG - 1 && !LAST) x0[3] = p2_rdx<ST ^ 1, 0, 3>(c); }
        if (mt == SPOS) {
            if constexpr (G >= NG - 3) { if constexpr (KS + 2 < 10) p2_dma1(c, ST, KS + 2, G - (NG - 3)); }
            else if constexpr (G + 3 < NP) { if constexpr (KS + 1 < 10) p2_dma1(c, ST ^ 1, KS + 1, G + 3); }
        }
        if constexpr (LAST && G >= NG - 3) {
            constexpr int p0 = (G - (NG - 3)) * 4;
            if (p0 + mt < 4) dma16h(c.sws, c.vl16, c.t0s + (p0 + mt) * 1024, c.t0l + (p0 + mt) * 1024);
            else if (p0 + mt < 8) { if (c.w < 4) dma16h(c.sws, c.vl16, c.t1s + (p0 + mt - 4) * 1024, 65536 + c.t0l + (p0 + mt - 4) * 1024); }
        }
        SBAR();
    }
    if constexpr (G == NP - 3 && KS >= 1) { p2_extras<HASF, KS - 1>(c, kv, isv); SBAR(); }
    if constexpr (HASF && KS == 6 && G == 5) { asm volatile("global_load_dwordx2 %0, %2, off\n\tglobal_load_dwordx2 %1, %2, off offset:32" : "=&v"(bs.a), "=&v"(bs.b) : "v"(bs.p) : "memory"); SBAR(); }
}
template <bool HASF, int KS, int... Gs> DI void p2_step(const P2Ctx& c, const Sync& y, P2Bias& bs, u64& seen, f32x4 (&acc)[6][4], bf16x8 (&wr)[4], bf16x8 (&x0)[4], bf16x8 (&x1)[4], const u32x4 (&kv)[16], bool isv, std::integer_sequence<int, Gs...>) {
    (p2_group<HASF, KS, Gs>(c, y, bs, seen, acc, wr, x0, x1, kv, isv), ...);
}
template <bool HASF, int... KSs> DI void p2_main(const P2Ctx& c, const Sync& y, P2Bias& bs, f32x4 (&acc)[6][4], const u32x4 (&kv)[16], bool isv, std::integer_sequence<int, KSs...>) {
    bf16x8 wr[4], x0[4], x1[4];
    u64 seen = 0;
    asm volatile("" ::: "memory");
    wr[0] = p2_rdw<HASF, 0, 0>(c); wr[1] = p2_rdw<HASF, 0, 1>(c); wr[2] = p2_rdw<HASF, 0, 2>(c);
    x0[0] = p2_rdx<0, 0, 0>(c); x0[1] = p2_rdx<0, 0, 1>(c); x0[2] = p2_rdx<0, 0, 2>(c); x0[3] = p2_rdx<0, 0, 3>(c);
    SBAR();
    (p2_step<HASF, KSs>(c, y, bs, seen, acc, wr, x0, x1, kv, isv, std::make_integer_sequence<int, (HASF ? 12 : 8)>{}), ...);
}
DI float mish(float h) {
    const float t = __builtin_amdgcn_exp2f(__builtin_fminf(h, 20.f) * 1.4426950408889634f);
    const float n = t * (t + 2.f);
    return h * n * __builtin_amdgcn_rcpf(n + 2.f);
}

constexpr int PD = 8;
#define PRIOM 1
#define PRIOV 0
#define ADDMODE 2
#define ABL_NOEARLY 0
#define APRE_EARLY 1
#define LATE_STEP 7
#define LATE_OFF 2
struct AttnRegs { f32x4 o[8][2]; f32x4 ls[2]; f32x4 s[4][2]; bf16x8 q[2][4]; u32x4 pk[2][2]; f32x4 negm[2]; bf16x8 ones; bf16x8 op[32]; };
template <int I> DI void a_read(AttnRegs& r, lcptr vp, lcptr kp) {
    if constexpr (I < 16) r.op[I] = ld128(vp + I * 1024);
    else if constexpr (I < 32) r.op[I] = ld128(kp + (((I - 16) & 3) * 4 + ((I - 16) >> 2)) * 1024);
}
struct Piece { rsrc_t sws; unsigned vl16, soff, ldso; bool on; };
template <int P, bool COND> DI void piece(const Piece& d) {
    if ((!COND && !ABL_NOEARLY) || (COND && d.on)) asm volatile("s_mov_b32 m0, %0\n\ts_nop 3\n\tbuffer_load_dwordx4 %1, %2, %3 offen offset:%4" HS " lds" ::"s"(d.ldso), "v"(d.vl16), "s"(d.sws), "s"(d.soff), "i"(P * 1024) : "memory");
}
template <int I, int DMA> DI void a_slot(AttnRegs& r, lcptr vp, lcptr kp, const Piece& late) {
    if constexpr (I < 16) { constexpr int ks = I >> 3, dt = I & 7;
        r.o[dt][0] = MF16(r.op[I], asbf(r.pk[0][ks]), r.o[dt][0]); r.o[dt][1] = MF16(r.op[I], asbf(r.pk[1][ks]), r.o[dt][1]);
        if constexpr (dt == 3) { r.ls[0] = MF16(r.ones, asbf(r.pk[0][ks]), r.ls[0]); r.ls[1] = MF16(r.ones, asbf(r.pk[1][ks]), r.ls[1]); } }
    else { constexpr int u = (I - 16) >> 2, kt = (I - 16) & 3;
        r.s[kt][0] = MF16(r.op[I], r.q[0][u], u == 0 ? r.negm[0] : r.s[kt][0]); r.s[kt][1] = MF16(r.op[I], r.q[1][u], u == 0 ? r.negm[1] : r.s[kt][1]); }
    a_read<I + PD>(r, vp, kp);
    SBAR();
    if constexpr (DMA != 0 && (I % LATE_STEP) == LATE_OFF && I / LATE_STEP < 4) { piece<I / LATE_STEP, DMA == 2>(late); SBAR(); }
}
template <int B, int DMA, int... Is> DI void a_run(AttnRegs& r, lcptr vp, lcptr kp, const Piece& late, std::integer_sequence<int, Is...>) { (a_slot<B + Is, DMA>(r, vp, kp, late), ...); }
template <int B, int... Is> DI void a_pre(AttnRegs& r, lcptr vp, lcptr kp, std::integer_sequence<int, Is...>) { (a_read<B + Is>(r, vp, kp), ...); }

DI unsigned pkmax(unsigned a, unsigned b) { return __builtin_bit_cast(unsigned, __builtin_elementwise_max(__builtin_bit_cast(u16x2, a), __builtin_bit_cast(u16x2, b))); }
DI unsigned hmax(unsigned v) { const unsigned lo = v & 0xFFFFu, hi = v >> 16; return lo > hi ? lo : hi; }
template <int Q> DI void a_exp_quarter(AttnRegs& r, unsigned& x0, unsigned& x1) {
    constexpr int ks = Q >> 1, qt = Q & 1;
    float e[8];
#pragma unroll
    for (int i = 0; i < 4; ++i) { e[i] = __builtin_amdgcn_exp2f(r.s[2 * ks][qt][i]); e[4 + i] = __builtin_amdgcn_exp2f(r.s[2 * ks + 1][qt][i]); }
    const u32x4 p = {cvtpk(e[0], e[1]), cvtpk(e[2], e[3]), cvtpk(e[4], e[5]), cvtpk(e[6], e[7])};
    r.pk[qt][ks] = p;
    const unsigned m = pkmax(pkmax(p[0], p[1]), pkmax(p[2], p[3]));
    if constexpr (Q == 0) x0 = m; else if constexpr (Q == 1) x1 = m; else if constexpr (qt == 0) x0 = pkmax(x0, m); else x1 = pkmax(x1, m);
}
template <bool DMA> DI void a_exp_pack(AttnRegs& r, unsigned& x0, unsigned& x1, const Piece& early) {
    if constexpr (DMA) { piece<0, false>(early); SBAR(); }
    a_exp_quarter<0>(r, x0, x1);
    if constexpr (DMA) { SBAR(); piece<1, false>(early); SBAR(); }
    a_exp_quarter<1>(r, x0, x1);
    if constexpr (DMA) { SBAR(); piece<2, false>(early); SBAR(); }
    a_exp_quarter<2>(r, x0, x1);
    if constexpr (DMA) { SBAR(); piece<3, false>(early); SBAR(); }
    a_exp_quarter<3>(r, x0, x1);
}
DI float xmax4(float v) {
    const u32x2 a = __builtin_amdgcn_permlane16_swap(__float_as_uint(v), __float_as_uint(v), false, false);
    v = __builtin_fmaxf(__uint_as_float(a[0]), __uint_as_float(a[1]));
    const u32x2 c = __builtin_amdgcn_permlane32_swap(__float_as_uint(v), __float_as_uint(v), false, false);
    return __builtin_fmaxf(__uint_as_float(c[0]), __uint_as_float(c[1]));
}
DI float xsum4(float v) {
    const u32x2 a = __builtin_amdgcn_permlane16_swap(__float_as_uint(v), __float_as_uint(v), false, false);
    v = __uint_as_float(a[0]) + __uint_as_float(a[1]);
    const u32x2 c = __builtin_amdgcn_permlane32_swap(__float_as_uint(v), __float_as_uint(v), false, false);
    return __uint_as_float(c[0]) + __uint_as_float(c[1]);
}
constexpr unsigned P_HI = 0x5D80u, P_LO = 0x2180u;

struct AttnCtx { rsrc_t sws; unsigned vl16, kbase, vbase, ctxl; int nL, grp, qg, j, rowrel, swap_s, first_s; lcptr kl, vl, ql; };
struct AttnSm { float m0, m1; };
DI void a_dmaK(const AttnCtx& c, int n) { dma16x4(c.sws, c.vl16, c.kbase + n * 16384 + c.qg * 4096, (n & 3) * 16384 + c.qg * 4096); }
DI void a_dmaV(const AttnCtx& c, int n) { dma16x4(c.sws, c.vl16, c.vbase + n * 16384 + c.qg * 4096, 65536 + (n & 3) * 16384 + c.qg * 4096); }
#define PT(i) do { } while (0)
#define PARG
#define PPASS
#define PPASSG
DI void a_slotfull(const AttnCtx& c, const Sync& y, AttnRegs& r, AttnSm& z, const int s PARG) {
    const int nL = c.nL, grp = c.grp;
    const bool SWAP = (s == c.swap_s), FIN = (s == c.swap_s + 1), FIRST = (s == c.first_s) || FIN;
    const int tcur = s < nL ? s : 2 * s - nL + grp;
    Piece early, late;
    {
        const int s2 = s + 2, s1 = s + 1;
        const bool eK = grp == 1 && s2 < nL;
        const int te = eK ? s2 : (s1 < nL ? s1 : 2 * s1 - nL + grp), tl = 2 * s2 - nL + grp;
        early.sws = c.sws; early.vl16 = c.vl16; late.sws = c.sws; late.vl16 = c.vl16;
        early.on = eK || (s1 <= 16 && (grp == 0 || s1 >= nL));
        early.soff = (eK ? c.kbase : c.vbase) + te * 16384 + c.qg * 4096; early.ldso = (eK ? 0 : 65536) + (te & 3) * 16384 + c.qg * 4096;
        late.on = s2 <= 16 && s2 >= nL;
        late.soff = c.kbase + tl * 16384 + c.qg * 4096; late.ldso = (tl & 3) * 16384 + c.qg * 4096;
    }
    const int snext = s + 1, tnext = snext < nL ? snext : 2 * snext - nL + grp;
    const lcptr vp = c.vl + (tcur & 3) * 16384, kp = c.kl + (tnext & 3) * 16384;
    u64 seenA = 0;
    if (s == 16) seenA = poll_asm(y.flags, FL_ACT + 32 * y.w + (lane_id() & 31));
    a_pre<0>(r, vp, kp, std::make_integer_sequence<int, PD>{});
    SBAR();
    if (SWAP) {
        asm volatile("ds_read_b128 %0, %8\n\tds_read_b128 %1, %8 offset:1024\n\tds_read_b128 %2, %8 offset:2048\n\tds_read_b128 %3, %8 offset:3072\n\t"
                     "ds_read_b128 %4, %8 offset:4096\n\tds_read_b128 %5, %8 offset:5120\n\tds_read_b128 %6, %8 offset:6144\n\tds_read_b128 %7, %8 offset:7168\n\ts_waitcnt lgkmcnt(0)"
                     : "=&v"(r.q[0][0]), "=&v"(r.q[0][1]), "=&v"(r.q[0][2]), "=&v"(r.q[0][3]), "=&v"(r.q[1][0]), "=&v"(r.q[1][1]), "=&v"(r.q[1][2]), "=&v"(r.q[1][3])
                     : "v"(c.ql) : "memory");
    }
    if (FIN) {
        const float i0 = __builtin_amdgcn_rcpf(r.ls[0][0]), i1 = __builtin_amdgcn_rcpf(r.ls[1][0]);
        const unsigned c1o = c.ctxl + 131072;
        st16_sc1o<0>(c.sws, pack8(r.o[0][0] * i0, r.o[1][0] * i0), c.vl16, c.ctxl); st16_sc1o<1024>(c.sws, pack8(r.o[2][0] * i0, r.o[3][0] * i0), c.vl16, c.ctxl);
        st16_sc1o<2048>(c.sws, pack8(r.o[4][0] * i0, r.o[5][0] * i0), c.vl16, c.ctxl); st16_sc1o<3072>(c.sws, pack8(r.o[6][0] * i0, r.o[7][0] * i0), c.vl16, c.ctxl);
        st16_sc1o<0>(c.sws, pack8(r.o[0][1] * i1, r.o[1][1] * i1), c.vl16, c1o); st16_sc1o<1024>(c.sws, pack8(r.o[2][1] * i1, r.o[3][1] * i1), c.vl16, c1o);
        st16_sc1o<2048>(c.sws, pack8(r.o[4][1] * i1, r.o[5][1] * i1), c.vl16, c1o); st16_sc1o<3072>(c.sws, pack8(r.o[6][1] * i1, r.o[7][1] * i1), c.vl16, c1o);
#pragma unroll
        for (int d = 0; d < 8; ++d) { r.o[d][0] = (f32x4){0.f, 0.f, 0.f, 0.f}; r.o[d][1] = (f32x4){0.f, 0.f, 0.f, 0.f}; }
        r.ls[0] = (f32x4){0.f, 0.f, 0.f, 0.f}; r.ls[1] = (f32x4){0.f, 0.f, 0.f, 0.f};
    }
    {
        const bool heavy = (grp == 1) || (s >= nL);
        const int qb = heavy ? 15 - c.j : c.j, kb = 64 * tcur - 128 * qb - 32 * c.qg;
        if (kb >= -32) {
            const float NI = -__builtin_inff();
            const f32x4 ninf = {NI, NI, NI, NI};
            f32x4 s00 = r.s[0][0], s10 = r.s[1][0], s20 = r.s[2][0], s30 = r.s[3][0], s01 = r.s[0][1], s11 = r.s[1][1], s21 = r.s[2][1], s31 = r.s[3][1];
            const int dq = c.rowrel;
            if (kb > 0) { s00 = ninf; s10 = ninf; s20 = ninf; s30 = ninf; s01 = ninf; s11 = ninf; s21 = ninf; s31 = ninf; }
            else if (kb == 0) {
#pragma unroll
                for (int i = 0; i < 4; ++i) { s00[i] = sel(i > dq, NI, s00[i]); s11[i] = sel(i > dq, NI, s11[i]); }
                s10 = ninf; s20 = ninf; s30 = ninf; s21 = ninf; s31 = ninf;
            } else {
#pragma unroll
                for (int i = 0; i < 4; ++i) { s20[i] = sel(i > dq, NI, s20[i]); s31[i] = sel(i > dq, NI, s31[i]); }
                s30 = ninf;
            }
            r.s[0][0] = s00; r.s[1][0] = s10; r.s[2][0] = s20; r.s[3][0] = s30; r.s[0][1] = s01; r.s[1][1] = s11; r.s[2][1] = s21; r.s[3][1] = s31;
        }
    }
    unsigned a0, a1;
    if (early.on) dma16x4(c.sws, c.vl16, early.soff, early.ldso);
    a_exp_pack<false>(r, a0, a1, early);
    a0 = hmax(a0); a1 = hmax(a1);
    const unsigned lo = FIRST ? P_LO : 0u;
    if (__builtin_expect(__any(a0 > P_HI || a1 > P_HI || a0 < lo || a1 < lo), 0)) {
        float x0 = r.s[0][0][0], x1 = r.s[0][1][0];
#pragma unroll
        for (int kt = 0; kt < 4; ++kt)
#pragma unroll
            for (int i = 0; i < 4; ++i) { x0 = __builtin_fmaxf(x0, r.s[kt][0][i]); x1 = __builtin_fmaxf(x1, r.s[kt][1][i]); }
        x0 = xmax4(x0); x1 = xmax4(x1);
        if (!FIRST) { x0 = __builtin_fmaxf(x0, 0.f); x1 = __builtin_fmaxf(x1, 0.f);
            const float f0 = __builtin_amdgcn_exp2f(-x0), f1 = __builtin_amdgcn_exp2f(-x1);
#pragma unroll
            for (int d = 0; d < 8; ++d) { r.o[d][0] *= f0; r.o[d][1] *= f1; }
            r.ls[0] *= f0; r.ls[1] *= f1; }
        z.m0 += x0; z.m1 += x1;
        r.negm[0] = (f32x4){-z.m0, -z.m0, -z.m0, -z.m0}; r.negm[1] = (f32x4){-z.m1, -z.m1, -z.m1, -z.m1};
#pragma unroll
        for (int kt = 0; kt < 4; ++kt) { r.s[kt][0] -= x0; r.s[kt][1] -= x1; }
        a_exp_pack<false>(r, a0, a1, early);
    }
    if (SWAP) {
        z.m0 = 0.f; z.m1 = 0.f;
        r.negm[0] = (f32x4){0.f, 0.f, 0.f, 0.f}; r.negm[1] = (f32x4){0.f, 0.f, 0.f, 0.f};
    }
    SBAR();
    PT(0);
    if (FIN) { if (early.on) WAITV(12); else WAITV(8); } else { if (early.on) WAITV(4); else WAITV(0); }
    if (s == 16) { asm volatile("" : "+v"(seenA)); check_flags(y.flags, FL_ACT + 32 * y.w + (lane_id() & 31), seenA, y.token); }
    PT(1);
    BARRIER(); SBAR();
    PT(2);
    __builtin_amdgcn_s_setprio(PRIOM);
    a_run<0, 2>(r, vp, kp, late, std::make_integer_sequence<int, 16>{});
    if (s < 16) a_run<16, 2>(r, vp, kp, late, std::make_integer_sequence<int, 16>{});
    __builtin_amdgcn_s_setprio(PRIOV);
    SBAR();
    PT(3);
    if (late.on) WAITV(4); else WAITV(0);
    PT(4);
    BARRIER(); SBAR();
    PT(5);
}

struct Lean { unsigned e_soff, e_lds, l_soff, l_lds, voff, koff; };
template <bool LATE> DI void a_slot_lean(const AttnCtx& c, AttnRegs& r, AttnSm& z, const Lean& st PARG) {
    const lcptr vp = c.vl + st.voff, kp = c.kl + st.koff;
    Piece early, late;
    early.sws = c.sws; early.vl16 = c.vl16; early.soff = st.e_soff; early.ldso = st.e_lds; early.on = true;
    late.sws = c.sws; late.vl16 = c.vl16; late.soff = st.l_soff; late.ldso = st.l_lds; late.on = true;
    a_pre<0>(r, vp, kp, std::make_integer_sequence<int, PD>{});
    SBAR();
    unsigned a0, a1;
    a_exp_pack<true>(r, a0, a1, early);
    if (__builtin_expect(__any(hmax(pkmax(a0, a1)) > P_HI), 0)) {
        float x0 = 0.f, x1 = 0.f;
#pragma unroll
        for (int kt = 0; kt < 4; ++kt)
#pragma unroll
            for (int i = 0; i < 4; ++i) { x0 = __builtin_fmaxf(x0, r.s[kt][0][i]); x1 = __builtin_fmaxf(x1, r.s[kt][1][i]); }
        x0 = xmax4(x0); x1 = xmax4(x1);
        const float f0 = __builtin_amdgcn_exp2f(-x0), f1 = __builtin_amdgcn_exp2f(-x1);
#pragma unroll
        for (int d = 0; d < 8; ++d) { r.o[d][0] *= f0; r.o[d][1] *= f1; }
        r.ls[0] *= f0; r.ls[1] *= f1;
        z.m0 += x0; z.m1 += x1;
        r.negm[0] = (f32x4){-z.m0, -z.m0, -z.m0, -z.m0}; r.negm[1] = (f32x4){-z.m1, -z.m1, -z.m1, -z.m1};
#pragma unroll
        for (int kt = 0; kt < 4; ++kt) { r.s[kt][0] -= x0; r.s[kt][1] -= x1; }
        a_exp_pack<false>(r, a0, a1, early);
    }
    SBAR();
    PT(0);
    WAITV(4);
    PT(1);
    BARRIER(); SBAR();
    PT(2);
    __builtin_amdgcn_s_setprio(PRIOM);
    a_run<0, LATE ? 1 : 0>(r, vp, kp, late, std::make_integer_sequence<int, 32>{});
    __builtin_amdgcn_s_setprio(PRIOV);
    SBAR();
    PT(3);
    if constexpr (LATE) WAITV(4); else WAITV(0);
    PT(4);
    BARRIER(); SBAR();
    PT(5);
}

#define P4W0 11
constexpr unsigned P4STG = 36864, P4TAG = 0x3FFu, P4OOB = 0xF0000000u;
struct P4Ctx { rsrc_t sws; unsigned vl16, ga, gw, gas, gws, dw0, dw1, dw2, lw, tag, so[3], so2[3], lo[3], lo2[3]; int w, q, o, ng, ra, rw; lcptr pa, pw; };
struct P4Regs { bf16x8 xa[2][2], wa[2][5]; };
struct P4Xch { f32x4 own[3]; u32x4 snd[9], rcv[9]; };
DI void mfa(f32x4& acc, bf16x8 a, bf16x8 b) { asm volatile("v_mfma_f32_16x16x32_bf16 %0, %1, %2, %0" : "+v"(acc) : "v"(a), "v"(b)); }
DI void mfz(f32x4& acc, bf16x8 a, bf16x8 b) { asm volatile("v_mfma_f32_16x16x32_bf16 %0, %1, %2, 0" : "=&v"(acc) : "v"(a), "v"(b)); }
template <int OFF> DI void dma_sc1(const P4Ctx& c, unsigned soff, unsigned ldso) {
    asm volatile("s_mov_b32 m0, %0\n\ts_nop 3\n\tbuffer_load_dwordx4 %1, %2, %3 offen offset:%4 sc1 lds" ::"s"(ldso), "v"(c.vl16), "s"(c.sws), "s"(soff), "i"(OFF) : "memory");
}
template <int P> DI void p4_piece(P4Ctx& c, unsigned st) {
    if constexpr (P == 0) dma_sc1<0>(c, c.ga, st + c.w * 2048);
    else if constexpr (P == 1) { dma_sc1<1024>(c, c.ga, st + c.w * 2048); ++c.ra; c.ga = c.ra == c.ng ? c.gas : c.ga + 2048; }
    else if constexpr (P == 2) dma_sc1<0>(c, c.gw + c.dw0, st + c.lw);
    else if constexpr (P == 3) dma_sc1<0>(c, c.gw + c.dw1, st + c.lw + 8192);
    else { if (c.w < 4) dma_sc1<0>(c, c.gw + c.dw2, st + c.lw + 16384); ++c.rw; c.gw = c.rw == c.ng ? c.gws : c.gw + 81920; }
}
template <int B, int UP, int I> DI void p4_rd(const P4Ctx& c, P4Regs& r, unsigned st) {
    if constexpr (I == 0) r.wa[B][0] = ld128(c.pw + st + UP * 10240);
    else if constexpr (I <= 2) r.xa[B][I - 1] = ld128(c.pa + st + (I - 1) * 2048 + UP * 1024);
    else if constexpr (I < 7) r.wa[B][I - 2] = ld128(c.pw + st + UP * 10240 + (I - 2) * 1024);
}
template <int N> DI void p4_put(const P4Ctx& c, const P4Xch& x, unsigned xoff) { st16_sc1o<(N % 3) * 1024>(c.sws, x.snd[N], c.vl16, (N % 3 == 2 ? c.so2[N / 3] : c.so[N / 3]) + xoff); }
template <int N> DI void p4_get(const P4Ctx& c, P4Xch& x, unsigned xoff) {
    asm volatile("buffer_load_dwordx4 %0, %1, %2, %3 offen offset:%4 sc1" : "=v"(x.rcv[N]) : "v"(c.vl16), "s"(c.sws), "s"((N % 3 == 2 ? c.lo2[N / 3] : c.lo[N / 3]) + xoff), "i"((N % 3) * 1024) : "memory");
}
template <int O> DI void p4_split(const f32x4 (&acc)[10], unsigned tag, P4Xch& x) {
#pragma unroll
    for (int k = 0; k < 3; ++k) x.own[k] = acc[O + 4 * k < 10 ? O + 4 * k : O];
#pragma unroll
    for (int n = 0; n < 9; ++n) { const int i = ((O + 1 + n / 3) & 3) + 4 * (n % 3);
        const u32x4 v = __builtin_bit_cast(u32x4, acc[i < 10 ? i : 0]);
#pragma unroll
        for (int e = 0; e < 4; ++e) x.snd[n][e] = (v[e] & ~P4TAG) | tag; }
}
DI void p4_split(const P4Ctx& c, f32x4 (&a)[10], P4Xch& x) {
    asm volatile("s_nop 7\n\ts_nop 7\n\ts_nop 7" : "+v"(a[0]), "+v"(a[1]), "+v"(a[2]), "+v"(a[3]), "+v"(a[4]), "+v"(a[5]), "+v"(a[6]), "+v"(a[7]), "+v"(a[8]), "+v"(a[9]) :: "memory");
    switch (c.o) { case 0: p4_split<0>(a, c.tag, x); break; case 1: p4_split<1>(a, c.tag, x); break; case 2: p4_split<2>(a, c.tag, x); break; default: p4_split<3>(a, c.tag, x); break; }
}
DI bool p4_tagged(const P4Ctx& c, const P4Xch& x) {
    unsigned a = 0, b = 0;
#pragma unroll
    for (int n = 0; n < 9; ++n)
#pragma unroll
        for (int e = 0; e < 4; ++e) { if (n % 3 == 2) b |= x.rcv[n][e] ^ c.tag; else a |= x.rcv[n][e] ^ c.tag; }
    return __all(((a | (c.o < 2 ? b : 0u)) & P4TAG) == 0);
}
template <int... Ns> DI void p4_refetch(const P4Ctx& c, P4Xch& x, unsigned xoff, std::integer_sequence<int, Ns...>) {
    const unsigned long long t0 = __builtin_amdgcn_s_memrealtime();
    for (;;) {
        (p4_get<Ns>(c, x, xoff), ...);
        asm volatile("s_waitcnt vmcnt(0)" : "+v"(x.rcv[0]), "+v"(x.rcv[1]), "+v"(x.rcv[2]), "+v"(x.rcv[3]), "+v"(x.rcv[4]), "+v"(x.rcv[5]), "+v"(x.rcv[6]), "+v"(x.rcv[7]), "+v"(x.rcv[8]) :: "memory");
        if (p4_tagged(c, x)) break;
        if (__builtin_amdgcn_s_memrealtime() - t0 > 2000000ull) break;
        __builtin_amdgcn_s_sleep(1);
    }
}
DI f32x4 p4_untag(u32x4 v) { return __builtin_bit_cast(f32x4, v & ~P4TAG); }
DI f32x4 p4_total(const P4Xch& x, int k) { return (x.own[k] + p4_untag(x.rcv[k])) + (p4_untag(x.rcv[3 + k]) + p4_untag(x.rcv[6 + k])); }
template <int B, bool ZC, bool RD, int DMA, int PUT, int GET, int... Is> DI void p4_half(P4Ctx& c, P4Regs& r, P4Xch& x, f32x4 (&acc)[10], unsigned rs, unsigned ds, std::integer_sequence<int, Is...>) {
    auto one = [&](auto ic) {
        constexpr int I = decltype(ic)::value;
        if constexpr (ZC) mfz(acc[I], r.wa[B][I >> 1], r.xa[B][I & 1]); else mfa(acc[I], r.wa[B][I >> 1], r.xa[B][I & 1]);
        if constexpr (RD) p4_rd<B ^ 1, B ^ 1, I>(c, r, rs);
        if constexpr (DMA == 1) { if constexpr (I == 1) p4_piece<0>(c, ds); if constexpr (I == 5) p4_piece<1>(c, ds); if constexpr (I == 9) p4_piece<2>(c, ds); }
        if constexpr (DMA == 2) { if constexpr (I == 3) p4_piece<3>(c, ds); if constexpr (I == 7) p4_piece<4>(c, ds); }
        if constexpr (PUT >= 0 && B == 0 && I == 5) p4_put<PUT>(c, x, OFF_XG);
        if constexpr (GET >= 0 && (I & 1) && GET + (I >> 1) < 9) p4_get<GET + (I >> 1)>(c, x, OFF_XG);
        SBAR();
    };
    (one(std::integral_constant<int, Is>{}), ...);
}
template <int WAITN, bool RA, bool RB, bool LAST, int EV, int PUT, int GETA, int GETB, bool ZC = false> DI void p4_step(P4Ctx& c, const Sync& y, P4Regs& r, P4Xch& x, f32x4 (&acc)[10], unsigned& sb, unsigned& sp, int t, unsigned fidx, u64& seen) {
    constexpr auto seq = std::make_integer_sequence<int, 10>{};
    const unsigned sn = sb == 3 * P4STG ? 0 : sb + P4STG;
    p4_half<0, ZC, true, RA ? 2 : 0, PUT, GETA>(c, r, x, acc, sb, sp, seq);
    if constexpr (!LAST) {
        asm volatile("s_waitcnt vmcnt(%0) lgkmcnt(0)" :: "i"(WAITN) : "memory");
        if constexpr (EV == 1) { if (t == c.ng - 4 && c.w == 0) { asm volatile("s_waitcnt vmcnt(4)" : "+v"(seen) :: "memory"); check_flags(y.flags, fidx, seen, y.token); } }
        BARRIER();
        if constexpr (EV == 1) {
            if (c.w == 0 && t == c.ng - 5) seen = poll_asm(y.flags, fidx);
        }
        if constexpr (EV == 2) { if (c.w == 0 && lane_id() == 0) set_flag(y.flags, FL_CTX + y.j * 32 + y.h, y.token); }
        SBAR();
    }
    p4_half<1, false, !LAST, RB ? 1 : 0, PUT, GETB>(c, r, x, acc, sn, sb, seq);
    sp = sb; sb = sn;
}

template <int... Is> DI void p4_ssteps(P4Ctx& c, const Sync& y, P4Regs& r, P4Xch& x, f32x4 (&acc)[10], unsigned& sb, unsigned& sp, u64& seen, std::integer_sequence<int, Is...>) {
    (p4_step<8 + (Is >= 2 && Is <= 10) + (Is >= 1 && Is <= 9) + (Is <= 8), true, true, false, 0, (Is <= 8 ? Is : -1), -1, -1, Is == 0>(c, y, r, x, acc, sb, sp, 0, 0, seen), ...);
}

__global__ __launch_bounds__(512, 2) void fused_kernel(const bf16_t* __restrict__ x, const bf16_t* __restrict__ ckv, const bf16_t* __restrict__ wq, const bf16_t* __restrict__ wuk,
                                                       const bf16_t* __restrict__ wuv, const bf16_t* __restrict__ wo, const bf16_t* __restrict__ w1, const bf16_t* __restrict__ b1,
                                                       const bf16_t* __restrict__ w2, bf16_t* __restrict__ out, char* __restrict__ ws) {
    __shared__ __attribute__((aligned(1024))) char smem[163840];
    const lptr lds = (lptr)smem;
    const int b = blockIdx.x, xcd = b & 7, bi = b >> 3, h = xcd * 4 + (bi >> 3), j = bi & 7;
    const int w = __builtin_amdgcn_readfirstlane(threadIdx.x >> 6);
    STAMP(0);
    u64 code_pc; unsigned code_len;
    asm volatile("s_getpc_b64 %0\n.Lcode_a:\n\ts_mov_b32 %1, .Lcode_z-.Lcode_a" : "=s"(code_pc), "=s"(code_len));
    {
        const unsigned off = (unsigned)(bi * 64 + w * 8 + (lane_id() & 7)) * 64;
        touch((const char*)code_pc + ((int)code_len > 0 && off < code_len ? off : 0u), 131072 + w * 256);
    }
    SBAR();
    const rsrc_t sws = mk_rsrc(ws, WS_BYTES);
    Sync y;
    y.flags = (gu64*)(ws + OFF_FLAGS); y.b = b; y.h = h; y.j = j; y.w = w;
    u64 epoch;
    asm volatile("s_load_dwordx2 %0, %1, 0x20" : "=s"(epoch) : "s"(ws) : "memory");
    u32x4 kv[16];
    P1Ctx c1;
    P2Ctx c2;
    const bool isv = (w & 3) >= 2;
    {
        const int l = lane_id(), g = l >> 4, q = (l & 15) >> 2;
        {
            const int nb = w >> 1;
            c1.w = w; c1.sws = sws; c1.wo = wo; c1.w2 = w2; c1.wq = wq; c1.w1 = w1; c1.lds = lds;
            c1.sx = mk_rsrc(ckv, 2048 * 512 * 2);
            c1.sw = mk_rsrc(nb < 2 ? wuk : wuv, 512 * 4096 * 2);
#pragma unroll
            for (int par = 0; par < 2; ++par) {
                c1.vx[par] = (l >> 3) * 1024 + (((l & 7) ^ (4 * par + (l >> 4))) * 16);
                c1.vw[par] = (l >> 3) * 8192 + (((l & 7) ^ ((((l >> 4) & 1) | (par << 1)) << 1)) * 16);
            }
            c1.sxo = (256 * j + 32 * w) * 1024;
            c1.swo = (32 * (w & 1)) * 8192 + (h * 128 + 64 * (nb & 1)) * 2;
            const int wn = w & 3, wm = w >> 2, f = (l & 15) >> 1, s = ((q >> 1) & 1) | ((g & 1) << 1);
            c1.xb[0] = lds + wm * 16384 + (l & 15) * 128 + ((g ^ f) * 16);
            c1.xb[1] = lds + wm * 16384 + (l & 15) * 128 + (((g ^ f) ^ 4) * 16);
#pragma unroll
            for (int nt = 0; nt < 4; ++nt) c1.wb[nt] = lds + 65536 + wn * 8192 + (8 * g + q) * 128 + ((nt ^ s) * 32) + (l & 3) * 8;
        }
        p1_dma(c1, 0, 0);
        u32x4 xo0, xo1;
        {
            const rsrc_t sxi = mk_rsrc(x, 2048 * 640 * 2);
            const unsigned vx = (l & 7) * 1280 + (l >> 3) * 16, so0 = b * 8 * 1280 + w * 128, so1 = b * 8 * 1280 + (8 + (w & 1)) * 128;
            asm volatile("s_nop 4\n\tbuffer_load_dwordx4 %0, %2, %3, %4 offen\n\tbuffer_load_dwordx4 %1, %2, %3, %5 offen" : "=&v"(xo0), "=&v"(xo1) : "v"(vx), "s"(sxi), "s"(so0), "s"(so1) : "memory");
        }
        BARRIER();
        p1_dma(c1, 1, 1);
        asm volatile("s_waitcnt lgkmcnt(0)" : "+s"(epoch) :: "memory");
        y.token = epoch + 1;
        STAMP(12);
        asm volatile("s_waitcnt vmcnt(8)" : "+v"(xo0), "+v"(xo1) :: "memory");
        STAMP(13);
        float v0[8], v1[8];
#pragma unroll
        for (int e = 0; e < 4; ++e) { v0[2 * e] = bflo(xo0[e]); v0[2 * e + 1] = bfhi(xo0[e]); v1[2 * e] = bflo(xo1[e]); v1[2 * e + 1] = bfhi(xo1[e]); }
        auto red8 = [](float t) { t += __uint_as_float(__builtin_amdgcn_update_dpp(0u, __float_as_uint(t), 0x128, 0xf, 0xf, false)); return xsum4(t); };
        {
            const float m0 = red8(((v0[0] + v0[1]) + (v0[2] + v0[3])) + ((v0[4] + v0[5]) + (v0[6] + v0[7]))) * (1.f / 64.f);
            const float m1 = red8(((v1[0] + v1[1]) + (v1[2] + v1[3])) + ((v1[4] + v1[5]) + (v1[6] + v1[7]))) * (1.f / 64.f);
            float q0 = 0.f, q1 = 0.f;
#pragma unroll
            for (int e = 0; e < 8; ++e) { const float d0 = v0[e] - m0, d1 = v1[e] - m1; q0 += d0 * d0; q1 += d1 * d1; }
            q0 = red8(q0); q1 = red8(q1);
            if (l < 8) { *(LAS f32x2*)(lds + 162816 + l * 80 + w * 8) = (f32x2){m0, q0}; if (w < 2) *(LAS f32x2*)(lds + 162816 + l * 80 + 64 + w * 8) = (f32x2){m1, q1}; }
        }
        asm volatile("s_waitcnt lgkmcnt(0)" ::: "memory"); BARRIER();
        STAMP(14);
        {
            f32x4 t[5];
#pragma unroll
            for (int i = 0; i < 5; ++i) t[i] = *(const LAS f32x4*)(lds + 162816 + (l & 7) * 80 + i * 16);
            float mean = 0.f;
#pragma unroll
            for (int i = 0; i < 5; ++i) mean += t[i][0] + t[i][2];
            mean *= 0.1f;
            float m2 = 0.f;
#pragma unroll
            for (int i = 0; i < 5; ++i) { const float d0 = t[i][0] - mean, d1 = t[i][2] - mean; m2 += (t[i][1] + t[i][3]) + 64.f * (d0 * d0 + d1 * d1); }
            const float rstd = 1.f / __builtin_sqrtf(m2 * (1.f / 640.f) + 1e-5f);
            const unsigned vo = (l >> 3) * 256 + (l & 7) * 16, xo = OFF_XN + (b >> 1) * 20480 + (b & 1) * 128;
            u32x4 o0, o1;
#pragma unroll
            for (int e = 0; e < 4; ++e) { o0[e] = cvtpk((v0[2 * e] - mean) * rstd, (v0[2 * e + 1] - mean) * rstd); o1[e] = cvtpk((v1[2 * e] - mean) * rstd, (v1[2 * e + 1] - mean) * rstd); }
            st16_sc1(sws, o0, vo, xo + w * 2048);
            if (w < 2) st16_sc1(sws, o1, vo, xo + (8 + w) * 2048);
        }
        {
            const char* pa = (const char*)ckv + (size_t)(256 * j + 64 * (h & 3) + 8 * w + (l >> 3)) * 1024 + (l & 7) * 128;
            const char* pw = (const char*)((w & 1) ? wuv : wuk) + (size_t)(64 * j + 16 * (w >> 1) + ((l & 31) >> 1)) * 8192 + h * 256 + (l & 1) * 128;
            touch(pa, 131072 + w * 256); touch(pw, 131072 + w * 256);
        }
        {
            c2.w = w; c2.sws = sws; c2.swq = mk_rsrc(wq, 640 * 4096 * 2); c2.sw1 = mk_rsrc(w1, 640 * 1728 * 2);
            c2.vl16 = l * 16;
            const int swz = ((l >> 4) & 1) | ((w & 1) << 1);
            c2.vwq = (l >> 3) * 8192 + (((l & 7) ^ (swz << 1)) * 16);
            c2.vw1 = (l >> 3) * 3456 + (((l & 7) ^ (swz << 1)) * 16);
            { const int mtl = 2 * w, mtg = mtl < 8 ? 8 * j + mtl : 8 * (15 - j) + (mtl - 8); c2.sxo = OFF_XN + mtg * 20480; }
            c2.swqo = (8 * w) * 8192 + h * 256;
            c2.sw1o = (8 * w) * 3456 + h * 128;
            c2.t0l = (w >> 2) * 16384 + (w & 3) * 4096; c2.t0s = OFF_K + (unsigned)h * 524288 + c2.t0l; c2.t1s = OFF_V + (unsigned)h * 524288 + (w & 3) * 4096;
            { const int wn = w & 3, wm = w >> 2;
              c2.skv = isv ? OFF_V + (unsigned)(h * 32 + 4 * j + 2 * wm) * 16384 + (wn - 2) * 4096 : OFF_K + (unsigned)((h * 128 + 16 * j + 8 * wm) * 4 + 2 * wn) * 1024; }
        }
    }
    STAMP(1); CSTAMP(14);
    if (isv) p1_main<true>(c1, c2, y, kv); else p1_main<false>(c1, c2, y, kv);
    STAMP(2); CSTAMP(15);
    p2_kvst<0>(c2, kv, isv); p2_kvst<1>(c2, kv, isv); p2_kvst<2>(c2, kv, isv); p2_kvst<3>(c2, kv, isv);
    const bool hasf = h < 27;
    AttnCtx c3;
    AttnRegs r;
    {
        const int l = lane_id(), g = l >> 4, q = (l & 15) >> 2;
        const int wn = w & 1, wm = w >> 1;
        const int s = ((q >> 1) & 1) | ((g & 1) << 1);
        c2.xb = lds + wm * 8192 + l * 16;
#pragma unroll
        for (int nt = 0; nt < 4; ++nt) c2.wq[nt] = lds + 65536 + wn * 8192 + (8 * g + q) * 128 + ((nt ^ s) * 32) + (l & 3) * 8;
#pragma unroll
        for (int ft = 0; ft < 2; ++ft) c2.wf[ft] = lds + 65536 + 16384 + (8 * g + q) * 128 + (((2 * wn + ft) ^ s) * 32) + (l & 3) * 8;
        f32x4 acc[6][4];
#pragma unroll
        for (int a = 0; a < 6; ++a)
#pragma unroll
            for (int m = 0; m < 4; ++m) acc[a][m] = (f32x4){0.f, 0.f, 0.f, 0.f};
        STAMP(3);
        P2Bias bs; bs.p = b1 + (64 * h + 32 * wn + 4 * g); bs.a = (u32x2){0u, 0u}; bs.b = (u32x2){0u, 0u};
        if (hasf) p2_main<true>(c2, y, bs, acc, kv, isv, std::make_integer_sequence<int, 10>{}); else p2_main<false>(c2, y, bs, acc, kv, isv, std::make_integer_sequence<int, 10>{});
        STAMP(4);
        constexpr float C2 = 0.08838834764831845f * 1.4426950408889634f;
#pragma unroll
        for (int mt = 0; mt < 4; ++mt)
#pragma unroll
            for (int a = 0; a < 2; ++a)
                *(LAS u32x4*)(lds + 98304 + ((4 * wm + mt) * 4 + 2 * wn + a) * 1024 + l * 16) = pack8(acc[2 * a][mt] * C2, acc[2 * a + 1][mt] * C2);
        u32x4 actf[4];
        if (hasf) {
            asm volatile("" : "+v"(bs.a), "+v"(bs.b));
            const u32x2 bA = bs.a, bB = bs.b;
            const f32x4 biasA = {bflo(bA[0]), bfhi(bA[0]), bflo(bA[1]), bfhi(bA[1])}, biasB = {bflo(bB[0]), bfhi(bB[0]), bflo(bB[1]), bfhi(bB[1])};
#pragma unroll
            for (int mt = 0; mt < 4; ++mt) {
                f32x4 hA = acc[4][mt] + biasA, hB = acc[5][mt] + biasB;
#pragma unroll
                for (int i = 0; i < 4; ++i) { hA[i] = mish(hA[i]); hB[i] = mish(hB[i]); }
                actf[mt] = pack8(hA, hB);
            }
        }
        asm volatile("s_waitcnt vmcnt(0) lgkmcnt(0)" ::: "memory"); BARRIER();
        const int grp = w >> 2, qg = w & 3;
        c3.sws = sws; c3.vl16 = l * 16; c3.kbase = OFF_K + (unsigned)h * 524288; c3.vbase = OFF_V + (unsigned)h * 524288;
        c3.ctxl = OFF_CTX + ((8 * j + 2 * qg) * 128 + 4 * h) * 1024;
        c3.nL = 2 * j + 2; c3.grp = grp; c3.qg = qg; c3.j = j; c3.rowrel = (l & 15) - 4 * g; c3.kl = lds + l * 16; c3.vl = lds + 65536 + l * 16;
        c3.ql = lds + 131072 + qg * 8192 + l * 16;
        if (hasf) {
#pragma unroll
            for (int mt = 0; mt < 4; ++mt) { const int mtl = 4 * wm + mt, mtg = mtl < 8 ? 8 * j + mtl : 8 * (15 - j) + (mtl - 8);
                st16_sc1(sws, actf[mt], l * 16, OFF_ACT + (mtg * 54 + 2 * h + wn) * 1024); }
        }
#pragma unroll
        for (int qt = 0; qt < 2; ++qt)
#pragma unroll
            for (int u = 0; u < 4; ++u) r.q[qt][u] = ld128(lds + 98304 + ((8 * grp + 2 * qg + qt) * 4 + u) * 1024 + l * 16);
    }
    STAMP(5);
    P4Ctx c4;
    {
        const int l = lane_id();
        const int grp = c3.grp, qg = c3.qg;
        const unsigned vl16 = c3.vl16;
#pragma unroll
        for (int d = 0; d < 8; ++d) { r.o[d][0] = (f32x4){0.f, 0.f, 0.f, 0.f}; r.o[d][1] = (f32x4){0.f, 0.f, 0.f, 0.f}; }
        r.negm[0] = (f32x4){0.f, 0.f, 0.f, 0.f}; r.negm[1] = (f32x4){0.f, 0.f, 0.f, 0.f};
        AttnSm z; z.m0 = 0.f; z.m1 = 0.f;
        r.ls[0] = (f32x4){0.f, 0.f, 0.f, 0.f}; r.ls[1] = (f32x4){0.f, 0.f, 0.f, 0.f};
        r.ones = (bf16x8){0x3F80, 0x3F80, 0x3F80, 0x3F80, 0x3F80, 0x3F80, 0x3F80, 0x3F80}; PIN(r.ones);
        asm volatile("s_waitcnt lgkmcnt(0)" ::: "memory");
        BARRIER();
        STAMP(6); CSTAMP(12);
        a_pre<16>(r, c3.vl, c3.kl, std::make_integer_sequence<int, PD>{});
        if (grp == 1) BARRIER();
        { Piece none; none.on = false; none.sws = sws; none.vl16 = 0; none.soff = 0; none.ldso = 0; a_run<16, 0>(r, c3.vl, c3.kl, none, std::make_integer_sequence<int, 16>{}); }
        BARRIER();
        c3.swap_s = grp == 0 ? c3.nL - 1 : 99; c3.first_s = 0;
        asm volatile("" : "+s"(c3.swap_s), "+s"(c3.first_s));
        {
            const int nL = c3.nL;
            auto lean_init = [&](int s) {
                Lean st;
                const bool eK = grp == 1 && s + 2 < nL;
                const int t0 = s < nL ? s : 2 * s - nL + grp, t1 = s + 1 < nL ? s + 1 : 2 * s + 2 - nL + grp, t2 = s + 2 < nL ? s + 2 : 2 * s + 4 - nL + grp;
                const int te = eK ? t2 : t1;
                st.e_soff = (eK ? c3.kbase : c3.vbase) + te * 16384 + qg * 4096; st.e_lds = (eK ? 0 : 65536) + (te & 3) * 16384 + qg * 4096;
                st.l_soff = c3.kbase + t2 * 16384 + qg * 4096; st.l_lds = (t2 & 3) * 16384 + qg * 4096;
                st.voff = (t0 & 3) * 16384; st.koff = (t1 & 3) * 16384;
                return st;
            };
            int s = 0, phase = 0;
#pragma nounroll
            for (;;) {
                const int gend = phase == 0 ? 1 : (phase == 1 ? (grp == 0 ? nL + 1 : nL - 1) : 17);
#pragma nounroll
                while (s < gend) { a_slotfull(c3, y, r, z, s PPASSG); ++s; }
                if (phase == 0) { if (w == 0 && l == 0) set_flag(y.flags, FL_ACT + j * 32 + h, y.token); }
                if (phase == 2 || s > 16) break;
                if (phase == 1 && s <= 14) {
                    Lean st = lean_init(s);
#pragma nounroll
                    do { a_slot_lean<true>(c3, r, z, st PPASS);
                         st.e_soff += 32768; st.e_lds ^= 32768; st.l_soff += 32768; st.l_lds ^= 32768; st.voff ^= 32768; st.koff ^= 32768; ++s; } while (s <= 14);
                }
                const int send = phase == 0 ? nL - 2 : 16;
                if (s < send) {
                    Lean st = lean_init(s);
#pragma nounroll
                    do { a_slot_lean<false>(c3, r, z, st PPASS);
                         st.e_soff += 16384; st.e_lds = (st.e_lds & ~0xC000u) | ((st.e_lds + 16384) & 0xC000u); st.voff = (st.voff + 16384) & 0xC000u; st.koff = (st.koff + 16384) & 0xC000u; ++s; } while (s < send);
                }
                ++phase;
            }
        }
        const float m0 = z.m0, m1 = z.m1, l0 = r.ls[0][0], l1 = r.ls[1][0];
        if (grp == 0) BARRIER();
        STAMP(7); CSTAMP(13);
        {
            const int q = xcd >> 1, rbk = (xcd & 1) * 8 + (bi >> 2), cb = bi & 3, wm = w >> 1, wn = w & 1;
            c4.sws = sws; c4.vl16 = vl16; c4.w = w; c4.q = q; c4.o = (q + 2 * wn) & 3; c4.ng = q == 3 ? 6 : 7; c4.ra = 0; c4.rw = 0;
            c4.ga = OFF_ACT + ((8 * rbk + w) * 54 + 14 * q) * 1024; c4.gas = OFF_CTX + ((8 * rbk + w) * 128 + 32 * q) * 1024;
            c4.gw = OFF_WP + ((128 + 14 * q) * 40 + 10 * cb) * 1024; c4.gws = OFF_WP + (32 * q * 40 + 10 * cb) * 1024;
            c4.dw0 = w * 1024; c4.dw1 = (w < 2 ? 8 + w : 38 + w) * 1024; c4.dw2 = (46 + w) * 1024; c4.lw = 16384 + w * 1024;
            c4.tag = (unsigned)y.token & P4TAG;
            const unsigned xb = (unsigned)(((xcd & 1) * 32 + bi) * 128 + w) * 3072;
#pragma unroll
            for (int d = 0; d < 3; ++d) { const int qq = (q + 1 + d) & 3;
                c4.so[d] = xb + qq * 98304 + q * 24576; c4.so2[d] = ((c4.o + 1 + d) & 3) < 2 ? c4.so[d] : P4OOB;
                c4.lo[d] = xb + q * 98304 + qq * 24576; c4.lo2[d] = c4.o < 2 ? c4.lo[d] : P4OOB; }
            c4.pa = lds + (4 * wm) * 1024 + vl16; c4.pw = lds + 16384 + (5 * wn) * 1024 + vl16;
        }
        p4_piece<0>(c4, 2 * P4STG); p4_piece<1>(c4, 2 * P4STG); p4_piece<2>(c4, 2 * P4STG); p4_piece<3>(c4, 2 * P4STG); p4_piece<4>(c4, 2 * P4STG);
        p4_piece<0>(c4, 3 * P4STG); p4_piece<1>(c4, 3 * P4STG); p4_piece<2>(c4, 3 * P4STG); p4_piece<3>(c4, 3 * P4STG); p4_piece<4>(c4, 3 * P4STG);
        {
            const lptr xo = lds + (qg * 2 + grp) * 9216;
#pragma unroll
            for (int d = 0; d < 4; ++d)
#pragma unroll
                for (int qt = 0; qt < 2; ++qt) *(LAS f32x4*)(xo + (d * 2 + qt) * 1024 + l * 16) = sel(grp == 0, r.o[4 + d][qt], r.o[d][qt]);
            *(LAS f32x4*)(xo + 8192 + l * 16) = (f32x4){m0, m1, l0, l1};
            asm volatile("s_waitcnt lgkmcnt(0)" ::: "memory"); BARRIER();
            const lcptr xi = lds + (qg * 2 + (grp ^ 1)) * 9216;
            const f32x4 st = *(const LAS f32x4*)(xi + 8192 + l * 16);
            const float M0 = __builtin_fmaxf(m0, st[0]), M1 = __builtin_fmaxf(m1, st[1]);
            const float wa0 = __builtin_amdgcn_exp2f(m0 - M0), wb0 = __builtin_amdgcn_exp2f(st[0] - M0), wa1 = __builtin_amdgcn_exp2f(m1 - M1), wb1 = __builtin_amdgcn_exp2f(st[1] - M1);
            const float i0 = __builtin_amdgcn_rcpf(wa0 * l0 + wb0 * st[2]), i1 = __builtin_amdgcn_rcpf(wa1 * l1 + wb1 * st[3]);
            const float ca0 = wa0 * i0, cb0 = wb0 * i0, ca1 = wa1 * i1, cb1 = wb1 * i1;
#pragma unroll
            for (int up = 0; up < 2; ++up)
#pragma unroll
                for (int qt = 0; qt < 2; ++qt) {
                    const f32x4 pa = *(const LAS f32x4*)(xi + ((2 * up) * 2 + qt) * 1024 + l * 16), pb = *(const LAS f32x4*)(xi + ((2 * up + 1) * 2 + qt) * 1024 + l * 16);
                    const f32x4 oa = sel(grp == 0, r.o[2 * up][qt], r.o[4 + 2 * up][qt]), ob = sel(grp == 0, r.o[2 * up + 1][qt], r.o[4 + 2 * up + 1][qt]);
                    const float ca = qt ? ca1 : ca0, cb = qt ? cb1 : cb0;
                    st16_sc1(sws, pack8(oa * ca + pa * cb, ob * ca + pb * cb), vl16, OFF_CTX + ((8 * (15 - j) + 2 * qg + qt) * 128 + 4 * h + 2 * grp + up) * 1024);
                }
        }
    }
    STAMP(8);
    {
        asm volatile("s_waitcnt vmcnt(8) lgkmcnt(0)" ::: "memory"); BARRIER();
        P4Regs r4;
        unsigned sb = 2 * P4STG, sp = P4STG;
        p4_rd<0, 0, 0>(c4, r4, sb); p4_rd<0, 0, 1>(c4, r4, sb); p4_rd<0, 0, 2>(c4, r4, sb); p4_rd<0, 0, 3>(c4, r4, sb); p4_rd<0, 0, 4>(c4, r4, sb); p4_rd<0, 0, 5>(c4, r4, sb); p4_rd<0, 0, 6>(c4, r4, sb);
        SBAR();
        p4_piece<0>(c4, 0); p4_piece<1>(c4, 0); p4_piece<2>(c4, 0); p4_piece<3>(c4, 0); p4_piece<4>(c4, 0);
        p4_piece<0>(c4, P4STG); p4_piece<1>(c4, P4STG); p4_piece<2>(c4, P4STG);
        const int l = lane_id();
        const int rbk = (xcd & 1) * 8 + (bi >> 2), cb = bi & 3;
        const unsigned cidx = FL_CTX + (rbk < 8 ? rbk : 15 - rbk) * 32 + 8 * c4.q + (l & 7);
        f32x4 accg[10], accs[10], gt[3];
        P4Xch xg;
        const int row0 = 128 * rbk + 32 * (w >> 1) + (l & 15), col0 = 160 * cb + 80 * (w & 1) + 4 * (l >> 4);
        u32x2 xv[3];
        {
            const rsrc_t sx = mk_rsrc(x, 2048 * 640 * 2);
#pragma unroll
            for (int k = 0; k < 3; ++k) { const int i = c4.o + 4 * k < 10 ? c4.o + 4 * k : c4.o;
                asm volatile("buffer_load_dwordx2 %0, %1, %2, 0 offen" : "=v"(xv[k]) : "v"((unsigned)((row0 + (i & 1) * 16) * 640 + col0 + (i >> 1) * 16) * 2), "s"(sx) : "memory"); }
        }
        u64 seen = 0;
        SBAR();
        STAMP(9);
        p4_step<P4W0, true, true, false, 2, -1, -1, -1, true>(c4, y, r4, xg, accg, sb, sp, 0, cidx, seen);
#pragma nounroll
        for (int t = 1; t < c4.ng; ++t) p4_step<8, true, true, false, 1, -1, -1, -1>(c4, y, r4, xg, accg, sb, sp, t, cidx, seen);
        p4_split(c4, accg, xg);
        p4_ssteps(c4, y, r4, xg, accs, sb, sp, seen, std::make_integer_sequence<int, 12>{});
        p4_step<8, true, false, false, 0, -1, -1, 0>(c4, y, r4, xg, accs, sb, sp, 0, 0, seen);
        p4_step<13, false, false, false, 0, -1, 5, -1>(c4, y, r4, xg, accs, sb, sp, 0, 0, seen);
        p4_step<9, false, false, false, 0, -1, -1, -1>(c4, y, r4, xg, accs, sb, sp, 0, 0, seen);
        p4_step<0, false, false, true, 0, -1, -1, -1>(c4, y, r4, xg, accs, sb, sp, 0, 0, seen);
        asm volatile("s_waitcnt vmcnt(0)" : "+v"(xg.rcv[0]), "+v"(xg.rcv[1]), "+v"(xg.rcv[2]), "+v"(xg.rcv[3]), "+v"(xg.rcv[4]), "+v"(xg.rcv[5]), "+v"(xg.rcv[6]), "+v"(xg.rcv[7]), "+v"(xg.rcv[8]),
                     "+v"(xv[0]), "+v"(xv[1]), "+v"(xv[2]) :: "memory");
        constexpr auto nine = std::make_integer_sequence<int, 9>{};
        P4Xch xs;
        p4_split(c4, accs, xs);
        p4_put<0>(c4, xs, OFF_XS); p4_put<1>(c4, xs, OFF_XS); p4_put<2>(c4, xs, OFF_XS); p4_put<3>(c4, xs, OFF_XS); p4_put<4>(c4, xs, OFF_XS);
        p4_put<5>(c4, xs, OFF_XS); p4_put<6>(c4, xs, OFF_XS); p4_put<7>(c4, xs, OFF_XS); p4_put<8>(c4, xs, OFF_XS);
        TS(0);
        if (!p4_tagged(c4, xg)) p4_refetch(c4, xg, OFF_XG, nine);
#pragma unroll
        for (int k = 0; k < 3; ++k) {
            gt[k] = p4_total(xg, k);
#pragma unroll
            for (int e = 0; e < 4; ++e) gt[k][e] = gt[k][e] * __builtin_amdgcn_rcpf(1.f + __builtin_amdgcn_exp2f(-1.4426950408889634f * gt[k][e]));
            PIN(gt[k]);
        }
        TS(1);
        p4_refetch(c4, xs, OFF_XS, nine);
        TS(2);
#pragma unroll
        for (int k = 0; k < 3; ++k) {
            if (k < 2 || c4.o < 2) { const int i = c4.o + 4 * k;
                const f32x4 sv = p4_total(xs, k);
                const f32x4 o = {bflo(xv[k][0]) + sv[0] * gt[k][0], bfhi(xv[k][0]) + sv[1] * gt[k][1], bflo(xv[k][1]) + sv[2] * gt[k][2], bfhi(xv[k][1]) + sv[3] * gt[k][3]};
                const u32x2 ov = {cvtpk(o[0], o[1]), cvtpk(o[2], o[3])};
                asm volatile("global_store_dwordx2 %0, %1, off sc0 sc1\n\ts_nop 1" :: "v"(out + (row0 + (i & 1) * 16) * 640 + col0 + (i >> 1) * 16), "v"(ov) : "memory"); }
        }
        asm volatile(".Lcode_z:");
        if (b == 0 && w == 0 && l == 0) __hip_atomic_store(y.flags + 4, y.token, __ATOMIC_RELAXED, __HIP_MEMORY_SCOPE_AGENT);
    }
    STAMP(11);
}

extern "C" void kernel_launch(void* const* d_in, const int* in_sizes, int n_in, void* d_out, int out_size, void* d_ws, size_t ws_size, hipStream_t stream) {
    (void)in_sizes; (void)n_in; (void)out_size; (void)ws_size;
    fused_kernel<<<256, 512, 0, stream>>>((const bf16_t*)d_in[0], (const bf16_t*)d_in[1], (const bf16_t*)d_in[2], (const bf16_t*)d_in[3], (const bf16_t*)d_in[4],
                                          (const bf16_t*)d_in[5], (const bf16_t*)d_in[7], (const bf16_t*)d_in[8], (const bf16_t*)d_in[9], (bf16_t*)d_out, (char*)d_ws);
}
```

```cpp
#include <hip/hip_runtime.h>
#include <stdint.h>
#include <utility>

typedef unsigned short bf16_t;
typedef short bf16x8 __attribute__((ext_vector_type(8)));
typedef short s16x4 __attribute__((ext_vector_type(4)));
typedef short v4i16_t __attribute__((ext_vector_type(4)));
typedef float f32x4 __attribute__((ext_vector_type(4)));
typedef float f32x2 __attribute__((ext_vector_type(2)));
typedef unsigned u32x4 __attribute__((ext_vector_type(4)));
typedef unsigned u32x2 __attribute__((ext_vector_type(2)));
typedef __bf16 bf16x2_t __attribute__((ext_vector_type(2)));
typedef unsigned short u16x2 __attribute__((ext_vector_type(2)));
typedef __amdgpu_buffer_rsrc_t rsrc_t;
typedef unsigned long long u64;
#define LAS __attribute__((address_space(3)))
#define GAS __attribute__((address_space(1)))
typedef LAS char* lptr;
typedef LAS const char* lcptr;
typedef GAS u64 gu64;
#define DI __device__ __forceinline__
#define SBAR() __builtin_amdgcn_sched_barrier(0)
#define PIN(x) asm volatile("" : "+v"(x))
#define MF16(a, b, c) __builtin_amdgcn_mfma_f32_16x16x32_bf16(a, b, c, 0, 0, 0)
#define WAITV(n) asm volatile("s_waitcnt vmcnt(" #n ")" ::: "memory")
#define WAITL(n) asm volatile("s_waitcnt lgkmcnt(" #n ")" ::: "memory")
#define BARRIER() do { asm volatile("" ::: "memory"); __builtin_amdgcn_s_barrier(); asm volatile("" ::: "memory"); } while (0)

#define UPTO 4
#define STAG 1
#define NOINV 1
#define P0EB 1
#define KVR 2
#define SPREAD 1
#define SPOS 1
#define XST 0
#define STAMPS 0

constexpr unsigned OFF_FLAGS = 0;
constexpr unsigned OFF_XN = 65536;
constexpr unsigned OFF_K = OFF_XN + 2621440;
constexpr unsigned OFF_V = OFF_K + 16777216;
constexpr unsigned OFF_ACT = OFF_V + 16777216 + 8388608;
constexpr unsigned OFF_CTX = OFF_ACT + 7077888;
constexpr unsigned OFF_WP = OFF_CTX + 16777216;
constexpr unsigned OFF_ST = OFF_WP + 182 * 40 * 1024;
constexpr unsigned OFF_XG = OFF_ST + 65536;
constexpr unsigned OFF_XS = OFF_XG + 25165824;
constexpr unsigned WS_BYTES = OFF_XS + 25165824;
constexpr unsigned FL_P0 = 0, FL_KV = 256, FL_ACT = 512, FL_CTX = 768;
constexpr int SC1 = 16;

DI unsigned cvtpk(float lo, float hi) { f32x2 v = {lo, hi}; bf16x2_t b = __builtin_convertvector(v, bf16x2_t); return __builtin_bit_cast(unsigned, b); }
DI u32x4 pack8(f32x4 a, f32x4 b) { u32x4 w = {cvtpk(a[0], a[1]), cvtpk(a[2], a[3]), cvtpk(b[0], b[1]), cvtpk(b[2], b[3])}; return w; }
DI float bflo(unsigned u) { return __uint_as_float(u << 16); }
DI float bfhi(unsigned u) { return __uint_as_float(u & 0xffff0000u); }
DI s16x4 vtr(lcptr p) { return __builtin_bit_cast(s16x4, __builtin_amdgcn_ds_read_tr16_b64_v4i16((LAS v4i16_t*)p)); }
DI bf16x8 cat(s16x4 a, s16x4 b) { return (bf16x8){a[0], a[1], a[2], a[3], b[0], b[1], b[2], b[3]}; }
DI bf16x8 ld128(lcptr p) { return *(const LAS bf16x8*)p; }
DI bf16x8 asbf(u32x4 v) { return __builtin_bit_cast(bf16x8, v); }
DI rsrc_t mk_rsrc(const void* p, unsigned bytes) { return __builtin_amdgcn_make_buffer_rsrc((void*)p, (short)0, (int)bytes, 0x00020000); }
DI void dma16(rsrc_t r, unsigned voff, unsigned soff, unsigned ldsoff) {
    asm volatile("s_mov_b32 m0, %0\n\ts_nop 3\n\tbuffer_load_dwordx4 %1, %2, %3 offen lds" ::"s"(ldsoff), "v"(voff), "s"(r), "s"(soff) : "memory");
}
#define HS " sc1"
DI void dma16h(rsrc_t r, unsigned voff, unsigned soff, unsigned ldsoff) {
    asm volatile("s_mov_b32 m0, %0\n\ts_nop 3\n\tbuffer_load_dwordx4 %1, %2, %3 offen" HS " lds" ::"s"(ldsoff), "v"(voff), "s"(r), "s"(soff) : "memory");
}
DI void dma16x4(rsrc_t r, unsigned voff, unsigned soff, unsigned ldsoff) {
    asm volatile("s_mov_b32 m0, %0\n\ts_nop 3\n\tbuffer_load_dwordx4 %1, %2, %3 offen" HS " lds\n\tbuffer_load_dwordx4 %1, %2, %3 offen offset:1024" HS " lds\n\t"
                 "buffer_load_dwordx4 %1, %2, %3 offen offset:2048" HS " lds\n\tbuffer_load_dwordx4 %1, %2, %3 offen offset:3072" HS " lds" ::"s"(ldsoff), "v"(voff), "s"(r), "s"(soff) : "memory");
}
DI void touch(const void* p, unsigned ldsoff) { asm volatile("s_mov_b32 m0, %0\n\ts_nop 3\n\tglobal_load_lds_dword %1, off" ::"s"(ldsoff), "v"(p) : "memory"); }
DI void st16_sc1(rsrc_t r, u32x4 v, unsigned voff, unsigned soff) { asm volatile("s_nop 4\n\tbuffer_store_dwordx4 %0, %1, %2, %3 offen sc1\n\ts_nop 1" ::"v"(v), "v"(voff), "s"(r), "s"(soff) : "memory"); }
template <int OFF> DI void st16_sc1o(rsrc_t r, u32x4 v, unsigned voff, unsigned soff) { asm volatile("s_nop 4\n\tbuffer_store_dwordx4 %0, %1, %2, %3 offen offset:%4 sc1\n\ts_nop 1" ::"v"(v), "v"(voff), "s"(r), "s"(soff), "i"(OFF) : "memory"); }
DI u32x4 ld16(rsrc_t r, unsigned voff, unsigned soff) { return __builtin_amdgcn_raw_buffer_load_b128(r, voff, soff, 0); }
#define P4AUX SC1
DI u32x4 ld16_sc1(rsrc_t r, unsigned voff, unsigned soff) { return __builtin_amdgcn_raw_buffer_load_b128(r, voff, soff, P4AUX); }
template <class T> DI T sel(bool c, T a, T b) { return c ? a : b; }
DI int lane_id() { int l; asm volatile("v_mbcnt_lo_u32_b32 %0, -1, 0\n\tv_mbcnt_hi_u32_b32 %0, -1, %0" : "=v"(l)); return l; }
DI void set_flag(gu64* flags, unsigned idx, u64 token) { __hip_atomic_store(flags + idx * 8, token, __ATOMIC_RELAXED, __HIP_MEMORY_SCOPE_AGENT); }
DI u64 poll_flag(gu64* flags, unsigned idx) { return __hip_atomic_load(flags + idx * 8, __ATOMIC_RELAXED, __HIP_MEMORY_SCOPE_AGENT); }
DI void spin_flags(gu64* flags, unsigned idx, u64 token) {
    const unsigned long long t0 = __builtin_amdgcn_s_memrealtime();
    for (;;) {
        __builtin_amdgcn_s_sleep(1);
        const u64 v = poll_flag(flags, idx);
        if (__all(v == token)) break;
        if (__builtin_amdgcn_s_memrealtime() - t0 > 2000000ull) break;
    }
}
DI u64 poll_asm(gu64* flags, unsigned idx) { u64 v; asm volatile("global_load_dwordx2 %0, %1, off sc1" : "=v"(v) : "v"(flags + idx * 8) : "memory"); return v; }
DI void check_flags(gu64* flags, unsigned idx, u64 seen, u64 token) { if (!__all(seen == token)) spin_flags(flags, idx, token); }
DI void acquire_issue() { asm volatile("buffer_inv sc1" ::: "memory"); }
DI u64 uni64(u64 v) { return ((u64)(unsigned)__builtin_amdgcn_readfirstlane((unsigned)(v >> 32)) << 32) | (unsigned)__builtin_amdgcn_readfirstlane((unsigned)v); }
#define SCLK __builtin_amdgcn_s_memrealtime
#define STAMP(i) do { } while (0)
#define STAMPW(i, wv) do { } while (0)
#define CSTAMP(i) do { } while (0)

#define TS(i) do { } while (0)
#define TSARG
#define TSPASS
struct Sync { gu64* flags; u64 token; int b, h, j, w; };
#define YSTAMP(i) do { } while (0)

struct P2Ctx { rsrc_t sws, swq, sw1; unsigned vl16, vwq, vw1; unsigned sxo, swqo, sw1o, skv, t0s, t0l, t1s; int w; lcptr xb; lcptr wq[4]; lcptr wf[2]; };
DI void p2_dma1(const P2Ctx& c, int st, int ks, int i) {
    if (i < 4) dma16h(c.sws, c.vl16, c.sxo + (i >> 1) * 20480 + (i & 1) * 1024 + ks * 2048, st * 32768 + (4 * c.w + i) * 1024);
    else if (i < 6) dma16(c.swq, c.vwq, c.swqo + (i - 4) * 128 + ks * 524288, 65536 + st * 24576 + (i - 4) * 8192 + c.w * 1024);
    else dma16(c.sw1, c.vw1, c.sw1o + ks * (64 * 3456), 65536 + st * 24576 + 16384 + c.w * 1024);
}

struct P1Ctx { rsrc_t sx, sw, sws; unsigned vx[2], vw[2]; unsigned sxo, swo; int w; lcptr xb[2]; lcptr wb[4]; const bf16_t *wo, *w2, *wq, *w1; lcptr lds; };
DI void pack_fetch(const P1Ctx& c, int b) {
    const int l = lane_id(), gw = b * 8 + c.w, pu = gw / 10, pq = gw - pu * 10;
    const rsrc_t sp = mk_rsrc(pu < 128 ? c.wo : c.w2, pu < 128 ? 4096 * 640 * 2 : 1728 * 640 * 2);
    const unsigned so = gw < 1820 ? (unsigned)((pu < 128 ? pu : pu - 128) * 32 * 1280 + pq * 128) : 0xF0000000u;
#pragma unroll
    for (int i = 0; i < 4; ++i) dma16(sp, (l >> 3) * 1280 + (l & 7) * 16, so + i * 8 * 1280, 131072 + c.w * 4096 + i * 1024);
}
DI void pack_store(const P1Ctx& c, int b) {
    const int l = lane_id(), g = l >> 4, q = (l & 15) >> 2, gw = b * 8 + c.w, pu = gw / 10, pq = gw - pu * 10;
    lcptr p = c.lds + 131072 + c.w * 4096 + (4 * g + q) * 128 + (l & 3) * 8;
    bf16x8 fr[4];
#pragma unroll
    for (int i = 0; i < 4; ++i) fr[i] = cat(vtr(p + i * 32), vtr(p + i * 32 + 16 * 128));
    if (gw < 1820) {
#pragma unroll
        for (int i = 0; i < 4; ++i) st16_sc1(c.sws, __builtin_bit_cast(u32x4, fr[i]), l * 16, OFF_WP + (pu * 40 + pq * 4 + i) * 1024);
    }
}
DI void p1_dma1(const P1Ctx& c, int st, int ks, int i) {
    if (i < 4) dma16(c.sx, c.vx[i & 1], c.sxo + i * 8192 + ks * 128, st * 32768 + (4 * c.w + i) * 1024);
    else dma16(c.sw, c.vw[i & 1], c.swo + (i - 4) * 65536 + ks * 524288, 65536 + st * 32768 + (4 * c.w + (i - 4)) * 1024);
}
DI void p1_dma(const P1Ctx& c, int st, int ks) {
#pragma unroll
    for (int i = 0; i < 8; ++i) p1_dma1(c, st, ks, i);
}
template <int ST, int UP, int I> DI void p1_rdw(const P1Ctx& c, bf16x8 (&wf)[4], s16x4 (&wl)[4]) {
    constexpr int nt = I >> 1, rd = I & 1;
    const s16x4 v = vtr(c.wb[nt] + ST * 32768 + UP * 4096 + rd * 512);
    if constexpr (rd == 0) wl[nt] = v; else wf[nt] = cat(wl[nt], v);
}
template <int ST, int G> DI bf16x8 p1_rdx(const P1Ctx& c) { return ld128(c.xb[G >> 3] + ST * 32768 + (G & 7) * 2048); }
template <int ST, int KSC, int K> DI bool p1_is(int ks) { if constexpr (KSC >= 0) return KSC == K; else if constexpr ((K & 1) != ST || K > 5) return false; else return ks == K; }
template <bool ISV, int ST, int KSC, int G> DI void p1_group(const P1Ctx& c, const P2Ctx& c2, const Sync& y, const int ks, u64& seen, f32x4 (&acc)[4][8], bf16x8 (&wA)[4], bf16x8 (&wB)[4], s16x4 (&wl)[4], bf16x8 (&xr)[4]) {
    constexpr int up = G >> 3, mt = G & 7;
    constexpr bool NEXT = KSC != 7;
    if constexpr (G == 13) {
        if (p1_is<ST, KSC, 0>(ks)) asm volatile("s_waitcnt vmcnt(3) lgkmcnt(0)" ::: "memory");
        else if (p1_is<ST, KSC, 1>(ks) || p1_is<ST, KSC, 3>(ks)) asm volatile("s_waitcnt vmcnt(1) lgkmcnt(0)" ::: "memory");
        else if (p1_is<ST, KSC, 5>(ks) || p1_is<ST, KSC, 7>(ks)) asm volatile("s_waitcnt vmcnt(4) lgkmcnt(0)" ::: "memory");
        else asm volatile("s_waitcnt vmcnt(0) lgkmcnt(0)" ::: "memory");
        if constexpr (!ISV && KSC == 6) { if (y.w == 0) { const int l = lane_id() & 31; check_flags(y.flags, FL_P0 + (l < 16 ? 16 * y.j + l : 16 * (15 - y.j) + (l - 16)), seen, y.token); } }
        BARRIER();
        if constexpr (!ISV) {
            if (p1_is<ST, KSC, 1>(ks)) { if (y.w == 0 && lane_id() == 0) set_flag(y.flags, FL_P0 + y.b, y.token); }
        }
        SBAR();
    }
    const bf16x8 x = xr[G & 3];
#pragma unroll
    for (int nt = 0; nt < 4; ++nt) {
        const bf16x8 wv = up == 0 ? wA[nt] : wB[nt];
        if constexpr (ISV) acc[nt][mt] = MF16(x, wv, acc[nt][mt]); else acc[nt][mt] = MF16(wv, x, acc[nt][mt]);
        if (nt == 0) {
            if constexpr (G + 3 < 16) xr[(G + 3) & 3] = p1_rdx<ST, G + 3>(c);
            else if constexpr (NEXT) xr[(G + 3) & 3] = p1_rdx<ST ^ 1, G + 3 - 16>(c);
        }
        if constexpr (G >= 2 && G < 6) { if (nt == 1) p1_rdw<ST, 1, 2 * (G - 2)>(c, wB, wl); if (nt == 2) p1_rdw<ST, 1, 2 * (G - 2) + 1>(c, wB, wl); }
        if constexpr (G >= 13 && NEXT) {
            if (nt == 1) p1_rdw<ST ^ 1, 0, 3 * (G - 13)>(c, wA, wl);
            if (nt == 2) p1_rdw<ST ^ 1, 0, 3 * (G - 13) + 1>(c, wA, wl);
            if constexpr (G < 15) { if (nt == 3) p1_rdw<ST ^ 1, 0, 3 * (G - 13) + 2>(c, wA, wl); }
        }
        if (nt == SPOS) {
            if constexpr (G >= 13) { constexpr int p = G - 13;
                if constexpr (KSC < 0) p1_dma1(c, ST, ks + 2, p);
                else if constexpr (KSC == 6) p2_dma1(c2, 0, 0, p);
                else p2_dma1(c2, 1, 1, p);
            } else if constexpr (G <= 4) { constexpr int p = G + 3;
                if constexpr (KSC == 7) { if constexpr (p < 7) p2_dma1(c2, 0, 0, p); }
                else { if (!p1_is<ST, KSC, 0>(ks)) p1_dma1(c, ST ^ 1, ks + 1, p); }
            }
        }
        SBAR();
    }
    if constexpr (G == 6) {
        if (p1_is<ST, KSC, 1>(ks)) {
            const int l = lane_id(), L = (c.w & 3) * 64 + l;
            const char* pb = (L >= 160 && L < 240 && y.h < 27) ? (const char*)c.w1 + (size_t)(80 * y.j + L - 160) * 3456 + y.h * 128
                                                               : (const char*)c.wq + (size_t)(80 * y.j + ((L < 160 ? L : 0) >> 1)) * 8192 + y.h * 256 + (L & 1) * 128;
            touch(pb, 131072 + c.w * 256);
        }
        if (p1_is<ST, KSC, 3>(ks)) {
            const int l = lane_id(), gw = y.b * 8 + c.w < 1820 ? y.b * 8 + c.w : 0, pu = gw / 10, pq = gw - pu * 10;
            const char* pc = (const char*)(pu < 128 ? c.wo : c.w2) + (size_t)((pu < 128 ? pu : pu - 128) * 32 + (l & 31)) * 1280 + pq * 128;
            touch(pc, 131072 + c.w * 256);
        }
        if constexpr (!ISV && KSC == 6) { if (y.w == 0) { const int l = lane_id() & 31; seen = poll_flag(y.flags, FL_P0 + (l < 16 ? 16 * y.j + l : 16 * (15 - y.j) + (l - 16))); } }
        if (p1_is<ST, KSC, 5>(ks)) { pack_fetch(c, y.b); if constexpr (!ISV && !NOINV) { if (y.w == 0) acquire_issue(); } }
        if constexpr (KSC == 7) pack_store(c, y.b);
        SBAR();
    }
}
template <bool ISV, int ST, int KSC, int... Gs> DI void p1_step(const P1Ctx& c, const P2Ctx& c2, const Sync& y, const int ks, u64& seen, f32x4 (&acc)[4][8], bf16x8 (&wA)[4], bf16x8 (&wB)[4], s16x4 (&wl)[4], bf16x8 (&xr)[4], std::integer_sequence<int, Gs...>) {
    (p1_group<ISV, ST, KSC, Gs>(c, c2, y, ks, seen, acc, wA, wB, wl, xr), ...);
}
template <bool ISV> DI void p1_main(const P1Ctx& c, const P2Ctx& c2, const Sync& y, u32x4 (&kv)[16]) {
    f32x4 acc[4][8];
#pragma unroll
    for (int a = 0; a < 4; ++a)
#pragma unroll
        for (int b = 0; b < 8; ++b) acc[a][b] = (f32x4){0.f, 0.f, 0.f, 0.f};
    bf16x8 wA[4], wB[4], xr[4]; s16x4 wl[4];
    u64 seen = 0;
    asm volatile("" ::: "memory");
    p1_rdw<0, 0, 0>(c, wA, wl); p1_rdw<0, 0, 1>(c, wA, wl); p1_rdw<0, 0, 2>(c, wA, wl); p1_rdw<0, 0, 3>(c, wA, wl);
    p1_rdw<0, 0, 4>(c, wA, wl); p1_rdw<0, 0, 5>(c, wA, wl); p1_rdw<0, 0, 6>(c, wA, wl); p1_rdw<0, 0, 7>(c, wA, wl);
    xr[0] = p1_rdx<0, 0>(c); xr[1] = p1_rdx<0, 1>(c); xr[2] = p1_rdx<0, 2>(c);
    SBAR();
    constexpr auto seq = std::make_integer_sequence<int, 16>{};
#pragma nounroll
    for (int ks = 0; ks < 6; ks += 2) {
        p1_step<ISV, 0, -1>(c, c2, y, ks, seen, acc, wA, wB, wl, xr, seq);
        p1_step<ISV, 1, -1>(c, c2, y, ks + 1, seen, acc, wA, wB, wl, xr, seq);
    }
    p1_step<ISV, 0, 6>(c, c2, y, 6, seen, acc, wA, wB, wl, xr, seq);
    p1_step<ISV, 1, 7>(c, c2, y, 7, seen, acc, wA, wB, wl, xr, seq);
#pragma unroll
    for (int a = 0; a < 8; ++a)
#pragma unroll
        for (int b = 0; b < 2; ++b) {
            if constexpr (ISV) kv[a * 2 + b] = pack8(acc[(a & 1) * 2 + b][(a >> 1) * 2], acc[(a & 1) * 2 + b][(a >> 1) * 2 + 1]);
            else kv[a * 2 + b] = pack8(acc[2 * b][a], acc[2 * b + 1][a]);
        }
}

template <bool HASF, int ST, int I> DI bf16x8 p2_rdw(const P2Ctx& c) {
    constexpr int NW = HASF ? 6 : 4, up = I / NW, n = I % NW;
    lcptr p = (n < 4 ? c.wq[n & 3] : c.wf[n & 1]) + ST * 24576 + up * 4096;
    return cat(vtr(p), vtr(p + 512));
}
template <int ST, int UP, int MT> DI bf16x8 p2_rdx(const P2Ctx& c) { return ld128(c.xb + ST * 32768 + MT * 2048 + UP * 1024); }
template <int I> DI void p2_kvst(const P2Ctx& c, const u32x4 (&kv)[16], bool isv) {
    st16_sc1(c.sws, kv[I], c.vl16, c.skv + (isv ? (unsigned)(((I >> 3) * 16 + ((I >> 2) & 1) * 8 + (I & 3)) * 1024) : (unsigned)((I >> 1) * 4096 + (I & 1) * 1024)));
}
template <bool HASF, int IV> DI void p2_extras(const P2Ctx& c, const u32x4 (&kv)[16], bool isv) {
    if constexpr (IV < 6) { p2_kvst<4 + 2 * IV>(c, kv, isv); p2_kvst<5 + 2 * IV>(c, kv, isv); }
}
struct P2Bias { u32x2 a, b; const bf16_t* p; };
template <bool HASF, int KS, int G> DI void p2_group(const P2Ctx& c, const Sync& y, P2Bias& bs, u64& seen, f32x4 (&acc)[6][4], bf16x8 (&wr)[4], bf16x8 (&x0)[4], bf16x8 (&x1)[4], const u32x4 (&kv)[16], bool isv) {
    constexpr int NW = HASF ? 6 : 4, NG = 2 * NW, ST = KS & 1, up = G / NW, n = G % NW;
    constexpr int NP = HASF ? 7 : 6, BD0 = HASF ? 2 : 0;
    constexpr bool LAST = (KS == 9);
    if constexpr (G == NG - 3) {
        if constexpr (KS == 0 && !SPREAD) asm volatile("s_waitcnt vmcnt(4) lgkmcnt(0)" ::: "memory");
        else if constexpr (KS == 0) asm volatile("s_waitcnt vmcnt(0) lgkmcnt(0)" ::: "memory");
        else if constexpr (HASF && KS == 6) asm volatile("s_waitcnt vmcnt(4) lgkmcnt(0)" ::: "memory");
        else if constexpr (KS <= 6) asm volatile("s_waitcnt vmcnt(2) lgkmcnt(0)" ::: "memory");
        else asm volatile("s_waitcnt vmcnt(0) lgkmcnt(0)" ::: "memory");
        if constexpr (KS == 9) { if (y.w == 0) check_flags(y.flags, FL_KV + y.h * 8 + (lane_id() & 7), seen, y.token); }
        BARRIER();
        if constexpr (KS == (KVR == 3 ? 5 : 7)) { if (y.w == 0 && lane_id() == 0) set_flag(y.flags, FL_KV + y.h * 8 + y.j, y.token); }
        if constexpr (KS == 8) { if (y.w == 0) seen = poll_flag(y.flags, FL_KV + y.h * 8 + (lane_id() & 7)); }
        SBAR();
    }
    const bf16x8 wcur = wr[G & 3];
#pragma unroll
    for (int mt = 0; mt < 4; ++mt) {
        acc[n][mt] = MF16(wcur, up == 0 ? x0[mt] : x1[mt], acc[n][mt]);
        if (mt == 0) {
            if constexpr (G + 3 < NG) wr[(G + 3) & 3] = p2_rdw<HASF, ST, G + 3>(c);
            else if constexpr (!LAST) wr[(G + 3) & 3] = p2_rdw<HASF, ST ^ 1, G + 3 - NG>(c);
        }
        if (mt == 1) {
            if constexpr (G < 4) x1[G] = p2_rdx<ST, 1, G>(c);
            else if constexpr (G >= NG - 3 && !LAST) x0[G - (NG - 3)] = p2_rdx<ST ^ 1, 0, G - (NG - 3)>(c);
        }
        if (mt == 2) { if constexpr (G == NG - 1 && !LAST) x0[3] = p2_rdx<ST ^ 1, 0, 3>(c); }
        if (mt == SPOS) {
            if constexpr (G >= NG - 3) { if constexpr (KS + 2 < 10) p2_dma1(c, ST, KS + 2, G - (NG - 3)); }
            else if constexpr (G + 3 < NP) { if constexpr (KS + 1 < 10) p2_dma1(c, ST ^ 1, KS + 1, G + 3); }
        }
        if constexpr (LAST && G >= NG - 3) {
            constexpr int p0 = (G - (NG - 3)) * 4;
            if (p0 + mt < 4) dma16h(c.sws, c.vl16, c.t0s + (p0 + mt) * 1024, c.t0l + (p0 + mt) * 1024);
            else if (p0 + mt < 8) { if (c.w < 4) dma16h(c.sws, c.vl16, c.t1s + (p0 + mt - 4) * 1024, 65536 + c.t0l + (p0 + mt - 4) * 1024); }
        }
        SBAR();
    }
    if constexpr (G == NP - 3 && KS >= 1) { p2_extras<HASF, KS - 1>(c, kv, isv); SBAR(); }
    if constexpr (HASF && KS == 6 && G == 5) { asm volatile("global_load_dwordx2 %0, %2, off\n\tglobal_load_dwordx2 %1, %2, off offset:32" : "=&v"(bs.a), "=&v"(bs.b) : "v"(bs.p) : "memory"); SBAR(); }
}
template <bool HASF, int KS, int... Gs> DI void p2_step(const P2Ctx& c, const Sync& y, P2Bias& bs, u64& seen, f32x4 (&acc)[6][4], bf16x8 (&wr)[4], bf16x8 (&x0)[4], bf16x8 (&x1)[4], const u32x4 (&kv)[16], bool isv, std::integer_sequence<int, Gs...>) {
    (p2_group<HASF, KS, Gs>(c, y, bs, seen, acc, wr, x0, x1, kv, isv), ...);
}
template <bool HASF, int... KSs> DI void p2_main(const P2Ctx& c, const Sync& y, P2Bias& bs, f32x4 (&acc)[6][4], const u32x4 (&kv)[16], bool isv, std::integer_sequence<int, KSs...>) {
    bf16x8 wr[4], x0[4], x1[4];
    u64 seen = 0;
    asm volatile("" ::: "memory");
    wr[0] = p2_rdw<HASF, 0, 0>(c); wr[1] = p2_rdw<HASF, 0, 1>(c); wr[2] = p2_rdw<HASF, 0, 2>(c);
    x0[0] = p2_rdx<0, 0, 0>(c); x0[1] = p2_rdx<0, 0, 1>(c); x0[2] = p2_rdx<0, 0, 2>(c); x0[3] = p2_rdx<0, 0, 3>(c);
    SBAR();
    (p2_step<HASF, KSs>(c, y, bs, seen, acc, wr, x0, x1, kv, isv, std::make_integer_sequence<int, (HASF ? 12 : 8)>{}), ...);
}
DI float mish(float h) {
    const float t = __builtin_amdgcn_exp2f(__builtin_fminf(h, 20.f) * 1.4426950408889634f);
    const float n = t * (t + 2.f);
    return h * n * __builtin_amdgcn_rcpf(n + 2.f);
}

constexpr int PD = 8;
#define PRIOM 1
#define PRIOV 0
#define ADDMODE 2
#define ABL_NOEARLY 0
#define APRE_EARLY 1
#define LATE_STEP 7
#define LATE_OFF 2
struct AttnRegs { f32x4 o[8][2]; f32x4 ls[2]; f32x4 s[4][2]; bf16x8 q[2][4]; u32x4 pk[2][2]; f32x4 negm[2]; bf16x8 ones; bf16x8 op[32]; };
template <int I> DI void a_read(AttnRegs& r, lcptr vp, lcptr kp) {
    if constexpr (I < 16) r.op[I] = ld128(vp + I * 1024);
    else if constexpr (I < 32) r.op[I] = ld128(kp + (((I - 16) & 3) * 4 + ((I - 16) >> 2)) * 1024);
}
struct Piece { rsrc_t sws; unsigned vl16, soff, ldso; bool on; };
template <int P, bool COND> DI void piece(const Piece& d) {
    if ((!COND && !ABL_NOEARLY) || (COND && d.on)) asm volatile("s_mov_b32 m0, %0\n\ts_nop 3\n\tbuffer_load_dwordx4 %1, %2, %3 offen offset:%4" HS " lds" ::"s"(d.ldso), "v"(d.vl16), "s"(d.sws), "s"(d.soff), "i"(P * 1024) : "memory");
}
template <int I, int DMA> DI void a_slot(AttnRegs& r, lcptr vp, lcptr kp, const Piece& late) {
    if constexpr (I < 16) { constexpr int ks = I >> 3, dt = I & 7;
        r.o[dt][0] = MF16(r.op[I], asbf(r.pk[0][ks]), r.o[dt][0]); r.o[dt][1] = MF16(r.op[I], asbf(r.pk[1][ks]), r.o[dt][1]);
        if constexpr (dt == 3) { r.ls[0] = MF16(r.ones, asbf(r.pk[0][ks]), r.ls[0]); r.ls[1] = MF16(r.ones, asbf(r.pk[1][ks]), r.ls[1]); } }
    else { constexpr int u = (I - 16) >> 2, kt = (I - 16) & 3;
        r.s[kt][0] = MF16(r.op[I], r.q[0][u], u == 0 ? r.negm[0] : r.s[kt][0]); r.s[kt][1] = MF16(r.op[I], r.q[1][u], u == 0 ? r.negm[1] : r.s[kt][1]); }
    a_read<I + PD>(r, vp, kp);
    SBAR();
    if constexpr (DMA != 0 && (I % LATE_STEP) == LATE_OFF && I / LATE_STEP < 4) { piece<I / LATE_STEP, DMA == 2>(late); SBAR(); }
}
template <int B, int DMA, int... Is> DI void a_run(AttnRegs& r, lcptr vp, lcptr kp, const Piece& late, std::integer_sequence<int, Is...>) { (a_slot<B + Is, DMA>(r, vp, kp, late), ...); }
template <int B, int... Is> DI void a_pre(AttnRegs& r, lcptr vp, lcptr kp, std::integer_sequence<int, Is...>) { (a_read<B + Is>(r, vp, kp), ...); }

DI unsigned pkmax(unsigned a, unsigned b) { return __builtin_bit_cast(unsigned, __builtin_elementwise_max(__builtin_bit_cast(u16x2, a), __builtin_bit_cast(u16x2, b))); }
DI unsigned hmax(unsigned v) { const unsigned lo = v & 0xFFFFu, hi = v >> 16; return lo > hi ? lo : hi; }
template <int Q> DI void a_exp_quarter(AttnRegs& r, unsigned& x0, unsigned& x1) {
    constexpr int ks = Q >> 1, qt = Q & 1;
    float e[8];
#pragma unroll
    for (int i = 0; i < 4; ++i) { e[i] = __builtin_amdgcn_exp2f(r.s[2 * ks][qt][i]); e[4 + i] = __builtin_amdgcn_exp2f(r.s[2 * ks + 1][qt][i]); }
    const u32x4 p = {cvtpk(e[0], e[1]), cvtpk(e[2], e[3]), cvtpk(e[4], e[5]), cvtpk(e[6], e[7])};
    r.pk[qt][ks] = p;
    const unsigned m = pkmax(pkmax(p[0], p[1]), pkmax(p[2], p[3]));
    if constexpr (Q == 0) x0 = m; else if constexpr (Q == 1) x1 = m; else if constexpr (qt == 0) x0 = pkmax(x0, m); else x1 = pkmax(x1, m);
}
template <bool DMA> DI void a_exp_pack(AttnRegs& r, unsigned& x0, unsigned& x1, const Piece& early) {
    if constexpr (DMA) { piece<0, false>(early); SBAR(); }
    a_exp_quarter<0>(r, x0, x1);
    if constexpr (DMA) { SBAR(); piece<1, false>(early); SBAR(); }
    a_exp_quarter<1>(r, x0, x1);
    if constexpr (DMA) { SBAR(); piece<2, false>(early); SBAR(); }
    a_exp_quarter<2>(r, x0, x1);
    if constexpr (DMA) { SBAR(); piece<3, false>(early); SBAR(); }
    a_exp_quarter<3>(r, x0, x1);
}
DI float xmax4(float v) {
    const u32x2 a = __builtin_amdgcn_permlane16_swap(__float_as_uint(v), __float_as_uint(v), false, false);
    v = __builtin_fmaxf(__uint_as_float(a[0]), __uint_as_float(a[1]));
    const u32x2 c = __builtin_amdgcn_permlane32_swap(__float_as_uint(v), __float_as_uint(v), false, false);
    return __builtin_fmaxf(__uint_as_float(c[0]), __uint_as_float(c[1]));
}
DI float xsum4(float v) {
    const u32x2 a = __builtin_amdgcn_permlane16_swap(__float_as_uint(v), __float_as_uint(v), false, false);
    v = __uint_as_float(a[0]) + __uint_as_float(a[1]);
    const u32x2 c = __builtin_amdgcn_permlane32_swap(__float_as_uint(v), __float_as_uint(v), false, false);
    return __uint_as_float(c[0]) + __uint_as_float(c[1]);
}
constexpr unsigned P_HI = 0x5D80u, P_LO = 0x2180u;

struct AttnCtx { rsrc_t sws; unsigned vl16, kbase, vbase, ctxl; int nL, grp, qg, j, rowrel, swap_s, first_s; lcptr kl, vl, ql; };
struct AttnSm { float m0, m1; };
DI void a_dmaK(const AttnCtx& c, int n) { dma16x4(c.sws, c.vl16, c.kbase + n * 16384 + c.qg * 4096, (n & 3) * 16384 + c.qg * 4096); }
DI void a_dmaV(const AttnCtx& c, int n) { dma16x4(c.sws, c.vl16, c.vbase + n * 16384 + c.qg * 4096, 65536 + (n & 3) * 16384 + c.qg * 4096); }
#define PT(i) do { } while (0)
#define PARG
#define PPASS
#define PPASSG
DI void a_slotfull(const AttnCtx& c, const Sync& y, AttnRegs& r, AttnSm& z, const int s PARG) {
    const int nL = c.nL, grp = c.grp;
    const bool SWAP = (s == c.swap_s), FIN = (s == c.swap_s + 1), FIRST = (s == c.first_s) || FIN;
    const int tcur = s < nL ? s : 2 * s - nL + grp;
    Piece early, late;
    {
        const int s2 = s + 2, s1 = s + 1;
        const bool eK = grp == 1 && s2 < nL;
        const int te = eK ? s2 : (s1 < nL ? s1 : 2 * s1 - nL + grp), tl = 2 * s2 - nL + grp;
        early.sws = c.sws; early.vl16 = c.vl16; late.sws = c.sws; late.vl16 = c.vl16;
        early.on = eK || (s1 <= 16 && (grp == 0 || s1 >= nL));
        early.soff = (eK ? c.kbase : c.vbase) + te * 16384 + c.qg * 4096; early.ldso = (eK ? 0 : 65536) + (te & 3) * 16384 + c.qg * 4096;
        late.on = s2 <= 16 && s2 >= nL;
        late.soff = c.kbase + tl * 16384 + c.qg * 4096; late.ldso = (tl & 3) * 16384 + c.qg * 4096;
    }
    const int snext = s + 1, tnext = snext < nL ? snext : 2 * snext - nL + grp;
    const lcptr vp = c.vl + (tcur & 3) * 16384, kp = c.kl + (tnext & 3) * 16384;
    u64 seenA = 0;
    if (s == 16) seenA = poll_asm(y.flags, FL_ACT + 32 * y.w + (lane_id() & 31));
    a_pre<0>(r, vp, kp, std::make_integer_sequence<int, PD>{});
    SBAR();
    if (SWAP) {
        asm volatile("ds_read_b128 %0, %8\n\tds_read_b128 %1, %8 offset:1024\n\tds_read_b128 %2, %8 offset:2048\n\tds_read_b128 %3, %8 offset:3072\n\t"
                     "ds_read_b128 %4, %8 offset:4096\n\tds_read_b128 %5, %8 offset:5120\n\tds_read_b128 %6, %8 offset:6144\n\tds_read_b128 %7, %8 offset:7168\n\ts_waitcnt lgkmcnt(0)"
                     : "=&v"(r.q[0][0]), "=&v"(r.q[0][1]), "=&v"(r.q[0][2]), "=&v"(r.q[0][3]), "=&v"(r.q[1][0]), "=&v"(r.q[1][1]), "=&v"(r.q[1][2]), "=&v"(r.q[1][3])
                     : "v"(c.ql) : "memory");
    }
    if (FIN) {
        const float i0 = __builtin_amdgcn_rcpf(r.ls[0][0]), i1 = __builtin_amdgcn_rcpf(r.ls[1][0]);
        const unsigned c1o = c.ctxl + 131072;
        st16_sc1o<0>(c.sws, pack8(r.o[0][0] * i0, r.o[1][0] * i0), c.vl16, c.ctxl); st16_sc1o<1024>(c.sws, pack8(r.o[2][0] * i0, r.o[3][0] * i0), c.vl16, c.ctxl);
        st16_sc1o<2048>(c.sws, pack8(r.o[4][0] * i0, r.o[5][0] * i0), c.vl16, c.ctxl); st16_sc1o<3072>(c.sws, pack8(r.o[6][0] * i0, r.o[7][0] * i0), c.vl16, c.ctxl);
        st16_sc1o<0>(c.sws, pack8(r.o[0][1] * i1, r.o[1][1] * i1), c.vl16, c1o); st16_sc1o<1024>(c.sws, pack8(r.o[2][1] * i1, r.o[3][1] * i1), c.vl16, c1o);
        st16_sc1o<2048>(c.sws, pack8(r.o[4][1] * i1, r.o[5][1] * i1), c.vl16, c1o); st16_sc1o<3072>(c.sws, pack8(r.o[6][1] * i1, r.o[7][1] * i1), c.vl16, c1o);
#pragma unroll
        for (int d = 0; d < 8; ++d) { r.o[d][0] = (f32x4){0.f, 0.f, 0.f, 0.f}; r.o[d][1] = (f32x4){0.f, 0.f, 0.f, 0.f}; }
        r.ls[0] = (f32x4){0.f, 0.f, 0.f, 0.f}; r.ls[1] = (f32x4){0.f, 0.f, 0.f, 0.f};
    }
    {
        const bool heavy = (grp == 1) || (s >= nL);
        const int qb = heavy ? 15 - c.j : c.j, kb = 64 * tcur - 128 * qb - 32 * c.qg;
        if (kb >= -32) {
            const float NI = -__builtin_inff();
            const f32x4 ninf = {NI, NI, NI, NI};
            f32x4 s00 = r.s[0][0], s10 = r.s[1][0], s20 = r.s[2][0], s30 = r.s[3][0], s01 = r.s[0][1], s11 = r.s[1][1], s21 = r.s[2][1], s31 = r.s[3][1];
            const int dq = c.rowrel;
            if (kb > 0) { s00 = ninf; s10 = ninf; s20 = ninf; s30 = ninf; s01 = ninf; s11 = ninf; s21 = ninf; s31 = ninf; }
            else if (kb == 0) {
#pragma unroll
                for (int i = 0; i < 4; ++i) { s00[i] = sel(i > dq, NI, s00[i]); s11[i] = sel(i > dq, NI, s11[i]); }
                s10 = ninf; s20 = ninf; s30 = ninf; s21 = ninf; s31 = ninf;
            } else {
#pragma unroll
                for (int i = 0; i < 4; ++i) { s20[i] = sel(i > dq, NI, s20[i]); s31[i] = sel(i > dq, NI, s31[i]); }
                s30 = ninf;
            }
            r.s[0][0] = s00; r.s[1][0] = s10; r.s[2][0] = s20; r.s[3][0] = s30; r.s[0][1] = s01; r.s[1][1] = s11; r.s[2][1] = s21; r.s[3][1] = s31;
        }
    }
    unsigned a0, a1;
    if (early.on) dma16x4(c.sws, c.vl16, early.soff, early.ldso);
    a_exp_pack<false>(r, a0, a1, early);
    a0 = hmax(a0); a1 = hmax(a1);
    const unsigned lo = FIRST ? P_LO : 0u;
    if (__builtin_expect(__any(a0 > P_HI || a1 > P_HI || a0 < lo || a1 < lo), 0)) {
        float x0 = r.s[0][0][0], x1 = r.s[0][1][0];
#pragma unroll
        for (int kt = 0; kt < 4; ++kt)
#pragma unroll
            for (int i = 0; i < 4; ++i) { x0 = __builtin_fmaxf(x0, r.s[kt][0][i]); x1 = __builtin_fmaxf(x1, r.s[kt][1][i]); }
        x0 = xmax4(x0); x1 = xmax4(x1);
        if (!FIRST) { x0 = __builtin_fmaxf(x0, 0.f); x1 = __builtin_fmaxf(x1, 0.f);
            const float f0 = __builtin_amdgcn_exp2f(-x0), f1 = __builtin_amdgcn_exp2f(-x1);
#pragma unroll
            for (int d = 0; d < 8; ++d) { r.o[d][0] *= f0; r.o[d][1] *= f1; }
            r.ls[0] *= f0; r.ls[1] *= f1; }
        z.m0 += x0; z.m1 += x1;
        r.negm[0] = (f32x4){-z.m0, -z.m0, -z.m0, -z.m0}; r.negm[1] = (f32x4){-z.m1, -z.m1, -z.m1, -z.m1};
#pragma unroll
        for (int kt = 0; kt < 4; ++kt) { r.s[kt][0] -= x0; r.s[kt][1] -= x1; }
        a_exp_pack<false>(r, a0, a1, early);
    }
    if (SWAP) {
        z.m0 = 0.f; z.m1 = 0.f;
        r.negm[0] = (f32x4){0.f, 0.f, 0.f, 0.f}; r.negm[1] = (f32x4){0.f, 0.f, 0.f, 0.f};
    }
    SBAR();
    PT(0);
    if (FIN) { if (early.on) WAITV(12); else WAITV(8); } else { if (early.on) WAITV(4); else WAITV(0); }
    if (s == 16) { asm volatile("" : "+v"(seenA)); check_flags(y.flags, FL_ACT + 32 * y.w + (lane_id() & 31), seenA, y.token); }
    PT(1);
    BARRIER(); SBAR();
    PT(2);
    __builtin_amdgcn_s_setprio(PRIOM);
    a_run<0, 2>(r, vp, kp, late, std::make_integer_sequence<int, 16>{});
    if (s < 16) a_run<16, 2>(r, vp, kp, late, std::make_integer_sequence<int, 16>{});
    __builtin_amdgcn_s_setprio(PRIOV);
    SBAR();
    PT(3);
    if (late.on) WAITV(4); else WAITV(0);
    PT(4);
    BARRIER(); SBAR();
    PT(5);
}

struct Lean { unsigned e_soff, e_lds, l_soff, l_lds, voff, koff; };
template <bool LATE> DI void a_slot_lean(const AttnCtx& c, AttnRegs& r, AttnSm& z, const Lean& st PARG) {
    const lcptr vp = c.vl + st.voff, kp = c.kl + st.koff;
    Piece early, late;
    early.sws = c.sws; early.vl16 = c.vl16; early.soff = st.e_soff; early.ldso = st.e_lds; early.on = true;
    late.sws = c.sws; late.vl16 = c.vl16; late.soff = st.l_soff; late.ldso = st.l_lds; late.on = true;
    a_pre<0>(r, vp, kp, std::make_integer_sequence<int, PD>{});
    SBAR();
    unsigned a0, a1;
    a_exp_pack<true>(r, a0, a1, early);
    if (__builtin_expect(__any(hmax(pkmax(a0, a1)) > P_HI), 0)) {
        float x0 = 0.f, x1 = 0.f;
#pragma unroll
        for (int kt = 0; kt < 4; ++kt)
#pragma unroll
            for (int i = 0; i < 4; ++i) { x0 = __builtin_fmaxf(x0, r.s[kt][0][i]); x1 = __builtin_fmaxf(x1, r.s[kt][1][i]); }
        x0 = xmax4(x0); x1 = xmax4(x1);
        const float f0 = __builtin_amdgcn_exp2f(-x0), f1 = __builtin_amdgcn_exp2f(-x1);
#pragma unroll
        for (int d = 0; d < 8; ++d) { r.o[d][0] *= f0; r.o[d][1] *= f1; }
        r.ls[0] *= f0; r.ls[1] *= f1;
        z.m0 += x0; z.m1 += x1;
        r.negm[0] = (f32x4){-z.m0, -z.m0, -z.m0, -z.m0}; r.negm[1] = (f32x4){-z.m1, -z.m1, -z.m1, -z.m1};
#pragma unroll
        for (int kt = 0; kt < 4; ++kt) { r.s[kt][0] -= x0; r.s[kt][1] -= x1; }
        a_exp_pack<false>(r, a0, a1, early);
    }
    SBAR();
    PT(0);
    WAITV(4);
    PT(1);
    BARRIER(); SBAR();
    PT(2);
    __builtin_amdgcn_s_setprio(PRIOM);
    a_run<0, LATE ? 1 : 0>(r, vp, kp, late, std::make_integer_sequence<int, 32>{});
    __builtin_amdgcn_s_setprio(PRIOV);
    SBAR();
    PT(3);
    if constexpr (LATE) WAITV(4); else WAITV(0);
    PT(4);
    BARRIER(); SBAR();
    PT(5);
}

#define P4W0 11
constexpr unsigned P4STG = 36864, P4TAG = 0x3FFu, P4OOB = 0xF0000000u;
struct P4Ctx { rsrc_t sws; unsigned vl16, ga, gw, gas, gws, dw0, dw1, dw2, lw, tag, so[3], so2[3], lo[3], lo2[3]; int w, q, o, ng, ra, rw; lcptr pa, pw; };
struct P4Regs { bf16x8 xa[2][2], wa[2][5]; };
struct P4Xch { f32x4 own[3]; u32x4 snd[9], rcv[9]; };
DI void mfa(f32x4& acc, bf16x8 a, bf16x8 b) { asm volatile("v_mfma_f32_16x16x32_bf16 %0, %1, %2, %0" : "+v"(acc) : "v"(a), "v"(b)); }
DI void mfz(f32x4& acc, bf16x8 a, bf16x8 b) { asm volatile("v_mfma_f32_16x16x32_bf16 %0, %1, %2, 0" : "=&v"(acc) : "v"(a), "v"(b)); }
template <int OFF> DI void dma_sc1(const P4Ctx& c, unsigned soff, unsigned ldso) {
    asm volatile("s_mov_b32 m0, %0\n\ts_nop 3\n\tbuffer_load_dwordx4 %1, %2, %3 offen offset:%4 sc1 lds" ::"s"(ldso), "v"(c.vl16), "s"(c.sws), "s"(soff), "i"(OFF) : "memory");
}
template <int P> DI void p4_piece(P4Ctx& c, unsigned st) {
    if constexpr (P == 0) dma_sc1<0>(c, c.ga, st + c.w * 2048);
    else if constexpr (P == 1) { dma_sc1<1024>(c, c.ga, st + c.w * 2048); ++c.ra; c.ga = c.ra == c.ng ? c.gas : c.ga + 2048; }
    else if constexpr (P == 2) dma_sc1<0>(c, c.gw + c.dw0, st + c.lw);
    else if constexpr (P == 3) dma_sc1<0>(c, c.gw + c.dw1, st + c.lw + 8192);
    else { if (c.w < 4) dma_sc1<0>(c, c.gw + c.dw2, st + c.lw + 16384); ++c.rw; c.gw = c.rw == c.ng ? c.gws : c.gw + 81920; }
}
template <int B, int UP, int I> DI void p4_rd(const P4Ctx& c, P4Regs& r, unsigned st) {
    if constexpr (I == 0) r.wa[B][0] = ld128(c.pw + st + UP * 10240);
    else if constexpr (I <= 2) r.xa[B][I - 1] = ld128(c.pa + st + (I - 1) * 2048 + UP * 1024);
    else if constexpr (I < 7) r.wa[B][I - 2] = ld128(c.pw + st + UP * 10240 + (I - 2) * 1024);
}
template <int N> DI void p4_put(const P4Ctx& c, const P4Xch& x, unsigned xoff) { st16_sc1o<(N % 3) * 1024>(c.sws, x.snd[N], c.vl16, (N % 3 == 2 ? c.so2[N / 3] : c.so[N / 3]) + xoff); }
template <int N> DI void p4_get(const P4Ctx& c, P4Xch& x, unsigned xoff) {
    asm volatile("buffer_load_dwordx4 %0, %1, %2, %3 offen offset:%4 sc1" : "=v"(x.rcv[N]) : "v"(c.vl16), "s"(c.sws), "s"((N % 3 == 2 ? c.lo2[N / 3] : c.lo[N / 3]) + xoff), "i"((N % 3) * 1024) : "memory");
}
template <int O> DI void p4_split(const f32x4 (&acc)[10], unsigned tag, P4Xch& x) {
#pragma unroll
    for (int k = 0; k < 3; ++k) x.own[k] = acc[O + 4 * k < 10 ? O + 4 * k : O];
#pragma unroll
    for (int n = 0; n < 9; ++n) { const int i = ((O + 1 + n / 3) & 3) + 4 * (n % 3);
        const u32x4 v = __builtin_bit_cast(u32x4, acc[i < 10 ? i : 0]);
#pragma unroll
        for (int e = 0; e < 4; ++e) x.snd[n][e] = (v[e] & ~P4TAG) | tag; }
}
DI void p4_split(const P4Ctx& c, f32x4 (&a)[10], P4Xch& x) {
    asm volatile("s_nop 7\n\ts_nop 7\n\ts_nop 7" : "+v"(a[0]), "+v"(a[1]), "+v"(a[2]), "+v"(a[3]), "+v"(a[4]), "+v"(a[5]), "+v"(a[6]), "+v"(a[7]), "+v"(a[8]), "+v"(a[9]) :: "memory");
    switch (c.o) { case 0: p4_split<0>(a, c.tag, x); break; case 1: p4_split<1>(a, c.tag, x); break; case 2: p4_split<2>(a, c.tag, x); break; default: p4_split<3>(a, c.tag, x); break; }
}
DI bool p4_tagged(const P4Ctx& c, const P4Xch& x) {
    unsigned a = 0, b = 0;
#pragma unroll
    for (int n = 0; n < 9; ++n)
#pragma unroll
        for (int e = 0; e < 4; ++e) { if (n % 3 == 2) b |= x.rcv[n][e] ^ c.tag; else a |= x.rcv[n][e] ^ c.tag; }
    return __all(((a | (c.o < 2 ? b : 0u)) & P4TAG) == 0);
}
template <int... Ns> DI void p4_refetch(const P4Ctx& c, P4Xch& x, unsigned xoff, std::integer_sequence<int, Ns...>) {
    const unsigned long long t0 = __builtin_amdgcn_s_memrealtime();
    for (;;) {
        (p4_get<Ns>(c, x, xoff), ...);
        asm volatile("s_waitcnt vmcnt(0)" : "+v"(x.rcv[0]), "+v"(x.rcv[1]), "+v"(x.rcv[2]), "+v"(x.rcv[3]), "+v"(x.rcv[4]), "+v"(x.rcv[5]), "+v"(x.rcv[6]), "+v"(x.rcv[7]), "+v"(x.rcv[8]) :: "memory");
        if (p4_tagged(c, x)) break;
        if (__builtin_amdgcn_s_memrealtime() - t0 > 2000000ull) break;
        __builtin_amdgcn_s_sleep(1);
    }
}
DI f32x4 p4_untag(u32x4 v) { return __builtin_bit_cast(f32x4, v & ~P4TAG); }
DI f32x4 p4_total(const P4Xch& x, int k) { return (x.own[k] + p4_untag(x.rcv[k])) + (p4_untag(x.rcv[3 + k]) + p4_untag(x.rcv[6 + k])); }
template <int B, bool ZC, bool RD, int DMA, int PUT, int GET, int... Is> DI void p4_half(P4Ctx& c, P4Regs& r, P4Xch& x, f32x4 (&acc)[10], unsigned rs, unsigned ds, std::integer_sequence<int, Is...>) {
    auto one = [&](auto ic) {
        constexpr int I = decltype(ic)::value;
        if constexpr (ZC) mfz(acc[I], r.wa[B][I >> 1], r.xa[B][I & 1]); else mfa(acc[I], r.wa[B][I >> 1], r.xa[B][I & 1]);
        if constexpr (RD) p4_rd<B ^ 1, B ^ 1, I>(c, r, rs);
        if constexpr (DMA == 1) { if constexpr (I == 1) p4_piece<0>(c, ds); if constexpr (I == 5) p4_piece<1>(c, ds); if constexpr (I == 9) p4_piece<2>(c, ds); }
        if constexpr (DMA == 2) { if constexpr (I == 3) p4_piece<3>(c, ds); if constexpr (I == 7) p4_piece<4>(c, ds); }
        if constexpr (PUT >= 0 && B == 0 && I == 5) p4_put<PUT>(c, x, OFF_XG);
        if constexpr (GET >= 0 && (I & 1) && GET + (I >> 1) < 9) p4_get<GET + (I >> 1)>(c, x, OFF_XG);
        SBAR();
    };
    (one(std::integral_constant<int, Is>{}), ...);
}
template <int WAITN, bool RA, bool RB, bool LAST, int EV, int PUT, int GETA, int GETB, bool ZC = false> DI void p4_step(P4Ctx& c, const Sync& y, P4Regs& r, P4Xch& x, f32x4 (&acc)[10], unsigned& sb, unsigned& sp, int t, unsigned fidx, u64& seen) {
    constexpr auto seq = std::make_integer_sequence<int, 10>{};
    const unsigned sn = sb == 3 * P4STG ? 0 : sb + P4STG;
    p4_half<0, ZC, true, RA ? 2 : 0, PUT, GETA>(c, r, x, acc, sb, sp, seq);
    if constexpr (!LAST) {
        asm volatile("s_waitcnt vmcnt(%0) lgkmcnt(0)" :: "i"(WAITN) : "memory");
        if constexpr (EV == 1) { if (t == c.ng - 4 && c.w == 0) { asm volatile("s_waitcnt vmcnt(4)" : "+v"(seen) :: "memory"); check_flags(y.flags, fidx, seen, y.token); } }
        BARRIER();
        if constexpr (EV == 1) {
            if (c.w == 0 && t == c.ng - 5) seen = poll_asm(y.flags, fidx);
        }
        if constexpr (EV == 2) { if (c.w == 0 && lane_id() == 0) set_flag(y.flags, FL_CTX + y.j * 32 + y.h, y.token); }
        SBAR();
    }
    p4_half<1, false, !LAST, RB ? 1 : 0, PUT, GETB>(c, r, x, acc, sn, sb, seq);
    sp = sb; sb = sn;
}

template <int... Is> DI void p4_ssteps(P4Ctx& c, const Sync& y, P4Regs& r, P4Xch& x, f32x4 (&acc)[10], unsigned& sb, unsigned& sp, u64& seen, std::integer_sequence<int, Is...>) {
    (p4_step<8 + (Is >= 2 && Is <= 10) + (Is >= 1 && Is <= 9) + (Is <= 8), true, true, false, 0, (Is <= 8 ? Is : -1), -1, -1, Is == 0>(c, y, r, x, acc, sb, sp, 0, 0, seen), ...);
}

__global__ __launch_bounds__(512, 2) void fused_kernel(const bf16_t* __restrict__ x, const bf16_t* __restrict__ ckv, const bf16_t* __restrict__ wq, const bf16_t* __restrict__ wuk,
                                                       const bf16_t* __restrict__ wuv, const bf16_t* __restrict__ wo, const bf16_t* __restrict__ w1, const bf16_t* __restrict__ b1,
                                                       const bf16_t* __restrict__ w2, bf16_t* __restrict__ out, char* __restrict__ ws) {
    __shared__ __attribute__((aligned(1024))) char smem[163840];
    {
        u64 pc; unsigned off;
        asm volatile("s_getpc_b64 %0\n.Lcode_a:\n\ts_mov_b32 m0, 0x20000\n\tv_lshlrev_b32 %1, 3, %2\n\tv_lshl_add_u32 %1, %3, 12, %1\n\tv_min_u32 %1, .Lcode_z-.Lcode_a-4, %1\n\t"
                     "global_load_lds_dword %1, %0" : "=&s"(pc), "=&v"(off) : "v"(threadIdx.x), "s"(blockIdx.x >> 3) : "memory");
        SBAR();
    }
    const lptr lds = (lptr)smem;
    const int b = blockIdx.x, xcd = b & 7, bi = b >> 3, h = xcd * 4 + (bi >> 3), j = bi & 7;
    const int w = __builtin_amdgcn_readfirstlane(threadIdx.x >> 6);
    STAMP(0);
    const rsrc_t sws = mk_rsrc(ws, WS_BYTES);
    Sync y;
    y.flags = (gu64*)(ws + OFF_FLAGS); y.b = b; y.h = h; y.j = j; y.w = w;
    u64 epoch;
    asm volatile("s_load_dwordx2 %0, %1, 0x20" : "=s"(epoch) : "s"(ws) : "memory");
    u32x4 kv[16];
    P1Ctx c1;
    P2Ctx c2;
    const bool isv = (w & 3) >= 2;
    {
        const int l = lane_id(), g = l >> 4, q = (l & 15) >> 2;
        {
            const int nb = w >> 1;
            c1.w = w; c1.sws = sws; c1.wo = wo; c1.w2 = w2; c1.wq = wq; c1.w1 = w1; c1.lds = lds;
            c1.sx = mk_rsrc(ckv, 2048 * 512 * 2);
            c1.sw = mk_rsrc(nb < 2 ? wuk : wuv, 512 * 4096 * 2);
#pragma unroll
            for (int par = 0; par < 2; ++par) {
                c1.vx[par] = (l >> 3) * 1024 + (((l & 7) ^ (4 * par + (l >> 4))) * 16);
                c1.vw[par] = (l >> 3) * 8192 + (((l & 7) ^ ((((l >> 4) & 1) | (par << 1)) << 1)) * 16);
            }
            c1.sxo = (256 * j + 32 * w) * 1024;
            c1.swo = (32 * (w & 1)) * 8192 + (h * 128 + 64 * (nb & 1)) * 2;
            const int wn = w & 3, wm = w >> 2, f = (l & 15) >> 1, s = ((q >> 1) & 1) | ((g & 1) << 1);
            c1.xb[0] = lds + wm * 16384 + (l & 15) * 128 + ((g ^ f) * 16);
            c1.xb[1] = lds + wm * 16384 + (l & 15) * 128 + (((g ^ f) ^ 4) * 16);
#pragma unroll
            for (int nt = 0; nt < 4; ++nt) c1.wb[nt] = lds + 65536 + wn * 8192 + (8 * g + q) * 128 + ((nt ^ s) * 32) + (l & 3) * 8;
        }
        p1_dma(c1, 0, 0);
        u32x4 xo0, xo1;
        {
            const rsrc_t sxi = mk_rsrc(x, 2048 * 640 * 2);
            const unsigned vx = (l & 7) * 1280 + (l >> 3) * 16, so0 = b * 8 * 1280 + w * 128, so1 = b * 8 * 1280 + (8 + (w & 1)) * 128;
            asm volatile("s_nop 4\n\tbuffer_load_dwordx4 %0, %2, %3, %4 offen\n\tbuffer_load_dwordx4 %1, %2, %3, %5 offen" : "=&v"(xo0), "=&v"(xo1) : "v"(vx), "s"(sxi), "s"(so0), "s"(so1) : "memory");
        }
        BARRIER();
        p1_dma(c1, 1, 1);
        {
            const char* pa = (const char*)ckv + (size_t)(256 * j + 64 * (h & 3) + 8 * w + (l >> 3)) * 1024 + (l & 7) * 128;
            const char* pw = (const char*)((w & 1) ? wuv : wuk) + (size_t)(64 * j + 16 * (w >> 1) + ((l & 31) >> 1)) * 8192 + h * 256 + (l & 1) * 128;
            touch(pa, 131072 + w * 256); touch(pw, 131072 + w * 256);
        }
        asm volatile("s_waitcnt lgkmcnt(0)" : "+s"(epoch) :: "memory");
        y.token = epoch + 1;
        STAMP(12);
        asm volatile("s_waitcnt vmcnt(10)" : "+v"(xo0), "+v"(xo1) :: "memory");
        STAMP(13);
        float v0[8], v1[8];
#pragma unroll
        for (int e = 0; e < 4; ++e) { v0[2 * e] = bflo(xo0[e]); v0[2 * e + 1] = bfhi(xo0[e]); v1[2 * e] = bflo(xo1[e]); v1[2 * e + 1] = bfhi(xo1[e]); }
        auto red8 = [](float t) { t += __uint_as_float(__builtin_amdgcn_update_dpp(0u, __float_as_uint(t), 0x128, 0xf, 0xf, false)); return xsum4(t); };
        {
            const float m0 = red8(((v0[0] + v0[1]) + (v0[2] + v0[3])) + ((v0[4] + v0[5]) + (v0[6] + v0[7]))) * (1.f / 64.f);
            const float m1 = red8(((v1[0] + v1[1]) + (v1[2] + v1[3])) + ((v1[4] + v1[5]) + (v1[6] + v1[7]))) * (1.f / 64.f);
            float q0 = 0.f, q1 = 0.f;
#pragma unroll
            for (int e = 0; e < 8; ++e) { const float d0 = v0[e] - m0, d1 = v1[e] - m1; q0 += d0 * d0; q1 += d1 * d1; }
            q0 = red8(q0); q1 = red8(q1);
            if (l < 8) { *(LAS f32x2*)(lds + 162816 + l * 80 + w * 8) = (f32x2){m0, q0}; if (w < 2) *(LAS f32x2*)(lds + 162816 + l * 80 + 64 + w * 8) = (f32x2){m1, q1}; }
        }
        asm volatile("s_waitcnt lgkmcnt(0)" ::: "memory"); BARRIER();
        STAMP(14);
        {
            f32x4 t[5];
#pragma unroll
            for (int i = 0; i < 5; ++i) t[i] = *(const LAS f32x4*)(lds + 162816 + (l & 7) * 80 + i * 16);
            float mean = 0.f;
#pragma unroll
            for (int i = 0; i < 5; ++i) mean += t[i][0] + t[i][2];
            mean *= 0.1f;
            float m2 = 0.f;
#pragma unroll
            for (int i = 0; i < 5; ++i) { const float d0 = t[i][0] - mean, d1 = t[i][2] - mean; m2 += (t[i][1] + t[i][3]) + 64.f * (d0 * d0 + d1 * d1); }
            const float rstd = 1.f / __builtin_sqrtf(m2 * (1.f / 640.f) + 1e-5f);
            const unsigned vo = (l >> 3) * 256 + (l & 7) * 16, xo = OFF_XN + (b >> 1) * 20480 + (b & 1) * 128;
            u32x4 o0, o1;
#pragma unroll
            for (int e = 0; e < 4; ++e) { o0[e] = cvtpk((v0[2 * e] - mean) * rstd, (v0[2 * e + 1] - mean) * rstd); o1[e] = cvtpk((v1[2 * e] - mean) * rstd, (v1[2 * e + 1] - mean) * rstd); }
            st16_sc1(sws, o0, vo, xo + w * 2048);
            if (w < 2) st16_sc1(sws, o1, vo, xo + (8 + w) * 2048);
        }
        {
            c2.w = w; c2.sws = sws; c2.swq = mk_rsrc(wq, 640 * 4096 * 2); c2.sw1 = mk_rsrc(w1, 640 * 1728 * 2);
            c2.vl16 = l * 16;
            const int swz = ((l >> 4) & 1) | ((w & 1) << 1);
            c2.vwq = (l >> 3) * 8192 + (((l & 7) ^ (swz << 1)) * 16);
            c2.vw1 = (l >> 3) * 3456 + (((l & 7) ^ (swz << 1)) * 16);
            { const int mtl = 2 * w, mtg = mtl < 8 ? 8 * j + mtl : 8 * (15 - j) + (mtl - 8); c2.sxo = OFF_XN + mtg * 20480; }
            c2.swqo = (8 * w) * 8192 + h * 256;
            c2.sw1o = (8 * w) * 3456 + h * 128;
            c2.t0l = (w >> 2) * 16384 + (w & 3) * 4096; c2.t0s = OFF_K + (unsigned)h * 524288 + c2.t0l; c2.t1s = OFF_V + (unsigned)h * 524288 + (w & 3) * 4096;
            { const int wn = w & 3, wm = w >> 2;
              c2.skv = isv ? OFF_V + (unsigned)(h * 32 + 4 * j + 2 * wm) * 16384 + (wn - 2) * 4096 : OFF_K + (unsigned)((h * 128 + 16 * j + 8 * wm) * 4 + 2 * wn) * 1024; }
        }
    }
    STAMP(1); CSTAMP(14);
    if (isv) p1_main<true>(c1, c2, y, kv); else p1_main<false>(c1, c2, y, kv);
    STAMP(2); CSTAMP(15);
    p2_kvst<0>(c2, kv, isv); p2_kvst<1>(c2, kv, isv); p2_kvst<2>(c2, kv, isv); p2_kvst<3>(c2, kv, isv);
    const bool hasf = h < 27;
    AttnCtx c3;
    AttnRegs r;
    {
        const int l = lane_id(), g = l >> 4, q = (l & 15) >> 2;
        const int wn = w & 1, wm = w >> 1;
        const int s = ((q >> 1) & 1) | ((g & 1) << 1);
        c2.xb = lds + wm * 8192 + l * 16;
#pragma unroll
        for (int nt = 0; nt < 4; ++nt) c2.wq[nt] = lds + 65536 + wn * 8192 + (8 * g + q) * 128 + ((nt ^ s) * 32) + (l & 3) * 8;
#pragma unroll
        for (int ft = 0; ft < 2; ++ft) c2.wf[ft] = lds + 65536 + 16384 + (8 * g + q) * 128 + (((2 * wn + ft) ^ s) * 32) + (l & 3) * 8;
        f32x4 acc[6][4];
#pragma unroll
        for (int a = 0; a < 6; ++a)
#pragma unroll
            for (int m = 0; m < 4; ++m) acc[a][m] = (f32x4){0.f, 0.f, 0.f, 0.f};
        STAMP(3);
        P2Bias bs; bs.p = b1 + (64 * h + 32 * wn + 4 * g); bs.a = (u32x2){0u, 0u}; bs.b = (u32x2){0u, 0u};
        if (hasf) p2_main<true>(c2, y, bs, acc, kv, isv, std::make_integer_sequence<int, 10>{}); else p2_main<false>(c2, y, bs, acc, kv, isv, std::make_integer_sequence<int, 10>{});
        STAMP(4);
        constexpr float C2 = 0.08838834764831845f * 1.4426950408889634f;
#pragma unroll
        for (int mt = 0; mt < 4; ++mt)
#pragma unroll
            for (int a = 0; a < 2; ++a)
                *(LAS u32x4*)(lds + 98304 + ((4 * wm + mt) * 4 + 2 * wn + a) * 1024 + l * 16) = pack8(acc[2 * a][mt] * C2, acc[2 * a + 1][mt] * C2);
        u32x4 actf[4];
        if (hasf) {
            asm volatile("" : "+v"(bs.a), "+v"(bs.b));
            const u32x2 bA = bs.a, bB = bs.b;
            const f32x4 biasA = {bflo(bA[0]), bfhi(bA[0]), bflo(bA[1]), bfhi(bA[1])}, biasB = {bflo(bB[0]), bfhi(bB[0]), bflo(bB[1]), bfhi(bB[1])};
#pragma unroll
            for (int mt = 0; mt < 4; ++mt) {
                f32x4 hA = acc[4][mt] + biasA, hB = acc[5][mt] + biasB;
#pragma unroll
                for (int i = 0; i < 4; ++i) { hA[i] = mish(hA[i]); hB[i] = mish(hB[i]); }
                actf[mt] = pack8(hA, hB);
            }
        }
        asm volatile("s_waitcnt vmcnt(0) lgkmcnt(0)" ::: "memory"); BARRIER();
        const int grp = w >> 2, qg = w & 3;
        c3.sws = sws; c3.vl16 = l * 16; c3.kbase = OFF_K + (unsigned)h * 524288; c3.vbase = OFF_V + (unsigned)h * 524288;
        c3.ctxl = OFF_CTX + ((8 * j + 2 * qg) * 128 + 4 * h) * 1024;
        c3.nL = 2 * j + 2; c3.grp = grp; c3.qg = qg; c3.j = j; c3.rowrel = (l & 15) - 4 * g; c3.kl = lds + l * 16; c3.vl = lds + 65536 + l * 16;
        c3.ql = lds + 131072 + qg * 8192 + l * 16;
        if (hasf) {
#pragma unroll
            for (int mt = 0; mt < 4; ++mt) { const int mtl = 4 * wm + mt, mtg = mtl < 8 ? 8 * j + mtl : 8 * (15 - j) + (mtl - 8);
                st16_sc1(sws, actf[mt], l * 16, OFF_ACT + (mtg * 54 + 2 * h + wn) * 1024); }
        }
#pragma unroll
        for (int qt = 0; qt < 2; ++qt)
#pragma unroll
            for (int u = 0; u < 4; ++u) r.q[qt][u] = ld128(lds + 98304 + ((8 * grp + 2 * qg + qt) * 4 + u) * 1024 + l * 16);
    }
    STAMP(5);
    P4Ctx c4;
    {
        const int l = lane_id();
        const int grp = c3.grp, qg = c3.qg;
        const unsigned vl16 = c3.vl16;
#pragma unroll
        for (int d = 0; d < 8; ++d) { r.o[d][0] = (f32x4){0.f, 0.f, 0.f, 0.f}; r.o[d][1] = (f32x4){0.f, 0.f, 0.f, 0.f}; }
        r.negm[0] = (f32x4){0.f, 0.f, 0.f, 0.f}; r.negm[1] = (f32x4){0.f, 0.f, 0.f, 0.f};
        AttnSm z; z.m0 = 0.f; z.m1 = 0.f;
        r.ls[0] = (f32x4){0.f, 0.f, 0.f, 0.f}; r.ls[1] = (f32x4){0.f, 0.f, 0.f, 0.f};
        r.ones = (bf16x8){0x3F80, 0x3F80, 0x3F80, 0x3F80, 0x3F80, 0x3F80, 0x3F80, 0x3F80}; PIN(r.ones);
        asm volatile("s_waitcnt lgkmcnt(0)" ::: "memory");
        BARRIER();
        STAMP(6); CSTAMP(12);
        a_pre<16>(r, c3.vl, c3.kl, std::make_integer_sequence<int, PD>{});
        if (grp == 1) BARRIER();
        { Piece none; none.on = false; none.sws = sws; none.vl16 = 0; none.soff = 0; none.ldso = 0; a_run<16, 0>(r, c3.vl, c3.kl, none, std::make_integer_sequence<int, 16>{}); }
        BARRIER();
        c3.swap_s = grp == 0 ? c3.nL - 1 : 99; c3.first_s = 0;
        asm volatile("" : "+s"(c3.swap_s), "+s"(c3.first_s));
        {
            const int nL = c3.nL;
            auto lean_init = [&](int s) {
                Lean st;
                const bool eK = grp == 1 && s + 2 < nL;
                const int t0 = s < nL ? s : 2 * s - nL + grp, t1 = s + 1 < nL ? s + 1 : 2 * s + 2 - nL + grp, t2 = s + 2 < nL ? s + 2 : 2 * s + 4 - nL + grp;
                const int te = eK ? t2 : t1;
                st.e_soff = (eK ? c3.kbase : c3.vbase) + te * 16384 + qg * 4096; st.e_lds = (eK ? 0 : 65536) + (te & 3) * 16384 + qg * 4096;
                st.l_soff = c3.kbase + t2 * 16384 + qg * 4096; st.l_lds = (t2 & 3) * 16384 + qg * 4096;
                st.voff = (t0 & 3) * 16384; st.koff = (t1 & 3) * 16384;
                return st;
            };
            int s = 0, phase = 0;
#pragma nounroll
            for (;;) {
                const int gend = phase == 0 ? 1 : (phase == 1 ? (grp == 0 ? nL + 1 : nL - 1) : 17);
#pragma nounroll
                while (s < gend) { a_slotfull(c3, y, r, z, s PPASSG); ++s; }
                if (phase == 0) { if (w == 0 && l == 0) set_flag(y.flags, FL_ACT + j * 32 + h, y.token); }
                if (phase == 2 || s > 16) break;
                if (phase == 1 && s <= 14) {
                    Lean st = lean_init(s);
#pragma nounroll
                    do { a_slot_lean<true>(c3, r, z, st PPASS);
                         st.e_soff += 32768; st.e_lds ^= 32768; st.l_soff += 32768; st.l_lds ^= 32768; st.voff ^= 32768; st.koff ^= 32768; ++s; } while (s <= 14);
                }
                const int send = phase == 0 ? nL - 2 : 16;
                if (s < send) {
                    Lean st = lean_init(s);
#pragma nounroll
                    do { a_slot_lean<false>(c3, r, z, st PPASS);
                         st.e_soff += 16384; st.e_lds = (st.e_lds & ~0xC000u) | ((st.e_lds + 16384) & 0xC000u); st.voff = (st.voff + 16384) & 0xC000u; st.koff = (st.koff + 16384) & 0xC000u; ++s; } while (s < send);
                }
                ++phase;
            }
        }
        const float m0 = z.m0, m1 = z.m1, l0 = r.ls[0][0], l1 = r.ls[1][0];
        if (grp == 0) BARRIER();
        STAMP(7); CSTAMP(13);
        {
            const int q = xcd >> 1, rbk = (xcd & 1) * 8 + (bi >> 2), cb = bi & 3, wm = w >> 1, wn = w & 1;
            c4.sws = sws; c4.vl16 = vl16; c4.w = w; c4.q = q; c4.o = (q + 2 * wn) & 3; c4.ng = q == 3 ? 6 : 7; c4.ra = 0; c4.rw = 0;
            c4.ga = OFF_ACT + ((8 * rbk + w) * 54 + 14 * q) * 1024; c4.gas = OFF_CTX + ((8 * rbk + w) * 128 + 32 * q) * 1024;
            c4.gw = OFF_WP + ((128 + 14 * q) * 40 + 10 * cb) * 1024; c4.gws = OFF_WP + (32 * q * 40 + 10 * cb) * 1024;
            c4.dw0 = w * 1024; c4.dw1 = (w < 2 ? 8 + w : 38 + w) * 1024; c4.dw2 = (46 + w) * 1024; c4.lw = 16384 + w * 1024;
            c4.tag = (unsigned)y.token & P4TAG;
            const unsigned xb = (unsigned)(((xcd & 1) * 32 + bi) * 128 + w) * 3072;
#pragma unroll
            for (int d = 0; d < 3; ++d) { const int qq = (q + 1 + d) & 3;
                c4.so[d] = xb + qq * 98304 + q * 24576; c4.so2[d] = ((c4.o + 1 + d) & 3) < 2 ? c4.so[d] : P4OOB;
                c4.lo[d] = xb + q * 98304 + qq * 24576; c4.lo2[d] = c4.o < 2 ? c4.lo[d] : P4OOB; }
            c4.pa = lds + (4 * wm) * 1024 + vl16; c4.pw = lds + 16384 + (5 * wn) * 1024 + vl16;
        }
        p4_piece<0>(c4, 2 * P4STG); p4_piece<1>(c4, 2 * P4STG); p4_piece<2>(c4, 2 * P4STG); p4_piece<3>(c4, 2 * P4STG); p4_piece<4>(c4, 2 * P4STG);
        p4_piece<0>(c4, 3 * P4STG); p4_piece<1>(c4, 3 * P4STG); p4_piece<2>(c4, 3 * P4STG); p4_piece<3>(c4, 3 * P4STG); p4_piece<4>(c4, 3 * P4STG);
        {
            const lptr xo = lds + (qg * 2 + grp) * 9216;
#pragma unroll
            for (int d = 0; d < 4; ++d)
#pragma unroll
                for (int qt = 0; qt < 2; ++qt) *(LAS f32x4*)(xo + (d * 2 + qt) * 1024 + l * 16) = sel(grp == 0, r.o[4 + d][qt], r.o[d][qt]);
            *(LAS f32x4*)(xo + 8192 + l * 16) = (f32x4){m0, m1, l0, l1};
            asm volatile("s_waitcnt lgkmcnt(0)" ::: "memory"); BARRIER();
            const lcptr xi = lds + (qg * 2 + (grp ^ 1)) * 9216;
            const f32x4 st = *(const LAS f32x4*)(xi + 8192 + l * 16);
            const float M0 = __builtin_fmaxf(m0, st[0]), M1 = __builtin_fmaxf(m1, st[1]);
            const float wa0 = __builtin_amdgcn_exp2f(m0 - M0), wb0 = __builtin_amdgcn_exp2f(st[0] - M0), wa1 = __builtin_amdgcn_exp2f(m1 - M1), wb1 = __builtin_amdgcn_exp2f(st[1] - M1);
            const float i0 = __builtin_amdgcn_rcpf(wa0 * l0 + wb0 * st[2]), i1 = __builtin_amdgcn_rcpf(wa1 * l1 + wb1 * st[3]);
            const float ca0 = wa0 * i0, cb0 = wb0 * i0, ca1 = wa1 * i1, cb1 = wb1 * i1;
#pragma unroll
            for (int up = 0; up < 2; ++up)
#pragma unroll
                for (int qt = 0; qt < 2; ++qt) {
                    const f32x4 pa = *(const LAS f32x4*)(xi + ((2 * up) * 2 + qt) * 1024 + l * 16), pb = *(const LAS f32x4*)(xi + ((2 * up + 1) * 2 + qt) * 1024 + l * 16);
                    const f32x4 oa = sel(grp == 0, r.o[2 * up][qt], r.o[4 + 2 * up][qt]), ob = sel(grp == 0, r.o[2 * up + 1][qt], r.o[4 + 2 * up + 1][qt]);
                    const float ca = qt ? ca1 : ca0, cb = qt ? cb1 : cb0;
                    st16_sc1(sws, pack8(oa * ca + pa * cb, ob * ca + pb * cb), vl16, OFF_CTX + ((8 * (15 - j) + 2 * qg + qt) * 128 + 4 * h + 2 * grp + up) * 1024);
                }
        }
    }
    STAMP(8);
    {
        asm volatile("s_waitcnt vmcnt(8) lgkmcnt(0)" ::: "memory"); BARRIER();
        P4Regs r4;
        unsigned sb = 2 * P4STG, sp = P4STG;
        p4_rd<0, 0, 0>(c4, r4, sb); p4_rd<0, 0, 1>(c4, r4, sb); p4_rd<0, 0, 2>(c4, r4, sb); p4_rd<0, 0, 3>(c4, r4, sb); p4_rd<0, 0, 4>(c4, r4, sb); p4_rd<0, 0, 5>(c4, r4, sb); p4_rd<0, 0, 6>(c4, r4, sb);
        SBAR();
        p4_piece<0>(c4, 0); p4_piece<1>(c4, 0); p4_piece<2>(c4, 0); p4_piece<3>(c4, 0); p4_piece<4>(c4, 0);
        p4_piece<0>(c4, P4STG); p4_piece<1>(c4, P4STG); p4_piece<2>(c4, P4STG);
        const int l = lane_id();
        const int rbk = (xcd & 1) * 8 + (bi >> 2), cb = bi & 3;
        const unsigned cidx = FL_CTX + (rbk < 8 ? rbk : 15 - rbk) * 32 + 8 * c4.q + (l & 7);
        f32x4 accg[10], accs[10], gt[3];
        P4Xch xg;
        const int row0 = 128 * rbk + 32 * (w >> 1) + (l & 15), col0 = 160 * cb + 80 * (w & 1) + 4 * (l >> 4);
        u32x2 xv[3];
        {
            const rsrc_t sx = mk_rsrc(x, 2048 * 640 * 2);
#pragma unroll
            for (int k = 0; k < 3; ++k) { const int i = c4.o + 4 * k < 10 ? c4.o + 4 * k : c4.o;
                asm volatile("buffer_load_dwordx2 %0, %1, %2, 0 offen" : "=v"(xv[k]) : "v"((unsigned)((row0 + (i & 1) * 16) * 640 + col0 + (i >> 1) * 16) * 2), "s"(sx) : "memory"); }
        }
        u64 seen = 0;
        SBAR();
        STAMP(9);
        p4_step<P4W0, true, true, false, 2, -1, -1, -1, true>(c4, y, r4, xg, accg, sb, sp, 0, cidx, seen);
#pragma nounroll
        for (int t = 1; t < c4.ng; ++t) p4_step<8, true, true, false, 1, -1, -1, -1>(c4, y, r4, xg, accg, sb, sp, t, cidx, seen);
        p4_split(c4, accg, xg);
        p4_ssteps(c4, y, r4, xg, accs, sb, sp, seen, std::make_integer_sequence<int, 12>{});
        p4_step<8, true, false, false, 0, -1, -1, 0>(c4, y, r4, xg, accs, sb, sp, 0, 0, seen);
        p4_step<13, false, false, false, 0, -1, 5, -1>(c4, y, r4, xg, accs, sb, sp, 0, 0, seen);
        p4_step<9, false, false, false, 0, -1, -1, -1>(c4, y, r4, xg, accs, sb, sp, 0, 0, seen);
        p4_step<0, false, false, true, 0, -1, -1, -1>(c4, y, r4, xg, accs, sb, sp, 0, 0, seen);
        asm volatile("s_waitcnt vmcnt(0)" : "+v"(xg.rcv[0]), "+v"(xg.rcv[1]), "+v"(xg.rcv[2]), "+v"(xg.rcv[3]), "+v"(xg.rcv[4]), "+v"(xg.rcv[5]), "+v"(xg.rcv[6]), "+v"(xg.rcv[7]), "+v"(xg.rcv[8]),
                     "+v"(xv[0]), "+v"(xv[1]), "+v"(xv[2]) :: "memory");
        constexpr auto nine = std::make_integer_sequence<int, 9>{};
        P4Xch xs;
        p4_split(c4, accs, xs);
        p4_put<0>(c4, xs, OFF_XS); p4_put<1>(c4, xs, OFF_XS); p4_put<2>(c4, xs, OFF_XS); p4_put<3>(c4, xs, OFF_XS); p4_put<4>(c4, xs, OFF_XS);
        p4_put<5>(c4, xs, OFF_XS); p4_put<6>(c4, xs, OFF_XS); p4_put<7>(c4, xs, OFF_XS); p4_put<8>(c4, xs, OFF_XS);
        TS(0);
        if (!p4_tagged(c4, xg)) p4_refetch(c4, xg, OFF_XG, nine);
#pragma unroll
        for (int k = 0; k < 3; ++k) {
            gt[k] = p4_total(xg, k);
#pragma unroll
            for (int e = 0; e < 4; ++e) gt[k][e] = gt[k][e] * __builtin_amdgcn_rcpf(1.f + __builtin_amdgcn_exp2f(-1.4426950408889634f * gt[k][e]));
            PIN(gt[k]);
        }
        TS(1);
        p4_refetch(c4, xs, OFF_XS, nine);
        TS(2);
#pragma unroll
        for (int k = 0; k < 3; ++k) {
            if (k < 2 || c4.o < 2) { const int i = c4.o + 4 * k;
                const f32x4 sv = p4_total(xs, k);
                const f32x4 o = {bflo(xv[k][0]) + sv[0] * gt[k][0], bfhi(xv[k][0]) + sv[1] * gt[k][1], bflo(xv[k][1]) + sv[2] * gt[k][2], bfhi(xv[k][1]) + sv[3] * gt[k][3]};
                const u32x2 ov = {cvtpk(o[0], o[1]), cvtpk(o[2], o[3])};
                asm volatile("global_store_dwordx2 %0, %1, off sc0 sc1\n\ts_nop 1" :: "v"(out + (row0 + (i & 1) * 16) * 640 + col0 + (i >> 1) * 16), "v"(ov) : "memory"); }
        }
        asm volatile(".Lcode_z:");
        if (b == 0 && w == 0 && l == 0) __hip_atomic_store(y.flags + 4, y.token, __ATOMIC_RELAXED, __HIP_MEMORY_SCOPE_AGENT);
    }
    STAMP(11);
}

extern "C" void kernel_launch(void* const* d_in, const int* in_sizes, int n_in, void* d_out, int out_size, void* d_ws, size_t ws_size, hipStream_t stream) {
    (void)in_sizes; (void)n_in; (void)out_size; (void)ws_size;
    fused_kernel<<<256, 512, 0, stream>>>((const bf16_t*)d_in[0], (const bf16_t*)d_in[1], (const bf16_t*)d_in[2], (const bf16_t*)d_in[3], (const bf16_t*)d_in[4],
                                          (const bf16_t*)d_in[5], (const bf16_t*)d_in[7], (const bf16_t*)d_in[8], (const bf16_t*)d_in[9], (bf16_t*)d_out, (char*)d_ws);
}
```

```cpp
#include <hip/hip_runtime.h>
#include <stdint.h>
#include <utility>

typedef unsigned short bf16_t;
typedef short bf16x8 __attribute__((ext_vector_type(8)));
typedef short s16x4 __attribute__((ext_vector_type(4)));
typedef short v4i16_t __attribute__((ext_vector_type(4)));
typedef float f32x4 __attribute__((ext_vector_type(4)));
typedef float f32x2 __attribute__((ext_vector_type(2)));
typedef unsigned u32x4 __attribute__((ext_vector_type(4)));
typedef unsigned u32x2 __attribute__((ext_vector_type(2)));
typedef __bf16 bf16x2_t __attribute__((ext_vector_type(2)));
typedef unsigned short u16x2 __attribute__((ext_vector_type(2)));
typedef __amdgpu_buffer_rsrc_t rsrc_t;
typedef unsigned long long u64;
#define LAS __attribute__((address_space(3)))
#define GAS __attribute__((address_space(1)))
typedef LAS char* lptr;
typedef LAS const char* lcptr;
typedef GAS u64 gu64;
#define DI __device__ __forceinline__
#define SBAR() __builtin_amdgcn_sched_barrier(0)
#define PIN(x) asm volatile("" : "+v"(x))
#define MF16(a, b, c) __builtin_amdgcn_mfma_f32_16x16x32_bf16(a, b, c, 0, 0, 0)
#define WAITV(n) asm volatile("s_waitcnt vmcnt(" #n ")" ::: "memory")
#define WAITL(n) asm volatile("s_waitcnt lgkmcnt(" #n ")" ::: "memory")
#define BARRIER() do { asm volatile("" ::: "memory"); __builtin_amdgcn_s_barrier(); asm volatile("" ::: "memory"); } while (0)

#define UPTO 4
#define STAG 1
#define NOINV 1
#define P0EB 1
#define KVR 2
#define SPREAD 1
#define SPOS 1
#define XST 0
#define STAMPS 0

constexpr unsigned OFF_FLAGS = 0;
constexpr unsigned OFF_XN = 65536;
constexpr unsigned OFF_K = OFF_XN + 2621440;
constexpr unsigned OFF_V = OFF_K + 16777216;
constexpr unsigned OFF_ACT = OFF_V + 16777216 + 8388608;
constexpr unsigned OFF_CTX = OFF_ACT + 7077888;
constexpr unsigned OFF_WP = OFF_CTX + 16777216;
constexpr unsigned OFF_ST = OFF_WP + 182 * 40 * 1024;
constexpr unsigned OFF_XG = OFF_ST + 65536;
constexpr unsigned OFF_XS = OFF_XG + 25165824;
constexpr unsigned WS_BYTES = OFF_XS + 25165824;
constexpr unsigned FL_P0 = 0, FL_KV = 256, FL_ACT = 512, FL_CTX = 768;
constexpr int SC1 = 16;

DI unsigned cvtpk(float lo, float hi) { f32x2 v = {lo, hi}; bf16x2_t b = __builtin_convertvector(v, bf16x2_t); return __builtin_bit_cast(unsigned, b); }
DI u32x4 pack8(f32x4 a, f32x4 b) { u32x4 w = {cvtpk(a[0], a[1]), cvtpk(a[2], a[3]), cvtpk(b[0], b[1]), cvtpk(b[2], b[3])}; return w; }
DI float bflo(unsigned u) { return __uint_as_float(u << 16); }
DI float bfhi(unsigned u) { return __uint_as_float(u & 0xffff0000u); }
DI s16x4 vtr(lcptr p) { return __builtin_bit_cast(s16x4, __builtin_amdgcn_ds_read_tr16_b64_v4i16((LAS v4i16_t*)p)); }
DI bf16x8 cat(s16x4 a, s16x4 b) { return (bf16x8){a[0], a[1], a[2], a[3], b[0], b[1], b[2], b[3]}; }
DI bf16x8 ld128(lcptr p) { return *(const LAS bf16x8*)p; }
DI bf16x8 asbf(u32x4 v) { return __builtin_bit_cast(bf16x8, v); }
DI rsrc_t mk_rsrc(const void* p, unsigned bytes) { return __builtin_amdgcn_make_buffer_rsrc((void*)p, (short)0, (int)bytes, 0x00020000); }
DI void dma16(rsrc_t r, unsigned voff, unsigned soff, unsigned ldsoff) {
    asm volatile("s_mov_b32 m0, %0\n\ts_nop 3\n\tbuffer_load_dwordx4 %1, %2, %3 offen lds" ::"s"(ldsoff), "v"(voff), "s"(r), "s"(soff) : "memory");
}
#define HS " sc1"
DI void dma16h(rsrc_t r, unsigned voff, unsigned soff, unsigned ldsoff) {
    asm volatile("s_mov_b32 m0, %0\n\ts_nop 3\n\tbuffer_load_dwordx4 %1, %2, %3 offen" HS " lds" ::"s"(ldsoff), "v"(voff), "s"(r), "s"(soff) : "memory");
}
DI void dma16x4(rsrc_t r, unsigned voff, unsigned soff, unsigned ldsoff) {
    asm volatile("s_mov_b32 m0, %0\n\ts_nop 3\n\tbuffer_load_dwordx4 %1, %2, %3 offen" HS " lds\n\tbuffer_load_dwordx4 %1, %2, %3 offen offset:1024" HS " lds\n\t"
                 "buffer_load_dwordx4 %1, %2, %3 offen offset:2048" HS " lds\n\tbuffer_load_dwordx4 %1, %2, %3 offen offset:3072" HS " lds" ::"s"(ldsoff), "v"(voff), "s"(r), "s"(soff) : "memory");
}
DI void touch(const void* p, unsigned ldsoff) { asm volatile("s_mov_b32 m0, %0\n\ts_nop 3\n\tglobal_load_lds_dword %1, off" ::"s"(ldsoff), "v"(p) : "memory"); }
DI void st16_sc1(rsrc_t r, u32x4 v, unsigned voff, unsigned soff) { asm volatile("s_nop 4\n\tbuffer_store_dwordx4 %0, %1, %2, %3 offen sc1\n\ts_nop 1" ::"v"(v), "v"(voff), "s"(r), "s"(soff) : "memory"); }
template <int OFF> DI void st16_sc1o(rsrc_t r, u32x4 v, unsigned voff, unsigned soff) { asm volatile("s_nop 4\n\tbuffer_store_dwordx4 %0, %1, %2, %3 offen offset:%4 sc1\n\ts_nop 1" ::"v"(v), "v"(voff), "s"(r), "s"(soff), "i"(OFF) : "memory"); }
DI u32x4 ld16(rsrc_t r, unsigned voff, unsigned soff) { return __builtin_amdgcn_raw_buffer_load_b128(r, voff, soff, 0); }
#define P4AUX SC1
DI u32x4 ld16_sc1(rsrc_t r, unsigned voff, unsigned soff) { return __builtin_amdgcn_raw_buffer_load_b128(r, voff, soff, P4AUX); }
template <class T> DI T sel(bool c, T a, T b) { return c ? a : b; }
DI int lane_id() { int l; asm volatile("v_mbcnt_lo_u32_b32 %0, -1, 0\n\tv_mbcnt_hi_u32_b32 %0, -1, %0" : "=v"(l)); return l; }
DI void set_flag(gu64* flags, unsigned idx, u64 token) { __hip_atomic_store(flags + idx * 8, token, __ATOMIC_RELAXED, __HIP_MEMORY_SCOPE_AGENT); }
DI u64 poll_flag(gu64* flags, unsigned idx) { return __hip_atomic_load(flags + idx * 8, __ATOMIC_RELAXED, __HIP_MEMORY_SCOPE_AGENT); }
DI void spin_flags(gu64* flags, unsigned idx, u64 token) {
    const unsigned long long t0 = __builtin_amdgcn_s_memrealtime();
    for (;;) {
        __builtin_amdgcn_s_sleep(1);
        const u64 v = poll_flag(flags, idx);
        if (__all(v == token)) break;
        if (__builtin_amdgcn_s_memrealtime() - t0 > 2000000ull) break;
    }
}
DI u64 poll_asm(gu64* flags, unsigned idx) { u64 v; asm volatile("global_load_dwordx2 %0, %1, off sc1" : "=v"(v) : "v"(flags + idx * 8) : "memory"); return v; }
DI void check_flags(gu64* flags, unsigned idx, u64 seen, u64 token) { if (!__all(seen == token)) spin_flags(flags, idx, token); }
DI void acquire_issue() { asm volatile("buffer_inv sc1" ::: "memory"); }
DI u64 uni64(u64 v) { return ((u64)(unsigned)__builtin_amdgcn_readfirstlane((unsigned)(v >> 32)) << 32) | (unsigned)__builtin_amdgcn_readfirstlane((unsigned)v); }
#define SCLK __builtin_amdgcn_s_memrealtime
#define STAMP(i) do { } while (0)
#define STAMPW(i, wv) do { } while (0)
#define CSTAMP(i) do { } while (0)

#define TS(i) do { } while (0)
#define TSARG
#define TSPASS
struct Sync { gu64* flags; u64 token; int b, h, j, w; };
#define YSTAMP(i) do { } while (0)

struct P2Ctx { rsrc_t sws, swq, sw1; unsigned vl16, vwq, vw1; unsigned sxo, swqo, sw1o, skv, t0s, t0l, t1s; int w; lcptr xb; lcptr wq[4]; lcptr wf[2]; };
DI void p2_dma1(const P2Ctx& c, int st, int ks, int i) {
    if (i < 4) dma16h(c.sws, c.vl16, c.sxo + (i >> 1) * 20480 + (i & 1) * 1024 + ks * 2048, st * 32768 + (4 * c.w + i) * 1024);
    else if (i < 6) dma16(c.swq, c.vwq, c.swqo + (i - 4) * 128 + ks * 524288, 65536 + st * 24576 + (i - 4) * 8192 + c.w * 1024);
    else dma16(c.sw1, c.vw1, c.sw1o + ks * (64 * 3456), 65536 + st * 24576 + 16384 + c.w * 1024);
}

struct P1Ctx { rsrc_t sx, sw, sws; unsigned vx[2], vw[2]; unsigned sxo, swo; int w; lcptr xb[2]; lcptr wb[4]; const bf16_t *wo, *w2, *wq, *w1; lcptr lds; };
DI void pack_fetch(const P1Ctx& c, int b) {
    const int l = lane_id(), gw = b * 8 + c.w, pu = gw / 10, pq = gw - pu * 10;
    const rsrc_t sp = mk_rsrc(pu < 128 ? c.wo : c.w2, pu < 128 ? 4096 * 640 * 2 : 1728 * 640 * 2);
    const unsigned so = gw < 1820 ? (unsigned)((pu < 128 ? pu : pu - 128) * 32 * 1280 + pq * 128) : 0xF0000000u;
#pragma unroll
    for (int i = 0; i < 4; ++i) dma16(sp, (l >> 3) * 1280 + (l & 7) * 16, so + i * 8 * 1280, 131072 + c.w * 4096 + i * 1024);
}
DI void pack_store(const P1Ctx& c, int b) {
    const int l = lane_id(), g = l >> 4, q = (l & 15) >> 2, gw = b * 8 + c.w, pu = gw / 10, pq = gw - pu * 10;
    lcptr p = c.lds + 131072 + c.w * 4096 + (4 * g + q) * 128 + (l & 3) * 8;
    bf16x8 fr[4];
#pragma unroll
    for (int i = 0; i < 4; ++i) fr[i] = cat(vtr(p + i * 32), vtr(p + i * 32 + 16 * 128));
    if (gw < 1820) {
#pragma unroll
        for (int i = 0; i < 4; ++i) st16_sc1(c.sws, __builtin_bit_cast(u32x4, fr[i]), l * 16, OFF_WP + (pu * 40 + pq * 4 + i) * 1024);
    }
}
DI void p1_dma1(const P1Ctx& c, int st, int ks, int i) {
    if (i < 4) dma16(c.sx, c.vx[i & 1], c.sxo + i * 8192 + ks * 128, st * 32768 + (4 * c.w + i) * 1024);
    else dma16(c.sw, c.vw[i & 1], c.swo + (i - 4) * 65536 + ks * 524288, 65536 + st * 32768 + (4 * c.w + (i - 4)) * 1024);
}
DI void p1_dma(const P1Ctx& c, int st, int ks) {
#pragma unroll
    for (int i = 0; i < 8; ++i) p1_dma1(c, st, ks, i);
}
template <int ST, int UP, int I> DI void p1_rdw(const P1Ctx& c, bf16x8 (&wf)[4], s16x4 (&wl)[4]) {
    constexpr int nt = I >> 1, rd = I & 1;
    const s16x4 v = vtr(c.wb[nt] + ST * 32768 + UP * 4096 + rd * 512);
    if constexpr (rd == 0) wl[nt] = v; else wf[nt] = cat(wl[nt], v);
}
template <int ST, int G> DI bf16x8 p1_rdx(const P1Ctx& c) { return ld128(c.xb[G >> 3] + ST * 32768 + (G & 7) * 2048); }
template <int ST, int KSC, int K> DI bool p1_is(int ks) { if constexpr (KSC >= 0) return KSC == K; else if constexpr ((K & 1) != ST || K > 5) return false; else return ks == K; }
template <bool ISV, int ST, int KSC, int G> DI void p1_group(const P1Ctx& c, const P2Ctx& c2, const Sync& y, const int ks, u64& seen, f32x4 (&acc)[4][8], bf16x8 (&wA)[4], bf16x8 (&wB)[4], s16x4 (&wl)[4], bf16x8 (&xr)[4]) {
    constexpr int up = G >> 3, mt = G & 7;
    constexpr bool NEXT = KSC != 7;
    if constexpr (G == 13) {
        if (p1_is<ST, KSC, 0>(ks)) asm volatile("s_waitcnt vmcnt(3) lgkmcnt(0)" ::: "memory");
        else if (p1_is<ST, KSC, 1>(ks) || p1_is<ST, KSC, 3>(ks)) asm volatile("s_waitcnt vmcnt(1) lgkmcnt(0)" ::: "memory");
        else if (p1_is<ST, KSC, 5>(ks) || p1_is<ST, KSC, 7>(ks)) asm volatile("s_waitcnt vmcnt(4) lgkmcnt(0)" ::: "memory");
        else asm volatile("s_waitcnt vmcnt(0) lgkmcnt(0)" ::: "memory");
        if constexpr (!ISV && KSC == 6) { if (y.w == 0) { const int l = lane_id() & 31; check_flags(y.flags, FL_P0 + (l < 16 ? 16 * y.j + l : 16 * (15 - y.j) + (l - 16)), seen, y.token); } }
        BARRIER();
        if constexpr (!ISV) {
            if (p1_is<ST, KSC, 1>(ks)) { if (y.w == 0 && lane_id() == 0) set_flag(y.flags, FL_P0 + y.b, y.token); }
        }
        SBAR();
    }
    const bf16x8 x = xr[G & 3];
#pragma unroll
    for (int nt = 0; nt < 4; ++nt) {
        const bf16x8 wv = up == 0 ? wA[nt] : wB[nt];
        if constexpr (ISV) acc[nt][mt] = MF16(x, wv, acc[nt][mt]); else acc[nt][mt] = MF16(wv, x, acc[nt][mt]);
        if (nt == 0) {
            if constexpr (G + 3 < 16) xr[(G + 3) & 3] = p1_rdx<ST, G + 3>(c);
            else if constexpr (NEXT) xr[(G + 3) & 3] = p1_rdx<ST ^ 1, G + 3 - 16>(c);
        }
        if constexpr (G >= 2 && G < 6) { if (nt == 1) p1_rdw<ST, 1, 2 * (G - 2)>(c, wB, wl); if (nt == 2) p1_rdw<ST, 1, 2 * (G - 2) + 1>(c, wB, wl); }
        if constexpr (G >= 13 && NEXT) {
            if (nt == 1) p1_rdw<ST ^ 1, 0, 3 * (G - 13)>(c, wA, wl);
            if (nt == 2) p1_rdw<ST ^ 1, 0, 3 * (G - 13) + 1>(c, wA, wl);
            if constexpr (G < 15) { if (nt == 3) p1_rdw<ST ^ 1, 0, 3 * (G - 13) + 2>(c, wA, wl); }
        }
        if (nt == SPOS) {
            if constexpr (G >= 13) { constexpr int p = G - 13;
                if constexpr (KSC < 0) p1_dma1(c, ST, ks + 2, p);
                else if constexpr (KSC == 6) p2_dma1(c2, 0, 0, p);
                else p2_dma1(c2, 1, 1, p);
            } else if constexpr (G <= 4) { constexpr int p = G + 3;
                if constexpr (KSC == 7) { if constexpr (p < 7) p2_dma1(c2, 0, 0, p); }
                else { if (!p1_is<ST, KSC, 0>(ks)) p1_dma1(c, ST ^ 1, ks + 1, p); }
            }
        }
        SBAR();
    }
    if constexpr (G == 6) {
        if (p1_is<ST, KSC, 1>(ks)) {
            const int l = lane_id(), L = (c.w & 3) * 64 + l;
            const char* pb = (L >= 160 && L < 240 && y.h < 27) ? (const char*)c.w1 + (size_t)(80 * y.j + L - 160) * 3456 + y.h * 128
                                                               : (const char*)c.wq + (size_t)(80 * y.j + ((L < 160 ? L : 0) >> 1)) * 8192 + y.h * 256 + (L & 1) * 128;
            touch(pb, 131072 + c.w * 256);
        }
        if (p1_is<ST, KSC, 3>(ks)) {
            const int l = lane_id(), gw = y.b * 8 + c.w < 1820 ? y.b * 8 + c.w : 0, pu = gw / 10, pq = gw - pu * 10;
            const char* pc = (const char*)(pu < 128 ? c.wo : c.w2) + (size_t)((pu < 128 ? pu : pu - 128) * 32 + (l & 31)) * 1280 + pq * 128;
            touch(pc, 131072 + c.w * 256);
        }
        if constexpr (!ISV && KSC == 6) { if (y.w == 0) { const int l = lane_id() & 31; seen = poll_flag(y.flags, FL_P0 + (l < 16 ? 16 * y.j + l : 16 * (15 - y.j) + (l - 16))); } }
        if (p1_is<ST, KSC, 5>(ks)) { pack_fetch(c, y.b); if constexpr (!ISV && !NOINV) { if (y.w == 0) acquire_issue(); } }
        if constexpr (KSC == 7) pack_store(c, y.b);
        SBAR();
    }
}
template <bool ISV, int ST, int KSC, int... Gs> DI void p1_step(const P1Ctx& c, const P2Ctx& c2, const Sync& y, const int ks, u64& seen, f32x4 (&acc)[4][8], bf16x8 (&wA)[4], bf16x8 (&wB)[4], s16x4 (&wl)[4], bf16x8 (&xr)[4], std::integer_sequence<int, Gs...>) {
    (p1_group<ISV, ST, KSC, Gs>(c, c2, y, ks, seen, acc, wA, wB, wl, xr), ...);
}
template <bool ISV> DI void p1_main(const P1Ctx& c, const P2Ctx& c2, const Sync& y, u32x4 (&kv)[16]) {
    f32x4 acc[4][8];
#pragma unroll
    for (int a = 0; a < 4; ++a)
#pragma unroll
        for (int b = 0; b < 8; ++b) acc[a][b] = (f32x4){0.f, 0.f, 0.f, 0.f};
    bf16x8 wA[4], wB[4], xr[4]; s16x4 wl[4];
    u64 seen = 0;
    asm volatile("" ::: "memory");
    p1_rdw<0, 0, 0>(c, wA, wl); p1_rdw<0, 0, 1>(c, wA, wl); p1_rdw<0, 0, 2>(c, wA, wl); p1_rdw<0, 0, 3>(c, wA, wl);
    p1_rdw<0, 0, 4>(c, wA, wl); p1_rdw<0, 0, 5>(c, wA, wl); p1_rdw<0, 0, 6>(c, wA, wl); p1_rdw<0, 0, 7>(c, wA, wl);
    xr[0] = p1_rdx<0, 0>(c); xr[1] = p1_rdx<0, 1>(c); xr[2] = p1_rdx<0, 2>(c);
    SBAR();
    constexpr auto seq = std::make_integer_sequence<int, 16>{};
#pragma nounroll
    for (int ks = 0; ks < 6; ks += 2) {
        p1_step<ISV, 0, -1>(c, c2, y, ks, seen, acc, wA, wB, wl, xr, seq);
        p1_step<ISV, 1, -1>(c, c2, y, ks + 1, seen, acc, wA, wB, wl, xr, seq);
    }
    p1_step<ISV, 0, 6>(c, c2, y, 6, seen, acc, wA, wB, wl, xr, seq);
    p1_step<ISV, 1, 7>(c, c2, y, 7, seen, acc, wA, wB, wl, xr, seq);
#pragma unroll
    for (int a = 0; a < 8; ++a)
#pragma unroll
        for (int b = 0; b < 2; ++b) {
            if constexpr (ISV) kv[a * 2 + b] = pack8(acc[(a & 1) * 2 + b][(a >> 1) * 2], acc[(a & 1) * 2 + b][(a >> 1) * 2 + 1]);
            else kv[a * 2 + b] = pack8(acc[2 * b][a], acc[2 * b + 1][a]);
        }
}

template <bool HASF, int ST, int I> DI bf16x8 p2_rdw(const P2Ctx& c) {
    constexpr int NW = HASF ? 6 : 4, up = I / NW, n = I % NW;
    lcptr p = (n < 4 ? c.wq[n & 3] : c.wf[n & 1]) + ST * 24576 + up * 4096;
    return cat(vtr(p), vtr(p + 512));
}
template <int ST, int UP, int MT> DI bf16x8 p2_rdx(const P2Ctx& c) { return ld128(c.xb + ST * 32768 + MT * 2048 + UP * 1024); }
template <int I> DI void p2_kvst(const P2Ctx& c, const u32x4 (&kv)[16], bool isv) {
    st16_sc1(c.sws, kv[I], c.vl16, c.skv + (isv ? (unsigned)(((I >> 3) * 16 + ((I >> 2) & 1) * 8 + (I & 3)) * 1024) : (unsigned)((I >> 1) * 4096 + (I & 1) * 1024)));
}
template <bool HASF, int IV> DI void p2_extras(const P2Ctx& c, const u32x4 (&kv)[16], bool isv) {
    if constexpr (IV < 6) { p2_kvst<4 + 2 * IV>(c, kv, isv); p2_kvst<5 + 2 * IV>(c, kv, isv); }
}
struct P2Bias { u32x2 a, b; const bf16_t* p; };
template <bool HASF, int KS, int G> DI void p2_group(const P2Ctx& c, const Sync& y, P2Bias& bs, u64& seen, f32x4 (&acc)[6][4], bf16x8 (&wr)[4], bf16x8 (&x0)[4], bf16x8 (&x1)[4], const u32x4 (&kv)[16], bool isv) {
    constexpr int NW = HASF ? 6 : 4, NG = 2 * NW, ST = KS & 1, up = G / NW, n = G % NW;
    constexpr int NP = HASF ? 7 : 6, BD0 = HASF ? 2 : 0;
    constexpr bool LAST = (KS == 9);
    if constexpr (G == NG - 3) {
        if constexpr (KS == 0 && !SPREAD) asm volatile("s_waitcnt vmcnt(4) lgkmcnt(0)" ::: "memory");
        else if constexpr (KS == 0) asm volatile("s_waitcnt vmcnt(0) lgkmcnt(0)" ::: "memory");
        else if constexpr (HASF && KS == 6) asm volatile("s_waitcnt vmcnt(4) lgkmcnt(0)" ::: "memory");
        else if constexpr (KS <= 6) asm volatile("s_waitcnt vmcnt(2) lgkmcnt(0)" ::: "memory");
        else asm volatile("s_waitcnt vmcnt(0) lgkmcnt(0)" ::: "memory");
        if constexpr (KS == 9) { if (y.w == 0) check_flags(y.flags, FL_KV + y.h * 8 + (lane_id() & 7), seen, y.token); }
        BARRIER();
        if constexpr (KS == (KVR == 3 ? 5 : 7)) { if (y.w == 0 && lane_id() == 0) set_flag(y.flags, FL_KV + y.h * 8 + y.j, y.token); }
        if constexpr (KS == 8) { if (y.w == 0) seen = poll_flag(y.flags, FL_KV + y.h * 8 + (lane_id() & 7)); }
        SBAR();
    }
    const bf16x8 wcur = wr[G & 3];
#pragma unroll
    for (int mt = 0; mt < 4; ++mt) {
        acc[n][mt] = MF16(wcur, up == 0 ? x0[mt] : x1[mt], acc[n][mt]);
        if (mt == 0) {
            if constexpr (G + 3 < NG) wr[(G + 3) & 3] = p2_rdw<HASF, ST, G + 3>(c);
            else if constexpr (!LAST) wr[(G + 3) & 3] = p2_rdw<HASF, ST ^ 1, G + 3 - NG>(c);
        }
        if (mt == 1) {
            if constexpr (G < 4) x1[G] = p2_rdx<ST, 1, G>(c);
            else if constexpr (G >= NG - 3 && !LAST) x0[G - (NG - 3)] = p2_rdx<ST ^ 1, 0, G - (NG - 3)>(c);
        }
        if (mt == 2) { if constexpr (G == NG - 1 && !LAST) x0[3] = p2_rdx<ST ^ 1, 0, 3>(c); }
        if (mt == SPOS) {
            if constexpr (G >= NG - 3) { if constexpr (KS + 2 < 10) p2_dma1(c, ST, KS + 2, G - (NG - 3)); }
            else if constexpr (G + 3 < NP) { if constexpr (KS + 1 < 10) p2_dma1(c, ST ^ 1, KS + 1, G + 3); }
        }
        if constexpr (LAST && G >= NG - 3) {
            constexpr int p0 = (G - (NG - 3)) * 4;
            if (p0 + mt < 4) dma16h(c.sws, c.vl16, c.t0s + (p0 + mt) * 1024, c.t0l + (p0 + mt) * 1024);
            else if (p0 + mt < 8) { if (c.w < 4) dma16h(c.sws, c.vl16, c.t1s + (p0 + mt - 4) * 1024, 65536 + c.t0l + (p0 + mt - 4) * 1024); }
        }
        SBAR();
    }
    if constexpr (G == NP - 3 && KS >= 1) { p2_extras<HASF, KS - 1>(c, kv, isv); SBAR(); }
    if constexpr (HASF && KS == 6 && G == 5) { asm volatile("global_load_dwordx2 %0, %2, off\n\tglobal_load_dwordx2 %1, %2, off offset:32" : "=&v"(bs.a), "=&v"(bs.b) : "v"(bs.p) : "memory"); SBAR(); }
}
template <bool HASF, int KS, int... Gs> DI void p2_step(const P2Ctx& c, const Sync& y, P2Bias& bs, u64& seen, f32x4 (&acc)[6][4], bf16x8 (&wr)[4], bf16x8 (&x0)[4], bf16x8 (&x1)[4], const u32x4 (&kv)[16], bool isv, std::integer_sequence<int, Gs...>) {
    (p2_group<HASF, KS, Gs>(c, y, bs, seen, acc, wr, x0, x1, kv, isv), ...);
}
template <bool HASF, int... KSs> DI void p2_main(const P2Ctx& c, const Sync& y, P2Bias& bs, f32x4 (&acc)[6][4], const u32x4 (&kv)[16], bool isv, std::integer_sequence<int, KSs...>) {
    bf16x8 wr[4], x0[4], x1[4];
    u64 seen = 0;
    asm volatile("" ::: "memory");
    wr[0] = p2_rdw<HASF, 0, 0>(c); wr[1] = p2_rdw<HASF, 0, 1>(c); wr[2] = p2_rdw<HASF, 0, 2>(c);
    x0[0] = p2_rdx<0, 0, 0>(c); x0[1] = p2_rdx<0, 0, 1>(c); x0[2] = p2_rdx<0, 0, 2>(c); x0[3] = p2_rdx<0, 0, 3>(c);
    SBAR();
    (p2_step<HASF, KSs>(c, y, bs, seen, acc, wr, x0, x1, kv, isv, std::make_integer_sequence<int, (HASF ? 12 : 8)>{}), ...);
}
DI float mish(float h) {
    const float t = __builtin_amdgcn_exp2f(__builtin_fminf(h, 20.f) * 1.4426950408889634f);
    const float n = t * (t + 2.f);
    return h * n * __builtin_amdgcn_rcpf(n + 2.f);
}

constexpr int PD = 8;
#define PRIOM 1
#define PRIOV 0
#define ADDMODE 2
#define ABL_NOEARLY 0
#define APRE_EARLY 1
#define LATE_STEP 7
#define LATE_OFF 2
struct AttnRegs { f32x4 o[8][2]; f32x4 ls[2]; f32x4 s[4][2]; bf16x8 q[2][4]; u32x4 pk[2][2]; f32x4 negm[2]; bf16x8 ones; bf16x8 op[32]; };
template <int I> DI void a_read(AttnRegs& r, lcptr vp, lcptr kp) {
    if constexpr (I < 16) r.op[I] = ld128(vp + I * 1024);
    else if constexpr (I < 32) r.op[I] = ld128(kp + (((I - 16) & 3) * 4 + ((I - 16) >> 2)) * 1024);
}
struct Piece { rsrc_t sws; unsigned vl16, soff, ldso; bool on; };
template <int P, bool COND> DI void piece(const Piece& d) {
    if ((!COND && !ABL_NOEARLY) || (COND && d.on)) asm volatile("s_mov_b32 m0, %0\n\ts_nop 3\n\tbuffer_load_dwordx4 %1, %2, %3 offen offset:%4" HS " lds" ::"s"(d.ldso), "v"(d.vl16), "s"(d.sws), "s"(d.soff), "i"(P * 1024) : "memory");
}
template <int I, int DMA> DI void a_slot(AttnRegs& r, lcptr vp, lcptr kp, const Piece& late) {
    if constexpr (I < 16) { constexpr int ks = I >> 3, dt = I & 7;
        r.o[dt][0] = MF16(r.op[I], asbf(r.pk[0][ks]), r.o[dt][0]); r.o[dt][1] = MF16(r.op[I], asbf(r.pk[1][ks]), r.o[dt][1]);
        if constexpr (dt == 3) { r.ls[0] = MF16(r.ones, asbf(r.pk[0][ks]), r.ls[0]); r.ls[1] = MF16(r.ones, asbf(r.pk[1][ks]), r.ls[1]); } }
    else { constexpr int u = (I - 16) >> 2, kt = (I - 16) & 3;
        r.s[kt][0] = MF16(r.op[I], r.q[0][u], u == 0 ? r.negm[0] : r.s[kt][0]); r.s[kt][1] = MF16(r.op[I], r.q[1][u], u == 0 ? r.negm[1] : r.s[kt][1]); }
    a_read<I + PD>(r, vp, kp);
    SBAR();
    if constexpr (DMA != 0 && (I % LATE_STEP) == LATE_OFF && I / LATE_STEP < 4) { piece<I / LATE_STEP, DMA == 2>(late); SBAR(); }
}
template <int B, int DMA, int... Is> DI void a_run(AttnRegs& r, lcptr vp, lcptr kp, const Piece& late, std::integer_sequence<int, Is...>) { (a_slot<B + Is, DMA>(r, vp, kp, late), ...); }
template <int B, int... Is> DI void a_pre(AttnRegs& r, lcptr vp, lcptr kp, std::integer_sequence<int, Is...>) { (a_read<B + Is>(r, vp, kp), ...); }

DI unsigned pkmax(unsigned a, unsigned b) { return __builtin_bit_cast(unsigned, __builtin_elementwise_max(__builtin_bit_cast(u16x2, a), __builtin_bit_cast(u16x2, b))); }
DI unsigned hmax(unsigned v) { const unsigned lo = v & 0xFFFFu, hi = v >> 16; return lo > hi ? lo : hi; }
template <int Q> DI void a_exp_quarter(AttnRegs& r, unsigned& x0, unsigned& x1) {
    constexpr int ks = Q >> 1, qt = Q & 1;
    float e[8];
#pragma unroll
    for (int i = 0; i < 4; ++i) { e[i] = __builtin_amdgcn_exp2f(r.s[2 * ks][qt][i]); e[4 + i] = __builtin_amdgcn_exp2f(r.s[2 * ks + 1][qt][i]); }
    const u32x4 p = {cvtpk(e[0], e[1]), cvtpk(e[2], e[3]), cvtpk(e[4], e[5]), cvtpk(e[6], e[7])};
    r.pk[qt][ks] = p;
    const unsigned m = pkmax(pkmax(p[0], p[1]), pkmax(p[2], p[3]));
    if constexpr (Q == 0) x0 = m; else if constexpr (Q == 1) x1 = m; else if constexpr (qt == 0) x0 = pkmax(x0, m); else x1 = pkmax(x1, m);
}
template <bool DMA> DI void a_exp_pack(AttnRegs& r, unsigned& x0, unsigned& x1, const Piece& early) {
    if constexpr (DMA) { piece<0, false>(early); SBAR(); }
    a_exp_quarter<0>(r, x0, x1);
    if constexpr (DMA) { SBAR(); piece<1, false>(early); SBAR(); }
    a_exp_quarter<1>(r, x0, x1);
    if constexpr (DMA) { SBAR(); piece<2, false>(early); SBAR(); }
    a_exp_quarter<2>(r, x0, x1);
    if constexpr (DMA) { SBAR(); piece<3, false>(early); SBAR(); }
    a_exp_quarter<3>(r, x0, x1);
}
DI float xmax4(float v) {
    const u32x2 a = __builtin_amdgcn_permlane16_swap(__float_as_uint(v), __float_as_uint(v), false, false);
    v = __builtin_fmaxf(__uint_as_float(a[0]), __uint_as_float(a[1]));
    const u32x2 c = __builtin_amdgcn_permlane32_swap(__float_as_uint(v), __float_as_uint(v), false, false);
    return __builtin_fmaxf(__uint_as_float(c[0]), __uint_as_float(c[1]));
}
DI float xsum4(float v) {
    const u32x2 a = __builtin_amdgcn_permlane16_swap(__float_as_uint(v), __float_as_uint(v), false, false);
    v = __uint_as_float(a[0]) + __uint_as_float(a[1]);
    const u32x2 c = __builtin_amdgcn_permlane32_swap(__float_as_uint(v), __float_as_uint(v), false, false);
    return __uint_as_float(c[0]) + __uint_as_float(c[1]);
}
constexpr unsigned P_HI = 0x5D80u, P_LO = 0x2180u;

struct AttnCtx { rsrc_t sws; unsigned vl16, kbase, vbase, ctxl; int nL, grp, qg, j, rowrel, swap_s, first_s; lcptr kl, vl, ql; };
struct AttnSm { float m0, m1; };
DI void a_dmaK(const AttnCtx& c, int n) { dma16x4(c.sws, c.vl16, c.kbase + n * 16384 + c.qg * 4096, (n & 3) * 16384 + c.qg * 4096); }
DI void a_dmaV(const AttnCtx& c, int n) { dma16x4(c.sws, c.vl16, c.vbase + n * 16384 + c.qg * 4096, 65536 + (n & 3) * 16384 + c.qg * 4096); }
#define PT(i) do { } while (0)
#define PARG
#define PPASS
#define PPASSG
DI void a_slotfull(const AttnCtx& c, const Sync& y, AttnRegs& r, AttnSm& z, const int s PARG) {
    const int nL = c.nL, grp = c.grp;
    const bool SWAP = (s == c.swap_s), FIN = (s == c.swap_s + 1), FIRST = (s == c.first_s) || FIN;
    const int tcur = s < nL ? s : 2 * s - nL + grp;
    Piece early, late;
    {
        const int s2 = s + 2, s1 = s + 1;
        const bool eK = grp == 1 && s2 < nL;
        const int te = eK ? s2 : (s1 < nL ? s1 : 2 * s1 - nL + grp), tl = 2 * s2 - nL + grp;
        early.sws = c.sws; early.vl16 = c.vl16; late.sws = c.sws; late.vl16 = c.vl16;
        early.on = eK || (s1 <= 16 && (grp == 0 || s1 >= nL));
        early.soff = (eK ? c.kbase : c.vbase) + te * 16384 + c.qg * 4096; early.ldso = (eK ? 0 : 65536) + (te & 3) * 16384 + c.qg * 4096;
        late.on = s2 <= 16 && s2 >= nL;
        late.soff = c.kbase + tl * 16384 + c.qg * 4096; late.ldso = (tl & 3) * 16384 + c.qg * 4096;
    }
    const int snext = s + 1, tnext = snext < nL ? snext : 2 * snext - nL + grp;
    const lcptr vp = c.vl + (tcur & 3) * 16384, kp = c.kl + (tnext & 3) * 16384;
    u64 seenA = 0;
    if (s == 16) seenA = poll_asm(y.flags, FL_ACT + 32 * y.w + (lane_id() & 31));
    a_pre<0>(r, vp, kp, std::make_integer_sequence<int, PD>{});
    SBAR();
    if (SWAP) {
        asm volatile("ds_read_b128 %0, %8\n\tds_read_b128 %1, %8 offset:1024\n\tds_read_b128 %2, %8 offset:2048\n\tds_read_b128 %3, %8 offset:3072\n\t"
                     "ds_read_b128 %4, %8 offset:4096\n\tds_read_b128 %5, %8 offset:5120\n\tds_read_b128 %6, %8 offset:6144\n\tds_read_b128 %7, %8 offset:7168\n\ts_waitcnt lgkmcnt(0)"
                     : "=&v"(r.q[0][0]), "=&v"(r.q[0][1]), "=&v"(r.q[0][2]), "=&v"(r.q[0][3]), "=&v"(r.q[1][0]), "=&v"(r.q[1][1]), "=&v"(r.q[1][2]), "=&v"(r.q[1][3])
                     : "v"(c.ql) : "memory");
    }
    if (FIN) {
        const float i0 = __builtin_amdgcn_rcpf(r.ls[0][0]), i1 = __builtin_amdgcn_rcpf(r.ls[1][0]);
        const unsigned c1o = c.ctxl + 131072;
        st16_sc1o<0>(c.sws, pack8(r.o[0][0] * i0, r.o[1][0] * i0), c.vl16, c.ctxl); st16_sc1o<1024>(c.sws, pack8(r.o[2][0] * i0, r.o[3][0] * i0), c.vl16, c.ctxl);
        st16_sc1o<2048>(c.sws, pack8(r.o[4][0] * i0, r.o[5][0] * i0), c.vl16, c.ctxl); st16_sc1o<3072>(c.sws, pack8(r.o[6][0] * i0, r.o[7][0] * i0), c.vl16, c.ctxl);
        st16_sc1o<0>(c.sws, pack8(r.o[0][1] * i1, r.o[1][1] * i1), c.vl16, c1o); st16_sc1o<1024>(c.sws, pack8(r.o[2][1] * i1, r.o[3][1] * i1), c.vl16, c1o);
        st16_sc1o<2048>(c.sws, pack8(r.o[4][1] * i1, r.o[5][1] * i1), c.vl16, c1o); st16_sc1o<3072>(c.sws, pack8(r.o[6][1] * i1, r.o[7][1] * i1), c.vl16, c1o);
#pragma unroll
        for (int d = 0; d < 8; ++d) { r.o[d][0] = (f32x4){0.f, 0.f, 0.f, 0.f}; r.o[d][1] = (f32x4){0.f, 0.f, 0.f, 0.f}; }
        r.ls[0] = (f32x4){0.f, 0.f, 0.f, 0.f}; r.ls[1] = (f32x4){0.f, 0.f, 0.f, 0.f};
    }
    {
        const bool heavy = (grp == 1) || (s >= nL);
        const int qb = heavy ? 15 - c.j : c.j, kb = 64 * tcur - 128 * qb - 32 * c.qg;
        if (kb >= -32) {
            const float NI = -__builtin_inff();
            const f32x4 ninf = {NI, NI, NI, NI};
            f32x4 s00 = r.s[0][0], s10 = r.s[1][0], s20 = r.s[2][0], s30 = r.s[3][0], s01 = r.s[0][1], s11 = r.s[1][1], s21 = r.s[2][1], s31 = r.s[3][1];
            const int dq = c.rowrel;
            if (kb > 0) { s00 = ninf; s10 = ninf; s20 = ninf; s30 = ninf; s01 = ninf; s11 = ninf; s21 = ninf; s31 = ninf; }
            else if (kb == 0) {
#pragma unroll
                for (int i = 0; i < 4; ++i) { s00[i] = sel(i > dq, NI, s00[i]); s11[i] = sel(i > dq, NI, s11[i]); }
                s10 = ninf; s20 = ninf; s30 = ninf; s21 = ninf; s31 = ninf;
            } else {
#pragma unroll
                for (int i = 0; i < 4; ++i) { s20[i] = sel(i > dq, NI, s20[i]); s31[i] = sel(i > dq, NI, s31[i]); }
                s30 = ninf;
            }
            r.s[0][0] = s00; r.s[1][0] = s10; r.s[2][0] = s20; r.s[3][0] = s30; r.s[0][1] = s01; r.s[1][1] = s11; r.s[2][1] = s21; r.s[3][1] = s31;
        }
    }
    unsigned a0, a1;
    if (early.on) dma16x4(c.sws, c.vl16, early.soff, early.ldso);
    a_exp_pack<false>(r, a0, a1, early);
    a0 = hmax(a0); a1 = hmax(a1);
    const unsigned lo = FIRST ? P_LO : 0u;
    if (__builtin_expect(__any(a0 > P_HI || a1 > P_HI || a0 < lo || a1 < lo), 0)) {
        float x0 = r.s[0][0][0], x1 = r.s[0][1][0];
#pragma unroll
        for (int kt = 0; kt < 4; ++kt)
#pragma unroll
            for (int i = 0; i < 4; ++i) { x0 = __builtin_fmaxf(x0, r.s[kt][0][i]); x1 = __builtin_fmaxf(x1, r.s[kt][1][i]); }
        x0 = xmax4(x0); x1 = xmax4(x1);
        if (!FIRST) { x0 = __builtin_fmaxf(x0, 0.f); x1 = __builtin_fmaxf(x1, 0.f);
            const float f0 = __builtin_amdgcn_exp2f(-x0), f1 = __builtin_amdgcn_exp2f(-x1);
#pragma unroll
            for (int d = 0; d < 8; ++d) { r.o[d][0] *= f0; r.o[d][1] *= f1; }
            r.ls[0] *= f0; r.ls[1] *= f1; }
        z.m0 += x0; z.m1 += x1;
        r.negm[0] = (f32x4){-z.m0, -z.m0, -z.m0, -z.m0}; r.negm[1] = (f32x4){-z.m1, -z.m1, -z.m1, -z.m1};
#pragma unroll
        for (int kt = 0; kt < 4; ++kt) { r.s[kt][0] -= x0; r.s[kt][1] -= x1; }
        a_exp_pack<false>(r, a0, a1, early);
    }
    if (SWAP) {
        z.m0 = 0.f; z.m1 = 0.f;
        r.negm[0] = (f32x4){0.f, 0.f, 0.f, 0.f}; r.negm[1] = (f32x4){0.f, 0.f, 0.f, 0.f};
    }
    SBAR();
    PT(0);
    if (FIN) { if (early.on) WAITV(12); else WAITV(8); } else { if (early.on) WAITV(4); else WAITV(0); }
    if (s == 16) { asm volatile("" : "+v"(seenA)); check_flags(y.flags, FL_ACT + 32 * y.w + (lane_id() & 31), seenA, y.token); }
    PT(1);
    BARRIER(); SBAR();
    PT(2);
    __builtin_amdgcn_s_setprio(PRIOM);
    a_run<0, 2>(r, vp, kp, late, std::make_integer_sequence<int, 16>{});
    if (s < 16) a_run<16, 2>(r, vp, kp, late, std::make_integer_sequence<int, 16>{});
    __builtin_amdgcn_s_setprio(PRIOV);
    SBAR();
    PT(3);
    if (late.on) WAITV(4); else WAITV(0);
    PT(4);
    BARRIER(); SBAR();
    PT(5);
}

struct Lean { unsigned e_soff, e_lds, l_soff, l_lds, voff, koff; };
template <bool LATE> DI void a_slot_lean(const AttnCtx& c, AttnRegs& r, AttnSm& z, const Lean& st PARG) {
    const lcptr vp = c.vl + st.voff, kp = c.kl + st.koff;
    Piece early, late;
    early.sws = c.sws; early.vl16 = c.vl16; early.soff = st.e_soff; early.ldso = st.e_lds; early.on = true;
    late.sws = c.sws; late.vl16 = c.vl16; late.soff = st.l_soff; late.ldso = st.l_lds; late.on = true;
    a_pre<0>(r, vp, kp, std::make_integer_sequence<int, PD>{});
    SBAR();
    unsigned a0, a1;
    a_exp_pack<true>(r, a0, a1, early);
    if (__builtin_expect(__any(hmax(pkmax(a0, a1)) > P_HI), 0)) {
        float x0 = 0.f, x1 = 0.f;
#pragma unroll
        for (int kt = 0; kt < 4; ++kt)
#pragma unroll
            for (int i = 0; i < 4; ++i) { x0 = __builtin_fmaxf(x0, r.s[kt][0][i]); x1 = __builtin_fmaxf(x1, r.s[kt][1][i]); }
        x0 = xmax4(x0); x1 = xmax4(x1);
        const float f0 = __builtin_amdgcn_exp2f(-x0), f1 = __builtin_amdgcn_exp2f(-x1);
#pragma unroll
        for (int d = 0; d < 8; ++d) { r.o[d][0] *= f0; r.o[d][1] *= f1; }
        r.ls[0] *= f0; r.ls[1] *= f1;
        z.m0 += x0; z.m1 += x1;
        r.negm[0] = (f32x4){-z.m0, -z.m0, -z.m0, -z.m0}; r.negm[1] = (f32x4){-z.m1, -z.m1, -z.m1, -z.m1};
#pragma unroll
        for (int kt = 0; kt < 4; ++kt) { r.s[kt][0] -= x0; r.s[kt][1] -= x1; }
        a_exp_pack<false>(r, a0, a1, early);
    }
    SBAR();
    PT(0);
    WAITV(4);
    PT(1);
    BARRIER(); SBAR();
    PT(2);
    __builtin_amdgcn_s_setprio(PRIOM);
    a_run<0, LATE ? 1 : 0>(r, vp, kp, late, std::make_integer_sequence<int, 32>{});
    __builtin_amdgcn_s_setprio(PRIOV);
    SBAR();
    PT(3);
    if constexpr (LATE) WAITV(4); else WAITV(0);
    PT(4);
    BARRIER(); SBAR();
    PT(5);
}

#define P4W0 11
constexpr unsigned P4STG = 36864, P4TAG = 0x3FFu, P4OOB = 0xF0000000u;
struct P4Ctx { rsrc_t sws; unsigned vl16, ga, gw, gas, gws, dw0, dw1, dw2, lw, tag, so[3], so2[3], lo[3], lo2[3]; int w, q, o, ng, ra, rw; lcptr pa, pw; };
struct P4Regs { bf16x8 xa[2][2], wa[2][5]; };
struct P4Xch { f32x4 own[3]; u32x4 snd[9], rcv[9]; };
DI void mfa(f32x4& acc, bf16x8 a, bf16x8 b) { asm volatile("v_mfma_f32_16x16x32_bf16 %0, %1, %2, %0" : "+v"(acc) : "v"(a), "v"(b)); }
DI void mfz(f32x4& acc, bf16x8 a, bf16x8 b) { asm volatile("v_mfma_f32_16x16x32_bf16 %0, %1, %2, 0" : "=&v"(acc) : "v"(a), "v"(b)); }
template <int OFF> DI void dma_sc1(const P4Ctx& c, unsigned soff, unsigned ldso) {
    asm volatile("s_mov_b32 m0, %0\n\ts_nop 3\n\tbuffer_load_dwordx4 %1, %2, %3 offen offset:%4 sc1 lds" ::"s"(ldso), "v"(c.vl16), "s"(c.sws), "s"(soff), "i"(OFF) : "memory");
}
template <int P> DI void p4_piece(P4Ctx& c, unsigned st) {
    if constexpr (P == 0) dma_sc1<0>(c, c.ga, st + c.w * 2048);
    else if constexpr (P == 1) { dma_sc1<1024>(c, c.ga, st + c.w * 2048); ++c.ra; c.ga = c.ra == c.ng ? c.gas : c.ga + 2048; }
    else if constexpr (P == 2) dma_sc1<0>(c, c.gw + c.dw0, st + c.lw);
    else if constexpr (P == 3) dma_sc1<0>(c, c.gw + c.dw1, st + c.lw + 8192);
    else { if (c.w < 4) dma_sc1<0>(c, c.gw + c.dw2, st + c.lw + 16384); ++c.rw; c.gw = c.rw == c.ng ? c.gws : c.gw + 81920; }
}
template <int B, int UP, int I> DI void p4_rd(const P4Ctx& c, P4Regs& r, unsigned st) {
    if constexpr (I == 0) r.wa[B][0] = ld128(c.pw + st + UP * 10240);
    else if constexpr (I <= 2) r.xa[B][I - 1] = ld128(c.pa + st + (I - 1) * 2048 + UP * 1024);
    else if constexpr (I < 7) r.wa[B][I - 2] = ld128(c.pw + st + UP * 10240 + (I - 2) * 1024);
}
template <int N> DI void p4_put(const P4Ctx& c, const P4Xch& x, unsigned xoff) { st16_sc1o<(N % 3) * 1024>(c.sws, x.snd[N], c.vl16, (N % 3 == 2 ? c.so2[N / 3] : c.so[N / 3]) + xoff); }
template <int N> DI void p4_get(const P4Ctx& c, P4Xch& x, unsigned xoff) {
    asm volatile("buffer_load_dwordx4 %0, %1, %2, %3 offen offset:%4 sc1" : "=v"(x.rcv[N]) : "v"(c.vl16), "s"(c.sws), "s"((N % 3 == 2 ? c.lo2[N / 3] : c.lo[N / 3]) + xoff), "i"((N % 3) * 1024) : "memory");
}
template <int O> DI void p4_split(const f32x4 (&acc)[10], unsigned tag, P4Xch& x) {
#pragma unroll
    for (int k = 0; k < 3; ++k) x.own[k] = acc[O + 4 * k < 10 ? O + 4 * k : O];
#pragma unroll
    for (int n = 0; n < 9; ++n) { const int i = ((O + 1 + n / 3) & 3) + 4 * (n % 3);
        const u32x4 v = __builtin_bit_cast(u32x4, acc[i < 10 ? i : 0]);
#pragma unroll
        for (int e = 0; e < 4; ++e) x.snd[n][e] = (v[e] & ~P4TAG) | tag; }
}
DI void p4_split(const P4Ctx& c, f32x4 (&a)[10], P4Xch& x) {
    asm volatile("s_nop 7\n\ts_nop 7\n\ts_nop 7" : "+v"(a[0]), "+v"(a[1]), "+v"(a[2]), "+v"(a[3]), "+v"(a[4]), "+v"(a[5]), "+v"(a[6]), "+v"(a[7]), "+v"(a[8]), "+v"(a[9]) :: "memory");
    switch (c.o) { case 0: p4_split<0>(a, c.tag, x); break; case 1: p4_split<1>(a, c.tag, x); break; case 2: p4_split<2>(a, c.tag, x); break; default: p4_split<3>(a, c.tag, x); break; }
}
DI bool p4_tagged(const P4Ctx& c, const P4Xch& x) {
    unsigned a = 0, b = 0;
#pragma unroll
    for (int n = 0; n < 9; ++n)
#pragma unroll
        for (int e = 0; e < 4; ++e) { if (n % 3 == 2) b |= x.rcv[n][e] ^ c.tag; else a |= x.rcv[n][e] ^ c.tag; }
    return __all(((a | (c.o < 2 ? b : 0u)) & P4TAG) == 0);
}
template <int... Ns> DI void p4_refetch(const P4Ctx& c, P4Xch& x, unsigned xoff, std::integer_sequence<int, Ns...>) {
    const unsigned long long t0 = __builtin_amdgcn_s_memrealtime();
    for (;;) {
        (p4_get<Ns>(c, x, xoff), ...);
        asm volatile("s_waitcnt vmcnt(0)" : "+v"(x.rcv[0]), "+v"(x.rcv[1]), "+v"(x.rcv[2]), "+v"(x.rcv[3]), "+v"(x.rcv[4]), "+v"(x.rcv[5]), "+v"(x.rcv[6]), "+v"(x.rcv[7]), "+v"(x.rcv[8]) :: "memory");
        if (p4_tagged(c, x)) break;
        if (__builtin_amdgcn_s_memrealtime() - t0 > 2000000ull) break;
        __builtin_amdgcn_s_sleep(1);
    }
}
DI f32x4 p4_untag(u32x4 v) { return __builtin_bit_cast(f32x4, v & ~P4TAG); }
DI f32x4 p4_total(const P4Xch& x, int k) { return (x.own[k] + p4_untag(x.rcv[k])) + (p4_untag(x.rcv[3 + k]) + p4_untag(x.rcv[6 + k])); }
template <int B, bool ZC, bool RD, int DMA, int PUT, int GET, int... Is> DI void p4_half(P4Ctx& c, P4Regs& r, P4Xch& x, f32x4 (&acc)[10], unsigned rs, unsigned ds, std::integer_sequence<int, Is...>) {
    auto one = [&](auto ic) {
        constexpr int I = decltype(ic)::value;
        if constexpr (ZC) mfz(acc[I], r.wa[B][I >> 1], r.xa[B][I & 1]); else mfa(acc[I], r.wa[B][I >> 1], r.xa[B][I & 1]);
        if constexpr (RD) p4_rd<B ^ 1, B ^ 1, I>(c, r, rs);
        if constexpr (DMA == 1) { if constexpr (I == 1) p4_piece<0>(c, ds); if constexpr (I == 5) p4_piece<1>(c, ds); if constexpr (I == 9) p4_piece<2>(c, ds); }
        if constexpr (DMA == 2) { if constexpr (I == 3) p4_piece<3>(c, ds); if constexpr (I == 7) p4_piece<4>(c, ds); }
        if constexpr (PUT >= 0 && B == 0 && I == 5) p4_put<PUT>(c, x, OFF_XG);
        if constexpr (GET >= 0 && (I & 1) && GET + (I >> 1) < 9) p4_get<GET + (I >> 1)>(c, x, OFF_XG);
        SBAR();
    };
    (one(std::integral_constant<int, Is>{}), ...);
}
template <int WAITN, bool RA, bool RB, bool LAST, int EV, int PUT, int GETA, int GETB, bool ZC = false> DI void p4_step(P4Ctx& c, const Sync& y, P4Regs& r, P4Xch& x, f32x4 (&acc)[10], unsigned& sb, unsigned& sp, int t, unsigned fidx, u64& seen) {
    constexpr auto seq = std::make_integer_sequence<int, 10>{};
    const unsigned sn = sb == 3 * P4STG ? 0 : sb + P4STG;
    p4_half<0, ZC, true, RA ? 2 : 0, PUT, GETA>(c, r, x, acc, sb, sp, seq);
    if constexpr (!LAST) {
        asm volatile("s_waitcnt vmcnt(%0) lgkmcnt(0)" :: "i"(WAITN) : "memory");
        if constexpr (EV == 1) { if (t == c.ng - 4 && c.w == 0) { asm volatile("s_waitcnt vmcnt(4)" : "+v"(seen) :: "memory"); check_flags(y.flags, fidx, seen, y.token); } }
        BARRIER();
        if constexpr (EV == 1) {
            if (c.w == 0 && t == c.ng - 5) seen = poll_asm(y.flags, fidx);
        }
        if constexpr (EV == 2) { if (c.w == 0 && lane_id() == 0) set_flag(y.flags, FL_CTX + y.j * 32 + y.h, y.token); }
        SBAR();
    }
    p4_half<1, false, !LAST, RB ? 1 : 0, PUT, GETB>(c, r, x, acc, sn, sb, seq);
    sp = sb; sb = sn;
}

template <int... Is> DI void p4_ssteps(P4Ctx& c, const Sync& y, P4Regs& r, P4Xch& x, f32x4 (&acc)[10], unsigned& sb, unsigned& sp, u64& seen, std::integer_sequence<int, Is...>) {
    (p4_step<8 + (Is >= 2 && Is <= 10) + (Is >= 1 && Is <= 9) + (Is <= 8), true, true, false, 0, (Is <= 8 ? Is : -1), -1, -1, Is == 0>(c, y, r, x, acc, sb, sp, 0, 0, seen), ...);
}

__global__ __launch_bounds__(512, 2) void fused_kernel(const bf16_t* __restrict__ x, const bf16_t* __restrict__ ckv, const bf16_t* __restrict__ wq, const bf16_t* __restrict__ wuk,
                                                       const bf16_t* __restrict__ wuv, const bf16_t* __restrict__ wo, const bf16_t* __restrict__ w1, const bf16_t* __restrict__ b1,
                                                       const bf16_t* __restrict__ w2, bf16_t* __restrict__ out, char* __restrict__ ws) {
    __shared__ __attribute__((aligned(1024))) char smem[163840];
    {
        u64 pc; unsigned off;
        asm volatile("s_getpc_b64 %0\n.Lcode_a:\n\ts_mov_b32 m0, 0x20000\n\tv_lshlrev_b32 %1, 3, %2\n\tv_lshl_add_u32 %1, %3, 12, %1\n\tv_min_u32 %1, .Lcode_z-.Lcode_a-4, %1\n\t"
                     "global_load_lds_dword %1, %0" : "=&s"(pc), "=&v"(off) : "v"(threadIdx.x), "s"(blockIdx.x >> 3) : "memory");
        SBAR();
    }
    const lptr lds = (lptr)smem;
    const int b = blockIdx.x, xcd = b & 7, bi = b >> 3, h = xcd * 4 + (bi >> 3), j = bi & 7;
    const int w = __builtin_amdgcn_readfirstlane(threadIdx.x >> 6);
    STAMP(0);
    const rsrc_t sws = mk_rsrc(ws, WS_BYTES);
    Sync y;
    y.flags = (gu64*)(ws + OFF_FLAGS); y.b = b; y.h = h; y.j = j; y.w = w;
    u64 epoch;
    asm volatile("s_load_dwordx2 %0, %1, 0x20" : "=s"(epoch) : "s"(ws) : "memory");
    u32x4 kv[16];
    P1Ctx c1;
    P2Ctx c2;
    const bool isv = (w & 3) >= 2;
    {
        const int l = lane_id(), g = l >> 4, q = (l & 15) >> 2;
        {
            const int nb = w >> 1;
            c1.w = w; c1.sws = sws; c1.wo = wo; c1.w2 = w2; c1.wq = wq; c1.w1 = w1; c1.lds = lds;
            c1.sx = mk_rsrc(ckv, 2048 * 512 * 2);
            c1.sw = mk_rsrc(nb < 2 ? wuk : wuv, 512 * 4096 * 2);
#pragma unroll
            for (int par = 0; par < 2; ++par) {
                c1.vx[par] = (l >> 3) * 1024 + (((l & 7) ^ (4 * par + (l >> 4))) * 16);
                c1.vw[par] = (l >> 3) * 8192 + (((l & 7) ^ ((((l >> 4) & 1) | (par << 1)) << 1)) * 16);
            }
            c1.sxo = (256 * j + 32 * w) * 1024;
            c1.swo = (32 * (w & 1)) * 8192 + (h * 128 + 64 * (nb & 1)) * 2;
            const int wn = w & 3, wm = w >> 2, f = (l & 15) >> 1, s = ((q >> 1) & 1) | ((g & 1) << 1);
            c1.xb[0] = lds + wm * 16384 + (l & 15) * 128 + ((g ^ f) * 16);
            c1.xb[1] = lds + wm * 16384 + (l & 15) * 128 + (((g ^ f) ^ 4) * 16);
#pragma unroll
            for (int nt = 0; nt < 4; ++nt) c1.wb[nt] = lds + 65536 + wn * 8192 + (8 * g + q) * 128 + ((nt ^ s) * 32) + (l & 3) * 8;
        }
        p1_dma(c1, 0, 0);
        u32x4 xo0, xo1;
        {
            const rsrc_t sxi = mk_rsrc(x, 2048 * 640 * 2);
            const unsigned vx = (l & 7) * 1280 + (l >> 3) * 16, so0 = b * 8 * 1280 + w * 128, so1 = b * 8 * 1280 + (8 + (w & 1)) * 128;
            asm volatile("s_nop 4\n\tbuffer_load_dwordx4 %0, %2, %3, %4 offen\n\tbuffer_load_dwordx4 %1, %2, %3, %5 offen" : "=&v"(xo0), "=&v"(xo1) : "v"(vx), "s"(sxi), "s"(so0), "s"(so1) : "memory");
        }
        BARRIER();
        p1_dma(c1, 1, 1);
        asm volatile("s_waitcnt lgkmcnt(0)" : "+s"(epoch) :: "memory");
        y.token = epoch + 1;
        STAMP(12);
        asm volatile("s_waitcnt vmcnt(8)" : "+v"(xo0), "+v"(xo1) :: "memory");
        {
            const int i = l - 16;
            const char* pc = (const char*)ckv + (size_t)(256 * j + 64 * (h & 3) + 8 * w + ((l & 15) >> 1)) * 1024 + (2 + (l & 1)) * 128;
            const char* pw = (const char*)((i & 2) ? wuv : wuk) + (size_t)(128 + 16 * j + 2 * w + ((i >> 2) & 1)) * 8192 + h * 256 + (i & 1) * 128;
            touch(l >= 16 && l < 24 ? pw : pc, 131072 + w * 256);
        }
        STAMP(13);
        float v0[8], v1[8];
#pragma unroll
        for (int e = 0; e < 4; ++e) { v0[2 * e] = bflo(xo0[e]); v0[2 * e + 1] = bfhi(xo0[e]); v1[2 * e] = bflo(xo1[e]); v1[2 * e + 1] = bfhi(xo1[e]); }
        auto red8 = [](float t) { t += __uint_as_float(__builtin_amdgcn_update_dpp(0u, __float_as_uint(t), 0x128, 0xf, 0xf, false)); return xsum4(t); };
        {
            const float m0 = red8(((v0[0] + v0[1]) + (v0[2] + v0[3])) + ((v0[4] + v0[5]) + (v0[6] + v0[7]))) * (1.f / 64.f);
            const float m1 = red8(((v1[0] + v1[1]) + (v1[2] + v1[3])) + ((v1[4] + v1[5]) + (v1[6] + v1[7]))) * (1.f / 64.f);
            float q0 = 0.f, q1 = 0.f;
#pragma unroll
            for (int e = 0; e < 8; ++e) { const float d0 = v0[e] - m0, d1 = v1[e] - m1; q0 += d0 * d0; q1 += d1 * d1; }
            q0 = red8(q0); q1 = red8(q1);
            if (l < 8) { *(LAS f32x2*)(lds + 162816 + l * 80 + w * 8) = (f32x2){m0, q0}; if (w < 2) *(LAS f32x2*)(lds + 162816 + l * 80 + 64 + w * 8) = (f32x2){m1, q1}; }
        }
        asm volatile("s_waitcnt lgkmcnt(0)" ::: "memory"); BARRIER();
        STAMP(14);
        {
            f32x4 t[5];
#pragma unroll
            for (int i = 0; i < 5; ++i) t[i] = *(const LAS f32x4*)(lds + 162816 + (l & 7) * 80 + i * 16);
            float mean = 0.f;
#pragma unroll
            for (int i = 0; i < 5; ++i) mean += t[i][0] + t[i][2];
            mean *= 0.1f;
            float m2 = 0.f;
#pragma unroll
            for (int i = 0; i < 5; ++i) { const float d0 = t[i][0] - mean, d1 = t[i][2] - mean; m2 += (t[i][1] + t[i][3]) + 64.f * (d0 * d0 + d1 * d1); }
            const float rstd = 1.f / __builtin_sqrtf(m2 * (1.f / 640.f) + 1e-5f);
            const unsigned vo = (l >> 3) * 256 + (l & 7) * 16, xo = OFF_XN + (b >> 1) * 20480 + (b & 1) * 128;
            u32x4 o0, o1;
#pragma unroll
            for (int e = 0; e < 4; ++e) { o0[e] = cvtpk((v0[2 * e] - mean) * rstd, (v0[2 * e + 1] - mean) * rstd); o1[e] = cvtpk((v1[2 * e] - mean) * rstd, (v1[2 * e + 1] - mean) * rstd); }
            st16_sc1(sws, o0, vo, xo + w * 2048);
            if (w < 2) st16_sc1(sws, o1, vo, xo + (8 + w) * 2048);
        }
        {
            const int i = l - 32;
            const char* pc = (const char*)ckv + (size_t)(256 * j + 64 * (h & 3) + 8 * w + ((l & 31) >> 2)) * 1024 + (4 + (l & 3)) * 128;
            const char* pw = (const char*)((i & 2) ? wuv : wuk) + (size_t)(256 + 32 * j + 4 * w + ((i >> 2) & 3)) * 8192 + h * 256 + (i & 1) * 128;
            touch(l >= 48 && h < 27 ? (const char*)(b1 + 64 * h) : (l >= 32 && l < 48 ? pw : pc), 131072 + w * 256);
        }
        {
            c2.w = w; c2.sws = sws; c2.swq = mk_rsrc(wq, 640 * 4096 * 2); c2.sw1 = mk_rsrc(w1, 640 * 1728 * 2);
            c2.vl16 = l * 16;
            const int swz = ((l >> 4) & 1) | ((w & 1) << 1);
            c2.vwq = (l >> 3) * 8192 + (((l & 7) ^ (swz << 1)) * 16);
            c2.vw1 = (l >> 3) * 3456 + (((l & 7) ^ (swz << 1)) * 16);
            { const int mtl = 2 * w, mtg = mtl < 8 ? 8 * j + mtl : 8 * (15 - j) + (mtl - 8); c2.sxo = OFF_XN + mtg * 20480; }
            c2.swqo = (8 * w) * 8192 + h * 256;
            c2.sw1o = (8 * w) * 3456 + h * 128;
            c2.t0l = (w >> 2) * 16384 + (w & 3) * 4096; c2.t0s = OFF_K + (unsigned)h * 524288 + c2.t0l; c2.t1s = OFF_V + (unsigned)h * 524288 + (w & 3) * 4096;
            { const int wn = w & 3, wm = w >> 2;
              c2.skv = isv ? OFF_V + (unsigned)(h * 32 + 4 * j + 2 * wm) * 16384 + (wn - 2) * 4096 : OFF_K + (unsigned)((h * 128 + 16 * j + 8 * wm) * 4 + 2 * wn) * 1024; }
        }
    }
    STAMP(1); CSTAMP(14);
    if (isv) p1_main<true>(c1, c2, y, kv); else p1_main<false>(c1, c2, y, kv);
    STAMP(2); CSTAMP(15);
    p2_kvst<0>(c2, kv, isv); p2_kvst<1>(c2, kv, isv); p2_kvst<2>(c2, kv, isv); p2_kvst<3>(c2, kv, isv);
    const bool hasf = h < 27;
    AttnCtx c3;
    AttnRegs r;
    {
        const int l = lane_id(), g = l >> 4, q = (l & 15) >> 2;
        const int wn = w & 1, wm = w >> 1;
        const int s = ((q >> 1) & 1) | ((g & 1) << 1);
        c2.xb = lds + wm * 8192 + l * 16;
#pragma unroll
        for (int nt = 0; nt < 4; ++nt) c2.wq[nt] = lds + 65536 + wn * 8192 + (8 * g + q) * 128 + ((nt ^ s) * 32) + (l & 3) * 8;
#pragma unroll
        for (int ft = 0; ft < 2; ++ft) c2.wf[ft] = lds + 65536 + 16384 + (8 * g + q) * 128 + (((2 * wn + ft) ^ s) * 32) + (l & 3) * 8;
        f32x4 acc[6][4];
#pragma unroll
        for (int a = 0; a < 6; ++a)
#pragma unroll
            for (int m = 0; m < 4; ++m) acc[a][m] = (f32x4){0.f, 0.f, 0.f, 0.f};
        STAMP(3);
        P2Bias bs; bs.p = b1 + (64 * h + 32 * wn + 4 * g); bs.a = (u32x2){0u, 0u}; bs.b = (u32x2){0u, 0u};
        if (hasf) p2_main<true>(c2, y, bs, acc, kv, isv, std::make_integer_sequence<int, 10>{}); else p2_main<false>(c2, y, bs, acc, kv, isv, std::make_integer_sequence<int, 10>{});
        STAMP(4);
        constexpr float C2 = 0.08838834764831845f * 1.4426950408889634f;
#pragma unroll
        for (int mt = 0; mt < 4; ++mt)
#pragma unroll
            for (int a = 0; a < 2; ++a)
                *(LAS u32x4*)(lds + 98304 + ((4 * wm + mt) * 4 + 2 * wn + a) * 1024 + l * 16) = pack8(acc[2 * a][mt] * C2, acc[2 * a + 1][mt] * C2);
        u32x4 actf[4];
        if (hasf) {
            asm volatile("" : "+v"(bs.a), "+v"(bs.b));
            const u32x2 bA = bs.a, bB = bs.b;
            const f32x4 biasA = {bflo(bA[0]), bfhi(bA[0]), bflo(bA[1]), bfhi(bA[1])}, biasB = {bflo(bB[0]), bfhi(bB[0]), bflo(bB[1]), bfhi(bB[1])};
#pragma unroll
            for (int mt = 0; mt < 4; ++mt) {
                f32x4 hA = acc[4][mt] + biasA, hB = acc[5][mt] + biasB;
#pragma unroll
                for (int i = 0; i < 4; ++i) { hA[i] = mish(hA[i]); hB[i] = mish(hB[i]); }
                actf[mt] = pack8(hA, hB);
            }
        }
        asm volatile("s_waitcnt vmcnt(0) lgkmcnt(0)" ::: "memory"); BARRIER();
        const int grp = w >> 2, qg = w & 3;
        c3.sws = sws; c3.vl16 = l * 16; c3.kbase = OFF_K + (unsigned)h * 524288; c3.vbase = OFF_V + (unsigned)h * 524288;
        c3.ctxl = OFF_CTX + ((8 * j + 2 * qg) * 128 + 4 * h) * 1024;
        c3.nL = 2 * j + 2; c3.grp = grp; c3.qg = qg; c3.j = j; c3.rowrel = (l & 15) - 4 * g; c3.kl = lds + l * 16; c3.vl = lds + 65536 + l * 16;
        c3.ql = lds + 131072 + qg * 8192 + l * 16;
        if (hasf) {
#pragma unroll
            for (int mt = 0; mt < 4; ++mt) { const int mtl = 4 * wm + mt, mtg = mtl < 8 ? 8 * j + mtl : 8 * (15 - j) + (mtl - 8);
                st16_sc1(sws, actf[mt], l * 16, OFF_ACT + (mtg * 54 + 2 * h + wn) * 1024); }
        }
#pragma unroll
        for (int qt = 0; qt < 2; ++qt)
#pragma unroll
            for (int u = 0; u < 4; ++u) r.q[qt][u] = ld128(lds + 98304 + ((8 * grp + 2 * qg + qt) * 4 + u) * 1024 + l * 16);
    }
    STAMP(5);
    P4Ctx c4;
    {
        const int l = lane_id();
        const int grp = c3.grp, qg = c3.qg;
        const unsigned vl16 = c3.vl16;
#pragma unroll
        for (int d = 0; d < 8; ++d) { r.o[d][0] = (f32x4){0.f, 0.f, 0.f, 0.f}; r.o[d][1] = (f32x4){0.f, 0.f, 0.f, 0.f}; }
        r.negm[0] = (f32x4){0.f, 0.f, 0.f, 0.f}; r.negm[1] = (f32x4){0.f, 0.f, 0.f, 0.f};
        AttnSm z; z.m0 = 0.f; z.m1 = 0.f;
        r.ls[0] = (f32x4){0.f, 0.f, 0.f, 0.f}; r.ls[1] = (f32x4){0.f, 0.f, 0.f, 0.f};
        r.ones = (bf16x8){0x3F80, 0x3F80, 0x3F80, 0x3F80, 0x3F80, 0x3F80, 0x3F80, 0x3F80}; PIN(r.ones);
        asm volatile("s_waitcnt lgkmcnt(0)" ::: "memory");
        BARRIER();
        STAMP(6); CSTAMP(12);
        a_pre<16>(r, c3.vl, c3.kl, std::make_integer_sequence<int, PD>{});
        if (grp == 1) BARRIER();
        { Piece none; none.on = false; none.sws = sws; none.vl16 = 0; none.soff = 0; none.ldso = 0; a_run<16, 0>(r, c3.vl, c3.kl, none, std::make_integer_sequence<int, 16>{}); }
        BARRIER();
        c3.swap_s = grp == 0 ? c3.nL - 1 : 99; c3.first_s = 0;
        asm volatile("" : "+s"(c3.swap_s), "+s"(c3.first_s));
        {
            const int nL = c3.nL;
            auto lean_init = [&](int s) {
                Lean st;
                const bool eK = grp == 1 && s + 2 < nL;
                const int t0 = s < nL ? s : 2 * s - nL + grp, t1 = s + 1 < nL ? s + 1 : 2 * s + 2 - nL + grp, t2 = s + 2 < nL ? s + 2 : 2 * s + 4 - nL + grp;
                const int te = eK ? t2 : t1;
                st.e_soff = (eK ? c3.kbase : c3.vbase) + te * 16384 + qg * 4096; st.e_lds = (eK ? 0 : 65536) + (te & 3) * 16384 + qg * 4096;
                st.l_soff = c3.kbase + t2 * 16384 + qg * 4096; st.l_lds = (t2 & 3) * 16384 + qg * 4096;
                st.voff = (t0 & 3) * 16384; st.koff = (t1 & 3) * 16384;
                return st;
            };
            int s = 0, phase = 0;
#pragma nounroll
            for (;;) {
                const int gend = phase == 0 ? 1 : (phase == 1 ? (grp == 0 ? nL + 1 : nL - 1) : 17);
#pragma nounroll
                while (s < gend) { a_slotfull(c3, y, r, z, s PPASSG); ++s; }
                if (phase == 0) { if (w == 0 && l == 0) set_flag(y.flags, FL_ACT + j * 32 + h, y.token); }
                if (phase == 2 || s > 16) break;
                if (phase == 1 && s <= 14) {
                    Lean st = lean_init(s);
#pragma nounroll
                    do { a_slot_lean<true>(c3, r, z, st PPASS);
                         st.e_soff += 32768; st.e_lds ^= 32768; st.l_soff += 32768; st.l_lds ^= 32768; st.voff ^= 32768; st.koff ^= 32768; ++s; } while (s <= 14);
                }
                const int send = phase == 0 ? nL - 2 : 16;
                if (s < send) {
                    Lean st = lean_init(s);
#pragma nounroll
                    do { a_slot_lean<false>(c3, r, z, st PPASS);
                         st.e_soff += 16384; st.e_lds = (st.e_lds & ~0xC000u) | ((st.e_lds + 16384) & 0xC000u); st.voff = (st.voff + 16384) & 0xC000u; st.koff = (st.koff + 16384) & 0xC000u; ++s; } while (s < send);
                }
                ++phase;
            }
        }
        const float m0 = z.m0, m1 = z.m1, l0 = r.ls[0][0], l1 = r.ls[1][0];
        if (grp == 0) BARRIER();
        STAMP(7); CSTAMP(13);
        {
            const int q = xcd >> 1, rbk = (xcd & 1) * 8 + (bi >> 2), cb = bi & 3, wm = w >> 1, wn = w & 1;
            c4.sws = sws; c4.vl16 = vl16; c4.w = w; c4.q = q; c4.o = (q + 2 * wn) & 3; c4.ng = q == 3 ? 6 : 7; c4.ra = 0; c4.rw = 0;
            c4.ga = OFF_ACT + ((8 * rbk + w) * 54 + 14 * q) * 1024; c4.gas = OFF_CTX + ((8 * rbk + w) * 128 + 32 * q) * 1024;
            c4.gw = OFF_WP + ((128 + 14 * q) * 40 + 10 * cb) * 1024; c4.gws = OFF_WP + (32 * q * 40 + 10 * cb) * 1024;
            c4.dw0 = w * 1024; c4.dw1 = (w < 2 ? 8 + w : 38 + w) * 1024; c4.dw2 = (46 + w) * 1024; c4.lw = 16384 + w * 1024;
            c4.tag = (unsigned)y.token & P4TAG;
            const unsigned xb = (unsigned)(((xcd & 1) * 32 + bi) * 128 + w) * 3072;
#pragma unroll
            for (int d = 0; d < 3; ++d) { const int qq = (q + 1 + d) & 3;
                c4.so[d] = xb + qq * 98304 + q * 24576; c4.so2[d] = ((c4.o + 1 + d) & 3) < 2 ? c4.so[d] : P4OOB;
                c4.lo[d] = xb + q * 98304 + qq * 24576; c4.lo2[d] = c4.o < 2 ? c4.lo[d] : P4OOB; }
            c4.pa = lds + (4 * wm) * 1024 + vl16; c4.pw = lds + 16384 + (5 * wn) * 1024 + vl16;
        }
        p4_piece<0>(c4, 2 * P4STG); p4_piece<1>(c4, 2 * P4STG); p4_piece<2>(c4, 2 * P4STG); p4_piece<3>(c4, 2 * P4STG); p4_piece<4>(c4, 2 * P4STG);
        p4_piece<0>(c4, 3 * P4STG); p4_piece<1>(c4, 3 * P4STG); p4_piece<2>(c4, 3 * P4STG); p4_piece<3>(c4, 3 * P4STG); p4_piece<4>(c4, 3 * P4STG);
        {
            const lptr xo = lds + (qg * 2 + grp) * 9216;
#pragma unroll
            for (int d = 0; d < 4; ++d)
#pragma unroll
                for (int qt = 0; qt < 2; ++qt) *(LAS f32x4*)(xo + (d * 2 + qt) * 1024 + l * 16) = sel(grp == 0, r.o[4 + d][qt], r.o[d][qt]);
            *(LAS f32x4*)(xo + 8192 + l * 16) = (f32x4){m0, m1, l0, l1};
            asm volatile("s_waitcnt lgkmcnt(0)" ::: "memory"); BARRIER();
            const lcptr xi = lds + (qg * 2 + (grp ^ 1)) * 9216;
            const f32x4 st = *(const LAS f32x4*)(xi + 8192 + l * 16);
            const float M0 = __builtin_fmaxf(m0, st[0]), M1 = __builtin_fmaxf(m1, st[1]);
            const float wa0 = __builtin_amdgcn_exp2f(m0 - M0), wb0 = __builtin_amdgcn_exp2f(st[0] - M0), wa1 = __builtin_amdgcn_exp2f(m1 - M1), wb1 = __builtin_amdgcn_exp2f(st[1] - M1);
            const float i0 = __builtin_amdgcn_rcpf(wa0 * l0 + wb0 * st[2]), i1 = __builtin_amdgcn_rcpf(wa1 * l1 + wb1 * st[3]);
            const float ca0 = wa0 * i0, cb0 = wb0 * i0, ca1 = wa1 * i1, cb1 = wb1 * i1;
#pragma unroll
            for (int up = 0; up < 2; ++up)
#pragma unroll
                for (int qt = 0; qt < 2; ++qt) {
                    const f32x4 pa = *(const LAS f32x4*)(xi + ((2 * up) * 2 + qt) * 1024 + l * 16), pb = *(const LAS f32x4*)(xi + ((2 * up + 1) * 2 + qt) * 1024 + l * 16);
                    const f32x4 oa = sel(grp == 0, r.o[2 * up][qt], r.o[4 + 2 * up][qt]), ob = sel(grp == 0, r.o[2 * up + 1][qt], r.o[4 + 2 * up + 1][qt]);
                    const float ca = qt ? ca1 : ca0, cb = qt ? cb1 : cb0;
                    st16_sc1(sws, pack8(oa * ca + pa * cb, ob * ca + pb * cb), vl16, OFF_CTX + ((8 * (15 - j) + 2 * qg + qt) * 128 + 4 * h + 2 * grp + up) * 1024);
                }
        }
    }
    STAMP(8);
    {
        asm volatile("s_waitcnt vmcnt(8) lgkmcnt(0)" ::: "memory"); BARRIER();
        P4Regs r4;
        unsigned sb = 2 * P4STG, sp = P4STG;
        p4_rd<0, 0, 0>(c4, r4, sb); p4_rd<0, 0, 1>(c4, r4, sb); p4_rd<0, 0, 2>(c4, r4, sb); p4_rd<0, 0, 3>(c4, r4, sb); p4_rd<0, 0, 4>(c4, r4, sb); p4_rd<0, 0, 5>(c4, r4, sb); p4_rd<0, 0, 6>(c4, r4, sb);
        SBAR();
        p4_piece<0>(c4, 0); p4_piece<1>(c4, 0); p4_piece<2>(c4, 0); p4_piece<3>(c4, 0); p4_piece<4>(c4, 0);
        p4_piece<0>(c4, P4STG); p4_piece<1>(c4, P4STG); p4_piece<2>(c4, P4STG);
        const int l = lane_id();
        const int rbk = (xcd & 1) * 8 + (bi >> 2), cb = bi & 3;
        const unsigned cidx = FL_CTX + (rbk < 8 ? rbk : 15 - rbk) * 32 + 8 * c4.q + (l & 7);
        f32x4 accg[10], accs[10], gt[3];
        P4Xch xg;
        const int row0 = 128 * rbk + 32 * (w >> 1) + (l & 15), col0 = 160 * cb + 80 * (w & 1) + 4 * (l >> 4);
        u32x2 xv[3];
        {
            const rsrc_t sx = mk_rsrc(x, 2048 * 640 * 2);
#pragma unroll
            for (int k = 0; k < 3; ++k) { const int i = c4.o + 4 * k < 10 ? c4.o + 4 * k : c4.o;
                asm volatile("buffer_load_dwordx2 %0, %1, %2, 0 offen" : "=v"(xv[k]) : "v"((unsigned)((row0 + (i & 1) * 16) * 640 + col0 + (i >> 1) * 16) * 2), "s"(sx) : "memory"); }
        }
        u64 seen = 0;
        SBAR();
        STAMP(9);
        p4_step<P4W0, true, true, false, 2, -1, -1, -1, true>(c4, y, r4, xg, accg, sb, sp, 0, cidx, seen);
#pragma nounroll
        for (int t = 1; t < c4.ng; ++t) p4_step<8, true, true, false, 1, -1, -1, -1>(c4, y, r4, xg, accg, sb, sp, t, cidx, seen);
        p4_split(c4, accg, xg);
        p4_ssteps(c4, y, r4, xg, accs, sb, sp, seen, std::make_integer_sequence<int, 12>{});
        p4_step<8, true, false, false, 0, -1, -1, 0>(c4, y, r4, xg, accs, sb, sp, 0, 0, seen);
        p4_step<13, false, false, false, 0, -1, 5, -1>(c4, y, r4, xg, accs, sb, sp, 0, 0, seen);
        p4_step<9, false, false, false, 0, -1, -1, -1>(c4, y, r4, xg, accs, sb, sp, 0, 0, seen);
        p4_step<0, false, false, true, 0, -1, -1, -1>(c4, y, r4, xg, accs, sb, sp, 0, 0, seen);
        asm volatile("s_waitcnt vmcnt(0)" : "+v"(xg.rcv[0]), "+v"(xg.rcv[1]), "+v"(xg.rcv[2]), "+v"(xg.rcv[3]), "+v"(xg.rcv[4]), "+v"(xg.rcv[5]), "+v"(xg.rcv[6]), "+v"(xg.rcv[7]), "+v"(xg.rcv[8]),
                     "+v"(xv[0]), "+v"(xv[1]), "+v"(xv[2]) :: "memory");
        constexpr auto nine = std::make_integer_sequence<int, 9>{};
        P4Xch xs;
        p4_split(c4, accs, xs);
        p4_put<0>(c4, xs, OFF_XS); p4_put<1>(c4, xs, OFF_XS); p4_put<2>(c4, xs, OFF_XS); p4_put<3>(c4, xs, OFF_XS); p4_put<4>(c4, xs, OFF_XS);
        p4_put<5>(c4, xs, OFF_XS); p4_put<6>(c4, xs, OFF_XS); p4_put<7>(c4, xs, OFF_XS); p4_put<8>(c4, xs, OFF_XS);
        TS(0);
        if (!p4_tagged(c4, xg)) p4_refetch(c4, xg, OFF_XG, nine);
#pragma unroll
        for (int k = 0; k < 3; ++k) {
            gt[k] = p4_total(xg, k);
#pragma unroll
            for (int e = 0; e < 4; ++e) gt[k][e] = gt[k][e] * __builtin_amdgcn_rcpf(1.f + __builtin_amdgcn_exp2f(-1.4426950408889634f * gt[k][e]));
            PIN(gt[k]);
        }
        TS(1);
        p4_refetch(c4, xs, OFF_XS, nine);
        TS(2);
#pragma unroll
        for (int k = 0; k < 3; ++k) {
            if (k < 2 || c4.o < 2) { const int i = c4.o + 4 * k;
                const f32x4 sv = p4_total(xs, k);
                const f32x4 o = {bflo(xv[k][0]) + sv[0] * gt[k][0], bfhi(xv[k][0]) + sv[1] * gt[k][1], bflo(xv[k][1]) + sv[2] * gt[k][2], bfhi(xv[k][1]) + sv[3] * gt[k][3]};
                const u32x2 ov = {cvtpk(o[0], o[1]), cvtpk(o[2], o[3])};
                asm volatile("global_store_dwordx2 %0, %1, off sc0 sc1\n\ts_nop 1" :: "v"(out + (row0 + (i & 1) * 16) * 640 + col0 + (i >> 1) * 16), "v"(ov) : "memory"); }
        }
        asm volatile(".Lcode_z:");
        if (b == 0 && w == 0 && l == 0) __hip_atomic_store(y.flags + 4, y.token, __ATOMIC_RELAXED, __HIP_MEMORY_SCOPE_AGENT);
    }
    STAMP(11);
}

extern "C" void kernel_launch(void* const* d_in, const int* in_sizes, int n_in, void* d_out, int out_size, void* d_ws, size_t ws_size, hipStream_t stream) {
    (void)in_sizes; (void)n_in; (void)out_size; (void)ws_size;
    fused_kernel<<<256, 512, 0, stream>>>((const bf16_t*)d_in[0], (const bf16_t*)d_in[1], (const bf16_t*)d_in[2], (const bf16_t*)d_in[3], (const bf16_t*)d_in[4],
                                          (const bf16_t*)d_in[5], (const bf16_t*)d_in[7], (const bf16_t*)d_in[8], (const bf16_t*)d_in[9], (bf16_t*)d_out, (char*)d_ws);
}
```

```cpp
#include <hip/hip_runtime.h>
#include <stdint.h>
#include <utility>

typedef unsigned short bf16_t;
typedef short bf16x8 __attribute__((ext_vector_type(8)));
typedef short s16x4 __attribute__((ext_vector_type(4)));
typedef short v4i16_t __attribute__((ext_vector_type(4)));
typedef float f32x4 __attribute__((ext_vector_type(4)));
typedef float f32x2 __attribute__((ext_vector_type(2)));
typedef unsigned u32x4 __attribute__((ext_vector_type(4)));
typedef unsigned u32x2 __attribute__((ext_vector_type(2)));
typedef __bf16 bf16x2_t __attribute__((ext_vector_type(2)));
typedef unsigned short u16x2 __attribute__((ext_vector_type(2)));
typedef __amdgpu_buffer_rsrc_t rsrc_t;
typedef unsigned long long u64;
#define LAS __attribute__((address_space(3)))
#define GAS __attribute__((address_space(1)))
typedef LAS char* lptr;
typedef LAS const char* lcptr;
typedef GAS u64 gu64;
#define DI __device__ __forceinline__
#define SBAR() __builtin_amdgcn_sched_barrier(0)
#define PIN(x) asm volatile("" : "+v"(x))
#define MF16(a, b, c) __builtin_amdgcn_mfma_f32_16x16x32_bf16(a, b, c, 0, 0, 0)
#define WAITV(n) asm volatile("s_waitcnt vmcnt(" #n ")" ::: "memory")
#define BARRIER() do { asm volatile("" ::: "memory"); __builtin_amdgcn_s_barrier(); asm volatile("" ::: "memory"); } while (0)

#define SPOS 1

constexpr unsigned OFF_FLAGS = 0;
constexpr unsigned OFF_XN = 65536;
constexpr unsigned OFF_K = OFF_XN + 2621440;
constexpr unsigned OFF_V = OFF_K + 16777216;
constexpr unsigned OFF_ACT = OFF_V + 16777216 + 8388608;
constexpr unsigned OFF_CTX = OFF_ACT + 7077888;
constexpr unsigned OFF_WP = OFF_CTX + 16777216;
constexpr unsigned OFF_ST = OFF_WP + 182 * 40 * 1024;
constexpr unsigned OFF_XG = OFF_ST + 65536;
constexpr unsigned OFF_XS = OFF_XG + 25165824;
constexpr unsigned WS_BYTES = OFF_XS + 25165824;
constexpr unsigned FL_P0 = 0, FL_KV = 256, FL_ACT = 512, FL_CTX = 768;

DI unsigned cvtpk(float lo, float hi) { f32x2 v = {lo, hi}; bf16x2_t b = __builtin_convertvector(v, bf16x2_t); return __builtin_bit_cast(unsigned, b); }
DI u32x4 pack8(f32x4 a, f32x4 b) { u32x4 w = {cvtpk(a[0], a[1]), cvtpk(a[2], a[3]), cvtpk(b[0], b[1]), cvtpk(b[2], b[3])}; return w; }
DI float bflo(unsigned u) { return __uint_as_float(u << 16); }
DI float bfhi(unsigned u) { return __uint_as_float(u & 0xffff0000u); }
DI s16x4 vtr(lcptr p) { return __builtin_bit_cast(s16x4, __builtin_amdgcn_ds_read_tr16_b64_v4i16((LAS v4i16_t*)p)); }
DI bf16x8 cat(s16x4 a, s16x4 b) { return (bf16x8){a[0], a[1], a[2], a[3], b[0], b[1], b[2], b[3]}; }
DI bf16x8 ld128(lcptr p) { return *(const LAS bf16x8*)p; }
DI bf16x8 asbf(u32x4 v) { return __builtin_bit_cast(bf16x8, v); }
DI rsrc_t mk_rsrc(const void* p, unsigned bytes) { return __builtin_amdgcn_make_buffer_rsrc((void*)p, (short)0, (int)bytes, 0x00020000); }
DI void dma16(rsrc_t r, unsigned voff, unsigned soff, unsigned ldsoff) {
    asm volatile("s_mov_b32 m0, %0\n\ts_nop 3\n\tbuffer_load_dwordx4 %1, %2, %3 offen lds" ::"s"(ldsoff), "v"(voff), "s"(r), "s"(soff) : "memory");
}
#define HS " sc1"
DI void dma16h(rsrc_t r, unsigned voff, unsigned soff, unsigned ldsoff) {
    asm volatile("s_mov_b32 m0, %0\n\ts_nop 3\n\tbuffer_load_dwordx4 %1, %2, %3 offen" HS " lds" ::"s"(ldsoff), "v"(voff), "s"(r), "s"(soff) : "memory");
}
DI void dma16x4(rsrc_t r, unsigned voff, unsigned soff, unsigned ldsoff) {
    asm volatile("s_mov_b32 m0, %0\n\ts_nop 3\n\tbuffer_load_dwordx4 %1, %2, %3 offen" HS " lds\n\tbuffer_load_dwordx4 %1, %2, %3 offen offset:1024" HS " lds\n\t"
                 "buffer_load_dwordx4 %1, %2, %3 offen offset:2048" HS " lds\n\tbuffer_load_dwordx4 %1, %2, %3 offen offset:3072" HS " lds" ::"s"(ldsoff), "v"(voff), "s"(r), "s"(soff) : "memory");
}
DI void touch(const void* p, unsigned ldsoff) { asm volatile("s_mov_b32 m0, %0\n\ts_nop 3\n\tglobal_load_lds_dword %1, off" ::"s"(ldsoff), "v"(p) : "memory"); }
DI void st16_sc1(rsrc_t r, u32x4 v, unsigned voff, unsigned soff) { asm volatile("s_nop 4\n\tbuffer_store_dwordx4 %0, %1, %2, %3 offen sc1\n\ts_nop 1" ::"v"(v), "v"(voff), "s"(r), "s"(soff) : "memory"); }
template <int OFF> DI void st16_sc1o(rsrc_t r, u32x4 v, unsigned voff, unsigned soff) { asm volatile("s_nop 4\n\tbuffer_store_dwordx4 %0, %1, %2, %3 offen offset:%4 sc1\n\ts_nop 1" ::"v"(v), "v"(voff), "s"(r), "s"(soff), "i"(OFF) : "memory"); }
template <class T> DI T sel(bool c, T a, T b) { return c ? a : b; }
DI int lane_id() { int l; asm volatile("v_mbcnt_lo_u32_b32 %0, -1, 0\n\tv_mbcnt_hi_u32_b32 %0, -1, %0" : "=v"(l)); return l; }
DI void set_flag(gu64* flags, unsigned idx, u64 token) { __hip_atomic_store(flags + idx * 8, token, __ATOMIC_RELAXED, __HIP_MEMORY_SCOPE_AGENT); }
DI u64 poll_flag(gu64* flags, unsigned idx) { return __hip_atomic_load(flags + idx * 8, __ATOMIC_RELAXED, __HIP_MEMORY_SCOPE_AGENT); }
DI void spin_flags(gu64* flags, unsigned idx, u64 token) {
    const unsigned long long t0 = __builtin_amdgcn_s_memrealtime();
    for (;;) {
        __builtin_amdgcn_s_sleep(1);
        const u64 v = poll_flag(flags, idx);
        if (__all(v == token)) break;
        if (__builtin_amdgcn_s_memrealtime() - t0 > 2000000ull) break;
    }
}
DI u64 poll_asm(gu64* flags, unsigned idx) { u64 v; asm volatile("global_load_dwordx2 %0, %1, off sc1" : "=v"(v) : "v"(flags + idx * 8) : "memory"); return v; }
DI void check_flags(gu64* flags, unsigned idx, u64 seen, u64 token) { if (!__all(seen == token)) spin_flags(flags, idx, token); }

struct Sync { gu64* flags; u64 token; int b, h, j, w; };

struct P2Ctx { rsrc_t sws, swq, sw1; unsigned vl16, vwq, vw1; unsigned sxo, swqo, sw1o, skv, t0s, t0l, t1s; int w; lcptr xb; lcptr wq[4]; lcptr wf[2]; };
DI void p2_dma1(const P2Ctx& c, int st, int ks, int i) {
    if (i < 4) dma16h(c.sws, c.vl16, c.sxo + (i >> 1) * 20480 + (i & 1) * 1024 + ks * 2048, st * 32768 + (4 * c.w + i) * 1024);
    else if (i < 6) dma16(c.swq, c.vwq, c.swqo + (i - 4) * 128 + ks * 524288, 65536 + st * 24576 + (i - 4) * 8192 + c.w * 1024);
    else dma16(c.sw1, c.vw1, c.sw1o + ks * (64 * 3456), 65536 + st * 24576 + 16384 + c.w * 1024);
}

struct P1Ctx { rsrc_t sx, sw, sws; unsigned vx[2], vw[2]; unsigned sxo, swo; int w; lcptr xb[2]; lcptr wb[4]; const bf16_t *wo, *w2, *wq, *w1; lcptr lds; };
DI void pack_fetch(const P1Ctx& c, int b) {
    const int l = lane_id(), gw = b * 8 + c.w, pu = gw / 10, pq = gw - pu * 10;
    const rsrc_t sp = mk_rsrc(pu < 128 ? c.wo : c.w2, pu < 128 ? 4096 * 640 * 2 : 1728 * 640 * 2);
    const unsigned so = gw < 1820 ? (unsigned)((pu < 128 ? pu : pu - 128) * 32 * 1280 + pq * 128) : 0xF0000000u;
#pragma unroll
    for (int i = 0; i < 4; ++i) dma16(sp, (l >> 3) * 1280 + (l & 7) * 16, so + i * 8 * 1280, 131072 + c.w * 4096 + i * 1024);
}
DI void pack_store(const P1Ctx& c, int b) {
    const int l = lane_id(), g = l >> 4, q = (l & 15) >> 2, gw = b * 8 + c.w, pu = gw / 10, pq = gw - pu * 10;
    lcptr p = c.lds + 131072 + c.w * 4096 + (4 * g + q) * 128 + (l & 3) * 8;
    bf16x8 fr[4];
#pragma unroll
    for (int i = 0; i < 4; ++i) fr[i] = cat(vtr(p + i * 32), vtr(p + i * 32 + 16 * 128));
    if (gw < 1820) {
#pragma unroll
        for (int i = 0; i < 4; ++i) st16_sc1(c.sws, __builtin_bit_cast(u32x4, fr[i]), l * 16, OFF_WP + (pu * 40 + pq * 4 + i) * 1024);
    }
}
DI void p1_dma1(const P1Ctx& c, int st, int ks, int i) {
    if (i < 4) dma16(c.sx, c.vx[i & 1], c.sxo + i * 8192 + ks * 128, st * 32768 + (4 * c.w + i) * 1024);
    else dma16(c.sw, c.vw[i & 1], c.swo + (i - 4) * 65536 + ks * 524288, 65536 + st * 32768 + (4 * c.w + (i - 4)) * 1024);
}
DI void p1_dma(const P1Ctx& c, int st, int ks) {
#pragma unroll
    for (int i = 0; i < 8; ++i) p1_dma1(c, st, ks, i);
}
template <int ST, int UP, int I> DI void p1_rdw(const P1Ctx& c, bf16x8 (&wf)[4], s16x4 (&wl)[4]) {
    constexpr int nt = I >> 1, rd = I & 1;
    const s16x4 v = vtr(c.wb[nt] + ST * 32768 + UP * 4096 + rd * 512);
    if constexpr (rd == 0) wl[nt] = v; else wf[nt] = cat(wl[nt], v);
}
template <int ST, int G> DI bf16x8 p1_rdx(const P1Ctx& c) { return ld128(c.xb[G >> 3] + ST * 32768 + (G & 7) * 2048); }
template <int ST, int KSC, int K> DI bool p1_is(int ks) { if constexpr (KSC >= 0) return KSC == K; else if constexpr ((K & 1) != ST || K > 5) return false; else return ks == K; }
template <bool ISV, int ST, int KSC, int G> DI void p1_group(const P1Ctx& c, const P2Ctx& c2, const Sync& y, const int ks, u64& seen, f32x4 (&acc)[4][8], bf16x8 (&wA)[4], bf16x8 (&wB)[4], s16x4 (&wl)[4], bf16x8 (&xr)[4]) {
    constexpr int up = G >> 3, mt = G & 7;
    constexpr bool NEXT = KSC != 7;
    if constexpr (G == 13) {
        if (p1_is<ST, KSC, 0>(ks)) asm volatile("s_waitcnt vmcnt(3) lgkmcnt(0)" ::: "memory");
        else if (p1_is<ST, KSC, 1>(ks) || p1_is<ST, KSC, 3>(ks)) asm volatile("s_waitcnt vmcnt(1) lgkmcnt(0)" ::: "memory");
        else if (p1_is<ST, KSC, 5>(ks) || p1_is<ST, KSC, 7>(ks)) asm volatile("s_waitcnt vmcnt(4) lgkmcnt(0)" ::: "memory");
        else asm volatile("s_waitcnt vmcnt(0) lgkmcnt(0)" ::: "memory");
        if constexpr (!ISV && KSC == 6) { if (y.w == 0) { const int l = lane_id() & 31; check_flags(y.flags, FL_P0 + (l < 16 ? 16 * y.j + l : 16 * (15 - y.j) + (l - 16)), seen, y.token); } }
        BARRIER();
        if constexpr (!ISV) {
            if (p1_is<ST, KSC, 1>(ks)) { if (y.w == 0 && lane_id() == 0) set_flag(y.flags, FL_P0 + y.b, y.token); }
        }
        SBAR();
    }
    const bf16x8 x = xr[G & 3];
#pragma unroll
    for (int nt = 0; nt < 4; ++nt) {
        const bf16x8 wv = up == 0 ? wA[nt] : wB[nt];
        if constexpr (ISV) acc[nt][mt] = MF16(x, wv, acc[nt][mt]); else acc[nt][mt] = MF16(wv, x, acc[nt][mt]);
        if (nt == 0) {
            if constexpr (G + 3 < 16) xr[(G + 3) & 3] = p1_rdx<ST, G + 3>(c);
            else if constexpr (NEXT) xr[(G + 3) & 3] = p1_rdx<ST ^ 1, G + 3 - 16>(c);
        }
        if constexpr (G >= 2 && G < 6) { if (nt == 1) p1_rdw<ST, 1, 2 * (G - 2)>(c, wB, wl); if (nt == 2) p1_rdw<ST, 1, 2 * (G - 2) + 1>(c, wB, wl); }
        if constexpr (G >= 13 && NEXT) {
            if (nt == 1) p1_rdw<ST ^ 1, 0, 3 * (G - 13)>(c, wA, wl);
            if (nt == 2) p1_rdw<ST ^ 1, 0, 3 * (G - 13) + 1>(c, wA, wl);
            if constexpr (G < 15) { if (nt == 3) p1_rdw<ST ^ 1, 0, 3 * (G - 13) + 2>(c, wA, wl); }
        }
        if (nt == SPOS) {
            if constexpr (G >= 13) { constexpr int p = G - 13;
                if constexpr (KSC < 0) p1_dma1(c, ST, ks + 2, p);
                else if constexpr (KSC == 6) p2_dma1(c2, 0, 0, p);
                else p2_dma1(c2, 1, 1, p);
            } else if constexpr (G <= 4) { constexpr int p = G + 3;
                if constexpr (KSC == 7) { if constexpr (p < 7) p2_dma1(c2, 0, 0, p); }
                else { if (!p1_is<ST, KSC, 0>(ks)) p1_dma1(c, ST ^ 1, ks + 1, p); }
            }
        }
        SBAR();
    }
    if constexpr (G == 6) {
        if (p1_is<ST, KSC, 1>(ks)) {
            const int l = lane_id(), L = (c.w & 3) * 64 + l;
            const char* pb = (L >= 160 && L < 240 && y.h < 27) ? (const char*)c.w1 + (size_t)(80 * y.j + L - 160) * 3456 + y.h * 128
                                                               : (const char*)c.wq + (size_t)(80 * y.j + ((L < 160 ? L : 0) >> 1)) * 8192 + y.h * 256 + (L & 1) * 128;
            touch(pb, 131072 + c.w * 256);
        }
        if (p1_is<ST, KSC, 3>(ks)) {
            const int l = lane_id(), gw = y.b * 8 + c.w < 1820 ? y.b * 8 + c.w : 0, pu = gw / 10, pq = gw - pu * 10;
            const char* pc = (const char*)(pu < 128 ? c.wo : c.w2) + (size_t)((pu < 128 ? pu : pu - 128) * 32 + (l & 31)) * 1280 + pq * 128;
            touch(pc, 131072 + c.w * 256);
        }
        if constexpr (!ISV && KSC == 6) { if (y.w == 0) { const int l = lane_id() & 31; seen = poll_flag(y.flags, FL_P0 + (l < 16 ? 16 * y.j + l : 16 * (15 - y.j) + (l - 16))); } }
        if (p1_is<ST, KSC, 5>(ks)) pack_fetch(c, y.b);
        if constexpr (KSC == 7) pack_store(c, y.b);
        SBAR();
    }
}
template <bool ISV, int ST, int KSC, int... Gs> DI void p1_step(const P1Ctx& c, const P2Ctx& c2, const Sync& y, const int ks, u64& seen, f32x4 (&acc)[4][8], bf16x8 (&wA)[4], bf16x8 (&wB)[4], s16x4 (&wl)[4], bf16x8 (&xr)[4], std::integer_sequence<int, Gs...>) {
    (p1_group<ISV, ST, KSC, Gs>(c, c2, y, ks, seen, acc, wA, wB, wl, xr), ...);
}
template <bool ISV> DI void p1_main(const P1Ctx& c, const P2Ctx& c2, const Sync& y, u32x4 (&kv)[16]) {
    f32x4 acc[4][8];
#pragma unroll
    for (int a = 0; a < 4; ++a)
#pragma unroll
        for (int b = 0; b < 8; ++b) acc[a][b] = (f32x4){0.f, 0.f, 0.f, 0.f};
    bf16x8 wA[4], wB[4], xr[4]; s16x4 wl[4];
    u64 seen = 0;
    asm volatile("" ::: "memory");
    p1_rdw<0, 0, 0>(c, wA, wl); p1_rdw<0, 0, 1>(c, wA, wl); p1_rdw<0, 0, 2>(c, wA, wl); p1_rdw<0, 0, 3>(c, wA, wl);
    p1_rdw<0, 0, 4>(c, wA, wl); p1_rdw<0, 0, 5>(c, wA, wl); p1_rdw<0, 0, 6>(c, wA, wl); p1_rdw<0, 0, 7>(c, wA, wl);
    xr[0] = p1_rdx<0, 0>(c); xr[1] = p1_rdx<0, 1>(c); xr[2] = p1_rdx<0, 2>(c);
    SBAR();
    constexpr auto seq = std::make_integer_sequence<int, 16>{};
#pragma nounroll
    for (int ks = 0; ks < 6; ks += 2) {
        p1_step<ISV, 0, -1>(c, c2, y, ks, seen, acc, wA, wB, wl, xr, seq);
        p1_step<ISV, 1, -1>(c, c2, y, ks + 1, seen, acc, wA, wB, wl, xr, seq);
    }
    p1_step<ISV, 0, 6>(c, c2, y, 6, seen, acc, wA, wB, wl, xr, seq);
    p1_step<ISV, 1, 7>(c, c2, y, 7, seen, acc, wA, wB, wl, xr, seq);
#pragma unroll
    for (int a = 0; a < 8; ++a)
#pragma unroll
        for (int b = 0; b < 2; ++b) {
            if constexpr (ISV) kv[a * 2 + b] = pack8(acc[(a & 1) * 2 + b][(a >> 1) * 2], acc[(a & 1) * 2 + b][(a >> 1) * 2 + 1]);
            else kv[a * 2 + b] = pack8(acc[2 * b][a], acc[2 * b + 1][a]);
        }
}

template <bool HASF, int ST, int I> DI bf16x8 p2_rdw(const P2Ctx& c) {
    constexpr int NW = HASF ? 6 : 4, up = I / NW, n = I % NW;
    lcptr p = (n < 4 ? c.wq[n & 3] : c.wf[n & 1]) + ST * 24576 + up * 4096;
    return cat(vtr(p), vtr(p + 512));
}
template <int ST, int UP, int MT> DI bf16x8 p2_rdx(const P2Ctx& c) { return ld128(c.xb + ST * 32768 + MT * 2048 + UP * 1024); }
template <int I> DI void p2_kvst(const P2Ctx& c, const u32x4 (&kv)[16], bool isv) {
    st16_sc1(c.sws, kv[I], c.vl16, c.skv + (isv ? (unsigned)(((I >> 3) * 16 + ((I >> 2) & 1) * 8 + (I & 3)) * 1024) : (unsigned)((I >> 1) * 4096 + (I & 1) * 1024)));
}
template <bool HASF, int IV> DI void p2_extras(const P2Ctx& c, const u32x4 (&kv)[16], bool isv) {
    if constexpr (IV < 6) { p2_kvst<4 + 2 * IV>(c, kv, isv); p2_kvst<5 + 2 * IV>(c, kv, isv); }
}
struct P2Bias { u32x2 a, b; const bf16_t* p; };
template <bool HASF, int KS, int G> DI void p2_group(const P2Ctx& c, const Sync& y, P2Bias& bs, u64& seen, f32x4 (&acc)[6][4], bf16x8 (&wr)[4], bf16x8 (&x0)[4], bf16x8 (&x1)[4], const u32x4 (&kv)[16], bool isv) {
    constexpr int NW = HASF ? 6 : 4, NG = 2 * NW, ST = KS & 1, up = G / NW, n = G % NW;
    constexpr int NP = HASF ? 7 : 6;
    constexpr bool LAST = (KS == 9);
    if constexpr (G == NG - 3) {
        if constexpr (KS == 0) asm volatile("s_waitcnt vmcnt(0) lgkmcnt(0)" ::: "memory");
        else if constexpr (HASF && KS == 6) asm volatile("s_waitcnt vmcnt(4) lgkmcnt(0)" ::: "memory");
        else if constexpr (KS <= 6) asm volatile("s_waitcnt vmcnt(2) lgkmcnt(0)" ::: "memory");
        else asm volatile("s_waitcnt vmcnt(0) lgkmcnt(0)" ::: "memory");
        if constexpr (KS == 9) { if (y.w == 0) check_flags(y.flags, FL_KV + y.h * 8 + (lane_id() & 7), seen, y.token); }
        BARRIER();
        if constexpr (KS == 7) { if (y.w == 0 && lane_id() == 0) set_flag(y.flags, FL_KV + y.h * 8 + y.j, y.token); }
        if constexpr (KS == 8) { if (y.w == 0) seen = poll_flag(y.flags, FL_KV + y.h * 8 + (lane_id() & 7)); }
        SBAR();
    }
    const bf16x8 wcur = wr[G & 3];
#pragma unroll
    for (int mt = 0; mt < 4; ++mt) {
        acc[n][mt] = MF16(wcur, up == 0 ? x0[mt] : x1[mt], acc[n][mt]);
        if (mt == 0) {
            if constexpr (G + 3 < NG) wr[(G + 3) & 3] = p2_rdw<HASF, ST, G + 3>(c);
            else if constexpr (!LAST) wr[(G + 3) & 3] = p2_rdw<HASF, ST ^ 1, G + 3 - NG>(c);
        }
        if (mt == 1) {
            if constexpr (G < 4) x1[G] = p2_rdx<ST, 1, G>(c);
            else if constexpr (G >= NG - 3 && !LAST) x0[G - (NG - 3)] = p2_rdx<ST ^ 1, 0, G - (NG - 3)>(c);
        }
        if (mt == 2) { if constexpr (G == NG - 1 && !LAST) x0[3] = p2_rdx<ST ^ 1, 0, 3>(c); }
        if (mt == SPOS) {
            if constexpr (G >= NG - 3) { if constexpr (KS + 2 < 10) p2_dma1(c, ST, KS + 2, G - (NG - 3)); }
            else if constexpr (G + 3 < NP) { if constexpr (KS + 1 < 10) p2_dma1(c, ST ^ 1, KS + 1, G + 3); }
        }
        if constexpr (LAST && G >= NG - 3) {
            constexpr int p0 = (G - (NG - 3)) * 4;
            if (p0 + mt < 4) dma16h(c.sws, c.vl16, c.t0s + (p0 + mt) * 1024, c.t0l + (p0 + mt) * 1024);
            else if (p0 + mt < 8) { if (c.w < 4) dma16h(c.sws, c.vl16, c.t1s + (p0 + mt - 4) * 1024, 65536 + c.t0l + (p0 + mt - 4) * 1024); }
        }
        SBAR();
    }
    if constexpr (G == NP - 3 && KS >= 1) { p2_extras<HASF, KS - 1>(c, kv, isv); SBAR(); }
    if constexpr (HASF && KS == 6 && G == 5) { asm volatile("global_load_dwordx2 %0, %2, off\n\tglobal_load_dwordx2 %1, %2, off offset:32" : "=&v"(bs.a), "=&v"(bs.b) : "v"(bs.p) : "memory"); SBAR(); }
}
template <bool HASF, int KS, int... Gs> DI void p2_step(const P2Ctx& c, const Sync& y, P2Bias& bs, u64& seen, f32x4 (&acc)[6][4], bf16x8 (&wr)[4], bf16x8 (&x0)[4], bf16x8 (&x1)[4], const u32x4 (&kv)[16], bool isv, std::integer_sequence<int, Gs...>) {
    (p2_group<HASF, KS, Gs>(c, y, bs, seen, acc, wr, x0, x1, kv, isv), ...);
}
template <bool HASF, int... KSs> DI void p2_main(const P2Ctx& c, const Sync& y, P2Bias& bs, f32x4 (&acc)[6][4], const u32x4 (&kv)[16], bool isv, std::integer_sequence<int, KSs...>) {
    bf16x8 wr[4], x0[4], x1[4];
    u64 seen = 0;
    asm volatile("" ::: "memory");
    wr[0] = p2_rdw<HASF, 0, 0>(c); wr[1] = p2_rdw<HASF, 0, 1>(c); wr[2] = p2_rdw<HASF, 0, 2>(c);
    x0[0] = p2_rdx<0, 0, 0>(c); x0[1] = p2_rdx<0, 0, 1>(c); x0[2] = p2_rdx<0, 0, 2>(c); x0[3] = p2_rdx<0, 0, 3>(c);
    SBAR();
    (p2_step<HASF, KSs>(c, y, bs, seen, acc, wr, x0, x1, kv, isv, std::make_integer_sequence<int, (HASF ? 12 : 8)>{}), ...);
}
DI float mish(float h) {
    const float t = __builtin_amdgcn_exp2f(__builtin_fminf(h, 20.f) * 1.4426950408889634f);
    const float n = t * (t + 2.f);
    return h * n * __builtin_amdgcn_rcpf(n + 2.f);
}

constexpr int PD = 8;
#define PRIOM 1
#define PRIOV 0
#define LATE_STEP 7
#define LATE_OFF 2
struct AttnRegs { f32x4 o[8][2]; f32x4 ls[2]; f32x4 s[4][2]; bf16x8 q[2][4]; u32x4 pk[2][2]; f32x4 negm[2]; bf16x8 ones; bf16x8 op[32]; };
template <int I> DI void a_read(AttnRegs& r, lcptr vp, lcptr kp) {
    if constexpr (I < 16) r.op[I] = ld128(vp + I * 1024);
    else if constexpr (I < 32) r.op[I] = ld128(kp + (((I - 16) & 3) * 4 + ((I - 16) >> 2)) * 1024);
}
struct Piece { rsrc_t sws; unsigned vl16, soff, ldso; bool on; };
template <int P, bool COND> DI void piece(const Piece& d) {
    if (!COND || d.on) asm volatile("s_mov_b32 m0, %0\n\ts_nop 3\n\tbuffer_load_dwordx4 %1, %2, %3 offen offset:%4" HS " lds" ::"s"(d.ldso), "v"(d.vl16), "s"(d.sws), "s"(d.soff), "i"(P * 1024) : "memory");
}
template <int I, int DMA> DI void a_slot(AttnRegs& r, lcptr vp, lcptr kp, const Piece& late) {
    if constexpr (I < 16) { constexpr int ks = I >> 3, dt = I & 7;
        r.o[dt][0] = MF16(r.op[I], asbf(r.pk[0][ks]), r.o[dt][0]); r.o[dt][1] = MF16(r.op[I], asbf(r.pk[1][ks]), r.o[dt][1]);
        if constexpr (dt == 3) { r.ls[0] = MF16(r.ones, asbf(r.pk[0][ks]), r.ls[0]); r.ls[1] = MF16(r.ones, asbf(r.pk[1][ks]), r.ls[1]); } }
    else { constexpr int u = (I - 16) >> 2, kt = (I - 16) & 3;
        r.s[kt][0] = MF16(r.op[I], r.q[0][u], u == 0 ? r.negm[0] : r.s[kt][0]); r.s[kt][1] = MF16(r.op[I], r.q[1][u], u == 0 ? r.negm[1] : r.s[kt][1]); }
    a_read<I + PD>(r, vp, kp);
    SBAR();
    if constexpr (DMA != 0 && (I % LATE_STEP) == LATE_OFF && I / LATE_STEP < 4) { piece<I / LATE_STEP, DMA == 2>(late); SBAR(); }
}
template <int B, int DMA, int... Is> DI void a_run(AttnRegs& r, lcptr vp, lcptr kp, const Piece& late, std::integer_sequence<int, Is...>) { (a_slot<B + Is, DMA>(r, vp, kp, late), ...); }
template <int B, int... Is> DI void a_pre(AttnRegs& r, lcptr vp, lcptr kp, std::integer_sequence<int, Is...>) { (a_read<B + Is>(r, vp, kp), ...); }

DI unsigned pkmax(unsigned a, unsigned b) { return __builtin_bit_cast(unsigned, __builtin_elementwise_max(__builtin_bit_cast(u16x2, a), __builtin_bit_cast(u16x2, b))); }
DI unsigned hmax(unsigned v) { const unsigned lo = v & 0xFFFFu, hi = v >> 16; return lo > hi ? lo : hi; }
template <int Q> DI void a_exp_quarter(AttnRegs& r, unsigned& x0, unsigned& x1) {
    constexpr int ks = Q >> 1, qt = Q & 1;
    float e[8];
#pragma unroll
    for (int i = 0; i < 4; ++i) { e[i] = __builtin_amdgcn_exp2f(r.s[2 * ks][qt][i]); e[4 + i] = __builtin_amdgcn_exp2f(r.s[2 * ks + 1][qt][i]); }
    const u32x4 p = {cvtpk(e[0], e[1]), cvtpk(e[2], e[3]), cvtpk(e[4], e[5]), cvtpk(e[6], e[7])};
    r.pk[qt][ks] = p;
    const unsigned m = pkmax(pkmax(p[0], p[1]), pkmax(p[2], p[3]));
    if constexpr (Q == 0) x0 = m; else if constexpr (Q == 1) x1 = m; else if constexpr (qt == 0) x0 = pkmax(x0, m); else x1 = pkmax(x1, m);
}
template <bool DMA> DI void a_exp_pack(AttnRegs& r, unsigned& x0, unsigned& x1, const Piece& early) {
    if constexpr (DMA) { piece<0, false>(early); SBAR(); }
    a_exp_quarter<0>(r, x0, x1);
    if constexpr (DMA) { SBAR(); piece<1, false>(early); SBAR(); }
    a_exp_quarter<1>(r, x0, x1);
    if constexpr (DMA) { SBAR(); piece<2, false>(early); SBAR(); }
    a_exp_quarter<2>(r, x0, x1);
    if constexpr (DMA) { SBAR(); piece<3, false>(early); SBAR(); }
    a_exp_quarter<3>(r, x0, x1);
}
DI float xmax4(float v) {
    const u32x2 a = __builtin_amdgcn_permlane16_swap(__float_as_uint(v), __float_as_uint(v), false, false);
    v = __builtin_fmaxf(__uint_as_float(a[0]), __uint_as_float(a[1]));
    const u32x2 c = __builtin_amdgcn_permlane32_swap(__float_as_uint(v), __float_as_uint(v), false, false);
    return __builtin_fmaxf(__uint_as_float(c[0]), __uint_as_float(c[1]));
}
DI float xsum4(float v) {
    const u32x2 a = __builtin_amdgcn_permlane16_swap(__float_as_uint(v), __float_as_uint(v), false, false);
    v = __uint_as_float(a[0]) + __uint_as_float(a[1]);
    const u32x2 c = __builtin_amdgcn_permlane32_swap(__float_as_uint(v), __float_as_uint(v), false, false);
    return __uint_as_float(c[0]) + __uint_as_float(c[1]);
}
constexpr unsigned P_HI = 0x5D80u, P_LO = 0x2180u;

struct AttnCtx { rsrc_t sws; unsigned vl16, kbase, vbase, ctxl; int nL, grp, qg, j, rowrel, swap_s, first_s; lcptr kl, vl, ql; };
struct AttnSm { float m0, m1; };
DI void a_slotfull(const AttnCtx& c, const Sync& y, AttnRegs& r, AttnSm& z, const int s) {
    const int nL = c.nL, grp = c.grp;
    const bool SWAP = (s == c.swap_s), FIN = (s == c.swap_s + 1), FIRST = (s == c.first_s) || FIN;
    const int tcur = s < nL ? s : 2 * s - nL + grp;
    Piece early, late;
    {
        const int s2 = s + 2, s1 = s + 1;
        const bool eK = grp == 1 && s2 < nL;
        const int te = eK ? s2 : (s1 < nL ? s1 : 2 * s1 - nL + grp), tl = 2 * s2 - nL + grp;
        early.sws = c.sws; early.vl16 = c.vl16; late.sws = c.sws; late.vl16 = c.vl16;
        early.on = eK || (s1 <= 16 && (grp == 0 || s1 >= nL));
        early.soff = (eK ? c.kbase : c.vbase) + te * 16384 + c.qg * 4096; early.ldso = (eK ? 0 : 65536) + (te & 3) * 16384 + c.qg * 4096;
        late.on = s2 <= 16 && s2 >= nL;
        late.soff = c.kbase + tl * 16384 + c.qg * 4096; late.ldso = (tl & 3) * 16384 + c.qg * 4096;
    }
    const int snext = s + 1, tnext = snext < nL ? snext : 2 * snext - nL + grp;
    const lcptr vp = c.vl + (tcur & 3) * 16384, kp = c.kl + (tnext & 3) * 16384;
    u64 seenA = 0;
    if (s == 16) seenA = poll_asm(y.flags, FL_ACT + 32 * y.w + (lane_id() & 31));
    a_pre<0>(r, vp, kp, std::make_integer_sequence<int, PD>{});
    SBAR();
    if (SWAP) {
        asm volatile("ds_read_b128 %0, %8\n\tds_read_b128 %1, %8 offset:1024\n\tds_read_b128 %2, %8 offset:2048\n\tds_read_b128 %3, %8 offset:3072\n\t"
                     "ds_read_b128 %4, %8 offset:4096\n\tds_read_b128 %5, %8 offset:5120\n\tds_read_b128 %6, %8 offset:6144\n\tds_read_b128 %7, %8 offset:7168\n\ts_waitcnt lgkmcnt(0)"
                     : "=&v"(r.q[0][0]), "=&v"(r.q[0][1]), "=&v"(r.q[0][2]), "=&v"(r.q[0][3]), "=&v"(r.q[1][0]), "=&v"(r.q[1][1]), "=&v"(r.q[1][2]), "=&v"(r.q[1][3])
                     : "v"(c.ql) : "memory");
    }
    if (FIN) {
        const float i0 = __builtin_amdgcn_rcpf(r.ls[0][0]), i1 = __builtin_amdgcn_rcpf(r.ls[1][0]);
        const unsigned c1o = c.ctxl + 131072;
        st16_sc1o<0>(c.sws, pack8(r.o[0][0] * i0, r.o[1][0] * i0), c.vl16, c.ctxl); st16_sc1o<1024>(c.sws, pack8(r.o[2][0] * i0, r.o[3][0] * i0), c.vl16, c.ctxl);
        st16_sc1o<2048>(c.sws, pack8(r.o[4][0] * i0, r.o[5][0] * i0), c.vl16, c.ctxl); st16_sc1o<3072>(c.sws, pack8(r.o[6][0] * i0, r.o[7][0] * i0), c.vl16, c.ctxl);
        st16_sc1o<0>(c.sws, pack8(r.o[0][1] * i1, r.o[1][1] * i1), c.vl16, c1o); st16_sc1o<1024>(c.sws, pack8(r.o[2][1] * i1, r.o[3][1] * i1), c.vl16, c1o);
        st16_sc1o<2048>(c.sws, pack8(r.o[4][1] * i1, r.o[5][1] * i1), c.vl16, c1o); st16_sc1o<3072>(c.sws, pack8(r.o[6][1] * i1, r.o[7][1] * i1), c.vl16, c1o);
#pragma unroll
        for (int d = 0; d < 8; ++d) { r.o[d][0] = (f32x4){0.f, 0.f, 0.f, 0.f}; r.o[d][1] = (f32x4){0.f, 0.f, 0.f, 0.f}; }
        r.ls[0] = (f32x4){0.f, 0.f, 0.f, 0.f}; r.ls[1] = (f32x4){0.f, 0.f, 0.f, 0.f};
    }
    {
        const bool heavy = (grp == 1) || (s >= nL);
        const int qb = heavy ? 15 - c.j : c.j, kb = 64 * tcur - 128 * qb - 32 * c.qg;
        if (kb >= -32) {
            const float NI = -__builtin_inff();
            const f32x4 ninf = {NI, NI, NI, NI};
            f32x4 s00 = r.s[0][0], s10 = r.s[1][0], s20 = r.s[2][0], s30 = r.s[3][0], s01 = r.s[0][1], s11 = r.s[1][1], s21 = r.s[2][1], s31 = r.s[3][1];
            const int dq = c.rowrel;
            if (kb > 0) { s00 = ninf; s10 = ninf; s20 = ninf; s30 = ninf; s01 = ninf; s11 = ninf; s21 = ninf; s31 = ninf; }
            else if (kb == 0) {
#pragma unroll
                for (int i = 0; i < 4; ++i) { s00[i] = sel(i > dq, NI, s00[i]); s11[i] = sel(i > dq, NI, s11[i]); }
                s10 = ninf; s20 = ninf; s30 = ninf; s21 = ninf; s31 = ninf;
            } else {
#pragma unroll
                for (int i = 0; i < 4; ++i) { s20[i] = sel(i > dq, NI, s20[i]); s31[i] = sel(i > dq, NI, s31[i]); }
                s30 = ninf;
            }
            r.s[0][0] = s00; r.s[1][0] = s10; r.s[2][0] = s20; r.s[3][0] = s30; r.s[0][1] = s01; r.s[1][1] = s11; r.s[2][1] = s21; r.s[3][1] = s31;
        }
    }
    unsigned a0, a1;
    if (early.on) dma16x4(c.sws, c.vl16, early.soff, early.ldso);
    a_exp_pack<false>(r, a0, a1, early);
    a0 = hmax(a0); a1 = hmax(a1);
    const unsigned lo = FIRST ? P_LO : 0u;
    if (__builtin_expect(__any(a0 > P_HI || a1 > P_HI || a0 < lo || a1 < lo), 0)) {
        float x0 = r.s[0][0][0], x1 = r.s[0][1][0];
#pragma unroll
        for (int kt = 0; kt < 4; ++kt)
#pragma unroll
            for (int i = 0; i < 4; ++i) { x0 = __builtin_fmaxf(x0, r.s[kt][0][i]); x1 = __builtin_fmaxf(x1, r.s[kt][1][i]); }
        x0 = xmax4(x0); x1 = xmax4(x1);
        if (!FIRST) { x0 = __builtin_fmaxf(x0, 0.f); x1 = __builtin_fmaxf(x1, 0.f);
            const float f0 = __builtin_amdgcn_exp2f(-x0), f1 = __builtin_amdgcn_exp2f(-x1);
#pragma unroll
            for (int d = 0; d < 8; ++d) { r.o[d][0] *= f0; r.o[d][1] *= f1; }
            r.ls[0] *= f0; r.ls[1] *= f1; }
        z.m0 += x0; z.m1 += x1;
        r.negm[0] = (f32x4){-z.m0, -z.m0, -z.m0, -z.m0}; r.negm[1] = (f32x4){-z.m1, -z.m1, -z.m1, -z.m1};
#pragma unroll
        for (int kt = 0; kt < 4; ++kt) { r.s[kt][0] -= x0; r.s[kt][1] -= x1; }
        a_exp_pack<false>(r, a0, a1, early);
    }
    if (SWAP) {
        z.m0 = 0.f; z.m1 = 0.f;
        r.negm[0] = (f32x4){0.f, 0.f, 0.f, 0.f}; r.negm[1] = (f32x4){0.f, 0.f, 0.f, 0.f};
    }
    SBAR();
    if (FIN) { if (early.on) WAITV(12); else WAITV(8); } else { if (early.on) WAITV(4); else WAITV(0); }
    if (s == 16) { asm volatile("" : "+v"(seenA)); check_flags(y.flags, FL_ACT + 32 * y.w + (lane_id() & 31), seenA, y.token); }
    BARRIER(); SBAR();
    __builtin_amdgcn_s_setprio(PRIOM);
    a_run<0, 2>(r, vp, kp, late, std::make_integer_sequence<int, 16>{});
    if (s < 16) a_run<16, 2>(r, vp, kp, late, std::make_integer_sequence<int, 16>{});
    __builtin_amdgcn_s_setprio(PRIOV);
    SBAR();
    if (late.on) WAITV(4); else WAITV(0);
    BARRIER(); SBAR();
}

struct Lean { unsigned e_soff, e_lds, l_soff, l_lds, voff, koff; };
template <bool LATE> DI void a_slot_lean(const AttnCtx& c, AttnRegs& r, AttnSm& z, const Lean& st) {
    const lcptr vp = c.vl + st.voff, kp = c.kl + st.koff;
    Piece early, late;
    early.sws = c.sws; early.vl16 = c.vl16; early.soff = st.e_soff; early.ldso = st.e_lds; early.on = true;
    late.sws = c.sws; late.vl16 = c.vl16; late.soff = st.l_soff; late.ldso = st.l_lds; late.on = true;
    a_pre<0>(r, vp, kp, std::make_integer_sequence<int, PD>{});
    SBAR();
    unsigned a0, a1;
    a_exp_pack<true>(r, a0, a1, early);
    if (__builtin_expect(__any(hmax(pkmax(a0, a1)) > P_HI), 0)) {
        float x0 = 0.f, x1 = 0.f;
#pragma unroll
        for (int kt = 0; kt < 4; ++kt)
#pragma unroll
            for (int i = 0; i < 4; ++i) { x0 = __builtin_fmaxf(x0, r.s[kt][0][i]); x1 = __builtin_fmaxf(x1, r.s[kt][1][i]); }
        x0 = xmax4(x0); x1 = xmax4(x1);
        const float f0 = __builtin_amdgcn_exp2f(-x0), f1 = __builtin_amdgcn_exp2f(-x1);
#pragma unroll
        for (int d = 0; d < 8; ++d) { r.o[d][0] *= f0; r.o[d][1] *= f1; }
        r.ls[0] *= f0; r.ls[1] *= f1;
        z.m0 += x0; z.m1 += x1;
        r.negm[0] = (f32x4){-z.m0, -z.m0, -z.m0, -z.m0}; r.negm[1] = (f32x4){-z.m1, -z.m1, -z.m1, -z.m1};
#pragma unroll
        for (int kt = 0; kt < 4; ++kt) { r.s[kt][0] -= x0; r.s[kt][1] -= x1; }
        a_exp_pack<false>(r, a0, a1, early);
    }
    SBAR();
    WAITV(4);
    BARRIER(); SBAR();
    __builtin_amdgcn_s_setprio(PRIOM);
    a_run<0, LATE ? 1 : 0>(r, vp, kp, late, std::make_integer_sequence<int, 32>{});
    __builtin_amdgcn_s_setprio(PRIOV);
    SBAR();
    if constexpr (LATE) WAITV(4); else WAITV(0);
    BARRIER(); SBAR();
}

#define P4W0 11
constexpr unsigned P4STG = 36864, P4TAG = 0x3FFu, P4OOB = 0xF0000000u;
struct P4Ctx { rsrc_t sws; unsigned vl16, ga, gw, gas, gws, dw0, dw1, dw2, lw, tag, so[3], so2[3], lo[3], lo2[3]; int w, q, o, ng, ra, rw; lcptr pa, pw; };
struct P4Regs { bf16x8 xa[2][2], wa[2][5]; };
struct P4Xch { f32x4 own[3]; u32x4 snd[9], rcv[9]; };
DI void mfa(f32x4& acc, bf16x8 a, bf16x8 b) { asm volatile("v_mfma_f32_16x16x32_bf16 %0, %1, %2, %0" : "+v"(acc) : "v"(a), "v"(b)); }
DI void mfz(f32x4& acc, bf16x8 a, bf16x8 b) { asm volatile("v_mfma_f32_16x16x32_bf16 %0, %1, %2, 0" : "=&v"(acc) : "v"(a), "v"(b)); }
template <int OFF> DI void dma_sc1(const P4Ctx& c, unsigned soff, unsigned ldso) {
    asm volatile("s_mov_b32 m0, %0\n\ts_nop 3\n\tbuffer_load_dwordx4 %1, %2, %3 offen offset:%4 sc1 lds" ::"s"(ldso), "v"(c.vl16), "s"(c.sws), "s"(soff), "i"(OFF) : "memory");
}
template <int P> DI void p4_piece(P4Ctx& c, unsigned st) {
    if constexpr (P == 0) dma_sc1<0>(c, c.ga, st + c.w * 2048);
    else if constexpr (P == 1) { dma_sc1<1024>(c, c.ga, st + c.w * 2048); ++c.ra; c.ga = c.ra == c.ng ? c.gas : c.ga + 2048; }
    else if constexpr (P == 2) dma_sc1<0>(c, c.gw + c.dw0, st + c.lw);
    else if constexpr (P == 3) dma_sc1<0>(c, c.gw + c.dw1, st + c.lw + 8192);
    else { if (c.w < 4) dma_sc1<0>(c, c.gw + c.dw2, st + c.lw + 16384); ++c.rw; c.gw = c.rw == c.ng ? c.gws : c.gw + 81920; }
}
template <int B, int UP, int I> DI void p4_rd(const P4Ctx& c, P4Regs& r, unsigned st) {
    if constexpr (I == 0) r.wa[B][0] = ld128(c.pw + st + UP * 10240);
    else if constexpr (I <= 2) r.xa[B][I - 1] = ld128(c.pa + st + (I - 1) * 2048 + UP * 1024);
    else if constexpr (I < 7) r.wa[B][I - 2] = ld128(c.pw + st + UP * 10240 + (I - 2) * 1024);
}
template <int N> DI void p4_put(const P4Ctx& c, const P4Xch& x, unsigned xoff) { st16_sc1o<(N % 3) * 1024>(c.sws, x.snd[N], c.vl16, (N % 3 == 2 ? c.so2[N / 3] : c.so[N / 3]) + xoff); }
template <int N> DI void p4_get(const P4Ctx& c, P4Xch& x, unsigned xoff) {
    asm volatile("buffer_load_dwordx4 %0, %1, %2, %3 offen offset:%4 sc1" : "=v"(x.rcv[N]) : "v"(c.vl16), "s"(c.sws), "s"((N % 3 == 2 ? c.lo2[N / 3] : c.lo[N / 3]) + xoff), "i"((N % 3) * 1024) : "memory");
}
template <int O> DI void p4_split(const f32x4 (&acc)[10], unsigned tag, P4Xch& x) {
#pragma unroll
    for (int k = 0; k < 3; ++k) x.own[k] = acc[O + 4 * k < 10 ? O + 4 * k : O];
#pragma unroll
    for (int n = 0; n < 9; ++n) { const int i = ((O + 1 + n / 3) & 3) + 4 * (n % 3);
        const u32x4 v = __builtin_bit_cast(u32x4, acc[i < 10 ? i : 0]);
#pragma unroll
        for (int e = 0; e < 4; ++e) x.snd[n][e] = (v[e] & ~P4TAG) | tag; }
}
DI void p4_split(const P4Ctx& c, f32x4 (&a)[10], P4Xch& x) {
    asm volatile("s_nop 7\n\ts_nop 7\n\ts_nop 7" : "+v"(a[0]), "+v"(a[1]), "+v"(a[2]), "+v"(a[3]), "+v"(a[4]), "+v"(a[5]), "+v"(a[6]), "+v"(a[7]), "+v"(a[8]), "+v"(a[9]) :: "memory");
    switch (c.o) { case 0: p4_split<0>(a, c.tag, x); break; case 1: p4_split<1>(a, c.tag, x); break; case 2: p4_split<2>(a, c.tag, x); break; default: p4_split<3>(a, c.tag, x); break; }
}
DI bool p4_tagged(const P4Ctx& c, const P4Xch& x) {
    unsigned a = 0, b = 0;
#pragma unroll
    for (int n = 0; n < 9; ++n)
#pragma unroll
        for (int e = 0; e < 4; ++e) { if (n % 3 == 2) b |= x.rcv[n][e] ^ c.tag; else a |= x.rcv[n][e] ^ c.tag; }
    return __all(((a | (c.o < 2 ? b : 0u)) & P4TAG) == 0);
}
template <int... Ns> DI void p4_refetch(const P4Ctx& c, P4Xch& x, unsigned xoff, std::integer_sequence<int, Ns...>) {
    const unsigned long long t0 = __builtin_amdgcn_s_memrealtime();
    for (;;) {
        (p4_get<Ns>(c, x, xoff), ...);
        asm volatile("s_waitcnt vmcnt(0)" : "+v"(x.rcv[0]), "+v"(x.rcv[1]), "+v"(x.rcv[2]), "+v"(x.rcv[3]), "+v"(x.rcv[4]), "+v"(x.rcv[5]), "+v"(x.rcv[6]), "+v"(x.rcv[7]), "+v"(x.rcv[8]) :: "memory");
        if (p4_tagged(c, x)) break;
        if (__builtin_amdgcn_s_memrealtime() - t0 > 2000000ull) break;
        __builtin_amdgcn_s_sleep(1);
    }
}
DI f32x4 p4_untag(u32x4 v) { return __builtin_bit_cast(f32x4, v & ~P4TAG); }
DI f32x4 p4_total(const P4Xch& x, int k) { return (x.own[k] + p4_untag(x.rcv[k])) + (p4_untag(x.rcv[3 + k]) + p4_untag(x.rcv[6 + k])); }
template <int B, bool ZC, bool RD, int DMA, int PUT, int GET, int... Is> DI void p4_half(P4Ctx& c, P4Regs& r, P4Xch& x, f32x4 (&acc)[10], unsigned rs, unsigned ds, std::integer_sequence<int, Is...>) {
    auto one = [&](auto ic) {
        constexpr int I = decltype(ic)::value;
        if constexpr (ZC) mfz(acc[I], r.wa[B][I >> 1], r.xa[B][I & 1]); else mfa(acc[I], r.wa[B][I >> 1], r.xa[B][I & 1]);
        if constexpr (RD) p4_rd<B ^ 1, B ^ 1, I>(c, r, rs);
        if constexpr (DMA == 1) { if constexpr (I == 1) p4_piece<0>(c, ds); if constexpr (I == 5) p4_piece<1>(c, ds); if constexpr (I == 9) p4_piece<2>(c, ds); }
        if constexpr (DMA == 2) { if constexpr (I == 3) p4_piece<3>(c, ds); if constexpr (I == 7) p4_piece<4>(c, ds); }
        if constexpr (PUT >= 0 && B == 0 && I == 5) p4_put<PUT>(c, x, OFF_XG);
        if constexpr (GET >= 0 && (I & 1) && GET + (I >> 1) < 9) p4_get<GET + (I >> 1)>(c, x, OFF_XG);
        SBAR();
    };
    (one(std::integral_constant<int, Is>{}), ...);
}
template <int WAITN, bool RA, bool RB, bool LAST, int EV, int PUT, int GETA, int GETB, bool ZC = false> DI void p4_step(P4Ctx& c, const Sync& y, P4Regs& r, P4Xch& x, f32x4 (&acc)[10], unsigned& sb, unsigned& sp, int t, unsigned fidx, u64& seen) {
    constexpr auto seq = std::make_integer_sequence<int, 10>{};
    const unsigned sn = sb == 3 * P4STG ? 0 : sb + P4STG;
    p4_half<0, ZC, true, RA ? 2 : 0, PUT, GETA>(c, r, x, acc, sb, sp, seq);
    if constexpr (!LAST) {
        asm volatile("s_waitcnt vmcnt(%0) lgkmcnt(0)" :: "i"(WAITN) : "memory");
        if constexpr (EV == 1) { if (t == c.ng - 4 && c.w == 0) { asm volatile("s_waitcnt vmcnt(4)" : "+v"(seen) :: "memory"); check_flags(y.flags, fidx, seen, y.token); } }
        BARRIER();
        if constexpr (EV == 1) {
            if (c.w == 0 && t == c.ng - 5) seen = poll_asm(y.flags, fidx);
        }
        if constexpr (EV == 2) { if (c.w == 0 && lane_id() == 0) set_flag(y.flags, FL_CTX + y.j * 32 + y.h, y.token); }
        SBAR();
    }
    p4_half<1, false, !LAST, RB ? 1 : 0, PUT, GETB>(c, r, x, acc, sn, sb, seq);
    sp = sb; sb = sn;
}

template <int... Is> DI void p4_ssteps(P4Ctx& c, const Sync& y, P4Regs& r, P4Xch& x, f32x4 (&acc)[10], unsigned& sb, unsigned& sp, u64& seen, std::integer_sequence<int, Is...>) {
    (p4_step<8 + (Is >= 2 && Is <= 10) + (Is >= 1 && Is <= 9) + (Is <= 8), true, true, false, 0, (Is <= 8 ? Is : -1), -1, -1, Is == 0>(c, y, r, x, acc, sb, sp, 0, 0, seen), ...);
}

__global__ __launch_bounds__(512, 2) void fused_kernel(const bf16_t* __restrict__ x, const bf16_t* __restrict__ ckv, const bf16_t* __restrict__ wq, const bf16_t* __restrict__ wuk,
                                                       const bf16_t* __restrict__ wuv, const bf16_t* __restrict__ wo, const bf16_t* __restrict__ w1, const bf16_t* __restrict__ b1,
                                                       const bf16_t* __restrict__ w2, bf16_t* __restrict__ out, char* __restrict__ ws) {
    __shared__ __attribute__((aligned(1024))) char smem[163840];
    {
        u64 pc; unsigned off;
        asm volatile("s_getpc_b64 %0\n.Lcode_a:\n\ts_mov_b32 m0, 0x20000\n\tv_lshlrev_b32 %1, 3, %2\n\tv_lshl_add_u32 %1, %3, 12, %1\n\tv_min_u32 %1, .Lcode_z-.Lcode_a-4, %1\n\t"
                     "global_load_lds_dword %1, %0" : "=&s"(pc), "=&v"(off) : "v"(threadIdx.x), "s"(blockIdx.x >> 3) : "memory");
        SBAR();
    }
    const lptr lds = (lptr)smem;
    const int b = blockIdx.x, xcd = b & 7, bi = b >> 3, h = xcd * 4 + (bi >> 3), j = bi & 7;
    const int w = __builtin_amdgcn_readfirstlane(threadIdx.x >> 6);
    const rsrc_t sws = mk_rsrc(ws, WS_BYTES);
    Sync y;
    y.flags = (gu64*)(ws + OFF_FLAGS); y.b = b; y.h = h; y.j = j; y.w = w;
    u64 epoch;
    asm volatile("s_load_dwordx2 %0, %1, 0x20" : "=s"(epoch) : "s"(ws) : "memory");
    u32x4 kv[16];
    P1Ctx c1;
    P2Ctx c2;
    const bool isv = (w & 3) >= 2;
    {
        const int l = lane_id(), g = l >> 4, q = (l & 15) >> 2;
        {
            const int nb = w >> 1;
            c1.w = w; c1.sws = sws; c1.wo = wo; c1.w2 = w2; c1.wq = wq; c1.w1 = w1; c1.lds = lds;
            c1.sx = mk_rsrc(ckv, 2048 * 512 * 2);
            c1.sw = mk_rsrc(nb < 2 ? wuk : wuv, 512 * 4096 * 2);
#pragma unroll
            for (int par = 0; par < 2; ++par) {
                c1.vx[par] = (l >> 3) * 1024 + (((l & 7) ^ (4 * par + (l >> 4))) * 16);
                c1.vw[par] = (l >> 3) * 8192 + (((l & 7) ^ ((((l >> 4) & 1) | (par << 1)) << 1)) * 16);
            }
            c1.sxo = (256 * j + 32 * w) * 1024;
            c1.swo = (32 * (w & 1)) * 8192 + (h * 128 + 64 * (nb & 1)) * 2;
            const int wn = w & 3, wm = w >> 2, f = (l & 15) >> 1, s = ((q >> 1) & 1) | ((g & 1) << 1);
            c1.xb[0] = lds + wm * 16384 + (l & 15) * 128 + ((g ^ f) * 16);
            c1.xb[1] = lds + wm * 16384 + (l & 15) * 128 + (((g ^ f) ^ 4) * 16);
#pragma unroll
            for (int nt = 0; nt < 4; ++nt) c1.wb[nt] = lds + 65536 + wn * 8192 + (8 * g + q) * 128 + ((nt ^ s) * 32) + (l & 3) * 8;
        }
        p1_dma(c1, 0, 0);
        u32x4 xo0, xo1;
        {
            const rsrc_t sxi = mk_rsrc(x, 2048 * 640 * 2);
            const unsigned vx = (l & 7) * 1280 + (l >> 3) * 16, so0 = b * 8 * 1280 + w * 128, so1 = b * 8 * 1280 + (8 + (w & 1)) * 128;
            asm volatile("s_nop 4\n\tbuffer_load_dwordx4 %0, %2, %3, %4 offen\n\tbuffer_load_dwordx4 %1, %2, %3, %5 offen" : "=&v"(xo0), "=&v"(xo1) : "v"(vx), "s"(sxi), "s"(so0), "s"(so1) : "memory");
        }
        BARRIER();
        p1_dma(c1, 1, 1);
        asm volatile("s_waitcnt lgkmcnt(0)" : "+s"(epoch) :: "memory");
        y.token = epoch + 1;
        asm volatile("s_waitcnt vmcnt(8)" : "+v"(xo0), "+v"(xo1) :: "memory");
        {
            const int i = l - 16;
            const char* pc = (const char*)ckv + (size_t)(256 * j + 64 * (h & 3) + 8 * w + ((l & 15) >> 1)) * 1024 + (2 + (l & 1)) * 128;
            const char* pw = (const char*)((i & 2) ? wuv : wuk) + (size_t)(128 + 16 * j + 2 * w + ((i >> 2) & 1)) * 8192 + h * 256 + (i & 1) * 128;
            const int m = l - 24;
            const char* pb = m < 10 ? (const char*)wq : (m < 15 ? (const char*)w1 : (m < 25 ? (const char*)wo : (m < 30 ? (const char*)w2 : (m < 34 ? (const char*)wuk : (const char*)wuv))));
            const unsigned k = m < 10 ? m : (m < 15 ? m - 10 : (m < 25 ? m - 15 : (m < 30 ? m - 25 : 4 + ((m - 30) & 3))));
            const unsigned lim = (m >= 10 && m < 15) || (m >= 25 && m < 30) ? 640u * 1728u * 2u - 128u : 0xFFFFFFFFu, po = k * 524288u + bi * 16384u;
            const char* pt = pb + (po < lim ? po : lim);
            touch(w == 7 && l >= 24 && l < 62 ? pt : (l >= 16 && l < 24 ? pw : pc), 131072 + w * 256);
        }
        float v0[8], v1[8];
#pragma unroll
        for (int e = 0; e < 4; ++e) { v0[2 * e] = bflo(xo0[e]); v0[2 * e + 1] = bfhi(xo0[e]); v1[2 * e] = bflo(xo1[e]); v1[2 * e + 1] = bfhi(xo1[e]); }
        auto red8 = [](float t) { t += __uint_as_float(__builtin_amdgcn_update_dpp(0u, __float_as_uint(t), 0x128, 0xf, 0xf, false)); return xsum4(t); };
        {
            const float m0 = red8(((v0[0] + v0[1]) + (v0[2] + v0[3])) + ((v0[4] + v0[5]) + (v0[6] + v0[7]))) * (1.f / 64.f);
            const float m1 = red8(((v1[0] + v1[1]) + (v1[2] + v1[3])) + ((v1[4] + v1[5]) + (v1[6] + v1[7]))) * (1.f / 64.f);
            float q0 = 0.f, q1 = 0.f;
#pragma unroll
            for (int e = 0; e < 8; ++e) { const float d0 = v0[e] - m0, d1 = v1[e] - m1; q0 += d0 * d0; q1 += d1 * d1; }
            q0 = red8(q0); q1 = red8(q1);
            if (l < 8) { *(LAS f32x2*)(lds + 162816 + l * 80 + w * 8) = (f32x2){m0, q0}; if (w < 2) *(LAS f32x2*)(lds + 162816 + l * 80 + 64 + w * 8) = (f32x2){m1, q1}; }
        }
        asm volatile("s_waitcnt lgkmcnt(0)" ::: "memory"); BARRIER();
        {
            f32x4 t[5];
#pragma unroll
            for (int i = 0; i < 5; ++i) t[i] = *(const LAS f32x4*)(lds + 162816 + (l & 7) * 80 + i * 16);
            float mean = 0.f;
#pragma unroll
            for (int i = 0; i < 5; ++i) mean += t[i][0] + t[i][2];
            mean *= 0.1f;
            float m2 = 0.f;
#pragma unroll
            for (int i = 0; i < 5; ++i) { const float d0 = t[i][0] - mean, d1 = t[i][2] - mean; m2 += (t[i][1] + t[i][3]) + 64.f * (d0 * d0 + d1 * d1); }
            const float rstd = 1.f / __builtin_sqrtf(m2 * (1.f / 640.f) + 1e-5f);
            const unsigned vo = (l >> 3) * 256 + (l & 7) * 16, xo = OFF_XN + (b >> 1) * 20480 + (b & 1) * 128;
            u32x4 o0, o1;
#pragma unroll
            for (int e = 0; e < 4; ++e) { o0[e] = cvtpk((v0[2 * e] - mean) * rstd, (v0[2 * e + 1] - mean) * rstd); o1[e] = cvtpk((v1[2 * e] - mean) * rstd, (v1[2 * e + 1] - mean) * rstd); }
            st16_sc1(sws, o0, vo, xo + w * 2048);
            if (w < 2) st16_sc1(sws, o1, vo, xo + (8 + w) * 2048);
        }
        {
            const int i = l - 32;
            const char* pc = (const char*)ckv + (size_t)(256 * j + 64 * (h & 3) + 8 * w + ((l & 31) >> 2)) * 1024 + (4 + (l & 3)) * 128;
            const char* pw = (const char*)((i & 2) ? wuv : wuk) + (size_t)(256 + 32 * j + 4 * w + ((i >> 2) & 3)) * 8192 + h * 256 + (i & 1) * 128;
            touch(l >= 48 && h < 27 ? (const char*)(b1 + 64 * h) : (l >= 32 && l < 48 ? pw : pc), 131072 + w * 256);
        }
        {
            c2.w = w; c2.sws = sws; c2.swq = mk_rsrc(wq, 640 * 4096 * 2); c2.sw1 = mk_rsrc(w1, 640 * 1728 * 2);
            c2.vl16 = l * 16;
            const int swz = ((l >> 4) & 1) | ((w & 1) << 1);
            c2.vwq = (l >> 3) * 8192 + (((l & 7) ^ (swz << 1)) * 16);
            c2.vw1 = (l >> 3) * 3456 + (((l & 7) ^ (swz << 1)) * 16);
            { const int mtl = 2 * w, mtg = mtl < 8 ? 8 * j + mtl : 8 * (15 - j) + (mtl - 8); c2.sxo = OFF_XN + mtg * 20480; }
            c2.swqo = (8 * w) * 8192 + h * 256;
            c2.sw1o = (8 * w) * 3456 + h * 128;
            c2.t0l = (w >> 2) * 16384 + (w & 3) * 4096; c2.t0s = OFF_K + (unsigned)h * 524288 + c2.t0l; c2.t1s = OFF_V + (unsigned)h * 524288 + (w & 3) * 4096;
            { const int wn = w & 3, wm = w >> 2;
              c2.skv = isv ? OFF_V + (unsigned)(h * 32 + 4 * j + 2 * wm) * 16384 + (wn - 2) * 4096 : OFF_K + (unsigned)((h * 128 + 16 * j + 8 * wm) * 4 + 2 * wn) * 1024; }
        }
    }
    if (isv) p1_main<true>(c1, c2, y, kv); else p1_main<false>(c1, c2, y, kv);
        p2_kvst<0>(c2, kv, isv); p2_kvst<1>(c2, kv, isv); p2_kvst<2>(c2, kv, isv); p2_kvst<3>(c2, kv, isv);
    const bool hasf = h < 27;
    AttnCtx c3;
    AttnRegs r;
    {
        const int l = lane_id(), g = l >> 4, q = (l & 15) >> 2;
        const int wn = w & 1, wm = w >> 1;
        const int s = ((q >> 1) & 1) | ((g & 1) << 1);
        c2.xb = lds + wm * 8192 + l * 16;
#pragma unroll
        for (int nt = 0; nt < 4; ++nt) c2.wq[nt] = lds + 65536 + wn * 8192 + (8 * g + q) * 128 + ((nt ^ s) * 32) + (l & 3) * 8;
#pragma unroll
        for (int ft = 0; ft < 2; ++ft) c2.wf[ft] = lds + 65536 + 16384 + (8 * g + q) * 128 + (((2 * wn + ft) ^ s) * 32) + (l & 3) * 8;
        f32x4 acc[6][4];
#pragma unroll
        for (int a = 0; a < 6; ++a)
#pragma unroll
            for (int m = 0; m < 4; ++m) acc[a][m] = (f32x4){0.f, 0.f, 0.f, 0.f};
        P2Bias bs; bs.p = b1 + (64 * h + 32 * wn + 4 * g); bs.a = (u32x2){0u, 0u}; bs.b = (u32x2){0u, 0u};
        if (hasf) p2_main<true>(c2, y, bs, acc, kv, isv, std::make_integer_sequence<int, 10>{}); else p2_main<false>(c2, y, bs, acc, kv, isv, std::make_integer_sequence<int, 10>{});
        constexpr float C2 = 0.08838834764831845f * 1.4426950408889634f;
#pragma unroll
        for (int mt = 0; mt < 4; ++mt)
#pragma unroll
            for (int a = 0; a < 2; ++a)
                *(LAS u32x4*)(lds + 98304 + ((4 * wm + mt) * 4 + 2 * wn + a) * 1024 + l * 16) = pack8(acc[2 * a][mt] * C2, acc[2 * a + 1][mt] * C2);
        u32x4 actf[4];
        if (hasf) {
            asm volatile("" : "+v"(bs.a), "+v"(bs.b));
            const u32x2 bA = bs.a, bB = bs.b;
            const f32x4 biasA = {bflo(bA[0]), bfhi(bA[0]), bflo(bA[1]), bfhi(bA[1])}, biasB = {bflo(bB[0]), bfhi(bB[0]), bflo(bB[1]), bfhi(bB[1])};
#pragma unroll
            for (int mt = 0; mt < 4; ++mt) {
                f32x4 hA = acc[4][mt] + biasA, hB = acc[5][mt] + biasB;
#pragma unroll
                for (int i = 0; i < 4; ++i) { hA[i] = mish(hA[i]); hB[i] = mish(hB[i]); }
                actf[mt] = pack8(hA, hB);
            }
        }
        asm volatile("s_waitcnt vmcnt(0) lgkmcnt(0)" ::: "memory"); BARRIER();
        const int grp = w >> 2, qg = w & 3;
        c3.sws = sws; c3.vl16 = l * 16; c3.kbase = OFF_K + (unsigned)h * 524288; c3.vbase = OFF_V + (unsigned)h * 524288;
        c3.ctxl = OFF_CTX + ((8 * j + 2 * qg) * 128 + 4 * h) * 1024;
        c3.nL = 2 * j + 2; c3.grp = grp; c3.qg = qg; c3.j = j; c3.rowrel = (l & 15) - 4 * g; c3.kl = lds + l * 16; c3.vl = lds + 65536 + l * 16;
        c3.ql = lds + 131072 + qg * 8192 + l * 16;
        if (hasf) {
#pragma unroll
            for (int mt = 0; mt < 4; ++mt) { const int mtl = 4 * wm + mt, mtg = mtl < 8 ? 8 * j + mtl : 8 * (15 - j) + (mtl - 8);
                st16_sc1(sws, actf[mt], l * 16, OFF_ACT + (mtg * 54 + 2 * h + wn) * 1024); }
        }
#pragma unroll
        for (int qt = 0; qt < 2; ++qt)
#pragma unroll
            for (int u = 0; u < 4; ++u) r.q[qt][u] = ld128(lds + 98304 + ((8 * grp + 2 * qg + qt) * 4 + u) * 1024 + l * 16);
    }
    P4Ctx c4;
    {
        const int l = lane_id();
        const int grp = c3.grp, qg = c3.qg;
        const unsigned vl16 = c3.vl16;
#pragma unroll
        for (int d = 0; d < 8; ++d) { r.o[d][0] = (f32x4){0.f, 0.f, 0.f, 0.f}; r.o[d][1] = (f32x4){0.f, 0.f, 0.f, 0.f}; }
        r.negm[0] = (f32x4){0.f, 0.f, 0.f, 0.f}; r.negm[1] = (f32x4){0.f, 0.f, 0.f, 0.f};
        AttnSm z; z.m0 = 0.f; z.m1 = 0.f;
        r.ls[0] = (f32x4){0.f, 0.f, 0.f, 0.f}; r.ls[1] = (f32x4){0.f, 0.f, 0.f, 0.f};
        r.ones = (bf16x8){0x3F80, 0x3F80, 0x3F80, 0x3F80, 0x3F80, 0x3F80, 0x3F80, 0x3F80}; PIN(r.ones);
        asm volatile("s_waitcnt lgkmcnt(0)" ::: "memory");
        BARRIER();
                a_pre<16>(r, c3.vl, c3.kl, std::make_integer_sequence<int, PD>{});
        if (grp == 1) BARRIER();
        { Piece none; none.on = false; none.sws = sws; none.vl16 = 0; none.soff = 0; none.ldso = 0; a_run<16, 0>(r, c3.vl, c3.kl, none, std::make_integer_sequence<int, 16>{}); }
        BARRIER();
        c3.swap_s = grp == 0 ? c3.nL - 1 : 99; c3.first_s = 0;
        asm volatile("" : "+s"(c3.swap_s), "+s"(c3.first_s));
        {
            const int nL = c3.nL;
            auto lean_init = [&](int s) {
                Lean st;
                const bool eK = grp == 1 && s + 2 < nL;
                const int t0 = s < nL ? s : 2 * s - nL + grp, t1 = s + 1 < nL ? s + 1 : 2 * s + 2 - nL + grp, t2 = s + 2 < nL ? s + 2 : 2 * s + 4 - nL + grp;
                const int te = eK ? t2 : t1;
                st.e_soff = (eK ? c3.kbase : c3.vbase) + te * 16384 + qg * 4096; st.e_lds = (eK ? 0 : 65536) + (te & 3) * 16384 + qg * 4096;
                st.l_soff = c3.kbase + t2 * 16384 + qg * 4096; st.l_lds = (t2 & 3) * 16384 + qg * 4096;
                st.voff = (t0 & 3) * 16384; st.koff = (t1 & 3) * 16384;
                return st;
            };
            int s = 0, phase = 0;
#pragma nounroll
            for (;;) {
                const int gend = phase == 0 ? 1 : (phase == 1 ? (grp == 0 ? nL + 1 : nL - 1) : 17);
#pragma nounroll
                while (s < gend) { a_slotfull(c3, y, r, z, s); ++s; }
                if (phase == 0) { if (w == 0 && l == 0) set_flag(y.flags, FL_ACT + j * 32 + h, y.token); }
                if (phase == 2 || s > 16) break;
                if (phase == 1 && s <= 14) {
                    Lean st = lean_init(s);
#pragma nounroll
                    do { a_slot_lean<true>(c3, r, z, st);
                         st.e_soff += 32768; st.e_lds ^= 32768; st.l_soff += 32768; st.l_lds ^= 32768; st.voff ^= 32768; st.koff ^= 32768; ++s; } while (s <= 14);
                }
                const int send = phase == 0 ? nL - 2 : 16;
                if (s < send) {
                    Lean st = lean_init(s);
#pragma nounroll
                    do { a_slot_lean<false>(c3, r, z, st);
                         st.e_soff += 16384; st.e_lds = (st.e_lds & ~0xC000u) | ((st.e_lds + 16384) & 0xC000u); st.voff = (st.voff + 16384) & 0xC000u; st.koff = (st.koff + 16384) & 0xC000u; ++s; } while (s < send);
                }
                ++phase;
            }
        }
        const float m0 = z.m0, m1 = z.m1, l0 = r.ls[0][0], l1 = r.ls[1][0];
        if (grp == 0) BARRIER();
                {
            const int q = xcd >> 1, rbk = (xcd & 1) * 8 + (bi >> 2), cb = bi & 3, wm = w >> 1, wn = w & 1;
            c4.sws = sws; c4.vl16 = vl16; c4.w = w; c4.q = q; c4.o = (q + 2 * wn) & 3; c4.ng = q == 3 ? 6 : 7; c4.ra = 0; c4.rw = 0;
            c4.ga = OFF_ACT + ((8 * rbk + w) * 54 + 14 * q) * 1024; c4.gas = OFF_CTX + ((8 * rbk + w) * 128 + 32 * q) * 1024;
            c4.gw = OFF_WP + ((128 + 14 * q) * 40 + 10 * cb) * 1024; c4.gws = OFF_WP + (32 * q * 40 + 10 * cb) * 1024;
            c4.dw0 = w * 1024; c4.dw1 = (w < 2 ? 8 + w : 38 + w) * 1024; c4.dw2 = (46 + w) * 1024; c4.lw = 16384 + w * 1024;
            c4.tag = (unsigned)y.token & P4TAG;
            const unsigned xb = (unsigned)(((xcd & 1) * 32 + bi) * 128 + w) * 3072;
#pragma unroll
            for (int d = 0; d < 3; ++d) { const int qq = (q + 1 + d) & 3;
                c4.so[d] = xb + qq * 98304 + q * 24576; c4.so2[d] = ((c4.o + 1 + d) & 3) < 2 ? c4.so[d] : P4OOB;
                c4.lo[d] = xb + q * 98304 + qq * 24576; c4.lo2[d] = c4.o < 2 ? c4.lo[d] : P4OOB; }
            c4.pa = lds + (4 * wm) * 1024 + vl16; c4.pw = lds + 16384 + (5 * wn) * 1024 + vl16;
        }
        p4_piece<0>(c4, 2 * P4STG); p4_piece<1>(c4, 2 * P4STG); p4_piece<2>(c4, 2 * P4STG); p4_piece<3>(c4, 2 * P4STG); p4_piece<4>(c4, 2 * P4STG);
        p4_piece<0>(c4, 3 * P4STG); p4_piece<1>(c4, 3 * P4STG); p4_piece<2>(c4, 3 * P4STG); p4_piece<3>(c4, 3 * P4STG); p4_piece<4>(c4, 3 * P4STG);
        {
            const lptr xo = lds + (qg * 2 + grp) * 9216;
#pragma unroll
            for (int d = 0; d < 4; ++d)
#pragma unroll
                for (int qt = 0; qt < 2; ++qt) *(LAS f32x4*)(xo + (d * 2 + qt) * 1024 + l * 16) = sel(grp == 0, r.o[4 + d][qt], r.o[d][qt]);
            *(LAS f32x4*)(xo + 8192 + l * 16) = (f32x4){m0, m1, l0, l1};
            asm volatile("s_waitcnt lgkmcnt(0)" ::: "memory"); BARRIER();
            const lcptr xi = lds + (qg * 2 + (grp ^ 1)) * 9216;
            const f32x4 st = *(const LAS f32x4*)(xi + 8192 + l * 16);
            const float M0 = __builtin_fmaxf(m0, st[0]), M1 = __builtin_fmaxf(m1, st[1]);
            const float wa0 = __builtin_amdgcn_exp2f(m0 - M0), wb0 = __builtin_amdgcn_exp2f(st[0] - M0), wa1 = __builtin_amdgcn_exp2f(m1 - M1), wb1 = __builtin_amdgcn_exp2f(st[1] - M1);
            const float i0 = __builtin_amdgcn_rcpf(wa0 * l0 + wb0 * st[2]), i1 = __builtin_amdgcn_rcpf(wa1 * l1 + wb1 * st[3]);
            const float ca0 = wa0 * i0, cb0 = wb0 * i0, ca1 = wa1 * i1, cb1 = wb1 * i1;
#pragma unroll
            for (int up = 0; up < 2; ++up)
#pragma unroll
                for (int qt = 0; qt < 2; ++qt) {
                    const f32x4 pa = *(const LAS f32x4*)(xi + ((2 * up) * 2 + qt) * 1024 + l * 16), pb = *(const LAS f32x4*)(xi + ((2 * up + 1) * 2 + qt) * 1024 + l * 16);
                    const f32x4 oa = sel(grp == 0, r.o[2 * up][qt], r.o[4 + 2 * up][qt]), ob = sel(grp == 0, r.o[2 * up + 1][qt], r.o[4 + 2 * up + 1][qt]);
                    const float ca = qt ? ca1 : ca0, cb = qt ? cb1 : cb0;
                    st16_sc1(sws, pack8(oa * ca + pa * cb, ob * ca + pb * cb), vl16, OFF_CTX + ((8 * (15 - j) + 2 * qg + qt) * 128 + 4 * h + 2 * grp + up) * 1024);
                }
        }
    }
    {
        asm volatile("s_waitcnt vmcnt(8) lgkmcnt(0)" ::: "memory"); BARRIER();
        P4Regs r4;
        unsigned sb = 2 * P4STG, sp = P4STG;
        p4_rd<0, 0, 0>(c4, r4, sb); p4_rd<0, 0, 1>(c4, r4, sb); p4_rd<0, 0, 2>(c4, r4, sb); p4_rd<0, 0, 3>(c4, r4, sb); p4_rd<0, 0, 4>(c4, r4, sb); p4_rd<0, 0, 5>(c4, r4, sb); p4_rd<0, 0, 6>(c4, r4, sb);
        SBAR();
        p4_piece<0>(c4, 0); p4_piece<1>(c4, 0); p4_piece<2>(c4, 0); p4_piece<3>(c4, 0); p4_piece<4>(c4, 0);
        p4_piece<0>(c4, P4STG); p4_piece<1>(c4, P4STG); p4_piece<2>(c4, P4STG);
        const int l = lane_id();
        const int rbk = (xcd & 1) * 8 + (bi >> 2), cb = bi & 3;
        const unsigned cidx = FL_CTX + (rbk < 8 ? rbk : 15 - rbk) * 32 + 8 * c4.q + (l & 7);
        f32x4 accg[10], accs[10], gt[3];
        P4Xch xg;
        const int row0 = 128 * rbk + 32 * (w >> 1) + (l & 15), col0 = 160 * cb + 80 * (w & 1) + 4 * (l >> 4);
        u32x2 xv[3];
        {
            const rsrc_t sx = mk_rsrc(x, 2048 * 640 * 2);
#pragma unroll
            for (int k = 0; k < 3; ++k) { const int i = c4.o + 4 * k < 10 ? c4.o + 4 * k : c4.o;
                asm volatile("buffer_load_dwordx2 %0, %1, %2, 0 offen" : "=v"(xv[k]) : "v"((unsigned)((row0 + (i & 1) * 16) * 640 + col0 + (i >> 1) * 16) * 2), "s"(sx) : "memory"); }
        }
        u64 seen = 0;
        SBAR();
        p4_step<P4W0, true, true, false, 2, -1, -1, -1, true>(c4, y, r4, xg, accg, sb, sp, 0, cidx, seen);
#pragma nounroll
        for (int t = 1; t < c4.ng; ++t) p4_step<8, true, true, false, 1, -1, -1, -1>(c4, y, r4, xg, accg, sb, sp, t, cidx, seen);
        p4_split(c4, accg, xg);
        p4_ssteps(c4, y, r4, xg, accs, sb, sp, seen, std::make_integer_sequence<int, 12>{});
        p4_step<8, true, false, false, 0, -1, -1, 0>(c4, y, r4, xg, accs, sb, sp, 0, 0, seen);
        p4_step<13, false, false, false, 0, -1, 5, -1>(c4, y, r4, xg, accs, sb, sp, 0, 0, seen);
        p4_step<9, false, false, false, 0, -1, -1, -1>(c4, y, r4, xg, accs, sb, sp, 0, 0, seen);
        p4_step<0, false, false, true, 0, -1, -1, -1>(c4, y, r4, xg, accs, sb, sp, 0, 0, seen);
        asm volatile("s_waitcnt vmcnt(0)" : "+v"(xg.rcv[0]), "+v"(xg.rcv[1]), "+v"(xg.rcv[2]), "+v"(xg.rcv[3]), "+v"(xg.rcv[4]), "+v"(xg.rcv[5]), "+v"(xg.rcv[6]), "+v"(xg.rcv[7]), "+v"(xg.rcv[8]),
                     "+v"(xv[0]), "+v"(xv[1]), "+v"(xv[2]) :: "memory");
        constexpr auto nine = std::make_integer_sequence<int, 9>{};
        P4Xch xs;
        p4_split(c4, accs, xs);
        p4_put<0>(c4, xs, OFF_XS); p4_put<1>(c4, xs, OFF_XS); p4_put<2>(c4, xs, OFF_XS); p4_put<3>(c4, xs, OFF_XS); p4_put<4>(c4, xs, OFF_XS);
        p4_put<5>(c4, xs, OFF_XS); p4_put<6>(c4, xs, OFF_XS); p4_put<7>(c4, xs, OFF_XS); p4_put<8>(c4, xs, OFF_XS);
        if (!p4_tagged(c4, xg)) p4_refetch(c4, xg, OFF_XG, nine);
#pragma unroll
        for (int k = 0; k < 3; ++k) {
            gt[k] = p4_total(xg, k);
#pragma unroll
            for (int e = 0; e < 4; ++e) gt[k][e] = gt[k][e] * __builtin_amdgcn_rcpf(1.f + __builtin_amdgcn_exp2f(-1.4426950408889634f * gt[k][e]));
            PIN(gt[k]);
        }
        p4_refetch(c4, xs, OFF_XS, nine);
#pragma unroll
        for (int k = 0; k < 3; ++k) {
            if (k < 2 || c4.o < 2) { const int i = c4.o + 4 * k;
                const f32x4 sv = p4_total(xs, k);
                const f32x4 o = {bflo(xv[k][0]) + sv[0] * gt[k][0], bfhi(xv[k][0]) + sv[1] * gt[k][1], bflo(xv[k][1]) + sv[2] * gt[k][2], bfhi(xv[k][1]) + sv[3] * gt[k][3]};
                const u32x2 ov = {cvtpk(o[0], o[1]), cvtpk(o[2], o[3])};
                asm volatile("global_store_dwordx2 %0, %1, off sc0 sc1\n\ts_nop 1" :: "v"(out + (row0 + (i & 1) * 16) * 640 + col0 + (i >> 1) * 16), "v"(ov) : "memory"); }
        }
        asm volatile(".Lcode_z:");
        if (b == 0 && w == 0 && l == 0) __hip_atomic_store(y.flags + 4, y.token, __ATOMIC_RELAXED, __HIP_MEMORY_SCOPE_AGENT);
    }
}

extern "C" void kernel_launch(void* const* d_in, const int* in_sizes, int n_in, void* d_out, int out_size, void* d_ws, size_t ws_size, hipStream_t stream) {
    (void)in_sizes; (void)n_in; (void)out_size; (void)ws_size;
    fused_kernel<<<256, 512, 0, stream>>>((const bf16_t*)d_in[0], (const bf16_t*)d_in[1], (const bf16_t*)d_in[2], (const bf16_t*)d_in[3], (const bf16_t*)d_in[4],
                                          (const bf16_t*)d_in[5], (const bf16_t*)d_in[7], (const bf16_t*)d_in[8], (const bf16_t*)d_in[9], (bf16_t*)d_out, (char*)d_ws);
}
```
